```python
import torch

def _softmax(x, axis=-1):
    return torch.softmax(x, dim=axis)
def _gelu(x):
    return torch.nn.functional.gelu(x, approximate="tanh")
def _silu(x):
    return torch.nn.functional.silu(x)
def _sigmoid(x):
    return torch.sigmoid(x)
def _layernorm(x, axis=-1, eps=1e-5):
    m = x.mean(dim=axis, keepdim=True)
    v = x.var(dim=axis, keepdim=True, unbiased=False)
    return (x - m) / torch.sqrt(v + eps)
def _scatter_add(tgt, idx, src, axis=0):
    return tgt.clone().index_add_(axis, idx, src)
def _segment_sum(src, seg, n):
    out = torch.zeros((n,) + src.shape[1:], dtype=src.dtype)
    return out.index_add_(0, seg, src)
def _pad(x, pw):
    flat = tuple(v for pair in reversed(pw) for v in pair)
    return torch.nn.functional.pad(x, flat)

kernel_name = 'recurrent_scan__sliding_block_cached__glu_gated_bilinear__prompt'

def setup_inputs():

    _w = (lambda a: (lambda t: t.to(torch.bfloat16) if t.is_floating_point() else t)(torch.as_tensor(a)))
    def _A_setup():
        B, Sq, D = 1, 2048, 448
        Hq, Hkv, Sk, Dh = 16, 8, 2048, 256
        assert Hq % Hkv == 0, (Hq, Hkv)
        return {
            "x": _w((lambda *s: torch.randn(*s, dtype=torch.bfloat16) * 0.05)(B, Sq, D)),
            "k_cache": _w((lambda *s: torch.randn(*s, dtype=torch.bfloat16) * 1.6)(B, Hkv, Sk, Dh)),
            "v_cache": _w((lambda *s: torch.randn(*s, dtype=torch.bfloat16) * 1.6)(B, Hkv, Sk, Dh)),
            "wq": _w((lambda *s: torch.randn(*s, dtype=torch.bfloat16) * 1.6)(D, Hq * Dh) * (D ** -0.5)),
            "wo": _w((lambda *s: torch.randn(*s, dtype=torch.bfloat16) * 1.6)(Hq * Dh, D) * ((Hq * Dh) ** -0.5)),
        }
    _A_in = _A_setup()

    def _B_setup():
        B, S, D, F = 1, 2048, 448, 1792
        return {
            "x": _w((lambda *s: torch.randn(*s, dtype=torch.bfloat16) * 0.05)(B, S, D)),
            "w_gate": _w((lambda *s: torch.randn(*s, dtype=torch.bfloat16) * 0.2)(D, F) * (D ** -0.5)),
            "w_up": _w((lambda *s: torch.randn(*s, dtype=torch.bfloat16) * 0.2)(D, F) * (D ** -0.5)),
            "w_down": _w((lambda *s: torch.randn(*s, dtype=torch.bfloat16) * 0.2)(F, D) * (F ** -0.5)),
        }
    _B_in = _B_setup()

    return {
        "A_x": _A_in["x"],
        "A_k_cache": _A_in["k_cache"],
        "A_v_cache": _A_in["v_cache"],
        "A_wq": _A_in["wq"],
        "A_wo": _A_in["wo"],
        "B_x": _B_in["x"],
        "B_w_gate": _B_in["w_gate"],
        "B_w_up": _B_in["w_up"],
        "B_w_down": _B_in["w_down"],
    }

def reference(A_x, A_k_cache, A_v_cache, A_wq, A_wo, B_x, B_w_gate, B_w_up, B_w_down):
    def _A_ref(x, k_cache, v_cache, wq, wo):
        B, Sq, D = x.shape
        Hq, Dh = 16, 256
        Hkv, Sk = k_cache.shape[1], k_cache.shape[2]
        W = min(4096, max(256, Sk // 4))
        rep = Hq // Hkv
        q = (lambda x, *d: x.permute(*d))((x @ wq).reshape(B, Sq, Hq, Dh), 0, 2, 1, 3)
        k = (lambda x, n, axis: torch.repeat_interleave(x, n, dim=axis))(k_cache[:, :, Sk - W:, :], rep, axis=1)
        v = (lambda x, n, axis: torch.repeat_interleave(x, n, dim=axis))(v_cache[:, :, Sk - W:, :], rep, axis=1)
        scores = torch.einsum("bhqd,bhkd->bhqk", q, k) * (Dh ** -0.5)
        p = _softmax(scores, axis=-1)
        o = torch.einsum("bhqk,bhkd->bhqd", p, v)
        return (lambda x, *d: x.permute(*d))(o, 0, 2, 1, 3).reshape(B, Sq, Hq * Dh) @ wo

    def _B_ref(x, w_gate, w_up, w_down):
        g = torch.einsum("bsd,df->bsf", x, w_gate)
        u = torch.einsum("bsd,df->bsf", x, w_up)
        h = g * u
        return torch.einsum("bsf,fd->bsd", h, w_down)

    x = A_x
    xn = _layernorm(x, axis=-1)
    s = _A_ref(xn, A_k_cache, A_v_cache, A_wq, A_wo)
    g = _silu(_B_ref(xn, B_w_gate, B_w_up, B_w_down))
    return x + s * g
```

```cpp
#include <hip/hip_runtime.h>
#include <hip/hip_bf16.h>
typedef unsigned short u16;
__device__ __forceinline__ float bf2f(u16 v){ return __uint_as_float(((unsigned)v)<<16); }
__device__ __forceinline__ u16 f2bf(float f){ unsigned u=__float_as_uint(f); u += 0x7fffu + ((u>>16)&1u); return (u16)(u>>16);}
__device__ __forceinline__ float rb(float f){ return bf2f(f2bf(f)); }
constexpr int S=2048,D=448,HQ=16,HKV=8,SK=2048,DH=256,W=512,F=1792;
__global__ void ln_k(const u16* x,u16* xn){
  int r=blockIdx.x; __shared__ float red[64];
  int t=threadIdx.x; float s=0;
  for(int i=t;i<D;i+=64) s+=bf2f(x[r*D+i]);
  for(int o=32;o;o>>=1) s+=__shfl_xor(s,o);
  float mean=s/D; float v=0;
  for(int i=t;i<D;i+=64){ float d=bf2f(x[r*D+i])-mean; v+=d*d; }
  for(int o=32;o;o>>=1) v+=__shfl_xor(v,o);
  float m=rb(mean), var=rb(v/D);
  float den=rb(sqrtf(rb(var+1e-5f)));
  for(int i=t;i<D;i+=64) xn[r*D+i]=f2bf(rb(bf2f(x[r*D+i])-m)/den);
}
__global__ void gemm_k(const u16* A,int lda,const u16* B,int ldb,u16* C,int ldc,int K){
  int n=blockIdx.x*64+threadIdx.x, m=blockIdx.y*4+threadIdx.y;
  float acc=0; for(int k=0;k<K;++k) acc+=bf2f(A[(long)m*lda+k])*bf2f(B[(long)k*ldb+n]);
  C[(long)m*ldc+n]=f2bf(acc);
}
__global__ void attn_k(const u16* q,const u16* kc,const u16* vc,u16* o){
  int r=blockIdx.x,h=blockIdx.y,t=threadIdx.x;
  __shared__ float qs[DH], p[W], red[4];
  qs[t]=bf2f(q[(long)r*HQ*DH+h*DH+t]); __syncthreads();
  const u16* K=kc+((long)(h/2)*SK+(SK-W))*DH; const u16* V=vc+((long)(h/2)*SK+(SK-W))*DH;
  float mx=-1e30f;
  for(int j=t;j<W;j+=256){ float a=0; for(int d=0;d<DH;++d) a+=qs[d]*bf2f(K[j*DH+d]); a=rb(rb(a)*0.0625f); p[j]=a; mx=fmaxf(mx,a);}
  for(int o2=32;o2;o2>>=1) mx=fmaxf(mx,__shfl_xor(mx,o2));
  if((t&63)==0) red[t>>6]=mx; __syncthreads();
  mx=fmaxf(fmaxf(red[0],red[1]),fmaxf(red[2],red[3])); __syncthreads();
  float sm=0; for(int j=t;j<W;j+=256){ float e=expf(p[j]-mx); p[j]=e; sm+=e; }
  for(int o2=32;o2;o2>>=1) sm+=__shfl_xor(sm,o2);
  if((t&63)==0) red[t>>6]=sm; __syncthreads();
  sm=red[0]+red[1]+red[2]+red[3];
  float acc=0; for(int j=0;j<W;++j) acc+=rb(p[j]/sm)*bf2f(V[j*DH+t]);
  o[(long)r*HQ*DH+h*DH+t]=f2bf(acc);
}
__global__ void mul_k(const u16* g,const u16* u,u16* h,int n){ int i=blockIdx.x*256+threadIdx.x; if(i<n) h[i]=f2bf(bf2f(g[i])*bf2f(u[i])); }
__global__ void fin_k(const u16* x,const u16* s,const u16* d,u16* out,int n){ int i=blockIdx.x*256+threadIdx.x; if(i<n){ float dd=bf2f(d[i]); float g=rb(dd/(1.f+expf(-dd))); out[i]=f2bf(bf2f(x[i])+rb(bf2f(s[i])*g)); } }
extern "C" void kernel_launch(void* const* d_in, const int* in_sizes, int n_in, void* d_out, int out_size, void* d_ws, size_t ws_size, hipStream_t stream){
  const u16* x=(const u16*)d_in[0]; const u16* kc=(const u16*)d_in[1]; const u16* vc=(const u16*)d_in[2];
  const u16* wq=(const u16*)d_in[3]; const u16* wo=(const u16*)d_in[4];
  const u16* wg=(const u16*)d_in[6]; const u16* wu=(const u16*)d_in[7]; const u16* wd=(const u16*)d_in[8];
  u16* ws=(u16*)d_ws; u16* xn=ws; u16* q=xn+S*D; u16* o=q+S*4096; u16* s=o+S*4096; u16* g=s+S*D; u16* u=g+S*F; u16* h=u+S*F; u16* d=h+S*F;
  ln_k<<<S,64,0,stream>>>(x,xn);
  gemm_k<<<dim3(4096/64,S/4),dim3(64,4),0,stream>>>(xn,D,wq,4096,q,4096,D);
  attn_k<<<dim3(S,HQ),256,0,stream>>>(q,kc,vc,o);
  gemm_k<<<dim3(D/64,S/4),dim3(64,4),0,stream>>>(o,4096,wo,D,s,D,4096);
  gemm_k<<<dim3(F/64,S/4),dim3(64,4),0,stream>>>(xn,D,wg,F,g,F,D);
  gemm_k<<<dim3(F/64,S/4),dim3(64,4),0,stream>>>(xn,D,wu,F,u,F,D);
  mul_k<<<S*F/256,256,0,stream>>>(g,u,h,S*F);
  gemm_k<<<dim3(D/64,S/4),dim3(64,4),0,stream>>>(h,F,wd,D,d,D,F);
  fin_k<<<S*D/256,256,0,stream>>>(x,s,d,(u16*)d_out,S*D);
}
```

```cpp
#include <hip/hip_runtime.h>
static __device__ const __attribute__((aligned(1024))) unsigned lane_tab[20480] = {
4096,31872,20000,47776,4112,31888,20016,47792,4128,31904,20032,47808,4144,31920,20048,47824,4160,31936,20064,51200,4176,31952,20080,51216,4192,35328,20096,51232,4208,35344,20112,51248,
4224,35360,20128,51264,4240,35376,20144,51280,4256,35392,20160,51296,4272,35408,20176,51312,4288,35424,23552,51328,4304,35440,23568,51344,7680,35456,23584,51360,7696,35472,23600,51376,
7712,35488,23616,51392,7728,35504,23632,51408,7744,35520,23648,54784,7760,35536,23664,54800,7776,38912,23680,54816,7792,38928,23696,54832,7808,38944,23712,54848,7824,38960,23728,54864,
7840,38976,23744,54880,7856,38992,23760,54896,7872,39008,27136,54912,7888,39024,27152,54928,11264,39040,27168,54944,11280,39056,27184,54960,11296,39072,27200,54976,11312,39088,27216,54992,
11328,39104,27232,58368,11344,39120,27248,58384,11360,42496,27264,58400,11376,42512,27280,58416,11392,42528,27296,58432,11408,42544,27312,58448,11424,42560,27328,58464,11440,42576,27344,58480,
11456,42592,45056,58496,11472,42608,45072,58512,14848,42624,45088,58528,14864,42640,45104,58544,14880,42656,45120,58560,14896,42672,45136,58576,14912,42688,45152,61952,14928,42704,45168,61968,
14944,17408,45184,61984,14960,17424,45200,62000,14976,17440,45216,62016,14992,17456,45232,62032,15008,17472,45248,62048,15024,17488,45264,62064,15040,17504,48640,62080,15056,17520,48656,62096,
32768,17536,48672,62112,32784,17552,48688,62128,32800,17568,48704,62144,32816,17584,48720,62160,32832,17600,48736,65536,32848,17616,48752,65552,32864,20992,48768,65568,32880,21008,48784,65584,
68672,96448,84576,0,68688,96464,84592,0,68704,99840,84608,0,68720,99856,84624,0,68736,99872,84640,0,68752,99888,84656,0,68768,99904,84672,0,68784,99920,84688,0,
68800,99936,102400,0,68816,99952,102416,0,72192,99968,102432,0,72208,99984,102448,0,72224,100000,102464,0,72240,100016,102480,0,72256,100032,102496,0,72272,100048,102512,0,
72288,74752,102528,0,72304,74768,102544,0,72320,74784,102560,0,72336,74800,102576,0,72352,74816,102592,0,72368,74832,102608,0,72384,74848,105984,0,72400,74864,106000,0,
90112,74880,106016,0,90128,74896,106032,0,90144,74912,106048,0,90160,74928,106064,0,90176,74944,106080,0,90192,74960,106096,0,90208,78336,106112,0,90224,78352,106128,0,
90240,78368,106144,0,90256,78384,106160,0,90272,78400,106176,0,90288,78416,106192,0,90304,78432,109568,0,90320,78448,109584,0,93696,78464,109600,0,93712,78480,109616,0,
93728,78496,109632,0,93744,78512,109648,0,93760,78528,109664,0,93776,78544,109680,0,93792,81920,109696,0,93808,81936,109712,0,93824,81952,109728,0,93840,81968,109744,0,
93856,81984,109760,0,93872,82000,109776,0,93888,82016,113152,0,93904,82032,113168,0,97280,82048,113184,0,97296,82064,113200,0,97312,82080,113216,0,97328,82096,113232,0,
97344,82112,113248,0,97360,82128,113264,0,97376,85504,113280,0,97392,85520,113296,0,97408,85536,113312,0,97424,85552,113328,0,97440,85568,113344,0,97456,85584,113360,0,
4128,31904,20032,47808,4144,31920,20048,47824,4160,31936,20064,47616,4176,31952,20080,47632,4192,31744,20096,51232,4208,31760,20112,51248,4224,35360,20128,51264,4240,35376,20144,51280,
4256,35392,20160,51296,4272,35408,20176,51312,4288,35424,19968,51328,4304,35440,19984,51344,4096,35456,23584,51360,4112,35472,23600,51376,7712,35488,23616,51392,7728,35504,23632,51408,
7744,35520,23648,51200,7760,35536,23664,51216,7776,35328,23680,54816,7792,35344,23696,54832,7808,38944,23712,54848,7824,38960,23728,54864,7840,38976,23744,54880,7856,38992,23760,54896,
7872,39008,23552,54912,7888,39024,23568,54928,7680,39040,27168,54944,7696,39056,27184,54960,11296,39072,27200,54976,11312,39088,27216,54992,11328,39104,27232,54784,11344,39120,27248,54800,
11360,38912,27264,58400,11376,38928,27280,58416,11392,42528,27296,58432,11408,42544,27312,58448,11424,42560,27328,58464,11440,42576,27344,58480,11456,42592,27136,58496,11472,42608,27152,58512,
11264,42624,45088,58528,11280,42640,45104,58544,14880,42656,45120,58560,14896,42672,45136,58576,14912,42688,45152,58368,14928,42704,45168,58384,14944,42496,45184,61984,14960,42512,45200,62000,
14976,17440,45216,62016,14992,17456,45232,62032,15008,17472,45248,62048,15024,17488,45264,62064,15040,17504,45056,62080,15056,17520,45072,62096,14848,17536,48672,62112,14864,17552,48688,62128,
32800,17568,48704,62144,32816,17584,48720,62160,32832,17600,48736,61952,32848,17616,48752,61968,32864,17408,48768,65568,32880,17424,48784,65584,32896,21024,48800,65600,32912,21040,48816,65616,
68704,96256,84608,0,68720,96272,84624,0,68736,99872,84640,0,68752,99888,84656,0,68768,99904,84672,0,68784,99920,84688,0,68800,99936,84480,0,68816,99952,84496,0,
68608,99968,102432,0,68624,99984,102448,0,72224,100000,102464,0,72240,100016,102480,0,72256,100032,102496,0,72272,100048,102512,0,72288,99840,102528,0,72304,99856,102544,0,
72320,74784,102560,0,72336,74800,102576,0,72352,74816,102592,0,72368,74832,102608,0,72384,74848,102400,0,72400,74864,102416,0,72192,74880,106016,0,72208,74896,106032,0,
90144,74912,106048,0,90160,74928,106064,0,90176,74944,106080,0,90192,74960,106096,0,90208,74752,106112,0,90224,74768,106128,0,90240,78368,106144,0,90256,78384,106160,0,
90272,78400,106176,0,90288,78416,106192,0,90304,78432,105984,0,90320,78448,106000,0,90112,78464,109600,0,90128,78480,109616,0,93728,78496,109632,0,93744,78512,109648,0,
93760,78528,109664,0,93776,78544,109680,0,93792,78336,109696,0,93808,78352,109712,0,93824,81952,109728,0,93840,81968,109744,0,93856,81984,109760,0,93872,82000,109776,0,
93888,82016,109568,0,93904,82032,109584,0,93696,82048,113184,0,93712,82064,113200,0,97312,82080,113216,0,97328,82096,113232,0,97344,82112,113248,0,97360,82128,113264,0,
97376,81920,113280,0,97392,81936,113296,0,97408,85536,113312,0,97424,85552,113328,0,97440,85568,113344,0,97456,85584,113360,0,97472,85600,113152,0,97488,85616,113168,0,
0,65536,0,0,64,65600,0,0,128,65664,0,0,192,65728,0,0,304,65840,0,0,368,65904,0,0,432,65968,0,0,496,66032,0,0,
544,66080,0,0,608,66144,0,0,672,66208,0,0,736,66272,0,0,784,66320,0,0,848,66384,0,0,912,66448,0,0,976,66512,0,0,
16,65552,0,0,80,65616,0,0,144,65680,0,0,208,65744,0,0,288,65824,0,0,352,65888,0,0,416,65952,0,0,480,66016,0,0,
560,66096,0,0,624,66160,0,0,688,66224,0,0,752,66288,0,0,768,66304,0,0,832,66368,0,0,896,66432,0,0,960,66496,0,0,
32,65568,0,0,96,65632,0,0,160,65696,0,0,224,65760,0,0,272,65808,0,0,336,65872,0,0,400,65936,0,0,464,66000,0,0,
512,66048,0,0,576,66112,0,0,640,66176,0,0,704,66240,0,0,816,66352,0,0,880,66416,0,0,944,66480,0,0,1008,66544,0,0,
48,65584,0,0,112,65648,0,0,176,65712,0,0,240,65776,0,0,256,65792,0,0,320,65856,0,0,384,65920,0,0,448,65984,0,0,
528,66064,0,0,592,66128,0,0,656,66192,0,0,720,66256,0,0,800,66336,0,0,864,66400,0,0,928,66464,0,0,992,66528,0,0,
0,65536,0,65536,8,65544,8,65544,16,65552,16,65552,24,65560,24,65560,224,65760,288,65824,232,65768,296,65832,240,65776,304,65840,248,65784,312,65848,
448,65984,576,66112,456,65992,584,66120,464,66000,592,66128,472,66008,600,66136,672,66208,864,66400,680,66216,872,66408,688,66224,880,66416,696,66232,888,66424,
896,66432,1152,66688,904,66440,1160,66696,912,66448,1168,66704,920,66456,1176,66712,1120,66656,1440,66976,1128,66664,1448,66984,1136,66672,1456,66992,1144,66680,1464,67000,
1344,66880,1728,67264,1352,66888,1736,67272,1360,66896,1744,67280,1368,66904,1752,67288,1568,67104,2016,67552,1576,67112,2024,67560,1584,67120,2032,67568,1592,67128,2040,67576,
3584,69120,4608,70144,3592,69128,4616,70152,3600,69136,4624,70160,3608,69144,4632,70168,3808,69344,4896,70432,3816,69352,4904,70440,3824,69360,4912,70448,3832,69368,4920,70456,
4032,69568,5184,70720,4040,69576,5192,70728,4048,69584,5200,70736,4056,69592,5208,70744,4256,69792,5472,71008,4264,69800,5480,71016,4272,69808,5488,71024,4280,69816,5496,71032,
4480,70016,5760,71296,4488,70024,5768,71304,4496,70032,5776,71312,4504,70040,5784,71320,4704,70240,6048,71584,4712,70248,6056,71592,4720,70256,6064,71600,4728,70264,6072,71608,
4928,70464,6336,71872,4936,70472,6344,71880,4944,70480,6352,71888,4952,70488,6360,71896,5152,70688,6624,72160,5160,70696,6632,72168,5168,70704,6640,72176,5176,70712,6648,72184,
65536,131072,131072,0,65552,131088,131088,0,65568,131104,131104,0,65584,131120,131120,0,65600,131136,131136,0,65616,131152,131152,0,65632,131168,131168,0,65648,131184,131184,0,
65664,131200,131200,0,65680,131216,131216,0,65696,131232,131232,0,65712,131248,131248,0,65728,131264,131264,0,65744,131280,131280,0,65760,131296,131296,0,65776,131312,131312,0,
65792,131080,131328,0,65808,131096,131344,0,65824,131112,131360,0,65840,131128,131376,0,65856,131144,131392,0,65872,131160,131408,0,65888,131176,131424,0,65904,131192,131440,0,
65920,131208,131456,0,65936,131224,131472,0,65952,131240,131488,0,65968,131256,131504,0,65984,131272,131520,0,66000,131288,131536,0,66016,131304,131552,0,66032,131320,131568,0,
66048,131328,131584,0,66064,131344,131600,0,66080,131360,131616,0,66096,131376,131632,0,66112,131392,131648,0,66128,131408,131664,0,66144,131424,131680,0,66160,131440,131696,0,
66176,131456,131712,0,66192,131472,131728,0,66208,131488,131744,0,66224,131504,131760,0,66240,131520,131776,0,66256,131536,131792,0,66272,131552,131808,0,66288,131568,131824,0,
66304,131336,131840,0,66320,131352,131856,0,66336,131368,131872,0,66352,131384,131888,0,66368,131400,131904,0,66384,131416,131920,0,66400,131432,131936,0,66416,131448,131952,0,
66432,131464,131968,0,66448,131480,131984,0,66464,131496,132000,0,66480,131512,132016,0,66496,131528,132032,0,66512,131544,132048,0,66528,131560,132064,0,66544,131576,132080,0,
131072,0,0,0,131136,72,128,72,131200,144,256,144,131264,216,384,216,131376,896,512,3584,131440,968,640,3656,131504,1040,768,3728,131568,1112,896,3800,
131616,1792,1024,7168,131680,1864,1152,7240,131744,1936,1280,7312,131808,2008,1408,7384,131856,2688,1536,10752,131920,2760,1664,10824,131984,2832,1792,10896,132048,2904,1920,10968,
131088,2904,1920,10968,131152,2904,1920,10968,131216,2904,1920,10968,131280,2904,1920,10968,131360,2904,1920,10968,131424,2904,1920,10968,131488,2904,1920,10968,131552,2904,1920,10968,
131632,2904,1920,10968,131696,2904,1920,10968,131760,2904,1920,10968,131824,2904,1920,10968,131840,2904,1920,10968,131904,2904,1920,10968,131968,2904,1920,10968,132032,2904,1920,10968,
131104,2904,1920,10968,131168,2904,1920,10968,131232,2904,1920,10968,131296,2904,1920,10968,131344,2904,1920,10968,131408,2904,1920,10968,131472,2904,1920,10968,131536,2904,1920,10968,
131584,2904,1920,10968,131648,2904,1920,10968,131712,2904,1920,10968,131776,2904,1920,10968,131888,2904,1920,10968,131952,2904,1920,10968,132016,2904,1920,10968,132080,2904,1920,10968,
131120,2904,1920,10968,131184,2904,1920,10968,131248,2904,1920,10968,131312,2904,1920,10968,131328,2904,1920,10968,131392,2904,1920,10968,131456,2904,1920,10968,131520,2904,1920,10968,
131600,2904,1920,10968,131664,2904,1920,10968,131728,2904,1920,10968,131792,2904,1920,10968,131872,2904,1920,10968,131936,2904,1920,10968,132000,2904,1920,10968,132064,2904,1920,10968,
4096,68736,43040,107680,4112,68752,43056,107696,4128,68768,43072,107712,4144,68784,43088,107728,4160,68800,43104,115712,4176,68816,43120,115728,4192,76800,43136,115744,4208,76816,43152,115760,
4224,76832,43168,115776,4240,76848,43184,115792,4256,76864,43200,115808,4272,76880,43216,115824,4288,76896,51200,115840,4304,76912,51216,115856,12288,76928,51232,115872,12304,76944,51248,115888,
12320,76960,51264,115904,12336,76976,51280,115920,12352,76992,51296,123904,12368,77008,51312,123920,12384,84992,51328,123936,12400,85008,51344,123952,12416,85024,51360,123968,12432,85040,51376,123984,
12448,85056,51392,124000,12464,85072,51408,124016,12480,85088,59392,124032,12496,85104,59408,124048,20480,85120,59424,124064,20496,85136,59440,124080,20512,85152,59456,124096,20528,85168,59472,124112,
20544,85184,59488,132096,20560,85200,59504,132112,20576,93184,59520,132128,20592,93200,59536,132144,20608,93216,59552,132160,20624,93232,59568,132176,20640,93248,59584,132192,20656,93264,59600,132208,
20672,93280,100352,132224,20688,93296,100368,132240,28672,93312,100384,132256,28688,93328,100400,132272,28704,93344,100416,132288,28720,93360,100432,132304,28736,93376,100448,140288,28752,93392,100464,140304,
28768,35840,100480,140320,28784,35856,100496,140336,28800,35872,100512,140352,28816,35888,100528,140368,28832,35904,100544,140384,28848,35920,100560,140400,28864,35936,108544,140416,28880,35952,108560,140432,
69632,35968,108576,140448,69648,35984,108592,140464,69664,36000,108608,140480,69680,36016,108624,140496,69696,36032,108640,148480,69712,36048,108656,148496,69728,44032,108672,148512,69744,44048,108688,148528,
4096,7808,131072,0,4112,7824,131080,16,4128,7840,131088,32,4144,7856,131096,48,4160,7872,131296,64,4176,7888,131304,80,4192,8576,131312,96,4208,8592,131320,112,
4224,8608,131520,128,4240,8624,131528,144,4256,8640,131536,160,4272,8656,131544,176,4288,8672,131744,192,4304,8688,131752,208,4992,8704,131760,224,5008,8720,131768,240,
5024,8736,131968,256,5040,8752,131976,272,5056,8768,131984,288,5072,8784,131992,304,5088,9472,132192,320,5104,9488,132200,336,5120,9504,132208,352,5136,9520,132216,368,
5152,9536,132416,384,5168,9552,132424,400,5184,9568,132432,416,5200,9584,132440,432,5888,9600,132640,448,5904,9616,132648,464,5920,9632,132656,480,5936,9648,132664,496,
5952,9664,134656,512,5968,9680,134664,528,5984,10368,134672,544,6000,10384,134680,560,6016,10400,134880,576,6032,10416,134888,592,6048,10432,134896,608,6064,10448,134904,624,
6080,10464,135104,640,6096,10480,135112,656,6784,10496,135120,672,6800,10512,135128,688,6816,10528,135328,704,6832,10544,135336,720,6848,10560,135344,736,6864,10576,135352,752,
6880,18432,135552,768,6896,18448,135560,784,6912,18464,135568,800,6928,18480,135576,816,6944,18496,135776,832,6960,18512,135784,848,6976,18528,135792,864,6992,18544,135800,880,
7680,18560,136000,896,7696,18576,136008,912,7712,18592,136016,928,7728,18608,136024,944,7744,18624,136224,960,7760,18640,136232,976,7776,19328,136240,992,7792,19344,136248,1008,
0,139264,139296,0,8,139328,139360,16,16,139392,139424,32,24,139456,139488,48,32,139568,139536,512,40,139632,139600,528,48,139696,139664,544,56,139760,139728,560,
64,139808,139776,1024,72,139872,139840,1040,80,139936,139904,1056,88,140000,139968,1072,96,140048,140080,1536,104,140112,140144,1552,112,140176,140208,1568,120,140240,140272,1584,
128,139272,139304,2096,136,139336,139368,2080,144,139400,139432,2064,152,139464,139496,2048,160,139576,139544,2608,168,139640,139608,2592,176,139704,139672,2576,184,139768,139736,2560,
192,139816,139784,3120,200,139880,139848,3104,208,139944,139912,3088,216,140008,139976,3072,224,140056,140088,3632,232,140120,140152,3616,240,140184,140216,3600,248,140248,140280,3584,
256,139280,139312,4128,264,139344,139376,4144,272,139408,139440,4096,280,139472,139504,4112,288,139552,139520,4640,296,139616,139584,4656,304,139680,139648,4608,312,139744,139712,4624,
320,139824,139792,5152,328,139888,139856,5168,336,139952,139920,5120,344,140016,139984,5136,352,140032,140064,5664,360,140096,140128,5680,368,140160,140192,5632,376,140224,140256,5648,
384,139288,139320,6160,392,139352,139384,6144,400,139416,139448,6192,408,139480,139512,6176,416,139560,139528,6672,424,139624,139592,6656,432,139688,139656,6704,440,139752,139720,6688,
440,139832,139800,7184,440,139896,139864,7168,440,139960,139928,7216,440,140024,139992,7200,440,140040,140072,7696,440,140104,140136,7680,440,140168,140200,7728,440,140232,140264,7712,
151616,216256,190560,1248,151632,216272,190576,1264,151648,224256,190592,1280,151664,224272,190608,1296,151680,224288,190624,1312,151696,224304,190640,1328,151712,224320,190656,1344,151728,224336,190672,1360,
151744,224352,231424,1376,151760,224368,231440,1392,159744,224384,231456,1408,159760,224400,231472,1424,159776,224416,231488,1440,159792,224432,231504,1456,159808,224448,231520,1472,159824,224464,231536,1488,
159840,166912,231552,1504,159856,166928,231568,1520,159872,166944,231584,9440,159888,166960,231600,9456,159904,166976,231616,9472,159920,166992,231632,9488,159936,167008,239616,9504,159952,167024,239632,9520,
200704,167040,239648,9536,200720,167056,239664,9552,200736,167072,239680,9568,200752,167088,239696,9584,200768,167104,239712,9600,200784,167120,239728,9616,200800,175104,239744,9632,200816,175120,239760,9648,
200832,175136,239776,9664,200848,175152,239792,9680,200864,175168,239808,9696,200880,175184,239824,9712,200896,175200,247808,17632,200912,175216,247824,17648,208896,175232,247840,17664,208912,175248,247856,17680,
208928,175264,247872,17696,208944,175280,247888,17712,208960,175296,247904,17728,208976,175312,247920,17744,208992,183296,247936,17760,209008,183312,247952,17776,209024,183328,247968,17792,209040,183344,247984,17808,
209056,183360,248000,17824,209072,183376,248016,17840,209088,183392,256000,17856,209104,183408,256016,17872,217088,183424,256032,17888,217104,183440,256048,17904,217120,183456,256064,25824,217136,183472,256080,25840,
217152,183488,256096,25856,217168,183504,256112,25872,217184,191488,256128,25888,217200,191504,256144,25904,217216,191520,256160,25920,217232,191536,256176,25936,217248,191552,256192,25952,217264,191568,256208,25968,
4096,10368,6560,12832,4112,10384,6576,12848,4128,10400,6592,12864,4144,10416,6608,12880,4160,10432,6624,13568,4176,10448,6640,13584,4192,11136,6656,13600,4208,11152,6672,13616,
4224,11168,6688,13632,4240,11184,6704,13648,4256,11200,6720,13664,4272,11216,6736,13680,4288,11232,7424,13696,4304,11248,7440,13712,4992,11264,7456,13728,5008,11280,7472,13744,
5024,11296,7488,13760,5040,11312,7504,13776,5056,11328,7520,14464,5072,11344,7536,14480,5088,12032,7552,14496,5104,12048,7568,14512,5120,12064,7584,14528,5136,12080,7600,14544,
5152,12096,7616,14560,5168,12112,7632,14576,5184,12128,8320,14592,5200,12144,8336,14608,5888,12160,8352,14624,5904,12176,8368,14640,5920,12192,8384,14656,5936,12208,8400,14672,
5952,12224,8416,15360,5968,12240,8432,15376,5984,12928,8448,15392,6000,12944,8464,15408,6016,12960,8480,15424,6032,12976,8496,15440,6048,12992,8512,15456,6064,13008,8528,15472,
6080,13024,12800,15488,6096,13040,12816,15504,6784,13056,12832,15520,6800,13072,12848,15536,6816,13088,12864,15552,6832,13104,12880,15568,6848,13120,12896,16256,6864,13136,12912,16272,
6880,6656,12928,16288,6896,6672,12944,16304,6912,6688,12960,16320,6928,6704,12976,16336,6944,6720,12992,16352,6960,6736,13008,16368,6976,6752,13696,16384,6992,6768,13712,16400,
11264,6784,13728,16416,11280,6800,13744,16432,11296,6816,13760,16448,11312,6832,13776,16464,11328,6848,13792,17152,11344,6864,13808,17168,11360,7552,13824,17184,11376,7568,13840,17200,
20288,26560,22752,0,20304,26576,22768,0,20320,27264,22784,0,20336,27280,22800,0,20352,27296,22816,0,20368,27312,22832,0,20384,27328,22848,0,20400,27344,22864,0,
20416,27360,27136,0,20432,27376,27152,0,21120,27392,27168,0,21136,27408,27184,0,21152,27424,27200,0,21168,27440,27216,0,21184,27456,27232,0,21200,27472,27248,0,
21216,20992,27264,0,21232,21008,27280,0,21248,21024,27296,0,21264,21040,27312,0,21280,21056,27328,0,21296,21072,27344,0,21312,21088,28032,0,21328,21104,28048,0,
25600,21120,28064,0,25616,21136,28080,0,25632,21152,28096,0,25648,21168,28112,0,25664,21184,28128,0,25680,21200,28144,0,25696,21888,28160,0,25712,21904,28176,0,
25728,21920,28192,0,25744,21936,28208,0,25760,21952,28224,0,25776,21968,28240,0,25792,21984,28928,0,25808,22000,28944,0,26496,22016,28960,0,26512,22032,28976,0,
26528,22048,28992,0,26544,22064,29008,0,26560,22080,29024,0,26576,22096,29040,0,26592,22784,29056,0,26608,22800,29072,0,26624,22816,29088,0,26640,22832,29104,0,
26656,22848,29120,0,26672,22864,29136,0,26688,22880,29824,0,26704,22896,29840,0,27392,22912,29856,0,27408,22928,29872,0,27424,22944,29888,0,27440,22960,29904,0,
27456,22976,29920,0,27472,22992,29936,0,27488,23680,29952,0,27504,23696,29968,0,27520,23712,29984,0,27536,23728,30000,0,27552,23744,30016,0,27568,23760,30032,0,
0,0,0,0,2130706432,32,8,8,2130706432,64,16,16,2130706432,96,24,24,2130706432,0,0,32,2130706432,32,8,40,2130706432,64,16,48,2130706432,96,24,56,
2130706432,0,0,64,2130706432,32,8,72,2130706432,64,16,80,2130706432,96,24,88,2130706432,0,0,96,2130706432,32,8,104,2130706432,64,16,112,2130706432,96,24,120,
2130706432,0,0,128,2130706432,32,8,136,2130706432,64,16,144,2130706432,96,24,152,2130706432,0,0,160,2130706432,32,8,168,2130706432,64,16,176,2130706432,96,24,184,
2130706432,0,0,192,2130706432,32,8,200,2130706432,64,16,208,2130706432,96,24,216,2130706432,0,0,224,2130706432,32,8,232,2130706432,64,16,240,2130706432,96,24,248,
2130706432,0,0,256,2130706432,32,8,264,2130706432,64,16,272,2130706432,96,24,280,2130706432,0,0,288,2130706432,32,8,296,2130706432,64,16,304,2130706432,96,24,312,
2130706432,0,0,320,2130706432,32,8,328,2130706432,64,16,336,2130706432,96,24,344,2130706432,0,0,352,2130706432,32,8,360,2130706432,64,16,368,2130706432,96,24,376,
2130706432,0,0,384,2130706432,32,8,392,2130706432,64,16,400,2130706432,96,24,408,2130706432,0,0,416,2130706432,32,8,424,2130706432,64,16,432,2130706432,96,24,440,
2130706432,0,0,448,2130706432,32,8,456,2130706432,64,16,464,2130706432,96,24,472,2130706432,0,0,480,2130706432,32,8,488,2130706432,64,16,496,2130706432,96,24,504,
0,0,0,0,16,8,0,0,32,16,0,0,48,24,0,0,64,32,0,0,80,40,0,0,96,48,0,0,112,56,0,0,
128,64,0,0,144,72,0,0,160,80,0,0,176,88,0,0,192,96,0,0,208,104,0,0,224,112,0,0,240,120,0,0,
256,128,0,0,272,136,0,0,288,144,0,0,304,152,0,0,320,160,0,0,336,168,0,0,352,176,0,0,368,184,0,0,
384,192,0,0,400,200,0,0,416,208,0,0,432,216,0,0,448,224,0,0,464,232,0,0,480,240,0,0,496,248,0,0,
512,0,0,0,528,8,0,0,544,16,0,0,560,24,0,0,576,32,0,0,592,40,0,0,608,48,0,0,624,56,0,0,
640,64,0,0,656,72,0,0,672,80,0,0,688,88,0,0,704,96,0,0,720,104,0,0,736,112,0,0,752,120,0,0,
768,128,0,0,784,136,0,0,800,144,0,0,816,152,0,0,832,160,0,0,848,168,0,0,864,176,0,0,880,184,0,0,
896,192,0,0,912,200,0,0,928,208,0,0,944,216,0,0,960,224,0,0,976,232,0,0,992,240,0,0,1008,248,0,0,
0,131072,131072,0,16,131152,131088,16,32,131232,131104,32,48,131312,131120,48,896,131392,131152,64,912,131472,131168,80,928,131552,131184,96,944,131632,131200,112,
1792,131712,131232,128,1808,131792,131248,144,1824,131872,131264,160,1840,131952,131280,176,2688,132032,131312,192,2704,132112,131328,208,2720,132192,131344,224,2736,132272,131360,240,
3584,131080,131392,256,3600,131160,131408,272,3616,131240,131424,288,3632,131320,131440,304,4480,131400,131472,320,4496,131480,131488,336,4512,131560,131504,352,4528,131640,131520,368,
5376,131720,131552,384,5392,131800,131568,400,5408,131880,131584,416,5424,131960,131600,432,6272,132040,131632,448,6288,132120,131648,464,6304,132200,131664,480,6320,132280,131680,496,
7168,131088,131712,512,7184,131168,131728,528,7200,131248,131744,544,7216,131328,131760,560,8064,131408,131792,576,8080,131488,131808,592,8096,131568,131824,608,8112,131648,131840,624,
8960,131728,131872,640,8976,131808,131888,656,8992,131888,131904,672,9008,131968,131920,688,9856,132048,131952,704,9872,132128,131968,720,9888,132208,131984,736,9904,132288,132000,752,
10752,131096,132032,768,10768,131176,132048,784,10784,131256,132064,800,10800,131336,132080,816,11648,131416,132112,832,11664,131496,132128,848,11680,131576,132144,864,11696,131656,132160,880,
12544,131736,132192,896,12560,131816,132208,912,12576,131896,132224,928,12592,131976,132240,944,13440,132056,132272,960,13456,132136,132288,976,13472,132216,132304,992,13488,132296,132320,1008,
0,7168,7200,0,16,7232,7264,16,32,7296,7328,32,48,7360,7392,48,896,7472,7440,64,912,7536,7504,80,928,7600,7568,96,944,7664,7632,112,
1792,7712,7680,128,1808,7776,7744,144,1824,7840,7808,160,1840,7904,7872,176,2688,7952,7984,192,2704,8016,8048,208,2720,8080,8112,224,2736,8144,8176,240,
3632,7176,7208,256,3616,7240,7272,272,3600,7304,7336,288,3584,7368,7400,304,4528,7480,7448,320,4512,7544,7512,336,4496,7608,7576,352,4480,7672,7640,368,
5424,7720,7688,384,5408,7784,7752,400,5392,7848,7816,416,5376,7912,7880,432,6320,7960,7992,448,6304,8024,8056,464,6288,8088,8120,480,6272,8152,8184,496,
7200,7184,7216,512,7216,7248,7280,528,7168,7312,7344,544,7184,7376,7408,560,8096,7456,7424,576,8112,7520,7488,592,8064,7584,7552,608,8080,7648,7616,624,
8992,7728,7696,640,9008,7792,7760,656,8960,7856,7824,672,8976,7920,7888,688,9888,7936,7968,704,9904,8000,8032,720,9856,8064,8096,736,9872,8128,8160,752,
10768,7192,7224,768,10752,7256,7288,784,10800,7320,7352,800,10784,7384,7416,816,11664,7464,7432,832,11648,7528,7496,848,11696,7592,7560,864,11680,7656,7624,880,
12560,7736,7704,896,12544,7800,7768,912,12592,7864,7832,928,12576,7928,7896,944,13456,7944,7976,960,13440,8008,8040,976,13488,8072,8104,992,13472,8136,8168,1008,
16256,0,0,0,1065353216,0,0,0,0,16256,0,0,0,1065353216,0,0,0,0,0,0,0,0,0,0,0,0,0,0,0,0,0,0,
0,0,0,0,0,0,0,0,0,0,0,0,0,0,0,0,0,0,0,0,0,0,0,0,0,0,0,0,0,0,0,0,
0,0,0,0,0,0,0,0,0,0,0,0,0,0,0,0,16256,0,0,0,1065353216,0,0,0,0,16256,0,0,0,1065353216,0,0,
0,0,0,0,0,0,0,0,0,0,0,0,0,0,0,0,0,0,0,0,0,0,0,0,0,0,0,0,0,0,0,0,
0,0,0,0,0,0,0,0,0,0,0,0,0,0,0,0,0,0,0,0,0,0,0,0,0,0,0,0,0,0,0,0,
16256,0,0,0,1065353216,0,0,0,0,16256,0,0,0,1065353216,0,0,0,0,0,0,0,0,0,0,0,0,0,0,0,0,0,0,
0,0,0,0,0,0,0,0,0,0,0,0,0,0,0,0,0,0,0,0,0,0,0,0,0,0,0,0,0,0,0,0,
0,0,0,0,0,0,0,0,0,0,0,0,0,0,0,0,16256,0,0,0,1065353216,0,0,0,0,16256,0,0,0,1065353216,0,0,
0,0,16256,0,0,0,1065353216,0,0,0,0,16256,0,0,0,1065353216,0,0,0,0,0,0,0,0,0,0,0,0,0,0,0,0,
0,0,0,0,0,0,0,0,0,0,0,0,0,0,0,0,0,0,0,0,0,0,0,0,0,0,0,0,0,0,0,0,
0,0,0,0,0,0,0,0,0,0,0,0,0,0,0,0,0,0,16256,0,0,0,1065353216,0,0,0,0,16256,0,0,0,1065353216,
0,0,0,0,0,0,0,0,0,0,0,0,0,0,0,0,0,0,0,0,0,0,0,0,0,0,0,0,0,0,0,0,
0,0,0,0,0,0,0,0,0,0,0,0,0,0,0,0,0,0,0,0,0,0,0,0,0,0,0,0,0,0,0,0,
0,0,16256,0,0,0,1065353216,0,0,0,0,16256,0,0,0,1065353216,0,0,0,0,0,0,0,0,0,0,0,0,0,0,0,0,
0,0,0,0,0,0,0,0,0,0,0,0,0,0,0,0,0,0,0,0,0,0,0,0,0,0,0,0,0,0,0,0,
0,0,0,0,0,0,0,0,0,0,0,0,0,0,0,0,0,0,16256,0,0,0,1065353216,0,0,0,0,16256,0,0,0,1065353216,
4096,31872,20000,47776,4112,31888,20016,47792,4128,31904,20032,47808,4144,31920,20048,47824,4160,31936,20064,51200,4176,31952,20080,51216,4192,35328,20096,51232,4208,35344,20112,51248,
4224,35360,20128,51264,4240,35376,20144,51280,4256,35392,20160,51296,4272,35408,20176,51312,4288,35424,23552,51328,4304,35440,23568,51344,7680,35456,23584,51360,7696,35472,23600,51376,
7712,35488,23616,51392,7728,35504,23632,51408,7744,35520,23648,54784,7760,35536,23664,54800,7776,38912,23680,54816,7792,38928,23696,54832,7808,38944,23712,54848,7824,38960,23728,54864,
7840,38976,23744,54880,7856,38992,23760,54896,7872,39008,27136,54912,7888,39024,27152,54928,11264,39040,27168,54944,11280,39056,27184,54960,11296,39072,27200,54976,11312,39088,27216,54992,
11328,39104,27232,58368,11344,39120,27248,58384,11360,42496,27264,58400,11376,42512,27280,58416,11392,42528,27296,58432,11408,42544,27312,58448,11424,42560,27328,58464,11440,42576,27344,58480,
11456,42592,45056,58496,11472,42608,45072,58512,14848,42624,45088,58528,14864,42640,45104,58544,14880,42656,45120,58560,14896,42672,45136,58576,14912,42688,45152,61952,14928,42704,45168,61968,
14944,17408,45184,61984,14960,17424,45200,62000,14976,17440,45216,62016,14992,17456,45232,62032,15008,17472,45248,62048,15024,17488,45264,62064,15040,17504,48640,62080,15056,17520,48656,62096,
32768,17536,48672,62112,32784,17552,48688,62128,32800,17568,48704,62144,32816,17584,48720,62160,32832,17600,48736,65536,32848,17616,48752,65552,32864,20992,48768,65568,32880,21008,48784,65584,
68672,96448,84576,0,68688,96464,84592,0,68704,99840,84608,0,68720,99856,84624,0,68736,99872,84640,0,68752,99888,84656,0,68768,99904,84672,0,68784,99920,84688,0,
68800,99936,102400,0,68816,99952,102416,0,72192,99968,102432,0,72208,99984,102448,0,72224,100000,102464,0,72240,100016,102480,0,72256,100032,102496,0,72272,100048,102512,0,
72288,74752,102528,0,72304,74768,102544,0,72320,74784,102560,0,72336,74800,102576,0,72352,74816,102592,0,72368,74832,102608,0,72384,74848,105984,0,72400,74864,106000,0,
90112,74880,106016,0,90128,74896,106032,0,90144,74912,106048,0,90160,74928,106064,0,90176,74944,106080,0,90192,74960,106096,0,90208,78336,106112,0,90224,78352,106128,0,
90240,78368,106144,0,90256,78384,106160,0,90272,78400,106176,0,90288,78416,106192,0,90304,78432,109568,0,90320,78448,109584,0,93696,78464,109600,0,93712,78480,109616,0,
93728,78496,109632,0,93744,78512,109648,0,93760,78528,109664,0,93776,78544,109680,0,93792,81920,109696,0,93808,81936,109712,0,93824,81952,109728,0,93840,81968,109744,0,
93856,81984,109760,0,93872,82000,109776,0,93888,82016,113152,0,93904,82032,113168,0,97280,82048,113184,0,97296,82064,113200,0,97312,82080,113216,0,97328,82096,113232,0,
97344,82112,113248,0,97360,82128,113264,0,97376,85504,113280,0,97392,85520,113296,0,97408,85536,113312,0,97424,85552,113328,0,97440,85568,113344,0,97456,85584,113360,0,
4128,31904,20032,47808,4144,31920,20048,47824,4160,31936,20064,47616,4176,31952,20080,47632,4192,31744,20096,51232,4208,31760,20112,51248,4224,35360,20128,51264,4240,35376,20144,51280,
4256,35392,20160,51296,4272,35408,20176,51312,4288,35424,19968,51328,4304,35440,19984,51344,4096,35456,23584,51360,4112,35472,23600,51376,7712,35488,23616,51392,7728,35504,23632,51408,
7744,35520,23648,51200,7760,35536,23664,51216,7776,35328,23680,54816,7792,35344,23696,54832,7808,38944,23712,54848,7824,38960,23728,54864,7840,38976,23744,54880,7856,38992,23760,54896,
7872,39008,23552,54912,7888,39024,23568,54928,7680,39040,27168,54944,7696,39056,27184,54960,11296,39072,27200,54976,11312,39088,27216,54992,11328,39104,27232,54784,11344,39120,27248,54800,
11360,38912,27264,58400,11376,38928,27280,58416,11392,42528,27296,58432,11408,42544,27312,58448,11424,42560,27328,58464,11440,42576,27344,58480,11456,42592,27136,58496,11472,42608,27152,58512,
11264,42624,45088,58528,11280,42640,45104,58544,14880,42656,45120,58560,14896,42672,45136,58576,14912,42688,45152,58368,14928,42704,45168,58384,14944,42496,45184,61984,14960,42512,45200,62000,
14976,17440,45216,62016,14992,17456,45232,62032,15008,17472,45248,62048,15024,17488,45264,62064,15040,17504,45056,62080,15056,17520,45072,62096,14848,17536,48672,62112,14864,17552,48688,62128,
32800,17568,48704,62144,32816,17584,48720,62160,32832,17600,48736,61952,32848,17616,48752,61968,32864,17408,48768,65568,32880,17424,48784,65584,32896,21024,48800,65600,32912,21040,48816,65616,
68704,96256,84608,0,68720,96272,84624,0,68736,99872,84640,0,68752,99888,84656,0,68768,99904,84672,0,68784,99920,84688,0,68800,99936,84480,0,68816,99952,84496,0,
68608,99968,102432,0,68624,99984,102448,0,72224,100000,102464,0,72240,100016,102480,0,72256,100032,102496,0,72272,100048,102512,0,72288,99840,102528,0,72304,99856,102544,0,
72320,74784,102560,0,72336,74800,102576,0,72352,74816,102592,0,72368,74832,102608,0,72384,74848,102400,0,72400,74864,102416,0,72192,74880,106016,0,72208,74896,106032,0,
90144,74912,106048,0,90160,74928,106064,0,90176,74944,106080,0,90192,74960,106096,0,90208,74752,106112,0,90224,74768,106128,0,90240,78368,106144,0,90256,78384,106160,0,
90272,78400,106176,0,90288,78416,106192,0,90304,78432,105984,0,90320,78448,106000,0,90112,78464,109600,0,90128,78480,109616,0,93728,78496,109632,0,93744,78512,109648,0,
93760,78528,109664,0,93776,78544,109680,0,93792,78336,109696,0,93808,78352,109712,0,93824,81952,109728,0,93840,81968,109744,0,93856,81984,109760,0,93872,82000,109776,0,
93888,82016,109568,0,93904,82032,109584,0,93696,82048,113184,0,93712,82064,113200,0,97312,82080,113216,0,97328,82096,113232,0,97344,82112,113248,0,97360,82128,113264,0,
97376,81920,113280,0,97392,81936,113296,0,97408,85536,113312,0,97424,85552,113328,0,97440,85568,113344,0,97456,85584,113360,0,97472,85600,113152,0,97488,85616,113168,0,
8192,73728,0,0,8256,73792,0,0,8320,73856,0,0,8384,73920,0,0,8496,74032,0,0,8560,74096,0,0,8624,74160,0,0,8688,74224,0,0,
8736,74272,0,0,8800,74336,0,0,8864,74400,0,0,8928,74464,0,0,8976,74512,0,0,9040,74576,0,0,9104,74640,0,0,9168,74704,0,0,
8208,73744,0,0,8272,73808,0,0,8336,73872,0,0,8400,73936,0,0,8480,74016,0,0,8544,74080,0,0,8608,74144,0,0,8672,74208,0,0,
8752,74288,0,0,8816,74352,0,0,8880,74416,0,0,8944,74480,0,0,8960,74496,0,0,9024,74560,0,0,9088,74624,0,0,9152,74688,0,0,
8224,73760,0,0,8288,73824,0,0,8352,73888,0,0,8416,73952,0,0,8464,74000,0,0,8528,74064,0,0,8592,74128,0,0,8656,74192,0,0,
8704,74240,0,0,8768,74304,0,0,8832,74368,0,0,8896,74432,0,0,9008,74544,0,0,9072,74608,0,0,9136,74672,0,0,9200,74736,0,0,
8240,73776,0,0,8304,73840,0,0,8368,73904,0,0,8432,73968,0,0,8448,73984,0,0,8512,74048,0,0,8576,74112,0,0,8640,74176,0,0,
8720,74256,0,0,8784,74320,0,0,8848,74384,0,0,8912,74448,0,0,8992,74528,0,0,9056,74592,0,0,9120,74656,0,0,9184,74720,0,0,
0,65536,0,65536,8,65544,8,65544,16,65552,16,65552,24,65560,24,65560,224,65760,288,65824,232,65768,296,65832,240,65776,304,65840,248,65784,312,65848,
448,65984,576,66112,456,65992,584,66120,464,66000,592,66128,472,66008,600,66136,672,66208,864,66400,680,66216,872,66408,688,66224,880,66416,696,66232,888,66424,
896,66432,1152,66688,904,66440,1160,66696,912,66448,1168,66704,920,66456,1176,66712,1120,66656,1440,66976,1128,66664,1448,66984,1136,66672,1456,66992,1144,66680,1464,67000,
1344,66880,1728,67264,1352,66888,1736,67272,1360,66896,1744,67280,1368,66904,1752,67288,1568,67104,2016,67552,1576,67112,2024,67560,1584,67120,2032,67568,1592,67128,2040,67576,
3584,69120,4608,70144,3592,69128,4616,70152,3600,69136,4624,70160,3608,69144,4632,70168,3808,69344,4896,70432,3816,69352,4904,70440,3824,69360,4912,70448,3832,69368,4920,70456,
4032,69568,5184,70720,4040,69576,5192,70728,4048,69584,5200,70736,4056,69592,5208,70744,4256,69792,5472,71008,4264,69800,5480,71016,4272,69808,5488,71024,4280,69816,5496,71032,
4480,70016,5760,71296,4488,70024,5768,71304,4496,70032,5776,71312,4504,70040,5784,71320,4704,70240,6048,71584,4712,70248,6056,71592,4720,70256,6064,71600,4728,70264,6072,71608,
4928,70464,6336,71872,4936,70472,6344,71880,4944,70480,6352,71888,4952,70488,6360,71896,5152,70688,6624,72160,5160,70696,6632,72168,5168,70704,6640,72176,5176,70712,6648,72184,
65536,139264,139264,0,65552,139280,139280,0,65568,139296,139296,0,65584,139312,139312,0,65600,139328,139328,0,65616,139344,139344,0,65632,139360,139360,0,65648,139376,139376,0,
65664,139392,139392,0,65680,139408,139408,0,65696,139424,139424,0,65712,139440,139440,0,65728,139456,139456,0,65744,139472,139472,0,65760,139488,139488,0,65776,139504,139504,0,
65792,139272,139520,0,65808,139288,139536,0,65824,139304,139552,0,65840,139320,139568,0,65856,139336,139584,0,65872,139352,139600,0,65888,139368,139616,0,65904,139384,139632,0,
65920,139400,139648,0,65936,139416,139664,0,65952,139432,139680,0,65968,139448,139696,0,65984,139464,139712,0,66000,139480,139728,0,66016,139496,139744,0,66032,139512,139760,0,
66048,139520,139776,0,66064,139536,139792,0,66080,139552,139808,0,66096,139568,139824,0,66112,139584,139840,0,66128,139600,139856,0,66144,139616,139872,0,66160,139632,139888,0,
66176,139648,139904,0,66192,139664,139920,0,66208,139680,139936,0,66224,139696,139952,0,66240,139712,139968,0,66256,139728,139984,0,66272,139744,140000,0,66288,139760,140016,0,
66304,139528,140032,0,66320,139544,140048,0,66336,139560,140064,0,66352,139576,140080,0,66368,139592,140096,0,66384,139608,140112,0,66400,139624,140128,0,66416,139640,140144,0,
66432,139656,140160,0,66448,139672,140176,0,66464,139688,140192,0,66480,139704,140208,0,66496,139720,140224,0,66512,139736,140240,0,66528,139752,140256,0,66544,139768,140272,0,
139264,0,0,0,139328,72,128,72,139392,144,256,144,139456,216,384,216,139568,896,512,3584,139632,968,640,3656,139696,1040,768,3728,139760,1112,896,3800,
139808,1792,1024,7168,139872,1864,1152,7240,139936,1936,1280,7312,140000,2008,1408,7384,140048,2688,1536,10752,140112,2760,1664,10824,140176,2832,1792,10896,140240,2904,1920,10968,
139280,2904,1920,10968,139344,2904,1920,10968,139408,2904,1920,10968,139472,2904,1920,10968,139552,2904,1920,10968,139616,2904,1920,10968,139680,2904,1920,10968,139744,2904,1920,10968,
139824,2904,1920,10968,139888,2904,1920,10968,139952,2904,1920,10968,140016,2904,1920,10968,140032,2904,1920,10968,140096,2904,1920,10968,140160,2904,1920,10968,140224,2904,1920,10968,
139296,2904,1920,10968,139360,2904,1920,10968,139424,2904,1920,10968,139488,2904,1920,10968,139536,2904,1920,10968,139600,2904,1920,10968,139664,2904,1920,10968,139728,2904,1920,10968,
139776,2904,1920,10968,139840,2904,1920,10968,139904,2904,1920,10968,139968,2904,1920,10968,140080,2904,1920,10968,140144,2904,1920,10968,140208,2904,1920,10968,140272,2904,1920,10968,
139312,2904,1920,10968,139376,2904,1920,10968,139440,2904,1920,10968,139504,2904,1920,10968,139520,2904,1920,10968,139584,2904,1920,10968,139648,2904,1920,10968,139712,2904,1920,10968,
139792,2904,1920,10968,139856,2904,1920,10968,139920,2904,1920,10968,139984,2904,1920,10968,140064,2904,1920,10968,140128,2904,1920,10968,140192,2904,1920,10968,140256,2904,1920,10968,
29056,93440,51616,116000,29072,93456,51632,116016,29088,93472,51648,116032,29104,93488,51664,116048,29120,93504,51680,116064,29136,93520,51696,116080,29152,93536,59616,116096,29168,93552,59632,116112,
69856,93568,59648,116128,69872,93584,59664,116144,69888,93600,59680,116160,69904,93616,59696,116176,69920,93632,59712,116192,69936,93648,59728,116208,69952,93664,59744,124128,69968,93680,59760,124144,
69984,36064,59776,124160,70000,36080,59792,124176,70016,36096,59808,124192,70032,36112,59824,124208,70048,36128,59840,124224,70064,36144,59856,124240,70080,36160,59872,124256,70096,36176,59888,124272,
70112,36192,100576,124288,70128,36208,100592,124304,78048,36224,100608,124320,78064,36240,100624,124336,78080,36256,100640,124352,78096,36272,100656,124368,78112,36288,100672,124384,78128,36304,100688,124400,
78144,36320,100704,132320,78160,36336,100720,132336,78176,44256,100736,132352,78192,44272,100752,132368,78208,44288,100768,132384,78224,44304,100784,132400,78240,44320,100800,132416,78256,44336,100816,132432,
78272,44352,100832,132448,78288,44368,100848,132464,78304,44384,108768,132480,78320,44400,108784,132496,86240,44416,108800,132512,86256,44432,108816,132528,86272,44448,108832,132544,86288,44464,108848,132560,
86304,44480,108864,132576,86320,44496,108880,132592,86336,44512,108896,140512,86352,44528,108912,140528,86368,52448,108928,140544,86384,52464,108944,140560,86400,52480,108960,140576,86416,52496,108976,140592,
86432,52512,108992,140608,86448,52528,109008,140624,86464,52544,109024,140640,86480,52560,109040,140656,86496,52576,116960,140672,86512,52592,116976,140688,94432,52608,116992,140704,94448,52624,117008,140720,
19360,23072,131136,0,19376,23088,131144,16,19392,23104,131152,32,19408,23120,131160,48,19424,23808,131360,64,19440,23824,131368,80,19456,23840,131376,96,19472,23856,131384,112,
19488,23872,131584,128,19504,23888,131592,144,19520,23904,131600,160,19536,23920,131608,176,20224,23936,131808,192,20240,23952,131816,208,20256,23968,131824,224,20272,23984,131832,240,
20288,24000,132032,256,20304,24016,132040,272,20320,24704,132048,288,20336,24720,132056,304,20352,24736,132256,320,20368,24752,132264,336,20384,24768,132272,352,20400,24784,132280,368,
20416,24800,132480,384,20432,24816,132488,400,21120,24832,132496,416,21136,24848,132504,432,21152,24864,132704,448,21168,24880,132712,464,21184,24896,132720,480,21200,24912,132728,496,
21216,11264,134720,512,21232,11280,134728,528,21248,11296,134736,544,21264,11312,134744,560,21280,11328,134944,576,21296,11344,134952,592,21312,11360,134960,608,21328,11376,134968,624,
22016,11392,135168,640,22032,11408,135176,656,22048,11424,135184,672,22064,11440,135192,688,22080,11456,135392,704,22096,11472,135400,720,22112,12160,135408,736,22128,12176,135416,752,
22144,12192,135616,768,22160,12208,135624,784,22176,12224,135632,800,22192,12240,135640,816,22208,12256,135840,832,22224,12272,135848,848,22912,12288,135856,864,22928,12304,135864,880,
22944,12320,136064,896,22960,12336,136072,912,22976,12352,136080,928,22992,12368,136088,944,23008,13056,136288,960,23024,13072,136296,976,23040,13088,136304,992,23056,13104,136312,1008,
0,141312,141344,0,8,141376,141408,16,16,141440,141472,32,24,141504,141536,48,32,141616,141584,512,40,141680,141648,528,48,141744,141712,544,56,141808,141776,560,
64,141856,141824,1024,72,141920,141888,1040,80,141984,141952,1056,88,142048,142016,1072,96,142096,142128,1536,104,142160,142192,1552,112,142224,142256,1568,120,142288,142320,1584,
128,141320,141352,2096,136,141384,141416,2080,144,141448,141480,2064,152,141512,141544,2048,160,141624,141592,2608,168,141688,141656,2592,176,141752,141720,2576,184,141816,141784,2560,
192,141864,141832,3120,200,141928,141896,3104,208,141992,141960,3088,216,142056,142024,3072,224,142104,142136,3632,232,142168,142200,3616,240,142232,142264,3600,248,142296,142328,3584,
256,141328,141360,4128,264,141392,141424,4144,272,141456,141488,4096,280,141520,141552,4112,288,141600,141568,4640,296,141664,141632,4656,304,141728,141696,4608,312,141792,141760,4624,
320,141872,141840,5152,328,141936,141904,5168,336,142000,141968,5120,344,142064,142032,5136,352,142080,142112,5664,360,142144,142176,5680,368,142208,142240,5632,376,142272,142304,5648,
384,141336,141368,6160,392,141400,141432,6144,400,141464,141496,6192,408,141528,141560,6176,416,141608,141576,6672,424,141672,141640,6656,432,141736,141704,6704,440,141800,141768,6688,
440,141880,141848,7184,440,141944,141912,7168,440,142008,141976,7216,440,142072,142040,7200,440,142088,142120,7696,440,142152,142184,7680,440,142216,142248,7728,440,142280,142312,7712,
143808,208192,166368,230752,143824,208208,166384,230768,143840,208224,174304,230784,143856,208240,174320,230800,151776,208256,174336,230816,151792,208272,174352,230832,151808,208288,174368,230848,151824,208304,174384,230864,
151840,208320,174400,230880,151856,208336,174416,230896,151872,208352,174432,238816,151888,208368,174448,238832,151904,216288,174464,238848,151920,216304,174480,238864,151936,216320,174496,238880,151952,216336,174512,238896,
151968,216352,174528,238912,151984,216368,174544,238928,152000,216384,174560,238944,152016,216400,174576,238960,152032,216416,182496,238976,152048,216432,182512,238992,159968,216448,182528,239008,159984,216464,182544,239024,
160000,216480,182560,239040,160016,216496,182576,239056,160032,216512,182592,239072,160048,216528,182608,239088,160064,216544,182624,247008,160080,216560,182640,247024,160096,224480,182656,247040,160112,224496,182672,247056,
160128,224512,182688,247072,160144,224528,182704,247088,160160,224544,182720,247104,160176,224560,182736,247120,160192,224576,182752,247136,160208,224592,182768,247152,160224,224608,190688,247168,160240,224624,190704,247184,
200928,224640,190720,247200,200944,224656,190736,247216,200960,224672,190752,247232,200976,224688,190768,247248,200992,224704,190784,247264,201008,224720,190800,247280,201024,224736,190816,255200,201040,224752,190832,255216,
201056,167136,190848,255232,201072,167152,190864,255248,201088,167168,190880,255264,201104,167184,190896,255280,201120,167200,190912,255296,201136,167216,190928,255312,201152,167232,190944,255328,201168,167248,190960,255344,
201184,167264,231648,255360,201200,167280,231664,255376,209120,167296,231680,255392,209136,167312,231696,255408,209152,167328,231712,255424,209168,167344,231728,255440,209184,167360,231744,255456,209200,167376,231760,255472,
4096,10368,6560,12832,4112,10384,6576,12848,4128,10400,6592,12864,4144,10416,6608,12880,4160,10432,6624,13568,4176,10448,6640,13584,4192,11136,6656,13600,4208,11152,6672,13616,
4224,11168,6688,13632,4240,11184,6704,13648,4256,11200,6720,13664,4272,11216,6736,13680,4288,11232,7424,13696,4304,11248,7440,13712,4992,11264,7456,13728,5008,11280,7472,13744,
5024,11296,7488,13760,5040,11312,7504,13776,5056,11328,7520,14464,5072,11344,7536,14480,5088,12032,7552,14496,5104,12048,7568,14512,5120,12064,7584,14528,5136,12080,7600,14544,
5152,12096,7616,14560,5168,12112,7632,14576,5184,12128,8320,14592,5200,12144,8336,14608,5888,12160,8352,14624,5904,12176,8368,14640,5920,12192,8384,14656,5936,12208,8400,14672,
5952,12224,8416,15360,5968,12240,8432,15376,5984,12928,8448,15392,6000,12944,8464,15408,6016,12960,8480,15424,6032,12976,8496,15440,6048,12992,8512,15456,6064,13008,8528,15472,
6080,13024,12800,15488,6096,13040,12816,15504,6784,13056,12832,15520,6800,13072,12848,15536,6816,13088,12864,15552,6832,13104,12880,15568,6848,13120,12896,16256,6864,13136,12912,16272,
6880,6656,12928,16288,6896,6672,12944,16304,6912,6688,12960,16320,6928,6704,12976,16336,6944,6720,12992,16352,6960,6736,13008,16368,6976,6752,13696,16384,6992,6768,13712,16400,
11264,6784,13728,16416,11280,6800,13744,16432,11296,6816,13760,16448,11312,6832,13776,16464,11328,6848,13792,17152,11344,6864,13808,17168,11360,7552,13824,17184,11376,7568,13840,17200,
20288,26560,22752,0,20304,26576,22768,0,20320,27264,22784,0,20336,27280,22800,0,20352,27296,22816,0,20368,27312,22832,0,20384,27328,22848,0,20400,27344,22864,0,
20416,27360,27136,0,20432,27376,27152,0,21120,27392,27168,0,21136,27408,27184,0,21152,27424,27200,0,21168,27440,27216,0,21184,27456,27232,0,21200,27472,27248,0,
21216,20992,27264,0,21232,21008,27280,0,21248,21024,27296,0,21264,21040,27312,0,21280,21056,27328,0,21296,21072,27344,0,21312,21088,28032,0,21328,21104,28048,0,
25600,21120,28064,0,25616,21136,28080,0,25632,21152,28096,0,25648,21168,28112,0,25664,21184,28128,0,25680,21200,28144,0,25696,21888,28160,0,25712,21904,28176,0,
25728,21920,28192,0,25744,21936,28208,0,25760,21952,28224,0,25776,21968,28240,0,25792,21984,28928,0,25808,22000,28944,0,26496,22016,28960,0,26512,22032,28976,0,
26528,22048,28992,0,26544,22064,29008,0,26560,22080,29024,0,26576,22096,29040,0,26592,22784,29056,0,26608,22800,29072,0,26624,22816,29088,0,26640,22832,29104,0,
26656,22848,29120,0,26672,22864,29136,0,26688,22880,29824,0,26704,22896,29840,0,27392,22912,29856,0,27408,22928,29872,0,27424,22944,29888,0,27440,22960,29904,0,
27456,22976,29920,0,27472,22992,29936,0,27488,23680,29952,0,27504,23696,29968,0,27520,23712,29984,0,27536,23728,30000,0,27552,23744,30016,0,27568,23760,30032,0,
0,0,0,0,2130706432,32,8,8,2130706432,64,16,16,2130706432,96,24,24,2130706432,0,0,32,2130706432,32,8,40,2130706432,64,16,48,2130706432,96,24,56,
2130706432,0,0,64,2130706432,32,8,72,2130706432,64,16,80,2130706432,96,24,88,2130706432,0,0,96,2130706432,32,8,104,2130706432,64,16,112,2130706432,96,24,120,
2130706432,0,0,128,2130706432,32,8,136,2130706432,64,16,144,2130706432,96,24,152,2130706432,0,0,160,2130706432,32,8,168,2130706432,64,16,176,2130706432,96,24,184,
2130706432,0,0,192,2130706432,32,8,200,2130706432,64,16,208,2130706432,96,24,216,2130706432,0,0,224,2130706432,32,8,232,2130706432,64,16,240,2130706432,96,24,248,
2130706432,0,0,256,2130706432,32,8,264,2130706432,64,16,272,2130706432,96,24,280,2130706432,0,0,288,2130706432,32,8,296,2130706432,64,16,304,2130706432,96,24,312,
2130706432,0,0,320,2130706432,32,8,328,2130706432,64,16,336,2130706432,96,24,344,2130706432,0,0,352,2130706432,32,8,360,2130706432,64,16,368,2130706432,96,24,376,
2130706432,0,0,384,2130706432,32,8,392,2130706432,64,16,400,2130706432,96,24,408,2130706432,0,0,416,2130706432,32,8,424,2130706432,64,16,432,2130706432,96,24,440,
2130706432,0,0,448,2130706432,32,8,456,2130706432,64,16,464,2130706432,96,24,472,2130706432,0,0,480,2130706432,32,8,488,2130706432,64,16,496,2130706432,96,24,504,
0,0,0,0,16,8,0,0,32,16,0,0,48,24,0,0,64,32,0,0,80,40,0,0,96,48,0,0,112,56,0,0,
128,64,0,0,144,72,0,0,160,80,0,0,176,88,0,0,192,96,0,0,208,104,0,0,224,112,0,0,240,120,0,0,
256,128,0,0,272,136,0,0,288,144,0,0,304,152,0,0,320,160,0,0,336,168,0,0,352,176,0,0,368,184,0,0,
384,192,0,0,400,200,0,0,416,208,0,0,432,216,0,0,448,224,0,0,464,232,0,0,480,240,0,0,496,248,0,0,
512,0,0,0,528,8,0,0,544,16,0,0,560,24,0,0,576,32,0,0,592,40,0,0,608,48,0,0,624,56,0,0,
640,64,0,0,656,72,0,0,672,80,0,0,688,88,0,0,704,96,0,0,720,104,0,0,736,112,0,0,752,120,0,0,
768,128,0,0,784,136,0,0,800,144,0,0,816,152,0,0,832,160,0,0,848,168,0,0,864,176,0,0,880,184,0,0,
896,192,0,0,912,200,0,0,928,208,0,0,944,216,0,0,960,224,0,0,976,232,0,0,992,240,0,0,1008,248,0,0,
0,139264,139264,4096,16,139344,139280,4112,32,139424,139296,4128,48,139504,139312,4144,896,139584,139344,4160,912,139664,139360,4176,928,139744,139376,4192,944,139824,139392,4208,
1792,139904,139424,4224,1808,139984,139440,4240,1824,140064,139456,4256,1840,140144,139472,4272,2688,140224,139504,4288,2704,140304,139520,4304,2720,140384,139536,4320,2736,140464,139552,4336,
3584,139272,139584,4352,3600,139352,139600,4368,3616,139432,139616,4384,3632,139512,139632,4400,4480,139592,139664,4416,4496,139672,139680,4432,4512,139752,139696,4448,4528,139832,139712,4464,
5376,139912,139744,4480,5392,139992,139760,4496,5408,140072,139776,4512,5424,140152,139792,4528,6272,140232,139824,4544,6288,140312,139840,4560,6304,140392,139856,4576,6320,140472,139872,4592,
7168,139280,139904,4608,7184,139360,139920,4624,7200,139440,139936,4640,7216,139520,139952,4656,8064,139600,139984,4672,8080,139680,140000,4688,8096,139760,140016,4704,8112,139840,140032,4720,
8960,139920,140064,4736,8976,140000,140080,4752,8992,140080,140096,4768,9008,140160,140112,4784,9856,140240,140144,4800,9872,140320,140160,4816,9888,140400,140176,4832,9904,140480,140192,4848,
10752,139288,140224,4864,10768,139368,140240,4880,10784,139448,140256,4896,10800,139528,140272,4912,11648,139608,140304,4928,11664,139688,140320,4944,11680,139768,140336,4960,11696,139848,140352,4976,
12544,139928,140384,4992,12560,140008,140400,5008,12576,140088,140416,5024,12592,140168,140432,5040,13440,140248,140464,5056,13456,140328,140480,5072,13472,140408,140496,5088,13488,140488,140512,5104,
0,15360,15392,8192,16,15424,15456,8208,32,15488,15520,8224,48,15552,15584,8240,896,15664,15632,8256,912,15728,15696,8272,928,15792,15760,8288,944,15856,15824,8304,
1792,15904,15872,8320,1808,15968,15936,8336,1824,16032,16000,8352,1840,16096,16064,8368,2688,16144,16176,8384,2704,16208,16240,8400,2720,16272,16304,8416,2736,16336,16368,8432,
3632,15368,15400,8448,3616,15432,15464,8464,3600,15496,15528,8480,3584,15560,15592,8496,4528,15672,15640,8512,4512,15736,15704,8528,4496,15800,15768,8544,4480,15864,15832,8560,
5424,15912,15880,8576,5408,15976,15944,8592,5392,16040,16008,8608,5376,16104,16072,8624,6320,16152,16184,8640,6304,16216,16248,8656,6288,16280,16312,8672,6272,16344,16376,8688,
7200,15376,15408,8704,7216,15440,15472,8720,7168,15504,15536,8736,7184,15568,15600,8752,8096,15648,15616,8768,8112,15712,15680,8784,8064,15776,15744,8800,8080,15840,15808,8816,
8992,15920,15888,8832,9008,15984,15952,8848,8960,16048,16016,8864,8976,16112,16080,8880,9888,16128,16160,8896,9904,16192,16224,8912,9856,16256,16288,8928,9872,16320,16352,8944,
10768,15384,15416,8960,10752,15448,15480,8976,10800,15512,15544,8992,10784,15576,15608,9008,11664,15656,15624,9024,11648,15720,15688,9040,11696,15784,15752,9056,11680,15848,15816,9072,
12560,15928,15896,9088,12544,15992,15960,9104,12592,16056,16024,9120,12576,16120,16088,9136,13456,16136,16168,9152,13440,16200,16232,9168,13488,16264,16296,9184,13472,16328,16360,9200,
16256,0,0,0,1065353216,0,0,0,0,16256,0,0,0,1065353216,0,0,0,0,0,0,0,0,0,0,0,0,0,0,0,0,0,0,
0,0,0,0,0,0,0,0,0,0,0,0,0,0,0,0,0,0,0,0,0,0,0,0,0,0,0,0,0,0,0,0,
0,0,0,0,0,0,0,0,0,0,0,0,0,0,0,0,16256,0,0,0,1065353216,0,0,0,0,16256,0,0,0,1065353216,0,0,
0,0,0,0,0,0,0,0,0,0,0,0,0,0,0,0,0,0,0,0,0,0,0,0,0,0,0,0,0,0,0,0,
0,0,0,0,0,0,0,0,0,0,0,0,0,0,0,0,0,0,0,0,0,0,0,0,0,0,0,0,0,0,0,0,
16256,0,0,0,1065353216,0,0,0,0,16256,0,0,0,1065353216,0,0,0,0,0,0,0,0,0,0,0,0,0,0,0,0,0,0,
0,0,0,0,0,0,0,0,0,0,0,0,0,0,0,0,0,0,0,0,0,0,0,0,0,0,0,0,0,0,0,0,
0,0,0,0,0,0,0,0,0,0,0,0,0,0,0,0,16256,0,0,0,1065353216,0,0,0,0,16256,0,0,0,1065353216,0,0,
0,0,16256,0,0,0,1065353216,0,0,0,0,16256,0,0,0,1065353216,0,0,0,0,0,0,0,0,0,0,0,0,0,0,0,0,
0,0,0,0,0,0,0,0,0,0,0,0,0,0,0,0,0,0,0,0,0,0,0,0,0,0,0,0,0,0,0,0,
0,0,0,0,0,0,0,0,0,0,0,0,0,0,0,0,0,0,16256,0,0,0,1065353216,0,0,0,0,16256,0,0,0,1065353216,
0,0,0,0,0,0,0,0,0,0,0,0,0,0,0,0,0,0,0,0,0,0,0,0,0,0,0,0,0,0,0,0,
0,0,0,0,0,0,0,0,0,0,0,0,0,0,0,0,0,0,0,0,0,0,0,0,0,0,0,0,0,0,0,0,
0,0,16256,0,0,0,1065353216,0,0,0,0,16256,0,0,0,1065353216,0,0,0,0,0,0,0,0,0,0,0,0,0,0,0,0,
0,0,0,0,0,0,0,0,0,0,0,0,0,0,0,0,0,0,0,0,0,0,0,0,0,0,0,0,0,0,0,0,
0,0,0,0,0,0,0,0,0,0,0,0,0,0,0,0,0,0,16256,0,0,0,1065353216,0,0,0,0,16256,0,0,0,1065353216,
4096,31872,20000,47776,4112,31888,20016,47792,4128,31904,20032,47808,4144,31920,20048,47824,4160,31936,20064,51200,4176,31952,20080,51216,4192,35328,20096,51232,4208,35344,20112,51248,
4224,35360,20128,51264,4240,35376,20144,51280,4256,35392,20160,51296,4272,35408,20176,51312,4288,35424,23552,51328,4304,35440,23568,51344,7680,35456,23584,51360,7696,35472,23600,51376,
7712,35488,23616,51392,7728,35504,23632,51408,7744,35520,23648,54784,7760,35536,23664,54800,7776,38912,23680,54816,7792,38928,23696,54832,7808,38944,23712,54848,7824,38960,23728,54864,
7840,38976,23744,54880,7856,38992,23760,54896,7872,39008,27136,54912,7888,39024,27152,54928,11264,39040,27168,54944,11280,39056,27184,54960,11296,39072,27200,54976,11312,39088,27216,54992,
11328,39104,27232,58368,11344,39120,27248,58384,11360,42496,27264,58400,11376,42512,27280,58416,11392,42528,27296,58432,11408,42544,27312,58448,11424,42560,27328,58464,11440,42576,27344,58480,
11456,42592,45056,58496,11472,42608,45072,58512,14848,42624,45088,58528,14864,42640,45104,58544,14880,42656,45120,58560,14896,42672,45136,58576,14912,42688,45152,61952,14928,42704,45168,61968,
14944,17408,45184,61984,14960,17424,45200,62000,14976,17440,45216,62016,14992,17456,45232,62032,15008,17472,45248,62048,15024,17488,45264,62064,15040,17504,48640,62080,15056,17520,48656,62096,
32768,17536,48672,62112,32784,17552,48688,62128,32800,17568,48704,62144,32816,17584,48720,62160,32832,17600,48736,65536,32848,17616,48752,65552,32864,20992,48768,65568,32880,21008,48784,65584,
68672,96448,84576,0,68688,96464,84592,0,68704,99840,84608,0,68720,99856,84624,0,68736,99872,84640,0,68752,99888,84656,0,68768,99904,84672,0,68784,99920,84688,0,
68800,99936,102400,0,68816,99952,102416,0,72192,99968,102432,0,72208,99984,102448,0,72224,100000,102464,0,72240,100016,102480,0,72256,100032,102496,0,72272,100048,102512,0,
72288,74752,102528,0,72304,74768,102544,0,72320,74784,102560,0,72336,74800,102576,0,72352,74816,102592,0,72368,74832,102608,0,72384,74848,105984,0,72400,74864,106000,0,
90112,74880,106016,0,90128,74896,106032,0,90144,74912,106048,0,90160,74928,106064,0,90176,74944,106080,0,90192,74960,106096,0,90208,78336,106112,0,90224,78352,106128,0,
90240,78368,106144,0,90256,78384,106160,0,90272,78400,106176,0,90288,78416,106192,0,90304,78432,109568,0,90320,78448,109584,0,93696,78464,109600,0,93712,78480,109616,0,
93728,78496,109632,0,93744,78512,109648,0,93760,78528,109664,0,93776,78544,109680,0,93792,81920,109696,0,93808,81936,109712,0,93824,81952,109728,0,93840,81968,109744,0,
93856,81984,109760,0,93872,82000,109776,0,93888,82016,113152,0,93904,82032,113168,0,97280,82048,113184,0,97296,82064,113200,0,97312,82080,113216,0,97328,82096,113232,0,
97344,82112,113248,0,97360,82128,113264,0,97376,85504,113280,0,97392,85520,113296,0,97408,85536,113312,0,97424,85552,113328,0,97440,85568,113344,0,97456,85584,113360,0,
4128,31904,20032,47808,4144,31920,20048,47824,4160,31936,20064,47616,4176,31952,20080,47632,4192,31744,20096,51232,4208,31760,20112,51248,4224,35360,20128,51264,4240,35376,20144,51280,
4256,35392,20160,51296,4272,35408,20176,51312,4288,35424,19968,51328,4304,35440,19984,51344,4096,35456,23584,51360,4112,35472,23600,51376,7712,35488,23616,51392,7728,35504,23632,51408,
7744,35520,23648,51200,7760,35536,23664,51216,7776,35328,23680,54816,7792,35344,23696,54832,7808,38944,23712,54848,7824,38960,23728,54864,7840,38976,23744,54880,7856,38992,23760,54896,
7872,39008,23552,54912,7888,39024,23568,54928,7680,39040,27168,54944,7696,39056,27184,54960,11296,39072,27200,54976,11312,39088,27216,54992,11328,39104,27232,54784,11344,39120,27248,54800,
11360,38912,27264,58400,11376,38928,27280,58416,11392,42528,27296,58432,11408,42544,27312,58448,11424,42560,27328,58464,11440,42576,27344,58480,11456,42592,27136,58496,11472,42608,27152,58512,
11264,42624,45088,58528,11280,42640,45104,58544,14880,42656,45120,58560,14896,42672,45136,58576,14912,42688,45152,58368,14928,42704,45168,58384,14944,42496,45184,61984,14960,42512,45200,62000,
14976,17440,45216,62016,14992,17456,45232,62032,15008,17472,45248,62048,15024,17488,45264,62064,15040,17504,45056,62080,15056,17520,45072,62096,14848,17536,48672,62112,14864,17552,48688,62128,
32800,17568,48704,62144,32816,17584,48720,62160,32832,17600,48736,61952,32848,17616,48752,61968,32864,17408,48768,65568,32880,17424,48784,65584,32896,21024,48800,65600,32912,21040,48816,65616,
68704,96256,84608,0,68720,96272,84624,0,68736,99872,84640,0,68752,99888,84656,0,68768,99904,84672,0,68784,99920,84688,0,68800,99936,84480,0,68816,99952,84496,0,
68608,99968,102432,0,68624,99984,102448,0,72224,100000,102464,0,72240,100016,102480,0,72256,100032,102496,0,72272,100048,102512,0,72288,99840,102528,0,72304,99856,102544,0,
72320,74784,102560,0,72336,74800,102576,0,72352,74816,102592,0,72368,74832,102608,0,72384,74848,102400,0,72400,74864,102416,0,72192,74880,106016,0,72208,74896,106032,0,
90144,74912,106048,0,90160,74928,106064,0,90176,74944,106080,0,90192,74960,106096,0,90208,74752,106112,0,90224,74768,106128,0,90240,78368,106144,0,90256,78384,106160,0,
90272,78400,106176,0,90288,78416,106192,0,90304,78432,105984,0,90320,78448,106000,0,90112,78464,109600,0,90128,78480,109616,0,93728,78496,109632,0,93744,78512,109648,0,
93760,78528,109664,0,93776,78544,109680,0,93792,78336,109696,0,93808,78352,109712,0,93824,81952,109728,0,93840,81968,109744,0,93856,81984,109760,0,93872,82000,109776,0,
93888,82016,109568,0,93904,82032,109584,0,93696,82048,113184,0,93712,82064,113200,0,97312,82080,113216,0,97328,82096,113232,0,97344,82112,113248,0,97360,82128,113264,0,
97376,81920,113280,0,97392,81936,113296,0,97408,85536,113312,0,97424,85552,113328,0,97440,85568,113344,0,97456,85584,113360,0,97472,85600,113152,0,97488,85616,113168,0,
16384,81920,0,0,16448,81984,0,0,16512,82048,0,0,16576,82112,0,0,16688,82224,0,0,16752,82288,0,0,16816,82352,0,0,16880,82416,0,0,
16928,82464,0,0,16992,82528,0,0,17056,82592,0,0,17120,82656,0,0,17168,82704,0,0,17232,82768,0,0,17296,82832,0,0,17360,82896,0,0,
16400,81936,0,0,16464,82000,0,0,16528,82064,0,0,16592,82128,0,0,16672,82208,0,0,16736,82272,0,0,16800,82336,0,0,16864,82400,0,0,
16944,82480,0,0,17008,82544,0,0,17072,82608,0,0,17136,82672,0,0,17152,82688,0,0,17216,82752,0,0,17280,82816,0,0,17344,82880,0,0,
16416,81952,0,0,16480,82016,0,0,16544,82080,0,0,16608,82144,0,0,16656,82192,0,0,16720,82256,0,0,16784,82320,0,0,16848,82384,0,0,
16896,82432,0,0,16960,82496,0,0,17024,82560,0,0,17088,82624,0,0,17200,82736,0,0,17264,82800,0,0,17328,82864,0,0,17392,82928,0,0,
16432,81968,0,0,16496,82032,0,0,16560,82096,0,0,16624,82160,0,0,16640,82176,0,0,16704,82240,0,0,16768,82304,0,0,16832,82368,0,0,
16912,82448,0,0,16976,82512,0,0,17040,82576,0,0,17104,82640,0,0,17184,82720,0,0,17248,82784,0,0,17312,82848,0,0,17376,82912,0,0,
0,65536,0,65536,8,65544,8,65544,16,65552,16,65552,24,65560,24,65560,224,65760,288,65824,232,65768,296,65832,240,65776,304,65840,248,65784,312,65848,
448,65984,576,66112,456,65992,584,66120,464,66000,592,66128,472,66008,600,66136,672,66208,864,66400,680,66216,872,66408,688,66224,880,66416,696,66232,888,66424,
896,66432,1152,66688,904,66440,1160,66696,912,66448,1168,66704,920,66456,1176,66712,1120,66656,1440,66976,1128,66664,1448,66984,1136,66672,1456,66992,1144,66680,1464,67000,
1344,66880,1728,67264,1352,66888,1736,67272,1360,66896,1744,67280,1368,66904,1752,67288,1568,67104,2016,67552,1576,67112,2024,67560,1584,67120,2032,67568,1592,67128,2040,67576,
3584,69120,4608,70144,3592,69128,4616,70152,3600,69136,4624,70160,3608,69144,4632,70168,3808,69344,4896,70432,3816,69352,4904,70440,3824,69360,4912,70448,3832,69368,4920,70456,
4032,69568,5184,70720,4040,69576,5192,70728,4048,69584,5200,70736,4056,69592,5208,70744,4256,69792,5472,71008,4264,69800,5480,71016,4272,69808,5488,71024,4280,69816,5496,71032,
4480,70016,5760,71296,4488,70024,5768,71304,4496,70032,5776,71312,4504,70040,5784,71320,4704,70240,6048,71584,4712,70248,6056,71592,4720,70256,6064,71600,4728,70264,6072,71608,
4928,70464,6336,71872,4936,70472,6344,71880,4944,70480,6352,71888,4952,70488,6360,71896,5152,70688,6624,72160,5160,70696,6632,72168,5168,70704,6640,72176,5176,70712,6648,72184,
65536,147456,147456,0,65552,147472,147472,0,65568,147488,147488,0,65584,147504,147504,0,65600,147520,147520,0,65616,147536,147536,0,65632,147552,147552,0,65648,147568,147568,0,
65664,147584,147584,0,65680,147600,147600,0,65696,147616,147616,0,65712,147632,147632,0,65728,147648,147648,0,65744,147664,147664,0,65760,147680,147680,0,65776,147696,147696,0,
65792,147464,147712,0,65808,147480,147728,0,65824,147496,147744,0,65840,147512,147760,0,65856,147528,147776,0,65872,147544,147792,0,65888,147560,147808,0,65904,147576,147824,0,
65920,147592,147840,0,65936,147608,147856,0,65952,147624,147872,0,65968,147640,147888,0,65984,147656,147904,0,66000,147672,147920,0,66016,147688,147936,0,66032,147704,147952,0,
66048,147712,147968,0,66064,147728,147984,0,66080,147744,148000,0,66096,147760,148016,0,66112,147776,148032,0,66128,147792,148048,0,66144,147808,148064,0,66160,147824,148080,0,
66176,147840,148096,0,66192,147856,148112,0,66208,147872,148128,0,66224,147888,148144,0,66240,147904,148160,0,66256,147920,148176,0,66272,147936,148192,0,66288,147952,148208,0,
66304,147720,148224,0,66320,147736,148240,0,66336,147752,148256,0,66352,147768,148272,0,66368,147784,148288,0,66384,147800,148304,0,66400,147816,148320,0,66416,147832,148336,0,
66432,147848,148352,0,66448,147864,148368,0,66464,147880,148384,0,66480,147896,148400,0,66496,147912,148416,0,66512,147928,148432,0,66528,147944,148448,0,66544,147960,148464,0,
147456,0,0,0,147520,72,128,128,147584,144,256,256,147648,216,384,384,147760,896,512,8192,147824,968,640,8320,147888,1040,768,8448,147952,1112,896,8576,
148000,1792,1024,16384,148064,1864,1152,16512,148128,1936,1280,16640,148192,2008,1408,16768,148240,2688,1536,24576,148304,2760,1664,24704,148368,2832,1792,24832,148432,2904,1920,24960,
147472,2904,1920,24960,147536,2904,1920,24960,147600,2904,1920,24960,147664,2904,1920,24960,147744,2904,1920,24960,147808,2904,1920,24960,147872,2904,1920,24960,147936,2904,1920,24960,
148016,2904,1920,24960,148080,2904,1920,24960,148144,2904,1920,24960,148208,2904,1920,24960,148224,2904,1920,24960,148288,2904,1920,24960,148352,2904,1920,24960,148416,2904,1920,24960,
147488,2904,1920,24960,147552,2904,1920,24960,147616,2904,1920,24960,147680,2904,1920,24960,147728,2904,1920,24960,147792,2904,1920,24960,147856,2904,1920,24960,147920,2904,1920,24960,
147968,2904,1920,24960,148032,2904,1920,24960,148096,2904,1920,24960,148160,2904,1920,24960,148272,2904,1920,24960,148336,2904,1920,24960,148400,2904,1920,24960,148464,2904,1920,24960,
147504,2904,1920,24960,147568,2904,1920,24960,147632,2904,1920,24960,147696,2904,1920,24960,147712,2904,1920,24960,147776,2904,1920,24960,147840,2904,1920,24960,147904,2904,1920,24960,
147984,2904,1920,24960,148048,2904,1920,24960,148112,2904,1920,24960,148176,2904,1920,24960,148256,2904,1920,24960,148320,2904,1920,24960,148384,2904,1920,24960,148448,2904,1920,24960,
4096,68736,43040,107680,4112,68752,43056,107696,4128,68768,43072,107712,4144,68784,43088,107728,4160,68800,43104,115712,4176,68816,43120,115728,4192,76800,43136,115744,4208,76816,43152,115760,
4224,76832,43168,115776,4240,76848,43184,115792,4256,76864,43200,115808,4272,76880,43216,115824,4288,76896,51200,115840,4304,76912,51216,115856,12288,76928,51232,115872,12304,76944,51248,115888,
12320,76960,51264,115904,12336,76976,51280,115920,12352,76992,51296,123904,12368,77008,51312,123920,12384,84992,51328,123936,12400,85008,51344,123952,12416,85024,51360,123968,12432,85040,51376,123984,
12448,85056,51392,124000,12464,85072,51408,124016,12480,85088,59392,124032,12496,85104,59408,124048,20480,85120,59424,124064,20496,85136,59440,124080,20512,85152,59456,124096,20528,85168,59472,124112,
20544,85184,59488,132096,20560,85200,59504,132112,20576,93184,59520,132128,20592,93200,59536,132144,20608,93216,59552,132160,20624,93232,59568,132176,20640,93248,59584,132192,20656,93264,59600,132208,
20672,93280,100352,132224,20688,93296,100368,132240,28672,93312,100384,132256,28688,93328,100400,132272,28704,93344,100416,132288,28720,93360,100432,132304,28736,93376,100448,140288,28752,93392,100464,140304,
28768,35840,100480,140320,28784,35856,100496,140336,28800,35872,100512,140352,28816,35888,100528,140368,28832,35904,100544,140384,28848,35920,100560,140400,28864,35936,108544,140416,28880,35952,108560,140432,
69632,35968,108576,140448,69648,35984,108592,140464,69664,36000,108608,140480,69680,36016,108624,140496,69696,36032,108640,148480,69712,36048,108656,148496,69728,44032,108672,148512,69744,44048,108688,148528,
13120,16832,131200,0,13136,16848,131208,16,13152,17536,131216,32,13168,17552,131224,48,13184,17568,131424,64,13200,17584,131432,80,13216,17600,131440,96,13232,17616,131448,112,
13248,17632,131648,128,13264,17648,131656,144,13952,17664,131664,160,13968,17680,131672,176,13984,17696,131872,192,14000,17712,131880,208,14016,17728,131888,224,14032,17744,131896,240,
14048,25600,132096,256,14064,25616,132104,272,14080,25632,132112,288,14096,25648,132120,304,14112,25664,132320,320,14128,25680,132328,336,14144,25696,132336,352,14160,25712,132344,368,
14848,25728,132544,384,14864,25744,132552,400,14880,25760,132560,416,14896,25776,132568,432,14912,25792,132768,448,14928,25808,132776,464,14944,26496,132784,480,14960,26512,132792,496,
14976,26528,134784,512,14992,26544,134792,528,15008,26560,134800,544,15024,26576,134808,560,15040,26592,135008,576,15056,26608,135016,592,15744,26624,135024,608,15760,26640,135032,624,
15776,26656,135232,640,15792,26672,135240,656,15808,26688,135248,672,15824,26704,135256,688,15840,27392,135456,704,15856,27408,135464,720,15872,27424,135472,736,15888,27440,135480,752,
15904,27456,135680,768,15920,27472,135688,784,15936,27488,135696,800,15952,27504,135704,816,16640,27520,135904,832,16656,27536,135912,848,16672,27552,135920,864,16688,27568,135928,880,
16704,27584,136128,896,16720,27600,136136,912,16736,28288,136144,928,16752,28304,136152,944,16768,28320,136352,960,16784,28336,136360,976,16800,28352,136368,992,16816,28368,136376,1008,
0,143360,143392,0,8,143424,143456,16,16,143488,143520,32,24,143552,143584,48,32,143664,143632,512,40,143728,143696,528,48,143792,143760,544,56,143856,143824,560,
64,143904,143872,1024,72,143968,143936,1040,80,144032,144000,1056,88,144096,144064,1072,96,144144,144176,1536,104,144208,144240,1552,112,144272,144304,1568,120,144336,144368,1584,
128,143368,143400,2096,136,143432,143464,2080,144,143496,143528,2064,152,143560,143592,2048,160,143672,143640,2608,168,143736,143704,2592,176,143800,143768,2576,184,143864,143832,2560,
192,143912,143880,3120,200,143976,143944,3104,208,144040,144008,3088,216,144104,144072,3072,224,144152,144184,3632,232,144216,144248,3616,240,144280,144312,3600,248,144344,144376,3584,
256,143376,143408,4128,264,143440,143472,4144,272,143504,143536,4096,280,143568,143600,4112,288,143648,143616,4640,296,143712,143680,4656,304,143776,143744,4608,312,143840,143808,4624,
320,143920,143888,5152,328,143984,143952,5168,336,144048,144016,5120,344,144112,144080,5136,352,144128,144160,5664,360,144192,144224,5680,368,144256,144288,5632,376,144320,144352,5648,
384,143384,143416,6160,392,143448,143480,6144,400,143512,143544,6192,408,143576,143608,6176,416,143656,143624,6672,424,143720,143688,6656,432,143784,143752,6704,440,143848,143816,6688,
440,143928,143896,7184,440,143992,143960,7168,440,144056,144024,7216,440,144120,144088,7200,440,144136,144168,7696,440,144200,144232,7680,440,144264,144296,7728,440,144328,144360,7712,
151616,216256,190560,1248,151632,216272,190576,1264,151648,224256,190592,1280,151664,224272,190608,1296,151680,224288,190624,1312,151696,224304,190640,1328,151712,224320,190656,1344,151728,224336,190672,1360,
151744,224352,231424,1376,151760,224368,231440,1392,159744,224384,231456,1408,159760,224400,231472,1424,159776,224416,231488,1440,159792,224432,231504,1456,159808,224448,231520,1472,159824,224464,231536,1488,
159840,166912,231552,1504,159856,166928,231568,1520,159872,166944,231584,9440,159888,166960,231600,9456,159904,166976,231616,9472,159920,166992,231632,9488,159936,167008,239616,9504,159952,167024,239632,9520,
200704,167040,239648,9536,200720,167056,239664,9552,200736,167072,239680,9568,200752,167088,239696,9584,200768,167104,239712,9600,200784,167120,239728,9616,200800,175104,239744,9632,200816,175120,239760,9648,
200832,175136,239776,9664,200848,175152,239792,9680,200864,175168,239808,9696,200880,175184,239824,9712,200896,175200,247808,17632,200912,175216,247824,17648,208896,175232,247840,17664,208912,175248,247856,17680,
208928,175264,247872,17696,208944,175280,247888,17712,208960,175296,247904,17728,208976,175312,247920,17744,208992,183296,247936,17760,209008,183312,247952,17776,209024,183328,247968,17792,209040,183344,247984,17808,
209056,183360,248000,17824,209072,183376,248016,17840,209088,183392,256000,17856,209104,183408,256016,17872,217088,183424,256032,17888,217104,183440,256048,17904,217120,183456,256064,25824,217136,183472,256080,25840,
217152,183488,256096,25856,217168,183504,256112,25872,217184,191488,256128,25888,217200,191504,256144,25904,217216,191520,256160,25920,217232,191536,256176,25936,217248,191552,256192,25952,217264,191568,256208,25968,
4096,10368,6560,12832,4112,10384,6576,12848,4128,10400,6592,12864,4144,10416,6608,12880,4160,10432,6624,13568,4176,10448,6640,13584,4192,11136,6656,13600,4208,11152,6672,13616,
4224,11168,6688,13632,4240,11184,6704,13648,4256,11200,6720,13664,4272,11216,6736,13680,4288,11232,7424,13696,4304,11248,7440,13712,4992,11264,7456,13728,5008,11280,7472,13744,
5024,11296,7488,13760,5040,11312,7504,13776,5056,11328,7520,14464,5072,11344,7536,14480,5088,12032,7552,14496,5104,12048,7568,14512,5120,12064,7584,14528,5136,12080,7600,14544,
5152,12096,7616,14560,5168,12112,7632,14576,5184,12128,8320,14592,5200,12144,8336,14608,5888,12160,8352,14624,5904,12176,8368,14640,5920,12192,8384,14656,5936,12208,8400,14672,
5952,12224,8416,15360,5968,12240,8432,15376,5984,12928,8448,15392,6000,12944,8464,15408,6016,12960,8480,15424,6032,12976,8496,15440,6048,12992,8512,15456,6064,13008,8528,15472,
6080,13024,12800,15488,6096,13040,12816,15504,6784,13056,12832,15520,6800,13072,12848,15536,6816,13088,12864,15552,6832,13104,12880,15568,6848,13120,12896,16256,6864,13136,12912,16272,
6880,6656,12928,16288,6896,6672,12944,16304,6912,6688,12960,16320,6928,6704,12976,16336,6944,6720,12992,16352,6960,6736,13008,16368,6976,6752,13696,16384,6992,6768,13712,16400,
11264,6784,13728,16416,11280,6800,13744,16432,11296,6816,13760,16448,11312,6832,13776,16464,11328,6848,13792,17152,11344,6864,13808,17168,11360,7552,13824,17184,11376,7568,13840,17200,
20288,26560,22752,0,20304,26576,22768,0,20320,27264,22784,0,20336,27280,22800,0,20352,27296,22816,0,20368,27312,22832,0,20384,27328,22848,0,20400,27344,22864,0,
20416,27360,27136,0,20432,27376,27152,0,21120,27392,27168,0,21136,27408,27184,0,21152,27424,27200,0,21168,27440,27216,0,21184,27456,27232,0,21200,27472,27248,0,
21216,20992,27264,0,21232,21008,27280,0,21248,21024,27296,0,21264,21040,27312,0,21280,21056,27328,0,21296,21072,27344,0,21312,21088,28032,0,21328,21104,28048,0,
25600,21120,28064,0,25616,21136,28080,0,25632,21152,28096,0,25648,21168,28112,0,25664,21184,28128,0,25680,21200,28144,0,25696,21888,28160,0,25712,21904,28176,0,
25728,21920,28192,0,25744,21936,28208,0,25760,21952,28224,0,25776,21968,28240,0,25792,21984,28928,0,25808,22000,28944,0,26496,22016,28960,0,26512,22032,28976,0,
26528,22048,28992,0,26544,22064,29008,0,26560,22080,29024,0,26576,22096,29040,0,26592,22784,29056,0,26608,22800,29072,0,26624,22816,29088,0,26640,22832,29104,0,
26656,22848,29120,0,26672,22864,29136,0,26688,22880,29824,0,26704,22896,29840,0,27392,22912,29856,0,27408,22928,29872,0,27424,22944,29888,0,27440,22960,29904,0,
27456,22976,29920,0,27472,22992,29936,0,27488,23680,29952,0,27504,23696,29968,0,27520,23712,29984,0,27536,23728,30000,0,27552,23744,30016,0,27568,23760,30032,0,
0,0,0,0,2130706432,32,8,8,2130706432,64,16,16,2130706432,96,24,24,2130706432,0,0,32,2130706432,32,8,40,2130706432,64,16,48,2130706432,96,24,56,
2130706432,0,0,64,2130706432,32,8,72,2130706432,64,16,80,2130706432,96,24,88,2130706432,0,0,96,2130706432,32,8,104,2130706432,64,16,112,2130706432,96,24,120,
2130706432,0,0,128,2130706432,32,8,136,2130706432,64,16,144,2130706432,96,24,152,2130706432,0,0,160,2130706432,32,8,168,2130706432,64,16,176,2130706432,96,24,184,
2130706432,0,0,192,2130706432,32,8,200,2130706432,64,16,208,2130706432,96,24,216,2130706432,0,0,224,2130706432,32,8,232,2130706432,64,16,240,2130706432,96,24,248,
2130706432,0,0,256,2130706432,32,8,264,2130706432,64,16,272,2130706432,96,24,280,2130706432,0,0,288,2130706432,32,8,296,2130706432,64,16,304,2130706432,96,24,312,
2130706432,0,0,320,2130706432,32,8,328,2130706432,64,16,336,2130706432,96,24,344,2130706432,0,0,352,2130706432,32,8,360,2130706432,64,16,368,2130706432,96,24,376,
2130706432,0,0,384,2130706432,32,8,392,2130706432,64,16,400,2130706432,96,24,408,2130706432,0,0,416,2130706432,32,8,424,2130706432,64,16,432,2130706432,96,24,440,
2130706432,0,0,448,2130706432,32,8,456,2130706432,64,16,464,2130706432,96,24,472,2130706432,0,0,480,2130706432,32,8,488,2130706432,64,16,496,2130706432,96,24,504,
0,0,0,0,16,8,0,0,32,16,0,0,48,24,0,0,64,32,0,0,80,40,0,0,96,48,0,0,112,56,0,0,
128,64,0,0,144,72,0,0,160,80,0,0,176,88,0,0,192,96,0,0,208,104,0,0,224,112,0,0,240,120,0,0,
256,128,0,0,272,136,0,0,288,144,0,0,304,152,0,0,320,160,0,0,336,168,0,0,352,176,0,0,368,184,0,0,
384,192,0,0,400,200,0,0,416,208,0,0,432,216,0,0,448,224,0,0,464,232,0,0,480,240,0,0,496,248,0,0,
512,0,0,0,528,8,0,0,544,16,0,0,560,24,0,0,576,32,0,0,592,40,0,0,608,48,0,0,624,56,0,0,
640,64,0,0,656,72,0,0,672,80,0,0,688,88,0,0,704,96,0,0,720,104,0,0,736,112,0,0,752,120,0,0,
768,128,0,0,784,136,0,0,800,144,0,0,816,152,0,0,832,160,0,0,848,168,0,0,864,176,0,0,880,184,0,0,
896,192,0,0,912,200,0,0,928,208,0,0,944,216,0,0,960,224,0,0,976,232,0,0,992,240,0,0,1008,248,0,0,
0,147456,147456,32768,16,147536,147472,32784,32,147616,147488,32800,48,147696,147504,32816,896,147776,147536,32832,912,147856,147552,32848,928,147936,147568,32864,944,148016,147584,32880,
1792,148096,147616,32896,1808,148176,147632,32912,1824,148256,147648,32928,1840,148336,147664,32944,2688,148416,147696,32960,2704,148496,147712,32976,2720,148576,147728,32992,2736,148656,147744,33008,
3584,147464,147776,33024,3600,147544,147792,33040,3616,147624,147808,33056,3632,147704,147824,33072,4480,147784,147856,33088,4496,147864,147872,33104,4512,147944,147888,33120,4528,148024,147904,33136,
5376,148104,147936,33152,5392,148184,147952,33168,5408,148264,147968,33184,5424,148344,147984,33200,6272,148424,148016,33216,6288,148504,148032,33232,6304,148584,148048,33248,6320,148664,148064,33264,
7168,147472,148096,33280,7184,147552,148112,33296,7200,147632,148128,33312,7216,147712,148144,33328,8064,147792,148176,33344,8080,147872,148192,33360,8096,147952,148208,33376,8112,148032,148224,33392,
8960,148112,148256,33408,8976,148192,148272,33424,8992,148272,148288,33440,9008,148352,148304,33456,9856,148432,148336,33472,9872,148512,148352,33488,9888,148592,148368,33504,9904,148672,148384,33520,
10752,147480,148416,33536,10768,147560,148432,33552,10784,147640,148448,33568,10800,147720,148464,33584,11648,147800,148496,33600,11664,147880,148512,33616,11680,147960,148528,33632,11696,148040,148544,33648,
12544,148120,148576,33664,12560,148200,148592,33680,12576,148280,148608,33696,12592,148360,148624,33712,13440,148440,148656,33728,13456,148520,148672,33744,13472,148600,148688,33760,13488,148680,148704,33776,
0,23552,23584,16384,16,23616,23648,16400,32,23680,23712,16416,48,23744,23776,16432,896,23856,23824,16448,912,23920,23888,16464,928,23984,23952,16480,944,24048,24016,16496,
1792,24096,24064,16512,1808,24160,24128,16528,1824,24224,24192,16544,1840,24288,24256,16560,2688,24336,24368,16576,2704,24400,24432,16592,2720,24464,24496,16608,2736,24528,24560,16624,
3632,23560,23592,16640,3616,23624,23656,16656,3600,23688,23720,16672,3584,23752,23784,16688,4528,23864,23832,16704,4512,23928,23896,16720,4496,23992,23960,16736,4480,24056,24024,16752,
5424,24104,24072,16768,5408,24168,24136,16784,5392,24232,24200,16800,5376,24296,24264,16816,6320,24344,24376,16832,6304,24408,24440,16848,6288,24472,24504,16864,6272,24536,24568,16880,
7200,23568,23600,16896,7216,23632,23664,16912,7168,23696,23728,16928,7184,23760,23792,16944,8096,23840,23808,16960,8112,23904,23872,16976,8064,23968,23936,16992,8080,24032,24000,17008,
8992,24112,24080,17024,9008,24176,24144,17040,8960,24240,24208,17056,8976,24304,24272,17072,9888,24320,24352,17088,9904,24384,24416,17104,9856,24448,24480,17120,9872,24512,24544,17136,
10768,23576,23608,17152,10752,23640,23672,17168,10800,23704,23736,17184,10784,23768,23800,17200,11664,23848,23816,17216,11648,23912,23880,17232,11696,23976,23944,17248,11680,24040,24008,17264,
12560,24120,24088,17280,12544,24184,24152,17296,12592,24248,24216,17312,12576,24312,24280,17328,13456,24328,24360,17344,13440,24392,24424,17360,13488,24456,24488,17376,13472,24520,24552,17392,
16256,0,0,0,1065353216,0,0,0,0,16256,0,0,0,1065353216,0,0,0,0,0,0,0,0,0,0,0,0,0,0,0,0,0,0,
0,0,0,0,0,0,0,0,0,0,0,0,0,0,0,0,0,0,0,0,0,0,0,0,0,0,0,0,0,0,0,0,
0,0,0,0,0,0,0,0,0,0,0,0,0,0,0,0,16256,0,0,0,1065353216,0,0,0,0,16256,0,0,0,1065353216,0,0,
0,0,0,0,0,0,0,0,0,0,0,0,0,0,0,0,0,0,0,0,0,0,0,0,0,0,0,0,0,0,0,0,
0,0,0,0,0,0,0,0,0,0,0,0,0,0,0,0,0,0,0,0,0,0,0,0,0,0,0,0,0,0,0,0,
16256,0,0,0,1065353216,0,0,0,0,16256,0,0,0,1065353216,0,0,0,0,0,0,0,0,0,0,0,0,0,0,0,0,0,0,
0,0,0,0,0,0,0,0,0,0,0,0,0,0,0,0,0,0,0,0,0,0,0,0,0,0,0,0,0,0,0,0,
0,0,0,0,0,0,0,0,0,0,0,0,0,0,0,0,16256,0,0,0,1065353216,0,0,0,0,16256,0,0,0,1065353216,0,0,
0,0,16256,0,0,0,1065353216,0,0,0,0,16256,0,0,0,1065353216,0,0,0,0,0,0,0,0,0,0,0,0,0,0,0,0,
0,0,0,0,0,0,0,0,0,0,0,0,0,0,0,0,0,0,0,0,0,0,0,0,0,0,0,0,0,0,0,0,
0,0,0,0,0,0,0,0,0,0,0,0,0,0,0,0,0,0,16256,0,0,0,1065353216,0,0,0,0,16256,0,0,0,1065353216,
0,0,0,0,0,0,0,0,0,0,0,0,0,0,0,0,0,0,0,0,0,0,0,0,0,0,0,0,0,0,0,0,
0,0,0,0,0,0,0,0,0,0,0,0,0,0,0,0,0,0,0,0,0,0,0,0,0,0,0,0,0,0,0,0,
0,0,16256,0,0,0,1065353216,0,0,0,0,16256,0,0,0,1065353216,0,0,0,0,0,0,0,0,0,0,0,0,0,0,0,0,
0,0,0,0,0,0,0,0,0,0,0,0,0,0,0,0,0,0,0,0,0,0,0,0,0,0,0,0,0,0,0,0,
0,0,0,0,0,0,0,0,0,0,0,0,0,0,0,0,0,0,16256,0,0,0,1065353216,0,0,0,0,16256,0,0,0,1065353216,
4096,31872,20000,47776,4112,31888,20016,47792,4128,31904,20032,47808,4144,31920,20048,47824,4160,31936,20064,51200,4176,31952,20080,51216,4192,35328,20096,51232,4208,35344,20112,51248,
4224,35360,20128,51264,4240,35376,20144,51280,4256,35392,20160,51296,4272,35408,20176,51312,4288,35424,23552,51328,4304,35440,23568,51344,7680,35456,23584,51360,7696,35472,23600,51376,
7712,35488,23616,51392,7728,35504,23632,51408,7744,35520,23648,54784,7760,35536,23664,54800,7776,38912,23680,54816,7792,38928,23696,54832,7808,38944,23712,54848,7824,38960,23728,54864,
7840,38976,23744,54880,7856,38992,23760,54896,7872,39008,27136,54912,7888,39024,27152,54928,11264,39040,27168,54944,11280,39056,27184,54960,11296,39072,27200,54976,11312,39088,27216,54992,
11328,39104,27232,58368,11344,39120,27248,58384,11360,42496,27264,58400,11376,42512,27280,58416,11392,42528,27296,58432,11408,42544,27312,58448,11424,42560,27328,58464,11440,42576,27344,58480,
11456,42592,45056,58496,11472,42608,45072,58512,14848,42624,45088,58528,14864,42640,45104,58544,14880,42656,45120,58560,14896,42672,45136,58576,14912,42688,45152,61952,14928,42704,45168,61968,
14944,17408,45184,61984,14960,17424,45200,62000,14976,17440,45216,62016,14992,17456,45232,62032,15008,17472,45248,62048,15024,17488,45264,62064,15040,17504,48640,62080,15056,17520,48656,62096,
32768,17536,48672,62112,32784,17552,48688,62128,32800,17568,48704,62144,32816,17584,48720,62160,32832,17600,48736,65536,32848,17616,48752,65552,32864,20992,48768,65568,32880,21008,48784,65584,
68672,96448,84576,0,68688,96464,84592,0,68704,99840,84608,0,68720,99856,84624,0,68736,99872,84640,0,68752,99888,84656,0,68768,99904,84672,0,68784,99920,84688,0,
68800,99936,102400,0,68816,99952,102416,0,72192,99968,102432,0,72208,99984,102448,0,72224,100000,102464,0,72240,100016,102480,0,72256,100032,102496,0,72272,100048,102512,0,
72288,74752,102528,0,72304,74768,102544,0,72320,74784,102560,0,72336,74800,102576,0,72352,74816,102592,0,72368,74832,102608,0,72384,74848,105984,0,72400,74864,106000,0,
90112,74880,106016,0,90128,74896,106032,0,90144,74912,106048,0,90160,74928,106064,0,90176,74944,106080,0,90192,74960,106096,0,90208,78336,106112,0,90224,78352,106128,0,
90240,78368,106144,0,90256,78384,106160,0,90272,78400,106176,0,90288,78416,106192,0,90304,78432,109568,0,90320,78448,109584,0,93696,78464,109600,0,93712,78480,109616,0,
93728,78496,109632,0,93744,78512,109648,0,93760,78528,109664,0,93776,78544,109680,0,93792,81920,109696,0,93808,81936,109712,0,93824,81952,109728,0,93840,81968,109744,0,
93856,81984,109760,0,93872,82000,109776,0,93888,82016,113152,0,93904,82032,113168,0,97280,82048,113184,0,97296,82064,113200,0,97312,82080,113216,0,97328,82096,113232,0,
97344,82112,113248,0,97360,82128,113264,0,97376,85504,113280,0,97392,85520,113296,0,97408,85536,113312,0,97424,85552,113328,0,97440,85568,113344,0,97456,85584,113360,0,
4128,31904,20032,47808,4144,31920,20048,47824,4160,31936,20064,47616,4176,31952,20080,47632,4192,31744,20096,51232,4208,31760,20112,51248,4224,35360,20128,51264,4240,35376,20144,51280,
4256,35392,20160,51296,4272,35408,20176,51312,4288,35424,19968,51328,4304,35440,19984,51344,4096,35456,23584,51360,4112,35472,23600,51376,7712,35488,23616,51392,7728,35504,23632,51408,
7744,35520,23648,51200,7760,35536,23664,51216,7776,35328,23680,54816,7792,35344,23696,54832,7808,38944,23712,54848,7824,38960,23728,54864,7840,38976,23744,54880,7856,38992,23760,54896,
7872,39008,23552,54912,7888,39024,23568,54928,7680,39040,27168,54944,7696,39056,27184,54960,11296,39072,27200,54976,11312,39088,27216,54992,11328,39104,27232,54784,11344,39120,27248,54800,
11360,38912,27264,58400,11376,38928,27280,58416,11392,42528,27296,58432,11408,42544,27312,58448,11424,42560,27328,58464,11440,42576,27344,58480,11456,42592,27136,58496,11472,42608,27152,58512,
11264,42624,45088,58528,11280,42640,45104,58544,14880,42656,45120,58560,14896,42672,45136,58576,14912,42688,45152,58368,14928,42704,45168,58384,14944,42496,45184,61984,14960,42512,45200,62000,
14976,17440,45216,62016,14992,17456,45232,62032,15008,17472,45248,62048,15024,17488,45264,62064,15040,17504,45056,62080,15056,17520,45072,62096,14848,17536,48672,62112,14864,17552,48688,62128,
32800,17568,48704,62144,32816,17584,48720,62160,32832,17600,48736,61952,32848,17616,48752,61968,32864,17408,48768,65568,32880,17424,48784,65584,32896,21024,48800,65600,32912,21040,48816,65616,
68704,96256,84608,0,68720,96272,84624,0,68736,99872,84640,0,68752,99888,84656,0,68768,99904,84672,0,68784,99920,84688,0,68800,99936,84480,0,68816,99952,84496,0,
68608,99968,102432,0,68624,99984,102448,0,72224,100000,102464,0,72240,100016,102480,0,72256,100032,102496,0,72272,100048,102512,0,72288,99840,102528,0,72304,99856,102544,0,
72320,74784,102560,0,72336,74800,102576,0,72352,74816,102592,0,72368,74832,102608,0,72384,74848,102400,0,72400,74864,102416,0,72192,74880,106016,0,72208,74896,106032,0,
90144,74912,106048,0,90160,74928,106064,0,90176,74944,106080,0,90192,74960,106096,0,90208,74752,106112,0,90224,74768,106128,0,90240,78368,106144,0,90256,78384,106160,0,
90272,78400,106176,0,90288,78416,106192,0,90304,78432,105984,0,90320,78448,106000,0,90112,78464,109600,0,90128,78480,109616,0,93728,78496,109632,0,93744,78512,109648,0,
93760,78528,109664,0,93776,78544,109680,0,93792,78336,109696,0,93808,78352,109712,0,93824,81952,109728,0,93840,81968,109744,0,93856,81984,109760,0,93872,82000,109776,0,
93888,82016,109568,0,93904,82032,109584,0,93696,82048,113184,0,93712,82064,113200,0,97312,82080,113216,0,97328,82096,113232,0,97344,82112,113248,0,97360,82128,113264,0,
97376,81920,113280,0,97392,81936,113296,0,97408,85536,113312,0,97424,85552,113328,0,97440,85568,113344,0,97456,85584,113360,0,97472,85600,113152,0,97488,85616,113168,0,
24576,90112,0,0,24640,90176,0,0,24704,90240,0,0,24768,90304,0,0,24880,90416,0,0,24944,90480,0,0,25008,90544,0,0,25072,90608,0,0,
25120,90656,0,0,25184,90720,0,0,25248,90784,0,0,25312,90848,0,0,25360,90896,0,0,25424,90960,0,0,25488,91024,0,0,25552,91088,0,0,
24592,90128,0,0,24656,90192,0,0,24720,90256,0,0,24784,90320,0,0,24864,90400,0,0,24928,90464,0,0,24992,90528,0,0,25056,90592,0,0,
25136,90672,0,0,25200,90736,0,0,25264,90800,0,0,25328,90864,0,0,25344,90880,0,0,25408,90944,0,0,25472,91008,0,0,25536,91072,0,0,
24608,90144,0,0,24672,90208,0,0,24736,90272,0,0,24800,90336,0,0,24848,90384,0,0,24912,90448,0,0,24976,90512,0,0,25040,90576,0,0,
25088,90624,0,0,25152,90688,0,0,25216,90752,0,0,25280,90816,0,0,25392,90928,0,0,25456,90992,0,0,25520,91056,0,0,25584,91120,0,0,
24624,90160,0,0,24688,90224,0,0,24752,90288,0,0,24816,90352,0,0,24832,90368,0,0,24896,90432,0,0,24960,90496,0,0,25024,90560,0,0,
25104,90640,0,0,25168,90704,0,0,25232,90768,0,0,25296,90832,0,0,25376,90912,0,0,25440,90976,0,0,25504,91040,0,0,25568,91104,0,0,
0,65536,0,65536,8,65544,8,65544,16,65552,16,65552,24,65560,24,65560,224,65760,288,65824,232,65768,296,65832,240,65776,304,65840,248,65784,312,65848,
448,65984,576,66112,456,65992,584,66120,464,66000,592,66128,472,66008,600,66136,672,66208,864,66400,680,66216,872,66408,688,66224,880,66416,696,66232,888,66424,
896,66432,1152,66688,904,66440,1160,66696,912,66448,1168,66704,920,66456,1176,66712,1120,66656,1440,66976,1128,66664,1448,66984,1136,66672,1456,66992,1144,66680,1464,67000,
1344,66880,1728,67264,1352,66888,1736,67272,1360,66896,1744,67280,1368,66904,1752,67288,1568,67104,2016,67552,1576,67112,2024,67560,1584,67120,2032,67568,1592,67128,2040,67576,
3584,69120,4608,70144,3592,69128,4616,70152,3600,69136,4624,70160,3608,69144,4632,70168,3808,69344,4896,70432,3816,69352,4904,70440,3824,69360,4912,70448,3832,69368,4920,70456,
4032,69568,5184,70720,4040,69576,5192,70728,4048,69584,5200,70736,4056,69592,5208,70744,4256,69792,5472,71008,4264,69800,5480,71016,4272,69808,5488,71024,4280,69816,5496,71032,
4480,70016,5760,71296,4488,70024,5768,71304,4496,70032,5776,71312,4504,70040,5784,71320,4704,70240,6048,71584,4712,70248,6056,71592,4720,70256,6064,71600,4728,70264,6072,71608,
4928,70464,6336,71872,4936,70472,6344,71880,4944,70480,6352,71888,4952,70488,6360,71896,5152,70688,6624,72160,5160,70696,6632,72168,5168,70704,6640,72176,5176,70712,6648,72184,
65536,155648,155648,0,65552,155664,155664,0,65568,155680,155680,0,65584,155696,155696,0,65600,155712,155712,0,65616,155728,155728,0,65632,155744,155744,0,65648,155760,155760,0,
65664,155776,155776,0,65680,155792,155792,0,65696,155808,155808,0,65712,155824,155824,0,65728,155840,155840,0,65744,155856,155856,0,65760,155872,155872,0,65776,155888,155888,0,
65792,155656,155904,0,65808,155672,155920,0,65824,155688,155936,0,65840,155704,155952,0,65856,155720,155968,0,65872,155736,155984,0,65888,155752,156000,0,65904,155768,156016,0,
65920,155784,156032,0,65936,155800,156048,0,65952,155816,156064,0,65968,155832,156080,0,65984,155848,156096,0,66000,155864,156112,0,66016,155880,156128,0,66032,155896,156144,0,
66048,155904,156160,0,66064,155920,156176,0,66080,155936,156192,0,66096,155952,156208,0,66112,155968,156224,0,66128,155984,156240,0,66144,156000,156256,0,66160,156016,156272,0,
66176,156032,156288,0,66192,156048,156304,0,66208,156064,156320,0,66224,156080,156336,0,66240,156096,156352,0,66256,156112,156368,0,66272,156128,156384,0,66288,156144,156400,0,
66304,155912,156416,0,66320,155928,156432,0,66336,155944,156448,0,66352,155960,156464,0,66368,155976,156480,0,66384,155992,156496,0,66400,156008,156512,0,66416,156024,156528,0,
66432,156040,156544,0,66448,156056,156560,0,66464,156072,156576,0,66480,156088,156592,0,66496,156104,156608,0,66512,156120,156624,0,66528,156136,156640,0,66544,156152,156656,0,
155648,0,0,0,155712,72,128,128,155776,144,256,256,155840,216,384,384,155952,896,512,512,156016,968,640,640,156080,1040,768,768,156144,1112,896,896,
156192,1792,1024,1024,156256,1864,1152,1152,156320,1936,1280,1280,156384,2008,1408,1408,156432,2688,1536,1536,156496,2760,1664,1664,156560,2832,1792,1792,156624,2904,1920,1920,
155664,2904,1920,1920,155728,2904,1920,1920,155792,2904,1920,1920,155856,2904,1920,1920,155936,2904,1920,1920,156000,2904,1920,1920,156064,2904,1920,1920,156128,2904,1920,1920,
156208,2904,1920,1920,156272,2904,1920,1920,156336,2904,1920,1920,156400,2904,1920,1920,156416,2904,1920,1920,156480,2904,1920,1920,156544,2904,1920,1920,156608,2904,1920,1920,
155680,2904,1920,1920,155744,2904,1920,1920,155808,2904,1920,1920,155872,2904,1920,1920,155920,2904,1920,1920,155984,2904,1920,1920,156048,2904,1920,1920,156112,2904,1920,1920,
156160,2904,1920,1920,156224,2904,1920,1920,156288,2904,1920,1920,156352,2904,1920,1920,156464,2904,1920,1920,156528,2904,1920,1920,156592,2904,1920,1920,156656,2904,1920,1920,
155696,2904,1920,1920,155760,2904,1920,1920,155824,2904,1920,1920,155888,2904,1920,1920,155904,2904,1920,1920,155968,2904,1920,1920,156032,2904,1920,1920,156096,2904,1920,1920,
156176,2904,1920,1920,156240,2904,1920,1920,156304,2904,1920,1920,156368,2904,1920,1920,156448,2904,1920,1920,156512,2904,1920,1920,156576,2904,1920,1920,156640,2904,1920,1920,
29056,93440,51616,116000,29072,93456,51632,116016,29088,93472,51648,116032,29104,93488,51664,116048,29120,93504,51680,116064,29136,93520,51696,116080,29152,93536,59616,116096,29168,93552,59632,116112,
69856,93568,59648,116128,69872,93584,59664,116144,69888,93600,59680,116160,69904,93616,59696,116176,69920,93632,59712,116192,69936,93648,59728,116208,69952,93664,59744,124128,69968,93680,59760,124144,
69984,36064,59776,124160,70000,36080,59792,124176,70016,36096,59808,124192,70032,36112,59824,124208,70048,36128,59840,124224,70064,36144,59856,124240,70080,36160,59872,124256,70096,36176,59888,124272,
70112,36192,100576,124288,70128,36208,100592,124304,78048,36224,100608,124320,78064,36240,100624,124336,78080,36256,100640,124352,78096,36272,100656,124368,78112,36288,100672,124384,78128,36304,100688,124400,
78144,36320,100704,132320,78160,36336,100720,132336,78176,44256,100736,132352,78192,44272,100752,132368,78208,44288,100768,132384,78224,44304,100784,132400,78240,44320,100800,132416,78256,44336,100816,132432,
78272,44352,100832,132448,78288,44368,100848,132464,78304,44384,108768,132480,78320,44400,108784,132496,86240,44416,108800,132512,86256,44432,108816,132528,86272,44448,108832,132544,86288,44464,108848,132560,
86304,44480,108864,132576,86320,44496,108880,132592,86336,44512,108896,140512,86352,44528,108912,140528,86368,52448,108928,140544,86384,52464,108944,140560,86400,52480,108960,140576,86416,52496,108976,140592,
86432,52512,108992,140608,86448,52528,109008,140624,86464,52544,109024,140640,86480,52560,109040,140656,86496,52576,116960,140672,86512,52592,116976,140688,94432,52608,116992,140704,94448,52624,117008,140720,
28384,1073741824,131264,2130706432,28400,1073741840,131272,2130706448,28416,1073741856,131280,2130706464,28432,1073741872,131288,2130706480,28448,1073741888,131488,2130706496,28464,1073741904,131496,2130706512,28480,1073741920,131504,2130706528,28496,1073741936,131512,2130706544,
29184,1073741952,131712,2130706560,29200,1073741968,131720,2130706576,29216,1073741984,131728,2130706592,29232,1073742000,131736,2130706608,29248,1073742016,131936,2130706624,29264,1073742032,131944,2130706640,29280,1073742048,131952,2130706656,29296,1073742064,131960,2130706672,
29312,1073742080,132160,2130706688,29328,1073742096,132168,2130706704,29344,1073742112,132176,2130706720,29360,1073742128,132184,2130706736,29376,1073742144,132384,2130706752,29392,1073742160,132392,2130706768,30080,1073742176,132400,2130706784,30096,1073742192,132408,2130706800,
30112,1073742208,132608,2130706816,30128,1073742224,132616,2130706832,30144,1073742240,132624,2130706848,30160,1073742256,132632,2130706864,30176,1073742272,132832,2130706880,30192,1073742288,132840,2130706896,30208,1073742304,132848,2130706912,30224,1073742320,132856,2130706928,
30240,1073742336,134848,2130706944,30256,1073742352,134856,2130706960,30272,1073742368,134864,2130706976,30288,1073742384,134872,2130706992,30976,1073742400,135072,2130707008,30992,1073742416,135080,2130707024,31008,1073742432,135088,2130707040,31024,1073742448,135096,2130707056,
31040,1073742464,135296,2130707072,31056,1073742480,135304,2130707088,31072,1073742496,135312,2130707104,31088,1073742512,135320,2130707120,31104,1073742528,135520,2130707136,31120,1073742544,135528,2130707152,31136,1073742560,135536,2130707168,31152,1073742576,135544,2130707184,
31168,1073742592,135744,2130707200,31184,1073742608,135752,2130707216,31872,1073742624,135760,2130707232,31888,1073742640,135768,2130707248,31904,1073742656,135968,2130707264,31920,1073742672,135976,2130707280,31936,1073742688,135984,2130707296,31952,1073742704,135992,2130707312,
31968,1073742720,136192,2130707328,31984,1073742736,136200,2130707344,32000,1073742752,136208,2130707360,32016,1073742768,136216,2130707376,32032,1073742784,136416,2130707392,32048,1073742800,136424,2130707408,32064,1073742816,136432,2130707424,32080,1073742832,136440,2130707440,
0,145408,145440,0,8,145472,145504,16,16,145536,145568,32,24,145600,145632,48,32,145712,145680,512,40,145776,145744,528,48,145840,145808,544,56,145904,145872,560,
64,145952,145920,1024,72,146016,145984,1040,80,146080,146048,1056,88,146144,146112,1072,96,146192,146224,1536,104,146256,146288,1552,112,146320,146352,1568,120,146384,146416,1584,
128,145416,145448,2096,136,145480,145512,2080,144,145544,145576,2064,152,145608,145640,2048,160,145720,145688,2608,168,145784,145752,2592,176,145848,145816,2576,184,145912,145880,2560,
192,145960,145928,3120,200,146024,145992,3104,208,146088,146056,3088,216,146152,146120,3072,224,146200,146232,3632,232,146264,146296,3616,240,146328,146360,3600,248,146392,146424,3584,
256,145424,145456,4128,264,145488,145520,4144,272,145552,145584,4096,280,145616,145648,4112,288,145696,145664,4640,296,145760,145728,4656,304,145824,145792,4608,312,145888,145856,4624,
320,145968,145936,5152,328,146032,146000,5168,336,146096,146064,5120,344,146160,146128,5136,352,146176,146208,5664,360,146240,146272,5680,368,146304,146336,5632,376,146368,146400,5648,
384,145432,145464,6160,392,145496,145528,6144,400,145560,145592,6192,408,145624,145656,6176,416,145704,145672,6672,424,145768,145736,6656,432,145832,145800,6704,440,145896,145864,6688,
440,145976,145944,7184,440,146040,146008,7168,440,146104,146072,7216,440,146168,146136,7200,440,146184,146216,7696,440,146248,146280,7680,440,146312,146344,7728,440,146376,146408,7712,
143808,208192,166368,230752,143824,208208,166384,230768,143840,208224,174304,230784,143856,208240,174320,230800,151776,208256,174336,230816,151792,208272,174352,230832,151808,208288,174368,230848,151824,208304,174384,230864,
151840,208320,174400,230880,151856,208336,174416,230896,151872,208352,174432,238816,151888,208368,174448,238832,151904,216288,174464,238848,151920,216304,174480,238864,151936,216320,174496,238880,151952,216336,174512,238896,
151968,216352,174528,238912,151984,216368,174544,238928,152000,216384,174560,238944,152016,216400,174576,238960,152032,216416,182496,238976,152048,216432,182512,238992,159968,216448,182528,239008,159984,216464,182544,239024,
160000,216480,182560,239040,160016,216496,182576,239056,160032,216512,182592,239072,160048,216528,182608,239088,160064,216544,182624,247008,160080,216560,182640,247024,160096,224480,182656,247040,160112,224496,182672,247056,
160128,224512,182688,247072,160144,224528,182704,247088,160160,224544,182720,247104,160176,224560,182736,247120,160192,224576,182752,247136,160208,224592,182768,247152,160224,224608,190688,247168,160240,224624,190704,247184,
200928,224640,190720,247200,200944,224656,190736,247216,200960,224672,190752,247232,200976,224688,190768,247248,200992,224704,190784,247264,201008,224720,190800,247280,201024,224736,190816,255200,201040,224752,190832,255216,
201056,167136,190848,255232,201072,167152,190864,255248,201088,167168,190880,255264,201104,167184,190896,255280,201120,167200,190912,255296,201136,167216,190928,255312,201152,167232,190944,255328,201168,167248,190960,255344,
201184,167264,231648,255360,201200,167280,231664,255376,209120,167296,231680,255392,209136,167312,231696,255408,209152,167328,231712,255424,209168,167344,231728,255440,209184,167360,231744,255456,209200,167376,231760,255472,
4096,10368,6560,12832,4112,10384,6576,12848,4128,10400,6592,12864,4144,10416,6608,12880,4160,10432,6624,13568,4176,10448,6640,13584,4192,11136,6656,13600,4208,11152,6672,13616,
4224,11168,6688,13632,4240,11184,6704,13648,4256,11200,6720,13664,4272,11216,6736,13680,4288,11232,7424,13696,4304,11248,7440,13712,4992,11264,7456,13728,5008,11280,7472,13744,
5024,11296,7488,13760,5040,11312,7504,13776,5056,11328,7520,14464,5072,11344,7536,14480,5088,12032,7552,14496,5104,12048,7568,14512,5120,12064,7584,14528,5136,12080,7600,14544,
5152,12096,7616,14560,5168,12112,7632,14576,5184,12128,8320,14592,5200,12144,8336,14608,5888,12160,8352,14624,5904,12176,8368,14640,5920,12192,8384,14656,5936,12208,8400,14672,
5952,12224,8416,15360,5968,12240,8432,15376,5984,12928,8448,15392,6000,12944,8464,15408,6016,12960,8480,15424,6032,12976,8496,15440,6048,12992,8512,15456,6064,13008,8528,15472,
6080,13024,12800,15488,6096,13040,12816,15504,6784,13056,12832,15520,6800,13072,12848,15536,6816,13088,12864,15552,6832,13104,12880,15568,6848,13120,12896,16256,6864,13136,12912,16272,
6880,6656,12928,16288,6896,6672,12944,16304,6912,6688,12960,16320,6928,6704,12976,16336,6944,6720,12992,16352,6960,6736,13008,16368,6976,6752,13696,16384,6992,6768,13712,16400,
11264,6784,13728,16416,11280,6800,13744,16432,11296,6816,13760,16448,11312,6832,13776,16464,11328,6848,13792,17152,11344,6864,13808,17168,11360,7552,13824,17184,11376,7568,13840,17200,
20288,26560,22752,0,20304,26576,22768,0,20320,27264,22784,0,20336,27280,22800,0,20352,27296,22816,0,20368,27312,22832,0,20384,27328,22848,0,20400,27344,22864,0,
20416,27360,27136,0,20432,27376,27152,0,21120,27392,27168,0,21136,27408,27184,0,21152,27424,27200,0,21168,27440,27216,0,21184,27456,27232,0,21200,27472,27248,0,
21216,20992,27264,0,21232,21008,27280,0,21248,21024,27296,0,21264,21040,27312,0,21280,21056,27328,0,21296,21072,27344,0,21312,21088,28032,0,21328,21104,28048,0,
25600,21120,28064,0,25616,21136,28080,0,25632,21152,28096,0,25648,21168,28112,0,25664,21184,28128,0,25680,21200,28144,0,25696,21888,28160,0,25712,21904,28176,0,
25728,21920,28192,0,25744,21936,28208,0,25760,21952,28224,0,25776,21968,28240,0,25792,21984,28928,0,25808,22000,28944,0,26496,22016,28960,0,26512,22032,28976,0,
26528,22048,28992,0,26544,22064,29008,0,26560,22080,29024,0,26576,22096,29040,0,26592,22784,29056,0,26608,22800,29072,0,26624,22816,29088,0,26640,22832,29104,0,
26656,22848,29120,0,26672,22864,29136,0,26688,22880,29824,0,26704,22896,29840,0,27392,22912,29856,0,27408,22928,29872,0,27424,22944,29888,0,27440,22960,29904,0,
27456,22976,29920,0,27472,22992,29936,0,27488,23680,29952,0,27504,23696,29968,0,27520,23712,29984,0,27536,23728,30000,0,27552,23744,30016,0,27568,23760,30032,0,
0,0,0,0,2130706432,32,8,8,2130706432,64,16,16,2130706432,96,24,24,2130706432,0,0,32,2130706432,32,8,40,2130706432,64,16,48,2130706432,96,24,56,
2130706432,0,0,64,2130706432,32,8,72,2130706432,64,16,80,2130706432,96,24,88,2130706432,0,0,96,2130706432,32,8,104,2130706432,64,16,112,2130706432,96,24,120,
2130706432,0,0,128,2130706432,32,8,136,2130706432,64,16,144,2130706432,96,24,152,2130706432,0,0,160,2130706432,32,8,168,2130706432,64,16,176,2130706432,96,24,184,
2130706432,0,0,192,2130706432,32,8,200,2130706432,64,16,208,2130706432,96,24,216,2130706432,0,0,224,2130706432,32,8,232,2130706432,64,16,240,2130706432,96,24,248,
2130706432,0,0,256,2130706432,32,8,264,2130706432,64,16,272,2130706432,96,24,280,2130706432,0,0,288,2130706432,32,8,296,2130706432,64,16,304,2130706432,96,24,312,
2130706432,0,0,320,2130706432,32,8,328,2130706432,64,16,336,2130706432,96,24,344,2130706432,0,0,352,2130706432,32,8,360,2130706432,64,16,368,2130706432,96,24,376,
2130706432,0,0,384,2130706432,32,8,392,2130706432,64,16,400,2130706432,96,24,408,2130706432,0,0,416,2130706432,32,8,424,2130706432,64,16,432,2130706432,96,24,440,
2130706432,0,0,448,2130706432,32,8,456,2130706432,64,16,464,2130706432,96,24,472,2130706432,0,0,480,2130706432,32,8,488,2130706432,64,16,496,2130706432,96,24,504,
2130706432,0,0,0,2130706448,8,0,0,2130706464,16,0,0,2130706480,24,0,0,2130706496,32,0,0,2130706512,40,0,0,2130706528,48,0,0,2130706544,56,0,0,
2130706560,64,0,0,2130706576,72,0,0,2130706592,80,0,0,2130706608,88,0,0,2130706624,96,0,0,2130706640,104,0,0,2130706656,112,0,0,2130706672,120,0,0,
2130706688,128,0,0,2130706704,136,0,0,2130706720,144,0,0,2130706736,152,0,0,2130706752,160,0,0,2130706768,168,0,0,2130706784,176,0,0,2130706800,184,0,0,
2130706816,192,0,0,2130706832,200,0,0,2130706848,208,0,0,2130706864,216,0,0,2130706880,224,0,0,2130706896,232,0,0,2130706912,240,0,0,2130706928,248,0,0,
2130706944,0,0,0,2130706960,8,0,0,2130706976,16,0,0,2130706992,24,0,0,2130707008,32,0,0,2130707024,40,0,0,2130707040,48,0,0,2130707056,56,0,0,
2130707072,64,0,0,2130707088,72,0,0,2130707104,80,0,0,2130707120,88,0,0,2130707136,96,0,0,2130707152,104,0,0,2130707168,112,0,0,2130707184,120,0,0,
2130707200,128,0,0,2130707216,136,0,0,2130707232,144,0,0,2130707248,152,0,0,2130707264,160,0,0,2130707280,168,0,0,2130707296,176,0,0,2130707312,184,0,0,
2130707328,192,0,0,2130707344,200,0,0,2130707360,208,0,0,2130707376,216,0,0,2130707392,224,0,0,2130707408,232,0,0,2130707424,240,0,0,2130707440,248,0,0,
0,155648,155648,36864,16,155728,155664,36880,2130706464,155808,155680,36896,2130706480,155888,155696,36912,896,155968,155728,36928,912,156048,155744,36944,2130707360,156128,155760,36960,2130707376,156208,155776,36976,
1792,156288,155808,36992,1808,156368,155824,37008,2130708256,156448,155840,37024,2130708272,156528,155856,37040,2688,156608,155888,37056,2704,156688,155904,37072,2130709152,156768,155920,37088,2130709168,156848,155936,37104,
3584,155656,155968,37120,3600,155736,155984,37136,2130710048,155816,156000,37152,2130710064,155896,156016,37168,4480,155976,156048,37184,4496,156056,156064,37200,2130710944,156136,156080,37216,2130710960,156216,156096,37232,
5376,156296,156128,37248,5392,156376,156144,37264,2130711840,156456,156160,37280,2130711856,156536,156176,37296,6272,156616,156208,37312,6288,156696,156224,37328,2130712736,156776,156240,37344,2130712752,156856,156256,37360,
7168,155664,156288,37376,7184,155744,156304,37392,2130713632,155824,156320,37408,2130713648,155904,156336,37424,8064,155984,156368,37440,8080,156064,156384,37456,2130714528,156144,156400,37472,2130714544,156224,156416,37488,
8960,156304,156448,37504,8976,156384,156464,37520,2130715424,156464,156480,37536,2130715440,156544,156496,37552,9856,156624,156528,37568,9872,156704,156544,37584,2130716320,156784,156560,37600,2130716336,156864,156576,37616,
10752,155672,156608,37632,10768,155752,156624,37648,2130717216,155832,156640,37664,2130717232,155912,156656,37680,11648,155992,156688,37696,11664,156072,156704,37712,2130718112,156152,156720,37728,2130718128,156232,156736,37744,
12544,156312,156768,37760,12560,156392,156784,37776,2130719008,156472,156800,37792,2130719024,156552,156816,37808,13440,156632,156848,37824,13456,156712,156864,37840,2130719904,156792,156880,37856,2130719920,156872,156896,37872,
0,31744,31776,24576,16,31808,31840,24592,2130706464,31872,31904,24608,2130706480,31936,31968,24624,896,32048,32016,24640,912,32112,32080,24656,2130707360,32176,32144,24672,2130707376,32240,32208,24688,
1792,32288,32256,24704,1808,32352,32320,24720,2130708256,32416,32384,24736,2130708272,32480,32448,24752,2688,32528,32560,24768,2704,32592,32624,24784,2130709152,32656,32688,24800,2130709168,32720,32752,24816,
2130710064,31752,31784,24832,2130710048,31816,31848,24848,3600,31880,31912,24864,3584,31944,31976,24880,2130710960,32056,32024,24896,2130710944,32120,32088,24912,4496,32184,32152,24928,4480,32248,32216,24944,
2130711856,32296,32264,24960,2130711840,32360,32328,24976,5392,32424,32392,24992,5376,32488,32456,25008,2130712752,32536,32568,25024,2130712736,32600,32632,25040,6288,32664,32696,25056,6272,32728,32760,25072,
2130713632,31760,31792,25088,2130713648,31824,31856,25104,7168,31888,31920,25120,7184,31952,31984,25136,2130714528,32032,32000,25152,2130714544,32096,32064,25168,8064,32160,32128,25184,8080,32224,32192,25200,
2130715424,32304,32272,25216,2130715440,32368,32336,25232,8960,32432,32400,25248,8976,32496,32464,25264,2130716320,32512,32544,25280,2130716336,32576,32608,25296,9856,32640,32672,25312,9872,32704,32736,25328,
10768,31768,31800,25344,10752,31832,31864,25360,2130717232,31896,31928,25376,2130717216,31960,31992,25392,11664,32040,32008,25408,11648,32104,32072,25424,2130718128,32168,32136,25440,2130718112,32232,32200,25456,
12560,32312,32280,25472,12544,32376,32344,25488,2130719024,32440,32408,25504,2130719008,32504,32472,25520,13456,32520,32552,25536,13440,32584,32616,25552,2130719920,32648,32680,25568,2130719904,32712,32744,25584,
16256,0,0,0,1065353216,0,0,0,0,16256,0,0,0,1065353216,0,0,0,0,0,0,0,0,0,0,0,0,0,0,0,0,0,0,
0,0,0,0,0,0,0,0,0,0,0,0,0,0,0,0,0,0,0,0,0,0,0,0,0,0,0,0,0,0,0,0,
0,0,0,0,0,0,0,0,0,0,0,0,0,0,0,0,16256,0,0,0,1065353216,0,0,0,0,16256,0,0,0,1065353216,0,0,
0,0,0,0,0,0,0,0,0,0,0,0,0,0,0,0,0,0,0,0,0,0,0,0,0,0,0,0,0,0,0,0,
0,0,0,0,0,0,0,0,0,0,0,0,0,0,0,0,0,0,0,0,0,0,0,0,0,0,0,0,0,0,0,0,
16256,0,0,0,1065353216,0,0,0,0,16256,0,0,0,1065353216,0,0,0,0,0,0,0,0,0,0,0,0,0,0,0,0,0,0,
0,0,0,0,0,0,0,0,0,0,0,0,0,0,0,0,0,0,0,0,0,0,0,0,0,0,0,0,0,0,0,0,
0,0,0,0,0,0,0,0,0,0,0,0,0,0,0,0,16256,0,0,0,1065353216,0,0,0,0,16256,0,0,0,1065353216,0,0,
0,0,16256,0,0,0,1065353216,0,0,0,0,16256,0,0,0,1065353216,0,0,0,0,0,0,0,0,0,0,0,0,0,0,0,0,
0,0,0,0,0,0,0,0,0,0,0,0,0,0,0,0,0,0,0,0,0,0,0,0,0,0,0,0,0,0,0,0,
0,0,0,0,0,0,0,0,0,0,0,0,0,0,0,0,0,0,16256,0,0,0,1065353216,0,0,0,0,16256,0,0,0,1065353216,
0,0,0,0,0,0,0,0,0,0,0,0,0,0,0,0,0,0,0,0,0,0,0,0,0,0,0,0,0,0,0,0,
0,0,0,0,0,0,0,0,0,0,0,0,0,0,0,0,0,0,0,0,0,0,0,0,0,0,0,0,0,0,0,0,
0,0,16256,0,0,0,1065353216,0,0,0,0,16256,0,0,0,1065353216,0,0,0,0,0,0,0,0,0,0,0,0,0,0,0,0,
0,0,0,0,0,0,0,0,0,0,0,0,0,0,0,0,0,0,0,0,0,0,0,0,0,0,0,0,0,0,0,0,
0,0,0,0,0,0,0,0,0,0,0,0,0,0,0,0,0,0,16256,0,0,0,1065353216,0,0,0,0,16256,0,0,0,1065353216};
extern "C" __global__ __launch_bounds__(256, 1) void fused_k(const void* x, const void* kc, const void* vc, const void* wq, const void* wo,
                                                              const void* wg, const void* wu, const void* wd, void* out, void* ws) {
  auto kp = __builtin_amdgcn_kernarg_segment_ptr();
  unsigned wid = __builtin_amdgcn_readfirstlane(threadIdx.x >> 6);
  asm volatile(
  "s_mov_b64 s[8:9], %0\n\t"
  "s_load_dwordx16 s[12:27], s[8:9], 0\n\t"
  "s_load_dwordx4 s[28:31], s[8:9], 64\n\t"
  "s_mov_b32 s10, %1\n\t"
  "s_mov_b32 s11, %2\n\t"
  "s_mov_b64 s[96:97], %3\n\t"
  "v_mbcnt_lo_u32_b32 v0, -1, 0\n\t"
  "v_mbcnt_hi_u32_b32 v0, -1, v0\n\t"
  "s_getpc_b64 s[98:99]\n\t"
  "s_lshr_b32 s91, s10, 3\n\t"
  "s_lshl_b32 s91, s91, 1\n\t"
  "s_lshr_b32 s92, s11, 1\n\t"
  "s_add_u32 s91, s91, s92\n\t"
  "s_and_b32 s92, s11, 1\n\t"
  "s_cselect_b32 s98, s96, s98\n\t"
  "s_cselect_b32 s99, s97, s99\n\t"
  "s_cselect_b32 s92, 10, 8\n\t"
  "s_mov_b32 s93, 0x1c9\n\t"
  "s_cselect_b32 s93, 0x27f, s93\n\t"
  "s_mul_i32 s91, s91, s92\n\t"
  "s_sub_u32 s92, s92, 1\n\t"
  "v_min_u32 v2, s92, v0\n\t"
  "v_add_u32 v2, s91, v2\n\t"
  "v_min_u32 v2, s93, v2\n\t"
  "v_lshlrev_b32 v2, 7, v2\n\t"
  "global_load_dword v47, v2, s[98:99]\n\t"
  "s_and_b32 s72, s10, 7\n\t"
  "s_lshl_b32 s72, s72, 1\n\t"
  "s_bfe_u32 s89, s10, 0x10003\n\t"
  "s_or_b32 s72, s72, s89\n\t"
  "s_lshr_b32 s73, s10, 4\n\t"
  "s_lshl_b32 s84, s11, 13\n\t"
  "s_lshl_b32 s89, s73, 2\n\t"
  "s_add_u32 s89, s89, s11\n\t"
  "s_mul_i32 s90, s89, 0x7000\n\t"
  "v_lshrrev_b32 v3, 4, v0\n\t"
  "v_sub_u32 v3, 4, v3\n\t"
  "v_xor_b32 v3, v3, v0\n\t"
  "v_and_b32 v3, 3, v3\n\t"
  "v_lshrrev_b32 v46, 2, v0\n\t"
  "v_mul_u32_u24 v46, 0x380, v46\n\t"
  "v_lshl_add_u32 v46, v3, 4, v46\n\t"
  "s_waitcnt lgkmcnt(0)\n\t"
  "s_add_u32 s32, s12, s90\n\t"
  "s_addc_u32 s33, s13, 0\n\t"
  "s_sub_u32 s32, s32, 0x1000\n\t"
  "s_subb_u32 s33, s33, 0\n\t"
  "s_and_b32 s33, s33, 0xffff\n\t"
  "s_mov_b32 s34, 0x8000\n\t"
  "s_mov_b32 s35, 0x20000\n\t"
  "s_and_b32 s4, s72, 1\n\t"
  "s_mul_i32 s4, s4, 0\n\t"
  "s_sub_u32 s5, 0, s4\n\t"
  "s_add_u32 m0, s84, 0x8000\n\t"
  "s_add_u32 s88, s4, 0x1000\n\t"
  "buffer_load_dwordx4 v46, s[32:35], s88 offen offset:0 lds\n\t"
  "s_mul_i32 s95, s11, 0x5000\n\t"
  "s_add_u32 s95, s95, 0x1000\n\t"
  "s_add_u32 s96, s96, s95\n\t"
  "s_addc_u32 s97, s97, 0\n\t"
  "v_lshlrev_b32 v40, 4, v0\n\t"
  "s_and_b32 s95, s72, 1\n\t"
  "s_lshl_b32 s95, s95, 11\n\t"
  "v_add_u32 v1, s95, v40\n\t"
  "v_mov_b32 v52, 0x3f803f80\n\t"
  "s_add_u32 s81, s4, 0x4400\n\t"
  "buffer_load_dwordx4 v46, s[32:35], s81 offen offset:1024 lds\n\t"
  "v_mov_b32 v53, 0x3f803f80\n\t"
  "v_mov_b32 v54, 0x3f803f80\n\t"
  "v_mov_b32 v55, 0x3f803f80\n\t"
  "v_mov_b32 v124, 0\n\t"
  "s_mul_i32 s90, s72, 0xe0\n\t"
  "s_sub_u32 s91, 0x189000, s90\n\t"
  "s_and_b32 s92, s11, 1\n\t"
  "s_cselect_b32 s93, s24, s22\n\t"
  "s_cselect_b32 s94, s25, s23\n\t"
  "s_add_u32 s88, s4, 0x840\n\t"
  "buffer_load_dwordx4 v46, s[32:35], s88 offen offset:2048 lds\n\t"
  "s_add_u32 s36, s93, s90\n\t"
  "s_addc_u32 s37, s94, 0\n\t"
  "s_sub_u32 s36, s36, 0x1000\n\t"
  "s_subb_u32 s37, s37, 0\n\t"
  "s_and_b32 s37, s37, 0xffff\n\t"
  "s_mov_b32 s38, s91\n\t"
  "s_mov_b32 s39, 0x20000\n\t"
  "s_and_b32 s74, s72, 1\n\t"
  "s_mul_i32 s74, s74, 0\n\t"
  "s_add_u32 s74, s74, s11\n\t"
  "s_lshr_b32 s74, s74, 1\n\t"
  "s_mul_i32 s74, s74, 0x1c000\n\t"
  "s_mov_b32 s83, 0xffff0000\n\t"
  "s_add_u32 s81, s4, 0x3c40\n\t"
  "buffer_load_dwordx4 v46, s[32:35], s81 offen offset:3072 lds\n\t"
  "s_lshl_b32 s90, s72, 9\n\t"
  "s_sub_u32 s75, 0x381000, s90\n\t"
  "s_add_u32 s98, s18, s90\n\t"
  "s_addc_u32 s99, s19, 0\n\t"
  "s_sub_u32 s98, s98, 0x1000\n\t"
  "s_subb_u32 s99, s99, 0\n\t"
  "s_and_b32 s99, s99, 0xffff\n\t"
  "s_lshr_b32 s89, s72, 1\n\t"
  "s_lshl_b32 s89, s89, 20\n\t"
  "s_add_u32 s89, s89, 0xc0000\n\t"
  "s_add_u32 s40, s14, s89\n\t"
  "s_addc_u32 s41, s15, 0\n\t"
  "s_sub_u32 s40, s40, 0x1000\n\t"
  "s_subb_u32 s41, s41, 0\n\t"
  "s_and_b32 s41, s41, 0xffff\n\t"
  "s_mov_b32 s42, 0x41000\n\t"
  "s_mov_b32 s43, 0x20000\n\t"
  "s_add_u32 m0, s84, 0x9000\n\t"
  "s_add_u32 s88, s4, 0x1080\n\t"
  "global_load_dwordx4 v[8:11], v1, s[96:97] offset:-4096\n\t"
  "global_load_dwordx4 v[12:15], v1, s[96:97] offset:-3072\n\t"
  "global_load_dwordx2 v[120:121], v40, s[96:97] offset:0\n\t"
  "global_load_dwordx4 v[28:31], v40, s[96:97] offset:3072\n\t"
  "global_load_dwordx4 v[32:35], v40, s[96:97] offset:1024\n\t"
  "global_load_dwordx4 v[42:45], v40, s[96:97] offset:2048\n\t"
  "buffer_load_dwordx4 v46, s[32:35], s88 offen offset:0 lds\n\t"
  "s_cmp_eq_u32 s11, 2\n\t"
  "s_cselect_b32 s68, s98, s40\n\t"
  "s_cselect_b32 s69, s99, s41\n\t"
  "s_cselect_b32 s70, s75, s42\n\t"
  "s_mov_b32 s9, 0x800\n\t"
  "s_cselect_b32 s9, 0x80000, s9\n\t"
  "s_mov_b32 s92, 0x2000\n\t"
  "s_cselect_b32 s92, 0x8000, s92\n\t"
  "s_cmp_lt_u32 s11, 2\n\t"
  "s_cselect_b32 s68, s36, s68\n\t"
  "s_cselect_b32 s69, s37, s69\n\t"
  "s_cselect_b32 s70, s38, s70\n\t"
  "s_cselect_b32 s9, 0x38000, s9\n\t"
  "s_cselect_b32 s92, 0x3800, s92\n\t"
  "s_mov_b32 s91, 0x1000\n\t"
  "s_cselect_b32 s91, 0x71000, s91\n\t"
  "s_mov_b32 s71, 0x20000\n\t"
  "s_add_u32 s81, s4, 0x4480\n\t"
  "buffer_load_dwordx4 v46, s[32:35], s81 offen offset:1024 lds\n\t"
  "s_and_b32 s93, s73, 15\n\t"
  "s_lshl_b32 s8, s93, 1\n\t"
  "s_and_b32 s90, s72, 1\n\t"
  "s_add_u32 s8, s8, s90\n\t"
  "s_cmp_eq_u32 s11, 3\n\t"
  "s_cselect_b32 s8, s8, s93\n\t"
  "s_mul_i32 s8, s8, s92\n\t"
  "s_add_u32 s8, s8, s91\n\t"
  "s_add_u32 s56, s30, 0\n\t"
  "s_addc_u32 s57, s31, 0\n\t"
  "s_and_b32 s57, s57, 0xffff\n\t"
  "s_mov_b32 s58, 0x3400000\n\t"
  "s_mov_b32 s59, 0x20000\n\t"
  "s_add_u32 s88, s4, 0x8c0\n\t"
  "buffer_load_dwordx4 v46, s[32:35], s88 offen offset:2048 lds\n\t"
  "s_mul_i32 s92, s10, 0x493\n\t"
  "s_lshr_b32 s92, s92, 16\n\t"
  "s_mul_i32 s93, s92, 56\n\t"
  "s_sub_u32 s93, s10, s93\n\t"
  "s_mul_i32 s93, s93, 0x7000\n\t"
  "s_mul_i32 s92, s92, 0xe0\n\t"
  "s_add_u32 s93, s93, s92\n\t"
  "s_cmp_lt_u32 s10, 0xe0\n\t"
  "s_cselect_b32 s92, 0x7d60, 0\n\t"
  "s_cselect_b32 s93, s93, 0\n\t"
  "s_add_u32 s81, s4, 0x3cc0\n\t"
  "buffer_load_dwordx4 v46, s[32:35], s81 offen offset:3072 lds\n\t"
  "s_add_u32 s0, s26, s93\n\t"
  "s_addc_u32 s1, s27, 0\n\t"
  "s_sub_u32 s0, s0, 0x1000\n\t"
  "s_subb_u32 s1, s1, 0\n\t"
  "s_and_b32 s1, s1, 0xffff\n\t"
  "s_mov_b32 s2, s92\n\t"
  "s_mov_b32 s3, 0x20000\n\t"
  "s_mul_i32 s52, s10, 0x1c00\n\t"
  "s_lshl_b32 s92, s11, 11\n\t"
  "s_add_u32 m0, s84, 0x10000\n\t"
  "s_add_u32 s88, s4, 0x1100\n\t"
  "buffer_load_dwordx4 v46, s[32:35], s88 offen offset:0 lds\n\t"
  "s_getpc_b64 s[22:23]\n\t"
  "HOPB_%=:\n\t"
  "s_add_u32 s22, s22, MAIN_%=-HOPB_%=\n\t"
  "s_addc_u32 s23, s23, 0\n\t"
  "s_lshl_b32 s24, s11, 12\n\t"
  "s_add_u32 s22, s22, s24\n\t"
  "s_addc_u32 s23, s23, 0\n\t"
  "s_mov_b64 exec, 0\n\t"
  "s_swappc_b64 s[24:25], s[22:23]\n\t"
  "s_mov_b64 exec, -1\n\t"
  "s_add_u32 s22, s22, 0x4000\n\t"
  "s_addc_u32 s23, s23, 0\n\t"
  "s_add_u32 s52, s52, s92\n\t"
  "s_add_u32 s52, s52, 0x2f00000\n\t"
  "s_cmp_lt_u32 s10, 0xe0\n\t"
  "s_cselect_b32 s52, s52, 0x7f000000\n\t"
  "s_lshl_b32 s53, s10, 2\n\t"
  "s_add_u32 s53, s53, s11\n\t"
  "s_lshl_b32 s53, s53, 3\n\t"
  "s_add_u32 s53, s53, 0x5000\n\t"
  "s_lshr_b32 s90, s73, 2\n\t"
  "s_add_u32 s81, s4, 0x4500\n\t"
  "buffer_load_dwordx4 v46, s[32:35], s81 offen offset:1024 lds\n\t"
  "s_and_b32 s91, s73, 3\n\t"
  "s_lshl_b32 s92, s90, 2\n\t"
  "s_add_u32 s92, s92, s11\n\t"
  "s_lshl_b32 s93, s92, 14\n\t"
  "s_add_u32 s89, s89, s93\n\t"
  "s_add_u32 s44, s16, s89\n\t"
  "s_addc_u32 s45, s17, 0\n\t"
  "s_sub_u32 s44, s44, 0x1000\n\t"
  "s_subb_u32 s45, s45, 0\n\t"
  "s_add_u32 s88, s4, 0x940\n\t"
  "buffer_load_dwordx4 v46, s[32:35], s88 offen offset:2048 lds\n\t"
  "s_and_b32 s45, s45, 0xffff\n\t"
  "s_mov_b32 s46, 0x5000\n\t"
  "s_mov_b32 s47, 0x20000\n\t"
  "s_lshl_b32 s31, s91, 1\n\t"
  "s_and_b32 s93, s72, 1\n\t"
  "s_add_u32 s31, s31, s93\n\t"
  "s_lshl_b32 s31, s31, 11\n\t"
  "s_add_u32 s31, s31, 0x1000\n\t"
  "s_mul_i32 s76, s92, 0x1c00\n\t"
  "s_add_u32 s81, s4, 0x3d40\n\t"
  "buffer_load_dwordx4 v46, s[32:35], s81 offen offset:3072 lds\n\t"
  "s_mul_i32 s89, s72, 0x38000\n\t"
  "s_mul_i32 s93, s91, 0xe0\n\t"
  "s_add_u32 s89, s89, s93\n\t"
  "s_sub_u32 s93, 0x39000, s93\n\t"
  "s_add_u32 s48, s20, s89\n\t"
  "s_addc_u32 s49, s21, 0\n\t"
  "s_sub_u32 s48, s48, 0x1000\n\t"
  "s_subb_u32 s49, s49, 0\n\t"
  "s_and_b32 s49, s49, 0xffff\n\t"
  "s_add_u32 m0, s84, 0x11000\n\t"
  "s_add_u32 s88, s4, 0x1180\n\t"
  "buffer_load_dwordx4 v46, s[32:35], s88 offen offset:0 lds\n\t"
  "s_mov_b64 exec, 0\n\t"
  "s_swappc_b64 s[24:25], s[22:23]\n\t"
  "s_mov_b64 exec, -1\n\t"
  "s_add_u32 s22, s22, 0x4000\n\t"
  "s_addc_u32 s23, s23, 0\n\t"
  "s_mov_b32 s50, s93\n\t"
  "s_mov_b32 s51, 0x20000\n\t"
  "s_and_b32 s95, s72, 3\n\t"
  "s_lshr_b32 s94, s72, 2\n\t"
  "s_mul_i32 s93, s95, 0xe0\n\t"
  "s_lshl_b32 s89, s95, 2\n\t"
  "s_add_u32 s89, s89, s11\n\t"
  "s_mul_i32 s54, s89, 0x1c0\n\t"
  "s_add_u32 s54, s54, 0x5000\n\t"
  "s_add_u32 s81, s4, 0x4580\n\t"
  "buffer_load_dwordx4 v46, s[32:35], s81 offen offset:1024 lds\n\t"
  "s_mul_i32 s79, s89, 0x18800\n\t"
  "s_add_u32 s79, s79, 0x2f00000\n\t"
  "s_lshl_b32 s89, s73, 2\n\t"
  "s_add_u32 s94, s94, s89\n\t"
  "s_mul_i32 s89, s94, 0x7000\n\t"
  "s_lshl_b32 s92, s11, 6\n\t"
  "s_add_u32 s93, s93, s92\n\t"
  "s_add_u32 s89, s89, s93\n\t"
  "s_sub_u32 s92, 0x7000, s93\n\t"
  "s_add_u32 s88, s5, 0x9c0\n\t"
  "buffer_load_dwordx4 v46, s[32:35], s88 offen offset:2048 lds\n\t"
  "s_add_u32 s60, s28, s89\n\t"
  "s_addc_u32 s61, s29, 0\n\t"
  "s_and_b32 s61, s61, 0xffff\n\t"
  "s_mov_b32 s62, s92\n\t"
  "s_mov_b32 s63, 0x20000\n\t"
  "s_add_u32 s64, s12, s89\n\t"
  "s_addc_u32 s65, s13, 0\n\t"
  "s_and_b32 s65, s65, 0xffff\n\t"
  "s_mov_b32 s66, s92\n\t"
  "s_add_u32 s81, s5, 0x3dc0\n\t"
  "buffer_load_dwordx4 v46, s[32:35], s81 offen offset:3072 lds\n\t"
  "s_mov_b32 s67, 0x20000\n\t"
  "s_mov_b32 s19, s95\n\t"
  "s_mul_i32 s77, s72, 0x70000\n\t"
  "s_add_u32 s77, s77, 0x900000\n\t"
  "s_mul_i32 s20, s91, 0x1c000\n\t"
  "s_add_u32 s20, s20, s76\n\t"
  "s_add_u32 s20, s20, s77\n\t"
  "s_lshr_b32 s92, s72, 1\n\t"
  "s_lshl_b32 s76, s92, 18\n\t"
  "s_add_u32 m0, s84, 0x18000\n\t"
  "s_add_u32 s88, s5, 0x1200\n\t"
  "buffer_load_dwordx4 v46, s[32:35], s88 offen offset:0 lds\n\t"
  "s_mov_b64 exec, 0\n\t"
  "s_swappc_b64 s[24:25], s[22:23]\n\t"
  "s_mov_b64 exec, -1\n\t"
  "s_add_u32 s22, s22, 0x4000\n\t"
  "s_addc_u32 s23, s23, 0\n\t"
  "s_waitcnt vmcnt(13)\n\t"
  "s_waitcnt vmcnt(19)\n\t"
  "ds_read_b128 v[128:131], v120 offset:32768\n\t"
  "ds_read_b128 v[132:135], v120 offset:33792\n\t"
  "ds_read_b128 v[136:139], v120 offset:34816\n\t"
  "ds_read_b128 v[140:143], v120 offset:35840\n\t"
  "s_add_u32 s76, s76, 0x3100000\n\t"
  "s_lshl_b32 s80, s73, 1\n\t"
  "s_and_b32 s93, s72, 1\n\t"
  "s_add_u32 s80, s80, s93\n\t"
  "s_mul_i32 s87, s11, 0x1c00\n\t"
  "s_add_u32 s77, s77, s87\n\t"
  "s_mul_i32 s78, s11, 0x7000\n\t"
  "s_and_b32 s29, s90, 3\n\t"
  "s_lshl_b32 s29, s29, 2\n\t"
  "s_add_u32 s81, s5, 0x4600\n\t"
  "buffer_load_dwordx4 v46, s[32:35], s81 offen offset:1024 lds\n\t"
  "s_add_u32 s29, s29, s11\n\t"
  "s_mul_i32 s29, s29, 0x3800\n\t"
  "s_add_u32 s29, s29, 0x1000\n\t"
  "s_lshl_b32 s89, s73, 2\n\t"
  "s_add_u32 s89, s89, s11\n\t"
  "s_mul_i32 s85, s89, 0x1c000\n\t"
  "s_add_u32 s85, s85, 0x100000\n\t"
  "s_mul_i32 s86, s89, 0x70000\n\t"
  "s_mul_i32 s90, s72, 0x1c00\n\t"
  "s_add_u32 s88, s5, 0xa40\n\t"
  "buffer_load_dwordx4 v46, s[32:35], s88 offen offset:2048 lds\n\t"
  "s_waitcnt lgkmcnt(0)\n\t"
  "v_mfma_f32_16x16x32_bf16 v[56:59], v[52:55], v[128:131], 0\n\t"
  "v_mfma_f32_16x16x32_bf16 v[88:91], v[128:131], v[128:131], 0\n\t"
  "v_mfma_f32_16x16x32_bf16 v[60:63], v[52:55], v[132:135], 0\n\t"
  "v_mfma_f32_16x16x32_bf16 v[92:95], v[132:135], v[132:135], 0\n\t"
  "v_mfma_f32_16x16x32_bf16 v[56:59], v[52:55], v[136:139], v[56:59]\n\t"
  "v_mfma_f32_16x16x32_bf16 v[88:91], v[136:139], v[136:139], v[88:91]\n\t"
  "v_mfma_f32_16x16x32_bf16 v[60:63], v[52:55], v[140:143], v[60:63]\n\t"
  "v_mfma_f32_16x16x32_bf16 v[92:95], v[140:143], v[140:143], v[92:95]\n\t"
  "s_add_u32 s86, s86, s90\n\t"
  "s_add_u32 s86, s86, 0x1100000\n\t"
  "s_mov_b32 s82, 0x42200000\n\t"
  "s_mov_b32 s21, 0x3800\n\t"
  "s_lshl_b32 s12, s89, 4\n\t"
  "s_add_u32 s12, s12, s72\n\t"
  "s_lshl_b32 s12, s12, 3\n\t"
  "s_add_u32 s12, s12, 0x1000\n\t"
  "s_lshl_b32 s13, s89, 9\n\t"
  "s_add_u32 s81, s5, 0x3e40\n\t"
  "buffer_load_dwordx4 v46, s[32:35], s81 offen offset:3072 lds\n\t"
  "s_lshl_b32 s91, s72, 3\n\t"
  "s_add_u32 s13, s13, s91\n\t"
  "s_add_u32 s13, s13, 0x8000\n\t"
  "s_lshl_b32 s55, s72, 9\n\t"
  "s_add_u32 s55, s55, 0x3000\n\t"
  "s_lshl_b32 s17, s89, 3\n\t"
  "s_add_u32 s17, s17, s55\n\t"
  "s_lshl_b32 s14, s94, 7\n\t"
  "s_lshl_b32 s91, s11, 5\n\t"
  "s_add_u32 m0, s84, 0x19000\n\t"
  "s_add_u32 s88, s5, 0x1280\n\t"
  "buffer_load_dwordx4 v46, s[32:35], s88 offen offset:0 lds\n\t"
  "s_mov_b64 exec, 0\n\t"
  "s_swappc_b64 s[24:25], s[22:23]\n\t"
  "s_mov_b64 exec, -1\n\t"
  "s_add_u32 s22, s22, 0x4000\n\t"
  "s_addc_u32 s23, s23, 0\n\t"
  "s_waitcnt vmcnt(13)\n\t"
  "ds_read_b128 v[144:147], v120 offset:36864\n\t"
  "ds_read_b128 v[148:151], v120 offset:37888\n\t"
  "ds_read_b128 v[152:155], v120 offset:38912\n\t"
  "ds_read_b128 v[156:159], v120 offset:39936\n\t"
  "s_add_u32 s14, s14, s91\n\t"
  "s_add_u32 s14, s14, 0x1000\n\t"
  "s_lshl_b32 s91, s94, 2\n\t"
  "s_add_u32 s91, s91, s19\n\t"
  "s_lshl_b32 s15, s91, 7\n\t"
  "s_add_u32 s15, s15, 0x8000\n\t"
  "s_mul_i32 s16, s91, 0x1c000\n\t"
  "s_lshl_b32 s91, s11, 11\n\t"
  "s_add_u32 s16, s16, s91\n\t"
  "s_add_u32 s81, s5, 0x4680\n\t"
  "buffer_load_dwordx4 v46, s[32:35], s81 offen offset:1024 lds\n\t"
  "s_add_u32 s16, s16, 0x1100000\n\t"
  "s_mul_i32 s18, s94, 0x1c000\n\t"
  "s_mul_i32 s91, s11, 0x7000\n\t"
  "s_add_u32 s18, s18, s91\n\t"
  "s_add_u32 s18, s18, 0x100000\n\t"
  "s_and_b32 s91, s72, 1\n\t"
  "s_mul_i32 s92, s72, 7\n\t"
  "s_add_u32 s92, s92, s91\n\t"
  "s_lshl_b32 s92, s92, 10\n\t"
  "s_add_u32 s88, s5, 0xac0\n\t"
  "buffer_load_dwordx4 v46, s[32:35], s88 offen offset:2048 lds\n\t"
  "s_waitcnt lgkmcnt(0)\n\t"
  "v_mfma_f32_16x16x32_bf16 v[56:59], v[52:55], v[144:147], v[56:59]\n\t"
  "v_mfma_f32_16x16x32_bf16 v[88:91], v[144:147], v[144:147], v[88:91]\n\t"
  "v_mfma_f32_16x16x32_bf16 v[60:63], v[52:55], v[148:151], v[60:63]\n\t"
  "v_mfma_f32_16x16x32_bf16 v[92:95], v[148:151], v[148:151], v[92:95]\n\t"
  "v_mfma_f32_16x16x32_bf16 v[56:59], v[52:55], v[152:155], v[56:59]\n\t"
  "v_mfma_f32_16x16x32_bf16 v[88:91], v[152:155], v[152:155], v[88:91]\n\t"
  "v_mfma_f32_16x16x32_bf16 v[60:63], v[52:55], v[156:159], v[60:63]\n\t"
  "v_mfma_f32_16x16x32_bf16 v[92:95], v[156:159], v[156:159], v[92:95]\n\t"
  "s_add_u32 s85, s85, s92\n\t"
  "s_cmp_eq_u32 s91, 0\n\t"
  "s_mov_b32 s90, 0xfffff808\n\t"
  "s_cselect_b32 s90, 0x1800, s90\n\t"
  "s_add_u32 s27, s20, 0x1000\n\t"
  "s_mov_b32 s92, s98\n\t"
  "s_mov_b32 s93, s99\n\t"
  "s_mov_b32 s94, s75\n\t"
  "s_mov_b32 s95, 0x20000\n\t"
  "s_add_u32 s81, s5, 0x3ec0\n\t"
  "buffer_load_dwordx4 v46, s[32:35], s81 offen offset:3072 lds\n\t"
  "s_add_u32 m0, s84, 0x20000\n\t"
  "s_add_u32 s88, s5, 0x1300\n\t"
  "buffer_load_dwordx4 v46, s[32:35], s88 offen offset:0 lds\n\t"
  "s_waitcnt vmcnt(13)\n\t"
  "ds_read_b128 v[160:163], v121 offset:0\n\t"
  "ds_read_b128 v[164:167], v121 offset:1024\n\t"
  "ds_read_b128 v[168:171], v121 offset:2048\n\t"
  "ds_read_b128 v[172:175], v121 offset:3072\n\t"
  "s_add_u32 s81, s5, 0x4700\n\t"
  "buffer_load_dwordx4 v46, s[32:35], s81 offen offset:1024 lds\n\t"
  "s_add_u32 s88, s5, 0xb40\n\t"
  "buffer_load_dwordx4 v46, s[32:35], s88 offen offset:2048 lds\n\t"
  "s_waitcnt lgkmcnt(0)\n\t"
  "v_mfma_f32_16x16x32_bf16 v[56:59], v[52:55], v[160:163], v[56:59]\n\t"
  "v_mfma_f32_16x16x32_bf16 v[88:91], v[160:163], v[160:163], v[88:91]\n\t"
  "v_mfma_f32_16x16x32_bf16 v[60:63], v[52:55], v[164:167], v[60:63]\n\t"
  "v_mfma_f32_16x16x32_bf16 v[92:95], v[164:167], v[164:167], v[92:95]\n\t"
  "v_mfma_f32_16x16x32_bf16 v[56:59], v[52:55], v[168:171], v[56:59]\n\t"
  "v_mfma_f32_16x16x32_bf16 v[88:91], v[168:171], v[168:171], v[88:91]\n\t"
  "v_mfma_f32_16x16x32_bf16 v[60:63], v[52:55], v[172:175], v[60:63]\n\t"
  "v_mfma_f32_16x16x32_bf16 v[92:95], v[172:175], v[172:175], v[92:95]\n\t"
  "s_add_u32 s81, s5, 0x3f40\n\t"
  "buffer_load_dwordx4 v46, s[32:35], s81 offen offset:3072 lds\n\t"
  "s_add_u32 s35, s85, s90\n\t"
  "s_add_u32 s88, s85, 0x1000\n\t"
  "s_mov_b32 s90, 0x22222222\n\t"
  "s_mov_b32 s91, 0x22222222\n\t"
  "s_mov_b32 s22, 0x44444444\n\t"
  "s_mov_b32 s23, 0x44444444\n\t"
  "s_mov_b32 s24, 0x88888888\n\t"
  "s_mov_b32 s25, 0x88888888\n\t"
  "s_mov_b32 vcc_lo, 0x00f0000f\n\t"
  "s_mov_b32 vcc_hi, 0xf0000f00\n\t"
  "s_add_u32 m0, s84, 0\n\t"
  "s_nop 0\n\t"
  "buffer_load_dwordx4 v8, s[36:39], s74 offen offset:0 lds\n\t"
  "buffer_load_dwordx4 v9, s[36:39], s74 offen offset:1024 lds\n\t"
  "s_waitcnt vmcnt(14)\n\t"
  "ds_read_b128 v[176:179], v121 offset:4096\n\t"
  "ds_read_b128 v[180:183], v121 offset:5120\n\t"
  "ds_read_b128 v[184:187], v121 offset:6144\n\t"
  "ds_read_b128 v[188:191], v121 offset:7168\n\t"
  "buffer_load_dwordx4 v10, s[36:39], s74 offen offset:2048 lds\n\t"
  "buffer_load_dwordx4 v11, s[36:39], s74 offen offset:3072 lds\n\t"
  "s_waitcnt vmcnt(12)\n\t"
  "ds_read_b128 v[192:195], v121 offset:32768\n\t"
  "ds_read_b128 v[196:199], v121 offset:33792\n\t"
  "ds_read_b128 v[200:203], v121 offset:34816\n\t"
  "ds_read_b128 v[204:207], v121 offset:35840\n\t"
  "s_waitcnt lgkmcnt(4)\n\t"
  "v_mfma_f32_16x16x32_bf16 v[56:59], v[52:55], v[176:179], v[56:59]\n\t"
  "v_mfma_f32_16x16x32_bf16 v[88:91], v[176:179], v[176:179], v[88:91]\n\t"
  "v_mfma_f32_16x16x32_bf16 v[60:63], v[52:55], v[180:183], v[60:63]\n\t"
  "v_mfma_f32_16x16x32_bf16 v[92:95], v[180:183], v[180:183], v[92:95]\n\t"
  "v_mfma_f32_16x16x32_bf16 v[56:59], v[52:55], v[184:187], v[56:59]\n\t"
  "v_mfma_f32_16x16x32_bf16 v[88:91], v[184:187], v[184:187], v[88:91]\n\t"
  "v_mfma_f32_16x16x32_bf16 v[60:63], v[52:55], v[188:191], v[60:63]\n\t"
  "v_mfma_f32_16x16x32_bf16 v[92:95], v[188:191], v[188:191], v[92:95]\n\t"
  "s_add_u32 m0, s84, 0x1000\n\t"
  "s_nop 0\n\t"
  "buffer_load_dwordx4 v12, s[36:39], s74 offen offset:0 lds\n\t"
  "buffer_load_dwordx4 v13, s[36:39], s74 offen offset:1024 lds\n\t"
  "s_waitcnt vmcnt(10)\n\t"
  "ds_read_b128 v[208:211], v121 offset:36864\n\t"
  "ds_read_b128 v[212:215], v121 offset:37888\n\t"
  "ds_read_b128 v[216:219], v121 offset:38912\n\t"
  "ds_read_b128 v[220:223], v121 offset:39936\n\t"
  "s_waitcnt lgkmcnt(4)\n\t"
  "v_mfma_f32_16x16x32_bf16 v[56:59], v[52:55], v[192:195], v[56:59]\n\t"
  "v_mfma_f32_16x16x32_bf16 v[88:91], v[192:195], v[192:195], v[88:91]\n\t"
  "v_mfma_f32_16x16x32_bf16 v[60:63], v[52:55], v[196:199], v[60:63]\n\t"
  "v_mfma_f32_16x16x32_bf16 v[92:95], v[196:199], v[196:199], v[92:95]\n\t"
  "v_mfma_f32_16x16x32_bf16 v[56:59], v[52:55], v[200:203], v[56:59]\n\t"
  "v_mfma_f32_16x16x32_bf16 v[88:91], v[200:203], v[200:203], v[88:91]\n\t"
  "v_mfma_f32_16x16x32_bf16 v[60:63], v[52:55], v[204:207], v[60:63]\n\t"
  "v_mfma_f32_16x16x32_bf16 v[92:95], v[204:207], v[204:207], v[92:95]\n\t"
  "buffer_load_dwordx4 v14, s[36:39], s74 offen offset:2048 lds\n\t"
  "s_add_u32 s74, s74, 0x38000\n\t"
  "s_waitcnt vmcnt(7)\n\t"
  "ds_read_b128 v[224:227], v28 offset:0\n\t"
  "ds_read_b128 v[228:231], v28 offset:1024\n\t"
  "ds_read_b128 v[232:235], v28 offset:2048\n\t"
  "ds_read_b128 v[236:239], v28 offset:3072\n\t"
  "s_waitcnt lgkmcnt(4)\n\t"
  "v_mfma_f32_16x16x32_bf16 v[56:59], v[52:55], v[208:211], v[56:59]\n\t"
  "v_mfma_f32_16x16x32_bf16 v[88:91], v[208:211], v[208:211], v[88:91]\n\t"
  "v_mfma_f32_16x16x32_bf16 v[60:63], v[52:55], v[212:215], v[60:63]\n\t"
  "v_mfma_f32_16x16x32_bf16 v[92:95], v[212:215], v[212:215], v[92:95]\n\t"
  "v_mfma_f32_16x16x32_bf16 v[56:59], v[52:55], v[216:219], v[56:59]\n\t"
  "v_mfma_f32_16x16x32_bf16 v[88:91], v[216:219], v[216:219], v[88:91]\n\t"
  "v_mfma_f32_16x16x32_bf16 v[60:63], v[52:55], v[220:223], v[60:63]\n\t"
  "v_mfma_f32_16x16x32_bf16 v[92:95], v[220:223], v[220:223], v[92:95]\n\t"
  "s_waitcnt lgkmcnt(0)\n\t"
  "v_mfma_f32_16x16x32_bf16 v[56:59], v[52:55], v[224:227], v[56:59]\n\t"
  "v_mfma_f32_16x16x32_bf16 v[88:91], v[224:227], v[224:227], v[88:91]\n\t"
  "v_mfma_f32_16x16x32_bf16 v[60:63], v[52:55], v[228:231], v[60:63]\n\t"
  "v_mfma_f32_16x16x32_bf16 v[92:95], v[228:231], v[228:231], v[92:95]\n\t"
  "v_mfma_f32_16x16x32_bf16 v[56:59], v[52:55], v[232:235], v[56:59]\n\t"
  "v_mfma_f32_16x16x32_bf16 v[88:91], v[232:235], v[232:235], v[88:91]\n\t"
  "v_mfma_f32_16x16x32_bf16 v[60:63], v[52:55], v[236:239], v[60:63]\n\t"
  "v_mfma_f32_16x16x32_bf16 v[92:95], v[236:239], v[236:239], v[92:95]\n\t"
  "s_add_u32 m0, s84, 0x8000\n\t"
  "s_nop 0\n\t"
  "buffer_load_dwordx4 v8, s[36:39], s74 offen offset:0 lds\n\t"
  "s_nop 2\n\t"
  "v_cndmask_b32 v1, v88, v89, s[90:91]\n\t"
  "v_cndmask_b32 v1, v1, v90, s[22:23]\n\t"
  "v_cndmask_b32 v1, v1, v91, s[24:25]\n\t"
  "v_cndmask_b32 v1, 0, v1, vcc\n\t"
  "v_cndmask_b32 v4, v92, v93, s[90:91]\n\t"
  "v_cndmask_b32 v4, v4, v94, s[22:23]\n\t"
  "v_cndmask_b32 v4, v4, v95, s[24:25]\n\t"
  "v_cndmask_b32 v4, 0, v4, vcc\n\t"
  "v_mov_b32 v2, v1\n\t"
  "v_mov_b32 v5, v4\n\t"
  "buffer_load_dwordx4 v9, s[36:39], s74 offen offset:1024 lds\n\t"
  "v_permlane32_swap_b32 v1, v2\n\t"
  "v_permlane32_swap_b32 v4, v5\n\t"
  "v_add_f32 v1, v1, v2\n\t"
  "v_add_f32 v4, v4, v5\n\t"
  "v_mov_b32 v2, v1\n\t"
  "v_mov_b32 v5, v4\n\t"
  "s_nop 0\n\t"
  "v_permlane16_swap_b32 v1, v2\n\t"
  "v_permlane16_swap_b32 v4, v5\n\t"
  "v_add_f32 v1, v1, v2\n\t"
  "v_add_f32 v4, v4, v5\n\t"
  "buffer_load_dwordx4 v10, s[36:39], s74 offen offset:2048 lds\n\t"
  "v_mul_f32 v3, 0x3b124925, v56\n\t"
  "v_mul_f32 v6, 0x3b124925, v60\n\t"
  "v_mul_f32 v1, 0x3b124925, v1\n\t"
  "v_mul_f32 v4, 0x3b124925, v4\n\t"
  "v_fma_f32 v1, -v3, v3, v1\n\t"
  "v_fma_f32 v4, -v6, v6, v4\n\t"
  "v_max_f32 v1, 0, v1\n\t"
  "v_max_f32 v4, 0, v4\n\t"
  "v_cvt_pk_bf16_f32 v1, v1, v1\n\t"
  "v_cvt_pk_bf16_f32 v4, v4, v4\n\t"
  "buffer_load_dwordx4 v11, s[36:39], s74 offen offset:3072 lds\n\t"
  "v_and_b32 v1, s83, v1\n\t"
  "v_and_b32 v4, s83, v4\n\t"
  "v_add_f32 v1, 0x3727c5ac, v1\n\t"
  "v_add_f32 v4, 0x3727c5ac, v4\n\t"
  "v_cvt_pk_bf16_f32 v1, v1, v1\n\t"
  "v_cvt_pk_bf16_f32 v4, v4, v4\n\t"
  "v_and_b32 v1, s83, v1\n\t"
  "v_and_b32 v4, s83, v4\n\t"
  "v_sqrt_f32 v1, v1\n\t"
  "v_sqrt_f32 v4, v4\n\t"
  "s_add_u32 m0, s84, 0x9000\n\t"
  "s_nop 0\n\t"
  "buffer_load_dwordx4 v12, s[36:39], s74 offen offset:0 lds\n\t"
  "v_cvt_pk_bf16_f32 v1, v1, v1\n\t"
  "v_cvt_pk_bf16_f32 v4, v4, v4\n\t"
  "v_and_b32 v1, s83, v1\n\t"
  "v_and_b32 v4, s83, v4\n\t"
  "v_rcp_f32 v1, v1\n\t"
  "v_rcp_f32 v4, v4\n\t"
  "v_cvt_pk_bf16_f32 v3, v3, v3\n\t"
  "v_cvt_pk_bf16_f32 v6, v6, v6\n\t"
  "v_and_b32 v3, s83, v3\n\t"
  "v_and_b32 v6, s83, v6\n\t"
  "buffer_load_dwordx4 v13, s[36:39], s74 offen offset:1024 lds\n\t"
  "v_mul_f32 v48, 0x3db8aa3b, v1\n\t"
  "v_mul_f32 v49, 0x3db8aa3b, v4\n\t"
  "v_mul_f32 v50, -v3, v48\n\t"
  "v_mul_f32 v51, -v6, v49\n\t"
  "v_lshlrev_b32 v2, 16, v128\n\t"
  "v_lshlrev_b32 v4, 16, v129\n\t"
  "v_lshlrev_b32 v6, 16, v130\n\t"
  "v_and_b32 v1, s83, v128\n\t"
  "v_and_b32 v3, s83, v129\n\t"
  "v_and_b32 v5, s83, v130\n\t"
  "buffer_load_dwordx4 v14, s[36:39], s74 offen offset:2048 lds\n\t"
  "s_add_u32 s74, s74, 0x38000\n\t"
  "v_fma_f32 v2, v2, v48, v50\n\t"
  "v_fma_f32 v4, v4, v48, v50\n\t"
  "v_fma_f32 v6, v6, v48, v50\n\t"
  "v_fma_f32 v1, v1, v48, v50\n\t"
  "v_fma_f32 v3, v3, v48, v50\n\t"
  "v_fma_f32 v5, v5, v48, v50\n\t"
  "v_cvt_pk_bf16_f32 v128, v2, v1\n\t"
  "v_cvt_pk_bf16_f32 v129, v4, v3\n\t"
  "v_cvt_pk_bf16_f32 v130, v6, v5\n\t"
  "v_lshlrev_b32 v2, 16, v131\n\t"
  "buffer_load_dwordx2 v[122:123], v124, s[56:59], 0 offen offset:0 sc1\n\t"
  "v_lshlrev_b32 v4, 16, v132\n\t"
  "v_lshlrev_b32 v6, 16, v133\n\t"
  "v_and_b32 v1, s83, v131\n\t"
  "v_and_b32 v3, s83, v132\n\t"
  "v_and_b32 v5, s83, v133\n\t"
  "v_fma_f32 v2, v2, v48, v50\n\t"
  "v_fma_f32 v4, v4, v49, v51\n\t"
  "v_fma_f32 v6, v6, v49, v51\n\t"
  "v_fma_f32 v1, v1, v48, v50\n\t"
  "v_fma_f32 v3, v3, v49, v51\n\t"
  "buffer_load_dword v47, v31, s[68:71], s8 offen offset:0\n\t"
  "s_add_u32 s8, s8, s9\n\t"
  "v_fma_f32 v5, v5, v49, v51\n\t"
  "v_cvt_pk_bf16_f32 v131, v2, v1\n\t"
  "v_cvt_pk_bf16_f32 v132, v4, v3\n\t"
  "v_cvt_pk_bf16_f32 v133, v6, v5\n\t"
  "v_lshlrev_b32 v2, 16, v134\n\t"
  "v_lshlrev_b32 v4, 16, v135\n\t"
  "v_and_b32 v1, s83, v134\n\t"
  "v_and_b32 v3, s83, v135\n\t"
  "v_fma_f32 v2, v2, v49, v51\n\t"
  "v_fma_f32 v4, v4, v49, v51\n\t"
  "buffer_load_dword v47, v31, s[68:71], s8 offen offset:0\n\t"
  "s_add_u32 s8, s8, s9\n\t"
  "v_fma_f32 v1, v1, v49, v51\n\t"
  "v_fma_f32 v3, v3, v49, v51\n\t"
  "v_cvt_pk_bf16_f32 v134, v2, v1\n\t"
  "v_cvt_pk_bf16_f32 v135, v4, v3\n\t"
  "s_branch MAIN_%=\n\t"
  "TRAMP_%=:\n\t"
  "s_setpc_b64 s[24:25]\n\t"
  ".p2align 12\n\t"
  "MAIN_%=:\n\t"
  "s_cbranch_execz TRAMP_%=\n\t"
  "s_waitcnt vmcnt(10)\n\t"
  "s_barrier\n\t"
  "ds_read_b64_tr_b16 a[224:225], v32 offset:0\n\t"
  "ds_read_b64_tr_b16 a[226:227], v32 offset:1792\n\t"
  "ds_read_b64_tr_b16 a[228:229], v32 offset:32\n\t"
  "ds_read_b64_tr_b16 a[230:231], v32 offset:1824\n\t"
  "s_waitcnt lgkmcnt(0)\n\t"
  "v_mfma_f32_16x16x32_bf16 a[112:115], a[224:227], v[128:131], 0\n\t"
  "ds_read_b64_tr_b16 a[232:233], v32 offset:64\n\t"
  "ds_read_b64_tr_b16 a[234:235], v32 offset:1856\n\t"
  "v_mfma_f32_16x16x32_bf16 a[116:119], a[224:227], v[132:135], 0\n\t"
  "ds_read_b64_tr_b16 a[236:237], v32 offset:96\n\t"
  "ds_read_b64_tr_b16 a[238:239], v32 offset:1888\n\t"
  "v_mfma_f32_16x16x32_bf16 a[120:123], a[228:231], v[128:131], 0\n\t"
  "s_add_u32 m0, s84, 0x10000\n\t"
  "ds_read_b64_tr_b16 a[240:241], v32 offset:128\n\t"
  "ds_read_b64_tr_b16 a[242:243], v32 offset:1920\n\t"
  "v_mfma_f32_16x16x32_bf16 a[124:127], a[228:231], v[132:135], 0\n\t"
  "s_waitcnt lgkmcnt(2)\n\t"
  "ds_read_b64_tr_b16 a[244:245], v32 offset:160\n\t"
  "ds_read_b64_tr_b16 a[246:247], v32 offset:1952\n\t"
  "v_mfma_f32_16x16x32_bf16 a[128:131], a[232:235], v[128:131], 0\n\t"
  "ds_read_b64_tr_b16 a[248:249], v32 offset:192\n\t"
  "ds_read_b64_tr_b16 a[250:251], v32 offset:1984\n\t"
  "v_mfma_f32_16x16x32_bf16 a[132:135], a[232:235], v[132:135], 0\n\t"
  "buffer_load_dwordx4 v8, s[36:39], s74 offen offset:0 lds\n\t"
  "v_mfma_f32_16x16x32_bf16 a[136:139], a[236:239], v[128:131], 0\n\t"
  "ds_read_b64_tr_b16 a[252:253], v32 offset:8192\n\t"
  "v_lshlrev_b32 v6, 16, v136\n\t"
  "v_mfma_f32_16x16x32_bf16 a[140:143], a[236:239], v[132:135], 0\n\t"
  "s_waitcnt lgkmcnt(3)\n\t"
  "ds_read_b64_tr_b16 a[254:255], v32 offset:9984\n\t"
  "ds_read_b64_tr_b16 a[224:225], v32 offset:8224\n\t"
  "v_mfma_f32_16x16x32_bf16 a[144:147], a[240:243], v[128:131], 0\n\t"
  "ds_read_b64_tr_b16 a[226:227], v32 offset:10016\n\t"
  "ds_read_b64_tr_b16 a[228:229], v32 offset:8256\n\t"
  "v_mfma_f32_16x16x32_bf16 a[148:151], a[240:243], v[132:135], 0\n\t"
  "buffer_load_dwordx4 v9, s[36:39], s74 offen offset:1024 lds\n\t"
  "v_mfma_f32_16x16x32_bf16 a[152:155], a[244:247], v[128:131], 0\n\t"
  "ds_read_b64_tr_b16 a[230:231], v32 offset:10048\n\t"
  "v_and_b32 v5, s83, v136\n\t"
  "v_mfma_f32_16x16x32_bf16 a[156:159], a[244:247], v[132:135], 0\n\t"
  "s_waitcnt lgkmcnt(4)\n\t"
  "ds_read_b64_tr_b16 a[232:233], v32 offset:8288\n\t"
  "ds_read_b64_tr_b16 a[234:235], v32 offset:10080\n\t"
  "v_mfma_f32_16x16x32_bf16 a[160:163], a[248:251], v[128:131], 0\n\t"
  "ds_read_b64_tr_b16 a[236:237], v32 offset:8320\n\t"
  "ds_read_b64_tr_b16 a[238:239], v32 offset:10112\n\t"
  "v_mfma_f32_16x16x32_bf16 a[164:167], a[248:251], v[132:135], 0\n\t"
  "buffer_load_dwordx4 v10, s[36:39], s74 offen offset:2048 lds\n\t"
  "v_mfma_f32_16x16x32_bf16 a[168:171], a[252:255], v[128:131], 0\n\t"
  "ds_read_b64_tr_b16 a[240:241], v32 offset:8352\n\t"
  "v_fma_f32 v6, v6, v48, v50\n\t"
  "v_mfma_f32_16x16x32_bf16 a[172:175], a[252:255], v[132:135], 0\n\t"
  "s_waitcnt lgkmcnt(5)\n\t"
  "ds_read_b64_tr_b16 a[242:243], v32 offset:10144\n\t"
  "v_fma_f32 v5, v5, v48, v50\n\t"
  "v_mfma_f32_16x16x32_bf16 a[176:179], a[224:227], v[128:131], 0\n\t"
  "ds_read_b64_tr_b16 a[244:245], v32 offset:8384\n\t"
  "ds_read_b64_tr_b16 a[246:247], v32 offset:10176\n\t"
  "v_mfma_f32_16x16x32_bf16 a[180:183], a[224:227], v[132:135], 0\n\t"
  "buffer_load_dwordx4 v11, s[36:39], s74 offen offset:3072 lds\n\t"
  "v_mfma_f32_16x16x32_bf16 a[184:187], a[228:231], v[128:131], 0\n\t"
  "s_add_u32 m0, s84, 0x11000\n\t"
  "ds_read_b64_tr_b16 a[248:249], v32 offset:16384\n\t"
  "v_cvt_pk_bf16_f32 v136, v6, v5\n\t"
  "v_mfma_f32_16x16x32_bf16 a[188:191], a[228:231], v[132:135], 0\n\t"
  "s_waitcnt lgkmcnt(5)\n\t"
  "ds_read_b64_tr_b16 a[250:251], v32 offset:18176\n\t"
  "v_lshlrev_b32 v2, 16, v137\n\t"
  "v_mfma_f32_16x16x32_bf16 a[192:195], a[232:235], v[128:131], 0\n\t"
  "ds_read_b64_tr_b16 a[252:253], v32 offset:16416\n\t"
  "ds_read_b64_tr_b16 a[254:255], v32 offset:18208\n\t"
  "v_mfma_f32_16x16x32_bf16 a[196:199], a[232:235], v[132:135], 0\n\t"
  "buffer_load_dwordx4 v12, s[36:39], s74 offen offset:0 lds\n\t"
  "v_mfma_f32_16x16x32_bf16 a[200:203], a[236:239], v[128:131], 0\n\t"
  "ds_read_b64_tr_b16 a[224:225], v32 offset:16448\n\t"
  "v_and_b32 v1, s83, v137\n\t"
  "v_mfma_f32_16x16x32_bf16 a[204:207], a[236:239], v[132:135], 0\n\t"
  "s_waitcnt lgkmcnt(5)\n\t"
  "ds_read_b64_tr_b16 a[226:227], v32 offset:18240\n\t"
  "v_fma_f32 v2, v2, v48, v50\n\t"
  "v_mfma_f32_16x16x32_bf16 a[208:211], a[240:243], v[128:131], 0\n\t"
  "ds_read_b64_tr_b16 a[228:229], v32 offset:16480\n\t"
  "ds_read_b64_tr_b16 a[230:231], v32 offset:18272\n\t"
  "v_mfma_f32_16x16x32_bf16 a[212:215], a[240:243], v[132:135], 0\n\t"
  "buffer_load_dwordx4 v13, s[36:39], s74 offen offset:1024 lds\n\t"
  "v_mfma_f32_16x16x32_bf16 a[216:219], a[244:247], v[128:131], 0\n\t"
  "ds_read_b64_tr_b16 a[232:233], v32 offset:16512\n\t"
  "v_fma_f32 v1, v1, v48, v50\n\t"
  "v_mfma_f32_16x16x32_bf16 a[220:223], a[244:247], v[132:135], 0\n\t"
  "s_waitcnt lgkmcnt(5)\n\t"
  "ds_read_b64_tr_b16 a[234:235], v32 offset:18304\n\t"
  "v_cvt_pk_bf16_f32 v137, v2, v1\n\t"
  "v_lshlrev_b32 v4, 16, v138\n\t"
  "v_and_b32 v3, s83, v138\n\t"
  "v_fma_f32 v4, v4, v48, v50\n\t"
  "v_fma_f32 v3, v3, v48, v50\n\t"
  "v_lshlrev_b32 v6, 16, v139\n\t"
  "v_cvt_pk_bf16_f32 v138, v4, v3\n\t"
  "v_and_b32 v5, s83, v139\n\t"
  "v_fma_f32 v6, v6, v48, v50\n\t"
  "v_fma_f32 v5, v5, v48, v50\n\t"
  "v_cvt_pk_bf16_f32 v139, v6, v5\n\t"
  "s_nop 1\n\t"
  "v_mfma_f32_16x16x32_bf16 a[112:115], a[248:251], v[136:139], a[112:115]\n\t"
  "ds_read_b64_tr_b16 a[236:237], v32 offset:16544\n\t"
  "ds_read_b64_tr_b16 a[238:239], v32 offset:18336\n\t"
  "v_lshlrev_b32 v2, 16, v140\n\t"
  "v_and_b32 v1, s83, v140\n\t"
  "v_fma_f32 v2, v2, v49, v51\n\t"
  "v_fma_f32 v1, v1, v49, v51\n\t"
  "v_lshlrev_b32 v4, 16, v141\n\t"
  "v_cvt_pk_bf16_f32 v140, v2, v1\n\t"
  "v_and_b32 v3, s83, v141\n\t"
  "v_fma_f32 v4, v4, v49, v51\n\t"
  "v_fma_f32 v3, v3, v49, v51\n\t"
  "v_lshlrev_b32 v6, 16, v142\n\t"
  "v_cvt_pk_bf16_f32 v141, v4, v3\n\t"
  "v_and_b32 v5, s83, v142\n\t"
  "v_fma_f32 v6, v6, v49, v51\n\t"
  "v_fma_f32 v5, v5, v49, v51\n\t"
  "v_lshlrev_b32 v2, 16, v143\n\t"
  "v_cvt_pk_bf16_f32 v142, v6, v5\n\t"
  "v_and_b32 v1, s83, v143\n\t"
  "v_fma_f32 v2, v2, v49, v51\n\t"
  "v_fma_f32 v1, v1, v49, v51\n\t"
  "v_cvt_pk_bf16_f32 v143, v2, v1\n\t"
  "s_nop 1\n\t"
  "v_mfma_f32_16x16x32_bf16 a[116:119], a[248:251], v[140:143], a[116:119]\n\t"
  "buffer_load_dwordx4 v14, s[36:39], s74 offen offset:2048 lds\n\t"
  "v_mfma_f32_16x16x32_bf16 a[120:123], a[252:255], v[136:139], a[120:123]\n\t"
  "s_add_u32 s74, s74, 0x38000\n\t"
  "ds_read_b64_tr_b16 a[240:241], v32 offset:16576\n\t"
  "v_lshlrev_b32 v4, 16, v144\n\t"
  "v_mfma_f32_16x16x32_bf16 a[124:127], a[252:255], v[140:143], a[124:127]\n\t"
  "s_waitcnt lgkmcnt(5)\n\t"
  "ds_read_b64_tr_b16 a[242:243], v32 offset:18368\n\t"
  "v_and_b32 v3, s83, v144\n\t"
  "v_mfma_f32_16x16x32_bf16 a[128:131], a[224:227], v[136:139], a[128:131]\n\t"
  "ds_read_b64_tr_b16 a[244:245], v32 offset:24576\n\t"
  "ds_read_b64_tr_b16 a[246:247], v32 offset:26368\n\t"
  "v_mfma_f32_16x16x32_bf16 a[132:135], a[224:227], v[140:143], a[132:135]\n\t"
  "v_fma_f32 v4, v4, v48, v50\n\t"
  "v_fma_f32 v3, v3, v48, v50\n\t"
  "v_mfma_f32_16x16x32_bf16 a[136:139], a[228:231], v[136:139], a[136:139]\n\t"
  "s_add_u32 s96, s96, 0x2000\n\t"
  "ds_read_b64_tr_b16 a[248:249], v32 offset:24608\n\t"
  "ds_read_b64_tr_b16 a[250:251], v32 offset:26400\n\t"
  "v_mfma_f32_16x16x32_bf16 a[140:143], a[228:231], v[140:143], a[140:143]\n\t"
  "s_waitcnt lgkmcnt(6)\n\t"
  "v_cvt_pk_bf16_f32 v144, v4, v3\n\t"
  "v_lshlrev_b32 v6, 16, v145\n\t"
  "v_mfma_f32_16x16x32_bf16 a[144:147], a[232:235], v[136:139], a[144:147]\n\t"
  "s_addc_u32 s97, s97, 0\n\t"
  "ds_read_b64_tr_b16 a[252:253], v32 offset:24640\n\t"
  "ds_read_b64_tr_b16 a[254:255], v32 offset:26432\n\t"
  "v_mfma_f32_16x16x32_bf16 a[148:151], a[232:235], v[140:143], a[148:151]\n\t"
  "global_load_dwordx4 v[20:23], v40, s[96:97] offset:-3072\n\t"
  "v_mfma_f32_16x16x32_bf16 a[152:155], a[236:239], v[136:139], a[152:155]\n\t"
  "ds_read_b64_tr_b16 a[224:225], v32 offset:24672\n\t"
  "v_and_b32 v5, s83, v145\n\t"
  "v_mfma_f32_16x16x32_bf16 a[156:159], a[236:239], v[140:143], a[156:159]\n\t"
  "s_waitcnt lgkmcnt(5)\n\t"
  "ds_read_b64_tr_b16 a[226:227], v32 offset:26464\n\t"
  "v_fma_f32 v6, v6, v48, v50\n\t"
  "v_mfma_f32_16x16x32_bf16 a[160:163], a[240:243], v[136:139], a[160:163]\n\t"
  "ds_read_b64_tr_b16 a[228:229], v32 offset:24704\n\t"
  "ds_read_b64_tr_b16 a[230:231], v32 offset:26496\n\t"
  "v_mfma_f32_16x16x32_bf16 a[164:167], a[240:243], v[140:143], a[164:167]\n\t"
  "global_load_dwordx4 v[24:27], v40, s[96:97] offset:-2048\n\t"
  "v_mfma_f32_16x16x32_bf16 a[168:171], a[244:247], v[136:139], a[168:171]\n\t"
  "ds_read_b64_tr_b16 a[232:233], v32 offset:24736\n\t"
  "v_fma_f32 v5, v5, v48, v50\n\t"
  "v_mfma_f32_16x16x32_bf16 a[172:175], a[244:247], v[140:143], a[172:175]\n\t"
  "s_waitcnt lgkmcnt(5)\n\t"
  "ds_read_b64_tr_b16 a[234:235], v32 offset:26528\n\t"
  "v_cvt_pk_bf16_f32 v145, v6, v5\n\t"
  "v_mfma_f32_16x16x32_bf16 a[176:179], a[248:251], v[136:139], a[176:179]\n\t"
  "s_waitcnt vmcnt(12)\n\t"
  "ds_read_b64_tr_b16 a[236:237], v32 offset:24768\n\t"
  "ds_read_b64_tr_b16 a[238:239], v32 offset:26560\n\t"
  "v_mfma_f32_16x16x32_bf16 a[180:183], a[248:251], v[140:143], a[180:183]\n\t"
  "s_barrier\n\t"
  "buffer_load_dword v47, v31, s[68:71], s8 offen offset:0\n\t"
  "v_mfma_f32_16x16x32_bf16 a[184:187], a[252:255], v[136:139], a[184:187]\n\t"
  "s_add_u32 s8, s8, s9\n\t"
  "ds_read_b64_tr_b16 a[240:241], v32 offset:32768\n\t"
  "v_lshlrev_b32 v2, 16, v146\n\t"
  "v_mfma_f32_16x16x32_bf16 a[188:191], a[252:255], v[140:143], a[188:191]\n\t"
  "s_waitcnt lgkmcnt(5)\n\t"
  "ds_read_b64_tr_b16 a[242:243], v32 offset:34560\n\t"
  "v_and_b32 v1, s83, v146\n\t"
  "v_mfma_f32_16x16x32_bf16 a[192:195], a[224:227], v[136:139], a[192:195]\n\t"
  "ds_read_b64_tr_b16 a[244:245], v32 offset:32800\n\t"
  "ds_read_b64_tr_b16 a[246:247], v32 offset:34592\n\t"
  "v_mfma_f32_16x16x32_bf16 a[196:199], a[224:227], v[140:143], a[196:199]\n\t"
  "buffer_load_dword v47, v31, s[68:71], s8 offen offset:0\n\t"
  "v_mfma_f32_16x16x32_bf16 a[200:203], a[228:231], v[136:139], a[200:203]\n\t"
  "s_add_u32 s8, s8, s9\n\t"
  "ds_read_b64_tr_b16 a[248:249], v32 offset:32832\n\t"
  "v_fma_f32 v2, v2, v48, v50\n\t"
  "v_mfma_f32_16x16x32_bf16 a[204:207], a[228:231], v[140:143], a[204:207]\n\t"
  "s_waitcnt lgkmcnt(5)\n\t"
  "ds_read_b64_tr_b16 a[250:251], v32 offset:34624\n\t"
  "v_fma_f32 v1, v1, v48, v50\n\t"
  "v_mfma_f32_16x16x32_bf16 a[208:211], a[232:235], v[136:139], a[208:211]\n\t"
  "ds_read_b64_tr_b16 a[252:253], v32 offset:32864\n\t"
  "ds_read_b64_tr_b16 a[254:255], v32 offset:34656\n\t"
  "v_mfma_f32_16x16x32_bf16 a[212:215], a[232:235], v[140:143], a[212:215]\n\t"
  "buffer_load_dword v47, v31, s[68:71], s8 offen offset:0\n\t"
  "v_mfma_f32_16x16x32_bf16 a[216:219], a[236:239], v[136:139], a[216:219]\n\t"
  "s_add_u32 s8, s8, s9\n\t"
  "ds_read_b64_tr_b16 a[224:225], v32 offset:32896\n\t"
  "v_cvt_pk_bf16_f32 v146, v2, v1\n\t"
  "v_mfma_f32_16x16x32_bf16 a[220:223], a[236:239], v[140:143], a[220:223]\n\t"
  "s_waitcnt lgkmcnt(5)\n\t"
  "ds_read_b64_tr_b16 a[226:227], v32 offset:34688\n\t"
  "v_lshlrev_b32 v4, 16, v147\n\t"
  "v_and_b32 v3, s83, v147\n\t"
  "v_fma_f32 v4, v4, v48, v50\n\t"
  "v_fma_f32 v3, v3, v48, v50\n\t"
  "v_cvt_pk_bf16_f32 v147, v4, v3\n\t"
  "s_nop 1\n\t"
  "v_mfma_f32_16x16x32_bf16 a[112:115], a[240:243], v[144:147], a[112:115]\n\t"
  "ds_read_b64_tr_b16 a[228:229], v32 offset:32928\n\t"
  "ds_read_b64_tr_b16 a[230:231], v32 offset:34720\n\t"
  "v_lshlrev_b32 v6, 16, v148\n\t"
  "v_and_b32 v5, s83, v148\n\t"
  "v_fma_f32 v6, v6, v49, v51\n\t"
  "v_fma_f32 v5, v5, v49, v51\n\t"
  "v_lshlrev_b32 v2, 16, v149\n\t"
  "v_cvt_pk_bf16_f32 v148, v6, v5\n\t"
  "v_and_b32 v1, s83, v149\n\t"
  "v_fma_f32 v2, v2, v49, v51\n\t"
  "v_fma_f32 v1, v1, v49, v51\n\t"
  "v_lshlrev_b32 v4, 16, v150\n\t"
  "v_cvt_pk_bf16_f32 v149, v2, v1\n\t"
  "v_and_b32 v3, s83, v150\n\t"
  "v_fma_f32 v4, v4, v49, v51\n\t"
  "v_fma_f32 v3, v3, v49, v51\n\t"
  "v_lshlrev_b32 v6, 16, v151\n\t"
  "v_cvt_pk_bf16_f32 v150, v4, v3\n\t"
  "v_and_b32 v5, s83, v151\n\t"
  "v_fma_f32 v6, v6, v49, v51\n\t"
  "v_fma_f32 v5, v5, v49, v51\n\t"
  "v_cvt_pk_bf16_f32 v151, v6, v5\n\t"
  "s_nop 1\n\t"
  "v_mfma_f32_16x16x32_bf16 a[116:119], a[240:243], v[148:151], a[116:119]\n\t"
  "v_lshlrev_b32 v2, 16, v152\n\t"
  "v_and_b32 v1, s83, v152\n\t"
  "v_mfma_f32_16x16x32_bf16 a[120:123], a[244:247], v[144:147], a[120:123]\n\t"
  "s_add_u32 m0, s84, 0x18000\n\t"
  "ds_read_b64_tr_b16 a[232:233], v32 offset:32960\n\t"
  "ds_read_b64_tr_b16 a[234:235], v32 offset:34752\n\t"
  "v_mfma_f32_16x16x32_bf16 a[124:127], a[244:247], v[148:151], a[124:127]\n\t"
  "s_waitcnt lgkmcnt(6)\n\t"
  "v_fma_f32 v2, v2, v48, v50\n\t"
  "v_fma_f32 v1, v1, v48, v50\n\t"
  "v_mfma_f32_16x16x32_bf16 a[128:131], a[248:251], v[144:147], a[128:131]\n\t"
  "ds_read_b64_tr_b16 a[236:237], v32 offset:40960\n\t"
  "ds_read_b64_tr_b16 a[238:239], v32 offset:42752\n\t"
  "v_mfma_f32_16x16x32_bf16 a[132:135], a[248:251], v[148:151], a[132:135]\n\t"
  "buffer_load_dwordx4 v8, s[36:39], s74 offen offset:0 lds\n\t"
  "v_mfma_f32_16x16x32_bf16 a[136:139], a[252:255], v[144:147], a[136:139]\n\t"
  "ds_read_b64_tr_b16 a[240:241], v32 offset:40992\n\t"
  "v_cvt_pk_bf16_f32 v152, v2, v1\n\t"
  "v_mfma_f32_16x16x32_bf16 a[140:143], a[252:255], v[148:151], a[140:143]\n\t"
  "s_waitcnt lgkmcnt(5)\n\t"
  "ds_read_b64_tr_b16 a[242:243], v32 offset:42784\n\t"
  "v_lshlrev_b32 v4, 16, v153\n\t"
  "v_mfma_f32_16x16x32_bf16 a[144:147], a[224:227], v[144:147], a[144:147]\n\t"
  "ds_read_b64_tr_b16 a[244:245], v32 offset:41024\n\t"
  "ds_read_b64_tr_b16 a[246:247], v32 offset:42816\n\t"
  "v_mfma_f32_16x16x32_bf16 a[148:151], a[224:227], v[148:151], a[148:151]\n\t"
  "buffer_load_dwordx4 v9, s[36:39], s74 offen offset:1024 lds\n\t"
  "v_mfma_f32_16x16x32_bf16 a[152:155], a[228:231], v[144:147], a[152:155]\n\t"
  "ds_read_b64_tr_b16 a[248:249], v32 offset:41056\n\t"
  "v_and_b32 v3, s83, v153\n\t"
  "v_mfma_f32_16x16x32_bf16 a[156:159], a[228:231], v[148:151], a[156:159]\n\t"
  "s_waitcnt lgkmcnt(5)\n\t"
  "ds_read_b64_tr_b16 a[250:251], v32 offset:42848\n\t"
  "v_fma_f32 v4, v4, v48, v50\n\t"
  "v_mfma_f32_16x16x32_bf16 a[160:163], a[232:235], v[144:147], a[160:163]\n\t"
  "ds_read_b64_tr_b16 a[252:253], v32 offset:41088\n\t"
  "ds_read_b64_tr_b16 a[254:255], v32 offset:42880\n\t"
  "v_mfma_f32_16x16x32_bf16 a[164:167], a[232:235], v[148:151], a[164:167]\n\t"
  "buffer_load_dwordx4 v10, s[36:39], s74 offen offset:2048 lds\n\t"
  "v_mfma_f32_16x16x32_bf16 a[168:171], a[236:239], v[144:147], a[168:171]\n\t"
  "ds_read_b64_tr_b16 a[224:225], v32 offset:41120\n\t"
  "v_fma_f32 v3, v3, v48, v50\n\t"
  "v_mfma_f32_16x16x32_bf16 a[172:175], a[236:239], v[148:151], a[172:175]\n\t"
  "s_waitcnt lgkmcnt(5)\n\t"
  "ds_read_b64_tr_b16 a[226:227], v32 offset:42912\n\t"
  "v_cvt_pk_bf16_f32 v153, v4, v3\n\t"
  "v_mfma_f32_16x16x32_bf16 a[176:179], a[240:243], v[144:147], a[176:179]\n\t"
  "ds_read_b64_tr_b16 a[228:229], v32 offset:41152\n\t"
  "ds_read_b64_tr_b16 a[230:231], v32 offset:42944\n\t"
  "v_mfma_f32_16x16x32_bf16 a[180:183], a[240:243], v[148:151], a[180:183]\n\t"
  "v_lshlrev_b32 v6, 16, v154\n\t"
  "v_and_b32 v5, s83, v154\n\t"
  "v_mfma_f32_16x16x32_bf16 a[184:187], a[244:247], v[144:147], a[184:187]\n\t"
  "ds_read_b64_tr_b16 a[232:233], v32 offset:49152\n\t"
  "ds_read_b64_tr_b16 a[234:235], v32 offset:50944\n\t"
  "v_mfma_f32_16x16x32_bf16 a[188:191], a[244:247], v[148:151], a[188:191]\n\t"
  "s_waitcnt lgkmcnt(6)\n\t"
  "v_fma_f32 v6, v6, v48, v50\n\t"
  "v_fma_f32 v5, v5, v48, v50\n\t"
  "v_mfma_f32_16x16x32_bf16 a[192:195], a[248:251], v[144:147], a[192:195]\n\t"
  "ds_read_b64_tr_b16 a[236:237], v32 offset:49184\n\t"
  "ds_read_b64_tr_b16 a[238:239], v32 offset:50976\n\t"
  "v_mfma_f32_16x16x32_bf16 a[196:199], a[248:251], v[148:151], a[196:199]\n\t"
  "buffer_load_dwordx4 v11, s[36:39], s74 offen offset:3072 lds\n\t"
  "v_mfma_f32_16x16x32_bf16 a[200:203], a[252:255], v[144:147], a[200:203]\n\t"
  "s_add_u32 m0, s84, 0x19000\n\t"
  "ds_read_b64_tr_b16 a[240:241], v32 offset:49216\n\t"
  "v_cvt_pk_bf16_f32 v154, v6, v5\n\t"
  "v_mfma_f32_16x16x32_bf16 a[204:207], a[252:255], v[148:151], a[204:207]\n\t"
  "s_waitcnt lgkmcnt(5)\n\t"
  "ds_read_b64_tr_b16 a[242:243], v32 offset:51008\n\t"
  "v_lshlrev_b32 v2, 16, v155\n\t"
  "v_mfma_f32_16x16x32_bf16 a[208:211], a[224:227], v[144:147], a[208:211]\n\t"
  "ds_read_b64_tr_b16 a[244:245], v32 offset:49248\n\t"
  "ds_read_b64_tr_b16 a[246:247], v32 offset:51040\n\t"
  "v_mfma_f32_16x16x32_bf16 a[212:215], a[224:227], v[148:151], a[212:215]\n\t"
  "buffer_load_dwordx4 v12, s[36:39], s74 offen offset:0 lds\n\t"
  "v_mfma_f32_16x16x32_bf16 a[216:219], a[228:231], v[144:147], a[216:219]\n\t"
  "ds_read_b64_tr_b16 a[248:249], v32 offset:49280\n\t"
  "v_and_b32 v1, s83, v155\n\t"
  "v_mfma_f32_16x16x32_bf16 a[220:223], a[228:231], v[148:151], a[220:223]\n\t"
  "s_waitcnt lgkmcnt(5)\n\t"
  "ds_read_b64_tr_b16 a[250:251], v32 offset:51072\n\t"
  "v_fma_f32 v2, v2, v48, v50\n\t"
  "v_fma_f32 v1, v1, v48, v50\n\t"
  "v_cvt_pk_bf16_f32 v155, v2, v1\n\t"
  "s_nop 1\n\t"
  "v_mfma_f32_16x16x32_bf16 a[112:115], a[232:235], v[152:155], a[112:115]\n\t"
  "ds_read_b64_tr_b16 a[252:253], v32 offset:49312\n\t"
  "ds_read_b64_tr_b16 a[254:255], v32 offset:51104\n\t"
  "v_lshlrev_b32 v4, 16, v156\n\t"
  "v_and_b32 v3, s83, v156\n\t"
  "v_fma_f32 v4, v4, v49, v51\n\t"
  "v_fma_f32 v3, v3, v49, v51\n\t"
  "v_lshlrev_b32 v6, 16, v157\n\t"
  "v_cvt_pk_bf16_f32 v156, v4, v3\n\t"
  "v_and_b32 v5, s83, v157\n\t"
  "v_fma_f32 v6, v6, v49, v51\n\t"
  "v_fma_f32 v5, v5, v49, v51\n\t"
  "v_lshlrev_b32 v2, 16, v158\n\t"
  "v_cvt_pk_bf16_f32 v157, v6, v5\n\t"
  "v_and_b32 v1, s83, v158\n\t"
  "v_fma_f32 v2, v2, v49, v51\n\t"
  "v_fma_f32 v1, v1, v49, v51\n\t"
  "v_lshlrev_b32 v4, 16, v159\n\t"
  "v_cvt_pk_bf16_f32 v158, v2, v1\n\t"
  "v_and_b32 v3, s83, v159\n\t"
  "v_fma_f32 v4, v4, v49, v51\n\t"
  "v_fma_f32 v3, v3, v49, v51\n\t"
  "v_cvt_pk_bf16_f32 v159, v4, v3\n\t"
  "s_nop 1\n\t"
  "v_mfma_f32_16x16x32_bf16 a[116:119], a[232:235], v[156:159], a[116:119]\n\t"
  "v_lshlrev_b32 v6, 16, v160\n\t"
  "v_and_b32 v5, s83, v160\n\t"
  "v_mfma_f32_16x16x32_bf16 a[120:123], a[236:239], v[152:155], a[120:123]\n\t"
  "ds_read_b64_tr_b16 a[224:225], v32 offset:49344\n\t"
  "ds_read_b64_tr_b16 a[226:227], v32 offset:51136\n\t"
  "v_mfma_f32_16x16x32_bf16 a[124:127], a[236:239], v[156:159], a[124:127]\n\t"
  "s_waitcnt lgkmcnt(6)\n\t"
  "v_fma_f32 v6, v6, v48, v50\n\t"
  "v_fma_f32 v5, v5, v48, v50\n\t"
  "v_mfma_f32_16x16x32_bf16 a[128:131], a[240:243], v[152:155], a[128:131]\n\t"
  "ds_read_b64_tr_b16 a[228:229], v32 offset:57344\n\t"
  "ds_read_b64_tr_b16 a[230:231], v32 offset:59136\n\t"
  "v_mfma_f32_16x16x32_bf16 a[132:135], a[240:243], v[156:159], a[132:135]\n\t"
  "buffer_load_dwordx4 v13, s[36:39], s74 offen offset:1024 lds\n\t"
  "v_mfma_f32_16x16x32_bf16 a[136:139], a[244:247], v[152:155], a[136:139]\n\t"
  "ds_read_b64_tr_b16 a[232:233], v32 offset:57376\n\t"
  "v_cvt_pk_bf16_f32 v160, v6, v5\n\t"
  "v_mfma_f32_16x16x32_bf16 a[140:143], a[244:247], v[156:159], a[140:143]\n\t"
  "s_waitcnt lgkmcnt(5)\n\t"
  "ds_read_b64_tr_b16 a[234:235], v32 offset:59168\n\t"
  "v_lshlrev_b32 v2, 16, v161\n\t"
  "v_mfma_f32_16x16x32_bf16 a[144:147], a[248:251], v[152:155], a[144:147]\n\t"
  "ds_read_b64_tr_b16 a[236:237], v32 offset:57408\n\t"
  "ds_read_b64_tr_b16 a[238:239], v32 offset:59200\n\t"
  "v_mfma_f32_16x16x32_bf16 a[148:151], a[248:251], v[156:159], a[148:151]\n\t"
  "buffer_load_dwordx4 v14, s[36:39], s74 offen offset:2048 lds\n\t"
  "v_mfma_f32_16x16x32_bf16 a[152:155], a[252:255], v[152:155], a[152:155]\n\t"
  "s_add_u32 s74, s74, 0x38000\n\t"
  "ds_read_b64_tr_b16 a[240:241], v32 offset:57440\n\t"
  "v_and_b32 v1, s83, v161\n\t"
  "v_mfma_f32_16x16x32_bf16 a[156:159], a[252:255], v[156:159], a[156:159]\n\t"
  "s_waitcnt lgkmcnt(5)\n\t"
  "ds_read_b64_tr_b16 a[242:243], v32 offset:59232\n\t"
  "v_fma_f32 v2, v2, v48, v50\n\t"
  "v_mfma_f32_16x16x32_bf16 a[160:163], a[224:227], v[152:155], a[160:163]\n\t"
  "ds_read_b64_tr_b16 a[244:245], v32 offset:57472\n\t"
  "ds_read_b64_tr_b16 a[246:247], v32 offset:59264\n\t"
  "v_mfma_f32_16x16x32_bf16 a[164:167], a[224:227], v[156:159], a[164:167]\n\t"
  "v_fma_f32 v1, v1, v48, v50\n\t"
  "v_lshlrev_b32 v4, 16, v162\n\t"
  "v_mfma_f32_16x16x32_bf16 a[168:171], a[228:231], v[152:155], a[168:171]\n\t"
  "ds_read_b64_tr_b16 a[248:249], v32 offset:57504\n\t"
  "ds_read_b64_tr_b16 a[250:251], v32 offset:59296\n\t"
  "v_mfma_f32_16x16x32_bf16 a[172:175], a[228:231], v[156:159], a[172:175]\n\t"
  "s_waitcnt lgkmcnt(6)\n\t"
  "v_cvt_pk_bf16_f32 v161, v2, v1\n\t"
  "v_and_b32 v3, s83, v162\n\t"
  "v_mfma_f32_16x16x32_bf16 a[176:179], a[232:235], v[152:155], a[176:179]\n\t"
  "s_waitcnt vmcnt(12)\n\t"
  "ds_read_b64_tr_b16 a[252:253], v32 offset:57536\n\t"
  "ds_read_b64_tr_b16 a[254:255], v32 offset:59328\n\t"
  "v_mfma_f32_16x16x32_bf16 a[180:183], a[232:235], v[156:159], a[180:183]\n\t"
  "s_barrier\n\t"
  "global_load_dwordx4 v[16:19], v40, s[96:97] offset:-4096\n\t"
  "v_mfma_f32_16x16x32_bf16 a[184:187], a[236:239], v[152:155], a[184:187]\n\t"
  "ds_read_b64_tr_b16 a[224:225], v33 offset:0\n\t"
  "v_fma_f32 v4, v4, v48, v50\n\t"
  "v_mfma_f32_16x16x32_bf16 a[188:191], a[236:239], v[156:159], a[188:191]\n\t"
  "s_waitcnt lgkmcnt(5)\n\t"
  "ds_read_b64_tr_b16 a[226:227], v33 offset:1792\n\t"
  "v_fma_f32 v3, v3, v48, v50\n\t"
  "v_mfma_f32_16x16x32_bf16 a[192:195], a[240:243], v[152:155], a[192:195]\n\t"
  "ds_read_b64_tr_b16 a[228:229], v33 offset:32\n\t"
  "ds_read_b64_tr_b16 a[230:231], v33 offset:1824\n\t"
  "v_mfma_f32_16x16x32_bf16 a[196:199], a[240:243], v[156:159], a[196:199]\n\t"
  "global_load_dwordx4 v[36:39], v40, s[96:97] offset:-1024\n\t"
  "v_mfma_f32_16x16x32_bf16 a[200:203], a[244:247], v[152:155], a[200:203]\n\t"
  "ds_read_b64_tr_b16 a[232:233], v33 offset:64\n\t"
  "v_cvt_pk_bf16_f32 v162, v4, v3\n\t"
  "v_mfma_f32_16x16x32_bf16 a[204:207], a[244:247], v[156:159], a[204:207]\n\t"
  "s_waitcnt lgkmcnt(5)\n\t"
  "ds_read_b64_tr_b16 a[234:235], v33 offset:1856\n\t"
  "v_lshlrev_b32 v6, 16, v163\n\t"
  "v_mfma_f32_16x16x32_bf16 a[208:211], a[248:251], v[152:155], a[208:211]\n\t"
  "ds_read_b64_tr_b16 a[236:237], v33 offset:96\n\t"
  "ds_read_b64_tr_b16 a[238:239], v33 offset:1888\n\t"
  "v_mfma_f32_16x16x32_bf16 a[212:215], a[248:251], v[156:159], a[212:215]\n\t"
  "v_and_b32 v5, s83, v163\n\t"
  "v_fma_f32 v6, v6, v48, v50\n\t"
  "v_mfma_f32_16x16x32_bf16 a[216:219], a[252:255], v[152:155], a[216:219]\n\t"
  "ds_read_b64_tr_b16 a[240:241], v33 offset:128\n\t"
  "ds_read_b64_tr_b16 a[242:243], v33 offset:1920\n\t"
  "v_mfma_f32_16x16x32_bf16 a[220:223], a[252:255], v[156:159], a[220:223]\n\t"
  "s_waitcnt lgkmcnt(6)\n\t"
  "v_fma_f32 v5, v5, v48, v50\n\t"
  "v_lshlrev_b32 v2, 16, v164\n\t"
  "v_cvt_pk_bf16_f32 v163, v6, v5\n\t"
  "s_nop 1\n\t"
  "v_mfma_f32_16x16x32_bf16 a[112:115], a[224:227], v[160:163], a[112:115]\n\t"
  "ds_read_b64_tr_b16 a[244:245], v33 offset:160\n\t"
  "ds_read_b64_tr_b16 a[246:247], v33 offset:1952\n\t"
  "v_and_b32 v1, s83, v164\n\t"
  "v_fma_f32 v2, v2, v49, v51\n\t"
  "v_fma_f32 v1, v1, v49, v51\n\t"
  "v_lshlrev_b32 v4, 16, v165\n\t"
  "v_cvt_pk_bf16_f32 v164, v2, v1\n\t"
  "v_and_b32 v3, s83, v165\n\t"
  "v_fma_f32 v4, v4, v49, v51\n\t"
  "v_fma_f32 v3, v3, v49, v51\n\t"
  "v_lshlrev_b32 v6, 16, v166\n\t"
  "v_cvt_pk_bf16_f32 v165, v4, v3\n\t"
  "v_and_b32 v5, s83, v166\n\t"
  "v_fma_f32 v6, v6, v49, v51\n\t"
  "v_fma_f32 v5, v5, v49, v51\n\t"
  "v_lshlrev_b32 v2, 16, v167\n\t"
  "v_cvt_pk_bf16_f32 v166, v6, v5\n\t"
  "v_and_b32 v1, s83, v167\n\t"
  "v_fma_f32 v2, v2, v49, v51\n\t"
  "v_fma_f32 v1, v1, v49, v51\n\t"
  "v_cvt_pk_bf16_f32 v167, v2, v1\n\t"
  "s_nop 1\n\t"
  "v_mfma_f32_16x16x32_bf16 a[116:119], a[224:227], v[164:167], a[116:119]\n\t"
  "v_lshlrev_b32 v4, 16, v168\n\t"
  "v_and_b32 v3, s83, v168\n\t"
  "v_mfma_f32_16x16x32_bf16 a[120:123], a[228:231], v[160:163], a[120:123]\n\t"
  "s_add_u32 m0, s84, 0\n\t"
  "ds_read_b64_tr_b16 a[248:249], v33 offset:192\n\t"
  "ds_read_b64_tr_b16 a[250:251], v33 offset:1984\n\t"
  "v_mfma_f32_16x16x32_bf16 a[124:127], a[228:231], v[164:167], a[124:127]\n\t"
  "s_waitcnt lgkmcnt(6)\n\t"
  "v_fma_f32 v4, v4, v48, v50\n\t"
  "v_fma_f32 v3, v3, v48, v50\n\t"
  "v_mfma_f32_16x16x32_bf16 a[128:131], a[232:235], v[160:163], a[128:131]\n\t"
  "ds_read_b64_tr_b16 a[252:253], v33 offset:8192\n\t"
  "ds_read_b64_tr_b16 a[254:255], v33 offset:9984\n\t"
  "v_mfma_f32_16x16x32_bf16 a[132:135], a[232:235], v[164:167], a[132:135]\n\t"
  "buffer_load_dwordx4 v8, s[36:39], s74 offen offset:0 lds\n\t"
  "v_mfma_f32_16x16x32_bf16 a[136:139], a[236:239], v[160:163], a[136:139]\n\t"
  "ds_read_b64_tr_b16 a[224:225], v33 offset:8224\n\t"
  "v_cvt_pk_bf16_f32 v168, v4, v3\n\t"
  "v_mfma_f32_16x16x32_bf16 a[140:143], a[236:239], v[164:167], a[140:143]\n\t"
  "s_waitcnt lgkmcnt(5)\n\t"
  "ds_read_b64_tr_b16 a[226:227], v33 offset:10016\n\t"
  "v_lshlrev_b32 v6, 16, v169\n\t"
  "v_mfma_f32_16x16x32_bf16 a[144:147], a[240:243], v[160:163], a[144:147]\n\t"
  "ds_read_b64_tr_b16 a[228:229], v33 offset:8256\n\t"
  "ds_read_b64_tr_b16 a[230:231], v33 offset:10048\n\t"
  "v_mfma_f32_16x16x32_bf16 a[148:151], a[240:243], v[164:167], a[148:151]\n\t"
  "buffer_load_dwordx4 v9, s[36:39], s74 offen offset:1024 lds\n\t"
  "v_mfma_f32_16x16x32_bf16 a[152:155], a[244:247], v[160:163], a[152:155]\n\t"
  "ds_read_b64_tr_b16 a[232:233], v33 offset:8288\n\t"
  "v_and_b32 v5, s83, v169\n\t"
  "v_mfma_f32_16x16x32_bf16 a[156:159], a[244:247], v[164:167], a[156:159]\n\t"
  "s_waitcnt lgkmcnt(5)\n\t"
  "ds_read_b64_tr_b16 a[234:235], v33 offset:10080\n\t"
  "v_fma_f32 v6, v6, v48, v50\n\t"
  "v_mfma_f32_16x16x32_bf16 a[160:163], a[248:251], v[160:163], a[160:163]\n\t"
  "ds_read_b64_tr_b16 a[236:237], v33 offset:8320\n\t"
  "ds_read_b64_tr_b16 a[238:239], v33 offset:10112\n\t"
  "v_mfma_f32_16x16x32_bf16 a[164:167], a[248:251], v[164:167], a[164:167]\n\t"
  "buffer_load_dwordx4 v10, s[36:39], s74 offen offset:2048 lds\n\t"
  "v_mfma_f32_16x16x32_bf16 a[168:171], a[252:255], v[160:163], a[168:171]\n\t"
  "ds_read_b64_tr_b16 a[240:241], v33 offset:8352\n\t"
  "v_fma_f32 v5, v5, v48, v50\n\t"
  "v_mfma_f32_16x16x32_bf16 a[172:175], a[252:255], v[164:167], a[172:175]\n\t"
  "s_waitcnt lgkmcnt(5)\n\t"
  "ds_read_b64_tr_b16 a[242:243], v33 offset:10144\n\t"
  "v_cvt_pk_bf16_f32 v169, v6, v5\n\t"
  "v_mfma_f32_16x16x32_bf16 a[176:179], a[224:227], v[160:163], a[176:179]\n\t"
  "ds_read_b64_tr_b16 a[244:245], v33 offset:8384\n\t"
  "ds_read_b64_tr_b16 a[246:247], v33 offset:10176\n\t"
  "v_mfma_f32_16x16x32_bf16 a[180:183], a[224:227], v[164:167], a[180:183]\n\t"
  "buffer_load_dwordx4 v11, s[36:39], s74 offen offset:3072 lds\n\t"
  "v_mfma_f32_16x16x32_bf16 a[184:187], a[228:231], v[160:163], a[184:187]\n\t"
  "s_add_u32 m0, s84, 0x1000\n\t"
  "ds_read_b64_tr_b16 a[248:249], v33 offset:16384\n\t"
  "v_lshlrev_b32 v2, 16, v170\n\t"
  "v_mfma_f32_16x16x32_bf16 a[188:191], a[228:231], v[164:167], a[188:191]\n\t"
  "s_waitcnt lgkmcnt(5)\n\t"
  "ds_read_b64_tr_b16 a[250:251], v33 offset:18176\n\t"
  "v_and_b32 v1, s83, v170\n\t"
  "v_mfma_f32_16x16x32_bf16 a[192:195], a[232:235], v[160:163], a[192:195]\n\t"
  "ds_read_b64_tr_b16 a[252:253], v33 offset:16416\n\t"
  "ds_read_b64_tr_b16 a[254:255], v33 offset:18208\n\t"
  "v_mfma_f32_16x16x32_bf16 a[196:199], a[232:235], v[164:167], a[196:199]\n\t"
  "buffer_load_dwordx4 v12, s[36:39], s74 offen offset:0 lds\n\t"
  "v_mfma_f32_16x16x32_bf16 a[200:203], a[236:239], v[160:163], a[200:203]\n\t"
  "ds_read_b64_tr_b16 a[224:225], v33 offset:16448\n\t"
  "v_fma_f32 v2, v2, v48, v50\n\t"
  "v_mfma_f32_16x16x32_bf16 a[204:207], a[236:239], v[164:167], a[204:207]\n\t"
  "s_waitcnt lgkmcnt(5)\n\t"
  "ds_read_b64_tr_b16 a[226:227], v33 offset:18240\n\t"
  "v_fma_f32 v1, v1, v48, v50\n\t"
  "v_mfma_f32_16x16x32_bf16 a[208:211], a[240:243], v[160:163], a[208:211]\n\t"
  "ds_read_b64_tr_b16 a[228:229], v33 offset:16480\n\t"
  "ds_read_b64_tr_b16 a[230:231], v33 offset:18272\n\t"
  "v_mfma_f32_16x16x32_bf16 a[212:215], a[240:243], v[164:167], a[212:215]\n\t"
  "buffer_load_dwordx4 v13, s[36:39], s74 offen offset:1024 lds\n\t"
  "v_mfma_f32_16x16x32_bf16 a[216:219], a[244:247], v[160:163], a[216:219]\n\t"
  "ds_read_b64_tr_b16 a[232:233], v33 offset:16512\n\t"
  "v_cvt_pk_bf16_f32 v170, v2, v1\n\t"
  "v_mfma_f32_16x16x32_bf16 a[220:223], a[244:247], v[164:167], a[220:223]\n\t"
  "s_waitcnt lgkmcnt(5)\n\t"
  "ds_read_b64_tr_b16 a[234:235], v33 offset:18304\n\t"
  "v_lshlrev_b32 v4, 16, v171\n\t"
  "v_and_b32 v3, s83, v171\n\t"
  "v_fma_f32 v4, v4, v48, v50\n\t"
  "v_fma_f32 v3, v3, v48, v50\n\t"
  "v_cvt_pk_bf16_f32 v171, v4, v3\n\t"
  "s_nop 1\n\t"
  "v_mfma_f32_16x16x32_bf16 a[112:115], a[248:251], v[168:171], a[112:115]\n\t"
  "ds_read_b64_tr_b16 a[236:237], v33 offset:16544\n\t"
  "ds_read_b64_tr_b16 a[238:239], v33 offset:18336\n\t"
  "v_lshlrev_b32 v6, 16, v172\n\t"
  "v_and_b32 v5, s83, v172\n\t"
  "v_fma_f32 v6, v6, v49, v51\n\t"
  "v_fma_f32 v5, v5, v49, v51\n\t"
  "v_lshlrev_b32 v2, 16, v173\n\t"
  "v_cvt_pk_bf16_f32 v172, v6, v5\n\t"
  "v_and_b32 v1, s83, v173\n\t"
  "v_fma_f32 v2, v2, v49, v51\n\t"
  "v_fma_f32 v1, v1, v49, v51\n\t"
  "v_lshlrev_b32 v4, 16, v174\n\t"
  "v_cvt_pk_bf16_f32 v173, v2, v1\n\t"
  "v_and_b32 v3, s83, v174\n\t"
  "v_fma_f32 v4, v4, v49, v51\n\t"
  "v_fma_f32 v3, v3, v49, v51\n\t"
  "v_lshlrev_b32 v6, 16, v175\n\t"
  "s_cbranch_execz TRAMP_%=\n\t"
  "v_cvt_pk_bf16_f32 v174, v4, v3\n\t"
  "v_and_b32 v5, s83, v175\n\t"
  "v_fma_f32 v6, v6, v49, v51\n\t"
  "v_fma_f32 v5, v5, v49, v51\n\t"
  "v_cvt_pk_bf16_f32 v175, v6, v5\n\t"
  "s_nop 1\n\t"
  "v_mfma_f32_16x16x32_bf16 a[116:119], a[248:251], v[172:175], a[116:119]\n\t"
  "buffer_load_dwordx4 v14, s[36:39], s74 offen offset:2048 lds\n\t"
  "v_mfma_f32_16x16x32_bf16 a[120:123], a[252:255], v[168:171], a[120:123]\n\t"
  "s_add_u32 s74, s74, 0x38000\n\t"
  "ds_read_b64_tr_b16 a[240:241], v33 offset:16576\n\t"
  "v_lshlrev_b32 v2, 16, v176\n\t"
  "v_mfma_f32_16x16x32_bf16 a[124:127], a[252:255], v[172:175], a[124:127]\n\t"
  "s_waitcnt lgkmcnt(5)\n\t"
  "ds_read_b64_tr_b16 a[242:243], v33 offset:18368\n\t"
  "v_and_b32 v1, s83, v176\n\t"
  "v_mfma_f32_16x16x32_bf16 a[128:131], a[224:227], v[168:171], a[128:131]\n\t"
  "ds_read_b64_tr_b16 a[244:245], v33 offset:24576\n\t"
  "ds_read_b64_tr_b16 a[246:247], v33 offset:26368\n\t"
  "v_mfma_f32_16x16x32_bf16 a[132:135], a[224:227], v[172:175], a[132:135]\n\t"
  "v_fma_f32 v2, v2, v48, v50\n\t"
  "v_fma_f32 v1, v1, v48, v50\n\t"
  "v_mfma_f32_16x16x32_bf16 a[136:139], a[228:231], v[168:171], a[136:139]\n\t"
  "ds_read_b64_tr_b16 a[248:249], v33 offset:24608\n\t"
  "ds_read_b64_tr_b16 a[250:251], v33 offset:26400\n\t"
  "v_mfma_f32_16x16x32_bf16 a[140:143], a[228:231], v[172:175], a[140:143]\n\t"
  "s_waitcnt lgkmcnt(6)\n\t"
  "v_cvt_pk_bf16_f32 v176, v2, v1\n\t"
  "v_lshlrev_b32 v4, 16, v177\n\t"
  "v_mfma_f32_16x16x32_bf16 a[144:147], a[232:235], v[168:171], a[144:147]\n\t"
  "ds_read_b64_tr_b16 a[252:253], v33 offset:24640\n\t"
  "ds_read_b64_tr_b16 a[254:255], v33 offset:26432\n\t"
  "v_mfma_f32_16x16x32_bf16 a[148:151], a[232:235], v[172:175], a[148:151]\n\t"
  "buffer_load_dword v47, v31, s[68:71], s8 offen offset:0\n\t"
  "v_mfma_f32_16x16x32_bf16 a[152:155], a[236:239], v[168:171], a[152:155]\n\t"
  "s_add_u32 s8, s8, s9\n\t"
  "ds_read_b64_tr_b16 a[224:225], v33 offset:24672\n\t"
  "v_and_b32 v3, s83, v177\n\t"
  "v_mfma_f32_16x16x32_bf16 a[156:159], a[236:239], v[172:175], a[156:159]\n\t"
  "s_waitcnt lgkmcnt(5)\n\t"
  "ds_read_b64_tr_b16 a[226:227], v33 offset:26464\n\t"
  "v_fma_f32 v4, v4, v48, v50\n\t"
  "v_mfma_f32_16x16x32_bf16 a[160:163], a[240:243], v[168:171], a[160:163]\n\t"
  "ds_read_b64_tr_b16 a[228:229], v33 offset:24704\n\t"
  "ds_read_b64_tr_b16 a[230:231], v33 offset:26496\n\t"
  "v_mfma_f32_16x16x32_bf16 a[164:167], a[240:243], v[172:175], a[164:167]\n\t"
  "buffer_load_dword v47, v31, s[68:71], s8 offen offset:0\n\t"
  "v_mfma_f32_16x16x32_bf16 a[168:171], a[244:247], v[168:171], a[168:171]\n\t"
  "s_add_u32 s8, s8, s9\n\t"
  "ds_read_b64_tr_b16 a[232:233], v33 offset:24736\n\t"
  "v_fma_f32 v3, v3, v48, v50\n\t"
  "v_mfma_f32_16x16x32_bf16 a[172:175], a[244:247], v[172:175], a[172:175]\n\t"
  "s_waitcnt lgkmcnt(5)\n\t"
  "ds_read_b64_tr_b16 a[234:235], v33 offset:26528\n\t"
  "v_cvt_pk_bf16_f32 v177, v4, v3\n\t"
  "v_mfma_f32_16x16x32_bf16 a[176:179], a[248:251], v[168:171], a[176:179]\n\t"
  "s_waitcnt vmcnt(11)\n\t"
  "ds_read_b64_tr_b16 a[236:237], v33 offset:24768\n\t"
  "ds_read_b64_tr_b16 a[238:239], v33 offset:26560\n\t"
  "v_mfma_f32_16x16x32_bf16 a[180:183], a[248:251], v[172:175], a[180:183]\n\t"
  "s_barrier\n\t"
  "buffer_load_dword v47, v29, s[48:51], s29 offen offset:0\n\t"
  "v_mfma_f32_16x16x32_bf16 a[184:187], a[252:255], v[168:171], a[184:187]\n\t"
  "s_add_u32 s29, s29, 0xe00\n\t"
  "ds_read_b64_tr_b16 a[240:241], v33 offset:32768\n\t"
  "v_lshlrev_b32 v6, 16, v178\n\t"
  "v_mfma_f32_16x16x32_bf16 a[188:191], a[252:255], v[172:175], a[188:191]\n\t"
  "s_waitcnt lgkmcnt(5)\n\t"
  "ds_read_b64_tr_b16 a[242:243], v33 offset:34560\n\t"
  "v_and_b32 v5, s83, v178\n\t"
  "v_mfma_f32_16x16x32_bf16 a[192:195], a[224:227], v[168:171], a[192:195]\n\t"
  "ds_read_b64_tr_b16 a[244:245], v33 offset:32800\n\t"
  "ds_read_b64_tr_b16 a[246:247], v33 offset:34592\n\t"
  "v_mfma_f32_16x16x32_bf16 a[196:199], a[224:227], v[172:175], a[196:199]\n\t"
  "buffer_load_dword v47, v29, s[48:51], s29 offen offset:0\n\t"
  "v_mfma_f32_16x16x32_bf16 a[200:203], a[228:231], v[168:171], a[200:203]\n\t"
  "s_add_u32 s29, s29, 0xe00\n\t"
  "ds_read_b64_tr_b16 a[248:249], v33 offset:32832\n\t"
  "v_fma_f32 v6, v6, v48, v50\n\t"
  "v_mfma_f32_16x16x32_bf16 a[204:207], a[228:231], v[172:175], a[204:207]\n\t"
  "s_waitcnt lgkmcnt(5)\n\t"
  "ds_read_b64_tr_b16 a[250:251], v33 offset:34624\n\t"
  "v_fma_f32 v5, v5, v48, v50\n\t"
  "v_mfma_f32_16x16x32_bf16 a[208:211], a[232:235], v[168:171], a[208:211]\n\t"
  "s_lshl_b32 s32, s11, 11\n\t"
  "ds_read_b64_tr_b16 a[252:253], v33 offset:32864\n\t"
  "ds_read_b64_tr_b16 a[254:255], v33 offset:34656\n\t"
  "s_add_u32 m0, s32, 0x20000\n\t"
  "v_mfma_f32_16x16x32_bf16 a[212:215], a[232:235], v[172:175], a[212:215]\n\t"
  "buffer_load_dwordx4 v20, s[0:3], 0 offen offset:0 lds\n\t"
  "v_mfma_f32_16x16x32_bf16 a[216:219], a[236:239], v[168:171], a[216:219]\n\t"
  "ds_read_b64_tr_b16 a[224:225], v33 offset:32896\n\t"
  "v_cvt_pk_bf16_f32 v178, v6, v5\n\t"
  "v_mfma_f32_16x16x32_bf16 a[220:223], a[236:239], v[172:175], a[220:223]\n\t"
  "s_waitcnt lgkmcnt(5)\n\t"
  "ds_read_b64_tr_b16 a[226:227], v33 offset:34688\n\t"
  "v_lshlrev_b32 v2, 16, v179\n\t"
  "v_and_b32 v1, s83, v179\n\t"
  "v_fma_f32 v2, v2, v48, v50\n\t"
  "v_fma_f32 v1, v1, v48, v50\n\t"
  "v_cvt_pk_bf16_f32 v179, v2, v1\n\t"
  "s_nop 1\n\t"
  "v_mfma_f32_16x16x32_bf16 a[112:115], a[240:243], v[176:179], a[112:115]\n\t"
  "ds_read_b64_tr_b16 a[228:229], v33 offset:32928\n\t"
  "ds_read_b64_tr_b16 a[230:231], v33 offset:34720\n\t"
  "v_lshlrev_b32 v4, 16, v180\n\t"
  "v_and_b32 v3, s83, v180\n\t"
  "v_fma_f32 v4, v4, v49, v51\n\t"
  "v_fma_f32 v3, v3, v49, v51\n\t"
  "v_lshlrev_b32 v6, 16, v181\n\t"
  "v_cvt_pk_bf16_f32 v180, v4, v3\n\t"
  "v_and_b32 v5, s83, v181\n\t"
  "v_fma_f32 v6, v6, v49, v51\n\t"
  "v_fma_f32 v5, v5, v49, v51\n\t"
  "v_lshlrev_b32 v2, 16, v182\n\t"
  "v_cvt_pk_bf16_f32 v181, v6, v5\n\t"
  "v_and_b32 v1, s83, v182\n\t"
  "v_fma_f32 v2, v2, v49, v51\n\t"
  "v_fma_f32 v1, v1, v49, v51\n\t"
  "v_lshlrev_b32 v4, 16, v183\n\t"
  "v_cvt_pk_bf16_f32 v182, v2, v1\n\t"
  "v_and_b32 v3, s83, v183\n\t"
  "v_fma_f32 v4, v4, v49, v51\n\t"
  "v_fma_f32 v3, v3, v49, v51\n\t"
  "v_cvt_pk_bf16_f32 v183, v4, v3\n\t"
  "s_nop 1\n\t"
  "v_mfma_f32_16x16x32_bf16 a[116:119], a[240:243], v[180:183], a[116:119]\n\t"
  "v_lshlrev_b32 v6, 16, v184\n\t"
  "v_and_b32 v5, s83, v184\n\t"
  "v_mfma_f32_16x16x32_bf16 a[120:123], a[244:247], v[176:179], a[120:123]\n\t"
  "s_add_u32 m0, s84, 0x8000\n\t"
  "ds_read_b64_tr_b16 a[232:233], v33 offset:32960\n\t"
  "ds_read_b64_tr_b16 a[234:235], v33 offset:34752\n\t"
  "v_mfma_f32_16x16x32_bf16 a[124:127], a[244:247], v[180:183], a[124:127]\n\t"
  "s_waitcnt lgkmcnt(6)\n\t"
  "v_fma_f32 v6, v6, v48, v50\n\t"
  "v_fma_f32 v5, v5, v48, v50\n\t"
  "v_mfma_f32_16x16x32_bf16 a[128:131], a[248:251], v[176:179], a[128:131]\n\t"
  "ds_read_b64_tr_b16 a[236:237], v33 offset:40960\n\t"
  "ds_read_b64_tr_b16 a[238:239], v33 offset:42752\n\t"
  "v_mfma_f32_16x16x32_bf16 a[132:135], a[248:251], v[180:183], a[132:135]\n\t"
  "buffer_load_dwordx4 v8, s[36:39], s74 offen offset:0 lds\n\t"
  "v_mfma_f32_16x16x32_bf16 a[136:139], a[252:255], v[176:179], a[136:139]\n\t"
  "ds_read_b64_tr_b16 a[240:241], v33 offset:40992\n\t"
  "v_cvt_pk_bf16_f32 v184, v6, v5\n\t"
  "v_mfma_f32_16x16x32_bf16 a[140:143], a[252:255], v[180:183], a[140:143]\n\t"
  "s_waitcnt lgkmcnt(5)\n\t"
  "ds_read_b64_tr_b16 a[242:243], v33 offset:42784\n\t"
  "v_lshlrev_b32 v2, 16, v185\n\t"
  "v_mfma_f32_16x16x32_bf16 a[144:147], a[224:227], v[176:179], a[144:147]\n\t"
  "ds_read_b64_tr_b16 a[244:245], v33 offset:41024\n\t"
  "ds_read_b64_tr_b16 a[246:247], v33 offset:42816\n\t"
  "v_mfma_f32_16x16x32_bf16 a[148:151], a[224:227], v[180:183], a[148:151]\n\t"
  "buffer_load_dwordx4 v9, s[36:39], s74 offen offset:1024 lds\n\t"
  "v_mfma_f32_16x16x32_bf16 a[152:155], a[228:231], v[176:179], a[152:155]\n\t"
  "ds_read_b64_tr_b16 a[248:249], v33 offset:41056\n\t"
  "v_and_b32 v1, s83, v185\n\t"
  "v_mfma_f32_16x16x32_bf16 a[156:159], a[228:231], v[180:183], a[156:159]\n\t"
  "s_waitcnt lgkmcnt(5)\n\t"
  "ds_read_b64_tr_b16 a[250:251], v33 offset:42848\n\t"
  "v_fma_f32 v2, v2, v48, v50\n\t"
  "v_mfma_f32_16x16x32_bf16 a[160:163], a[232:235], v[176:179], a[160:163]\n\t"
  "ds_read_b64_tr_b16 a[252:253], v33 offset:41088\n\t"
  "ds_read_b64_tr_b16 a[254:255], v33 offset:42880\n\t"
  "v_mfma_f32_16x16x32_bf16 a[164:167], a[232:235], v[180:183], a[164:167]\n\t"
  "buffer_load_dwordx4 v10, s[36:39], s74 offen offset:2048 lds\n\t"
  "v_mfma_f32_16x16x32_bf16 a[168:171], a[236:239], v[176:179], a[168:171]\n\t"
  "ds_read_b64_tr_b16 a[224:225], v33 offset:41120\n\t"
  "v_fma_f32 v1, v1, v48, v50\n\t"
  "v_mfma_f32_16x16x32_bf16 a[172:175], a[236:239], v[180:183], a[172:175]\n\t"
  "s_waitcnt lgkmcnt(5)\n\t"
  "ds_read_b64_tr_b16 a[226:227], v33 offset:42912\n\t"
  "v_cvt_pk_bf16_f32 v185, v2, v1\n\t"
  "v_mfma_f32_16x16x32_bf16 a[176:179], a[240:243], v[176:179], a[176:179]\n\t"
  "ds_read_b64_tr_b16 a[228:229], v33 offset:41152\n\t"
  "ds_read_b64_tr_b16 a[230:231], v33 offset:42944\n\t"
  "v_mfma_f32_16x16x32_bf16 a[180:183], a[240:243], v[180:183], a[180:183]\n\t"
  "v_lshlrev_b32 v4, 16, v186\n\t"
  "v_and_b32 v3, s83, v186\n\t"
  "v_mfma_f32_16x16x32_bf16 a[184:187], a[244:247], v[176:179], a[184:187]\n\t"
  "ds_read_b64_tr_b16 a[232:233], v33 offset:49152\n\t"
  "ds_read_b64_tr_b16 a[234:235], v33 offset:50944\n\t"
  "v_mfma_f32_16x16x32_bf16 a[188:191], a[244:247], v[180:183], a[188:191]\n\t"
  "s_waitcnt lgkmcnt(6)\n\t"
  "v_fma_f32 v4, v4, v48, v50\n\t"
  "v_fma_f32 v3, v3, v48, v50\n\t"
  "v_mfma_f32_16x16x32_bf16 a[192:195], a[248:251], v[176:179], a[192:195]\n\t"
  "ds_read_b64_tr_b16 a[236:237], v33 offset:49184\n\t"
  "ds_read_b64_tr_b16 a[238:239], v33 offset:50976\n\t"
  "v_mfma_f32_16x16x32_bf16 a[196:199], a[248:251], v[180:183], a[196:199]\n\t"
  "buffer_load_dwordx4 v11, s[36:39], s74 offen offset:3072 lds\n\t"
  "v_mfma_f32_16x16x32_bf16 a[200:203], a[252:255], v[176:179], a[200:203]\n\t"
  "s_add_u32 m0, s84, 0x9000\n\t"
  "ds_read_b64_tr_b16 a[240:241], v33 offset:49216\n\t"
  "v_cvt_pk_bf16_f32 v186, v4, v3\n\t"
  "v_mfma_f32_16x16x32_bf16 a[204:207], a[252:255], v[180:183], a[204:207]\n\t"
  "s_waitcnt lgkmcnt(5)\n\t"
  "ds_read_b64_tr_b16 a[242:243], v33 offset:51008\n\t"
  "v_lshlrev_b32 v6, 16, v187\n\t"
  "v_mfma_f32_16x16x32_bf16 a[208:211], a[224:227], v[176:179], a[208:211]\n\t"
  "ds_read_b64_tr_b16 a[244:245], v33 offset:49248\n\t"
  "ds_read_b64_tr_b16 a[246:247], v33 offset:51040\n\t"
  "v_mfma_f32_16x16x32_bf16 a[212:215], a[224:227], v[180:183], a[212:215]\n\t"
  "buffer_load_dwordx4 v12, s[36:39], s74 offen offset:0 lds\n\t"
  "v_mfma_f32_16x16x32_bf16 a[216:219], a[228:231], v[176:179], a[216:219]\n\t"
  "ds_read_b64_tr_b16 a[248:249], v33 offset:49280\n\t"
  "v_and_b32 v5, s83, v187\n\t"
  "v_mfma_f32_16x16x32_bf16 a[220:223], a[228:231], v[180:183], a[220:223]\n\t"
  "s_waitcnt lgkmcnt(5)\n\t"
  "ds_read_b64_tr_b16 a[250:251], v33 offset:51072\n\t"
  "v_fma_f32 v6, v6, v48, v50\n\t"
  "v_fma_f32 v5, v5, v48, v50\n\t"
  "v_cvt_pk_bf16_f32 v187, v6, v5\n\t"
  "s_nop 1\n\t"
  "v_mfma_f32_16x16x32_bf16 a[112:115], a[232:235], v[184:187], a[112:115]\n\t"
  "ds_read_b64_tr_b16 a[252:253], v33 offset:49312\n\t"
  "ds_read_b64_tr_b16 a[254:255], v33 offset:51104\n\t"
  "v_lshlrev_b32 v2, 16, v188\n\t"
  "v_and_b32 v1, s83, v188\n\t"
  "v_fma_f32 v2, v2, v49, v51\n\t"
  "v_fma_f32 v1, v1, v49, v51\n\t"
  "v_lshlrev_b32 v4, 16, v189\n\t"
  "v_cvt_pk_bf16_f32 v188, v2, v1\n\t"
  "v_and_b32 v3, s83, v189\n\t"
  "v_fma_f32 v4, v4, v49, v51\n\t"
  "v_fma_f32 v3, v3, v49, v51\n\t"
  "v_lshlrev_b32 v6, 16, v190\n\t"
  "v_cvt_pk_bf16_f32 v189, v4, v3\n\t"
  "v_and_b32 v5, s83, v190\n\t"
  "v_fma_f32 v6, v6, v49, v51\n\t"
  "v_fma_f32 v5, v5, v49, v51\n\t"
  "v_lshlrev_b32 v2, 16, v191\n\t"
  "v_cvt_pk_bf16_f32 v190, v6, v5\n\t"
  "v_and_b32 v1, s83, v191\n\t"
  "v_fma_f32 v2, v2, v49, v51\n\t"
  "v_fma_f32 v1, v1, v49, v51\n\t"
  "v_cvt_pk_bf16_f32 v191, v2, v1\n\t"
  "s_nop 1\n\t"
  "v_mfma_f32_16x16x32_bf16 a[116:119], a[232:235], v[188:191], a[116:119]\n\t"
  "v_lshlrev_b32 v4, 16, v192\n\t"
  "v_and_b32 v3, s83, v192\n\t"
  "v_mfma_f32_16x16x32_bf16 a[120:123], a[236:239], v[184:187], a[120:123]\n\t"
  "ds_read_b64_tr_b16 a[224:225], v33 offset:49344\n\t"
  "ds_read_b64_tr_b16 a[226:227], v33 offset:51136\n\t"
  "v_mfma_f32_16x16x32_bf16 a[124:127], a[236:239], v[188:191], a[124:127]\n\t"
  "s_waitcnt lgkmcnt(6)\n\t"
  "v_fma_f32 v4, v4, v48, v50\n\t"
  "v_fma_f32 v3, v3, v48, v50\n\t"
  "v_mfma_f32_16x16x32_bf16 a[128:131], a[240:243], v[184:187], a[128:131]\n\t"
  "ds_read_b64_tr_b16 a[228:229], v33 offset:57344\n\t"
  "ds_read_b64_tr_b16 a[230:231], v33 offset:59136\n\t"
  "v_mfma_f32_16x16x32_bf16 a[132:135], a[240:243], v[188:191], a[132:135]\n\t"
  "buffer_load_dwordx4 v13, s[36:39], s74 offen offset:1024 lds\n\t"
  "v_mfma_f32_16x16x32_bf16 a[136:139], a[244:247], v[184:187], a[136:139]\n\t"
  "ds_read_b64_tr_b16 a[232:233], v33 offset:57376\n\t"
  "v_cvt_pk_bf16_f32 v192, v4, v3\n\t"
  "v_mfma_f32_16x16x32_bf16 a[140:143], a[244:247], v[188:191], a[140:143]\n\t"
  "s_waitcnt lgkmcnt(5)\n\t"
  "ds_read_b64_tr_b16 a[234:235], v33 offset:59168\n\t"
  "v_lshlrev_b32 v6, 16, v193\n\t"
  "v_mfma_f32_16x16x32_bf16 a[144:147], a[248:251], v[184:187], a[144:147]\n\t"
  "ds_read_b64_tr_b16 a[236:237], v33 offset:57408\n\t"
  "ds_read_b64_tr_b16 a[238:239], v33 offset:59200\n\t"
  "v_mfma_f32_16x16x32_bf16 a[148:151], a[248:251], v[188:191], a[148:151]\n\t"
  "buffer_load_dwordx4 v14, s[36:39], s74 offen offset:2048 lds\n\t"
  "v_mfma_f32_16x16x32_bf16 a[152:155], a[252:255], v[184:187], a[152:155]\n\t"
  "s_add_u32 s74, s74, 0x38000\n\t"
  "ds_read_b64_tr_b16 a[240:241], v33 offset:57440\n\t"
  "v_and_b32 v5, s83, v193\n\t"
  "v_mfma_f32_16x16x32_bf16 a[156:159], a[252:255], v[188:191], a[156:159]\n\t"
  "s_waitcnt lgkmcnt(5)\n\t"
  "ds_read_b64_tr_b16 a[242:243], v33 offset:59232\n\t"
  "v_fma_f32 v6, v6, v48, v50\n\t"
  "v_mfma_f32_16x16x32_bf16 a[160:163], a[224:227], v[184:187], a[160:163]\n\t"
  "ds_read_b64_tr_b16 a[244:245], v33 offset:57472\n\t"
  "ds_read_b64_tr_b16 a[246:247], v33 offset:59264\n\t"
  "v_mfma_f32_16x16x32_bf16 a[164:167], a[224:227], v[188:191], a[164:167]\n\t"
  "v_fma_f32 v5, v5, v48, v50\n\t"
  "v_lshlrev_b32 v2, 16, v194\n\t"
  "v_mfma_f32_16x16x32_bf16 a[168:171], a[228:231], v[184:187], a[168:171]\n\t"
  "s_lshl_b32 s32, s11, 11\n\t"
  "ds_read_b64_tr_b16 a[248:249], v33 offset:57504\n\t"
  "ds_read_b64_tr_b16 a[250:251], v33 offset:59296\n\t"
  "v_mfma_f32_16x16x32_bf16 a[172:175], a[228:231], v[188:191], a[172:175]\n\t"
  "s_waitcnt lgkmcnt(6)\n\t"
  "v_cvt_pk_bf16_f32 v193, v6, v5\n\t"
  "v_and_b32 v1, s83, v194\n\t"
  "v_mfma_f32_16x16x32_bf16 a[176:179], a[232:235], v[184:187], a[176:179]\n\t"
  "s_add_u32 m0, s32, 0x20400\n\t"
  "ds_read_b64_tr_b16 a[252:253], v33 offset:57536\n\t"
  "ds_read_b64_tr_b16 a[254:255], v33 offset:59328\n\t"
  "v_mfma_f32_16x16x32_bf16 a[180:183], a[232:235], v[188:191], a[180:183]\n\t"
  "s_waitcnt vmcnt(12)\n\t"
  "buffer_load_dwordx4 v21, s[0:3], 0 offen offset:0 lds\n\t"
  "v_mfma_f32_16x16x32_bf16 a[184:187], a[236:239], v[184:187], a[184:187]\n\t"
  "s_lshl_b32 s32, s11, 11\n\t"
  "v_fma_f32 v2, v2, v48, v50\n\t"
  "v_fma_f32 v1, v1, v48, v50\n\t"
  "v_mfma_f32_16x16x32_bf16 a[188:191], a[236:239], v[188:191], a[188:191]\n\t"
  "s_waitcnt lgkmcnt(4)\n\t"
  "v_cvt_pk_bf16_f32 v194, v2, v1\n\t"
  "v_lshlrev_b32 v4, 16, v195\n\t"
  "v_mfma_f32_16x16x32_bf16 a[192:195], a[240:243], v[184:187], a[192:195]\n\t"
  "s_add_u32 m0, s32, 0x22000\n\t"
  "s_lshl_b32 s33, s80, 13\n\t"
  "s_lshl_b32 s32, s11, 7\n\t"
  "s_add_u32 s33, s33, s32\n\t"
  "s_add_u32 s33, s33, 0x1000\n\t"
  "v_mfma_f32_16x16x32_bf16 a[196:199], a[240:243], v[188:191], a[196:199]\n\t"
  "s_barrier\n\t"
  "buffer_load_dwordx4 v27, s[40:43], s33 offen offset:0 lds\n\t"
  "v_mfma_f32_16x16x32_bf16 a[200:203], a[244:247], v[184:187], a[200:203]\n\t"
  "s_lshl_b32 s32, s11, 11\n\t"
  "ds_read_b64_tr_b16 a[224:225], v32 offset:0\n\t"
  "v_and_b32 v3, s83, v195\n\t"
  "v_mfma_f32_16x16x32_bf16 a[204:207], a[244:247], v[188:191], a[204:207]\n\t"
  "s_waitcnt lgkmcnt(1)\n\t"
  "ds_read_b64_tr_b16 a[226:227], v32 offset:1792\n\t"
  "ds_read_b64_tr_b16 a[228:229], v32 offset:32\n\t"
  "v_mfma_f32_16x16x32_bf16 a[208:211], a[248:251], v[184:187], a[208:211]\n\t"
  "s_add_u32 m0, s32, 0x22400\n\t"
  "ds_read_b64_tr_b16 a[230:231], v32 offset:1824\n\t"
  "ds_read_b64_tr_b16 a[232:233], v32 offset:64\n\t"
  "s_lshl_b32 s33, s80, 13\n\t"
  "s_lshl_b32 s32, s11, 7\n\t"
  "s_add_u32 s33, s33, s32\n\t"
  "s_add_u32 s33, s33, 0x1040\n\t"
  "v_mfma_f32_16x16x32_bf16 a[212:215], a[248:251], v[188:191], a[212:215]\n\t"
  "buffer_load_dwordx4 v27, s[40:43], s33 offen offset:0 lds\n\t"
  "v_mfma_f32_16x16x32_bf16 a[216:219], a[252:255], v[184:187], a[216:219]\n\t"
  "ds_read_b64_tr_b16 a[234:235], v32 offset:1856\n\t"
  "v_fma_f32 v4, v4, v48, v50\n\t"
  "v_mfma_f32_16x16x32_bf16 a[220:223], a[252:255], v[188:191], a[220:223]\n\t"
  "s_waitcnt lgkmcnt(2)\n\t"
  "ds_read_b64_tr_b16 a[236:237], v32 offset:96\n\t"
  "ds_read_b64_tr_b16 a[238:239], v32 offset:1888\n\t"
  "v_fma_f32 v3, v3, v48, v50\n\t"
  "v_cvt_pk_bf16_f32 v195, v4, v3\n\t"
  "s_nop 1\n\t"
  "v_mfma_f32_16x16x32_bf16 a[112:115], a[224:227], v[192:195], a[112:115]\n\t"
  "ds_read_b64_tr_b16 a[240:241], v32 offset:128\n\t"
  "ds_read_b64_tr_b16 a[242:243], v32 offset:1920\n\t"
  "v_lshlrev_b32 v6, 16, v196\n\t"
  "v_and_b32 v5, s83, v196\n\t"
  "v_fma_f32 v6, v6, v49, v51\n\t"
  "v_fma_f32 v5, v5, v49, v51\n\t"
  "v_lshlrev_b32 v2, 16, v197\n\t"
  "v_cvt_pk_bf16_f32 v196, v6, v5\n\t"
  "v_and_b32 v1, s83, v197\n\t"
  "v_fma_f32 v2, v2, v49, v51\n\t"
  "v_fma_f32 v1, v1, v49, v51\n\t"
  "v_lshlrev_b32 v4, 16, v198\n\t"
  "v_cvt_pk_bf16_f32 v197, v2, v1\n\t"
  "v_and_b32 v3, s83, v198\n\t"
  "v_fma_f32 v4, v4, v49, v51\n\t"
  "v_fma_f32 v3, v3, v49, v51\n\t"
  "v_lshlrev_b32 v6, 16, v199\n\t"
  "v_cvt_pk_bf16_f32 v198, v4, v3\n\t"
  "v_and_b32 v5, s83, v199\n\t"
  "v_fma_f32 v6, v6, v49, v51\n\t"
  "v_fma_f32 v5, v5, v49, v51\n\t"
  "v_cvt_pk_bf16_f32 v199, v6, v5\n\t"
  "s_nop 1\n\t"
  "v_mfma_f32_16x16x32_bf16 a[116:119], a[224:227], v[196:199], a[116:119]\n\t"
  "ds_read_b64_tr_b16 a[244:245], v32 offset:160\n\t"
  "ds_read_b64_tr_b16 a[246:247], v32 offset:1952\n\t"
  "v_mfma_f32_16x16x32_bf16 a[120:123], a[228:231], v[192:195], a[120:123]\n\t"
  "s_add_u32 m0, s84, 0x10000\n\t"
  "ds_read_b64_tr_b16 a[248:249], v32 offset:192\n\t"
  "ds_read_b64_tr_b16 a[250:251], v32 offset:1984\n\t"
  "v_mfma_f32_16x16x32_bf16 a[124:127], a[228:231], v[196:199], a[124:127]\n\t"
  "s_waitcnt lgkmcnt(6)\n\t"
  "v_lshlrev_b32 v2, 16, v200\n\t"
  "v_and_b32 v1, s83, v200\n\t"
  "v_mfma_f32_16x16x32_bf16 a[128:131], a[232:235], v[192:195], a[128:131]\n\t"
  "ds_read_b64_tr_b16 a[252:253], v32 offset:8192\n\t"
  "ds_read_b64_tr_b16 a[254:255], v32 offset:9984\n\t"
  "v_mfma_f32_16x16x32_bf16 a[132:135], a[232:235], v[196:199], a[132:135]\n\t"
  "buffer_load_dwordx4 v8, s[36:39], s74 offen offset:0 lds\n\t"
  "v_mfma_f32_16x16x32_bf16 a[136:139], a[236:239], v[192:195], a[136:139]\n\t"
  "ds_read_b64_tr_b16 a[224:225], v32 offset:8224\n\t"
  "v_fma_f32 v2, v2, v48, v50\n\t"
  "v_mfma_f32_16x16x32_bf16 a[140:143], a[236:239], v[196:199], a[140:143]\n\t"
  "s_waitcnt lgkmcnt(5)\n\t"
  "ds_read_b64_tr_b16 a[226:227], v32 offset:10016\n\t"
  "v_fma_f32 v1, v1, v48, v50\n\t"
  "v_mfma_f32_16x16x32_bf16 a[144:147], a[240:243], v[192:195], a[144:147]\n\t"
  "ds_read_b64_tr_b16 a[228:229], v32 offset:8256\n\t"
  "ds_read_b64_tr_b16 a[230:231], v32 offset:10048\n\t"
  "v_mfma_f32_16x16x32_bf16 a[148:151], a[240:243], v[196:199], a[148:151]\n\t"
  "buffer_load_dwordx4 v9, s[36:39], s74 offen offset:1024 lds\n\t"
  "v_mfma_f32_16x16x32_bf16 a[152:155], a[244:247], v[192:195], a[152:155]\n\t"
  "ds_read_b64_tr_b16 a[232:233], v32 offset:8288\n\t"
  "v_cvt_pk_bf16_f32 v200, v2, v1\n\t"
  "v_mfma_f32_16x16x32_bf16 a[156:159], a[244:247], v[196:199], a[156:159]\n\t"
  "s_waitcnt lgkmcnt(5)\n\t"
  "ds_read_b64_tr_b16 a[234:235], v32 offset:10080\n\t"
  "v_lshlrev_b32 v4, 16, v201\n\t"
  "v_mfma_f32_16x16x32_bf16 a[160:163], a[248:251], v[192:195], a[160:163]\n\t"
  "ds_read_b64_tr_b16 a[236:237], v32 offset:8320\n\t"
  "ds_read_b64_tr_b16 a[238:239], v32 offset:10112\n\t"
  "v_mfma_f32_16x16x32_bf16 a[164:167], a[248:251], v[196:199], a[164:167]\n\t"
  "buffer_load_dwordx4 v10, s[36:39], s74 offen offset:2048 lds\n\t"
  "v_mfma_f32_16x16x32_bf16 a[168:171], a[252:255], v[192:195], a[168:171]\n\t"
  "ds_read_b64_tr_b16 a[240:241], v32 offset:8352\n\t"
  "v_and_b32 v3, s83, v201\n\t"
  "v_mfma_f32_16x16x32_bf16 a[172:175], a[252:255], v[196:199], a[172:175]\n\t"
  "s_waitcnt lgkmcnt(5)\n\t"
  "ds_read_b64_tr_b16 a[242:243], v32 offset:10144\n\t"
  "v_fma_f32 v4, v4, v48, v50\n\t"
  "v_mfma_f32_16x16x32_bf16 a[176:179], a[224:227], v[192:195], a[176:179]\n\t"
  "ds_read_b64_tr_b16 a[244:245], v32 offset:8384\n\t"
  "ds_read_b64_tr_b16 a[246:247], v32 offset:10176\n\t"
  "v_mfma_f32_16x16x32_bf16 a[180:183], a[224:227], v[196:199], a[180:183]\n\t"
  "v_fma_f32 v3, v3, v48, v50\n\t"
  "v_lshlrev_b32 v6, 16, v202\n\t"
  "v_mfma_f32_16x16x32_bf16 a[184:187], a[228:231], v[192:195], a[184:187]\n\t"
  "ds_read_b64_tr_b16 a[248:249], v32 offset:16384\n\t"
  "ds_read_b64_tr_b16 a[250:251], v32 offset:18176\n\t"
  "v_mfma_f32_16x16x32_bf16 a[188:191], a[228:231], v[196:199], a[188:191]\n\t"
  "s_waitcnt lgkmcnt(6)\n\t"
  "v_cvt_pk_bf16_f32 v201, v4, v3\n\t"
  "v_and_b32 v5, s83, v202\n\t"
  "v_mfma_f32_16x16x32_bf16 a[192:195], a[232:235], v[192:195], a[192:195]\n\t"
  "ds_read_b64_tr_b16 a[252:253], v32 offset:16416\n\t"
  "ds_read_b64_tr_b16 a[254:255], v32 offset:18208\n\t"
  "v_mfma_f32_16x16x32_bf16 a[196:199], a[232:235], v[196:199], a[196:199]\n\t"
  "buffer_load_dwordx4 v11, s[36:39], s74 offen offset:3072 lds\n\t"
  "v_mfma_f32_16x16x32_bf16 a[200:203], a[236:239], v[192:195], a[200:203]\n\t"
  "s_add_u32 m0, s84, 0x11000\n\t"
  "ds_read_b64_tr_b16 a[224:225], v32 offset:16448\n\t"
  "v_fma_f32 v6, v6, v48, v50\n\t"
  "v_mfma_f32_16x16x32_bf16 a[204:207], a[236:239], v[196:199], a[204:207]\n\t"
  "s_waitcnt lgkmcnt(5)\n\t"
  "ds_read_b64_tr_b16 a[226:227], v32 offset:18240\n\t"
  "v_fma_f32 v5, v5, v48, v50\n\t"
  "v_mfma_f32_16x16x32_bf16 a[208:211], a[240:243], v[192:195], a[208:211]\n\t"
  "ds_read_b64_tr_b16 a[228:229], v32 offset:16480\n\t"
  "ds_read_b64_tr_b16 a[230:231], v32 offset:18272\n\t"
  "v_mfma_f32_16x16x32_bf16 a[212:215], a[240:243], v[196:199], a[212:215]\n\t"
  "buffer_load_dwordx4 v12, s[36:39], s74 offen offset:0 lds\n\t"
  "v_mfma_f32_16x16x32_bf16 a[216:219], a[244:247], v[192:195], a[216:219]\n\t"
  "ds_read_b64_tr_b16 a[232:233], v32 offset:16512\n\t"
  "v_cvt_pk_bf16_f32 v202, v6, v5\n\t"
  "v_mfma_f32_16x16x32_bf16 a[220:223], a[244:247], v[196:199], a[220:223]\n\t"
  "s_waitcnt lgkmcnt(5)\n\t"
  "ds_read_b64_tr_b16 a[234:235], v32 offset:18304\n\t"
  "v_lshlrev_b32 v2, 16, v203\n\t"
  "v_and_b32 v1, s83, v203\n\t"
  "v_fma_f32 v2, v2, v48, v50\n\t"
  "v_fma_f32 v1, v1, v48, v50\n\t"
  "v_cvt_pk_bf16_f32 v203, v2, v1\n\t"
  "s_nop 1\n\t"
  "v_mfma_f32_16x16x32_bf16 a[112:115], a[248:251], v[200:203], a[112:115]\n\t"
  "ds_read_b64_tr_b16 a[236:237], v32 offset:16544\n\t"
  "ds_read_b64_tr_b16 a[238:239], v32 offset:18336\n\t"
  "v_lshlrev_b32 v4, 16, v204\n\t"
  "v_and_b32 v3, s83, v204\n\t"
  "v_fma_f32 v4, v4, v49, v51\n\t"
  "v_fma_f32 v3, v3, v49, v51\n\t"
  "v_lshlrev_b32 v6, 16, v205\n\t"
  "v_cvt_pk_bf16_f32 v204, v4, v3\n\t"
  "v_and_b32 v5, s83, v205\n\t"
  "v_fma_f32 v6, v6, v49, v51\n\t"
  "v_fma_f32 v5, v5, v49, v51\n\t"
  "v_lshlrev_b32 v2, 16, v206\n\t"
  "v_cvt_pk_bf16_f32 v205, v6, v5\n\t"
  "v_and_b32 v1, s83, v206\n\t"
  "v_fma_f32 v2, v2, v49, v51\n\t"
  "v_fma_f32 v1, v1, v49, v51\n\t"
  "v_lshlrev_b32 v4, 16, v207\n\t"
  "v_cvt_pk_bf16_f32 v206, v2, v1\n\t"
  "v_and_b32 v3, s83, v207\n\t"
  "v_fma_f32 v4, v4, v49, v51\n\t"
  "v_fma_f32 v3, v3, v49, v51\n\t"
  "v_cvt_pk_bf16_f32 v207, v4, v3\n\t"
  "s_nop 1\n\t"
  "v_mfma_f32_16x16x32_bf16 a[116:119], a[248:251], v[204:207], a[116:119]\n\t"
  "v_lshlrev_b32 v6, 16, v208\n\t"
  "v_and_b32 v5, s83, v208\n\t"
  "v_mfma_f32_16x16x32_bf16 a[120:123], a[252:255], v[200:203], a[120:123]\n\t"
  "ds_read_b64_tr_b16 a[240:241], v32 offset:16576\n\t"
  "ds_read_b64_tr_b16 a[242:243], v32 offset:18368\n\t"
  "v_mfma_f32_16x16x32_bf16 a[124:127], a[252:255], v[204:207], a[124:127]\n\t"
  "s_waitcnt lgkmcnt(6)\n\t"
  "v_fma_f32 v6, v6, v48, v50\n\t"
  "v_fma_f32 v5, v5, v48, v50\n\t"
  "v_mfma_f32_16x16x32_bf16 a[128:131], a[224:227], v[200:203], a[128:131]\n\t"
  "ds_read_b64_tr_b16 a[244:245], v32 offset:24576\n\t"
  "ds_read_b64_tr_b16 a[246:247], v32 offset:26368\n\t"
  "v_mfma_f32_16x16x32_bf16 a[132:135], a[224:227], v[204:207], a[132:135]\n\t"
  "buffer_load_dwordx4 v13, s[36:39], s74 offen offset:1024 lds\n\t"
  "v_mfma_f32_16x16x32_bf16 a[136:139], a[228:231], v[200:203], a[136:139]\n\t"
  "ds_read_b64_tr_b16 a[248:249], v32 offset:24608\n\t"
  "v_cvt_pk_bf16_f32 v208, v6, v5\n\t"
  "v_mfma_f32_16x16x32_bf16 a[140:143], a[228:231], v[204:207], a[140:143]\n\t"
  "s_waitcnt lgkmcnt(5)\n\t"
  "ds_read_b64_tr_b16 a[250:251], v32 offset:26400\n\t"
  "v_lshlrev_b32 v2, 16, v209\n\t"
  "v_mfma_f32_16x16x32_bf16 a[144:147], a[232:235], v[200:203], a[144:147]\n\t"
  "ds_read_b64_tr_b16 a[252:253], v32 offset:24640\n\t"
  "ds_read_b64_tr_b16 a[254:255], v32 offset:26432\n\t"
  "v_mfma_f32_16x16x32_bf16 a[148:151], a[232:235], v[204:207], a[148:151]\n\t"
  "buffer_load_dwordx4 v14, s[36:39], s74 offen offset:2048 lds\n\t"
  "v_mfma_f32_16x16x32_bf16 a[152:155], a[236:239], v[200:203], a[152:155]\n\t"
  "s_add_u32 s74, s74, 0x38000\n\t"
  "ds_read_b64_tr_b16 a[224:225], v32 offset:24672\n\t"
  "v_and_b32 v1, s83, v209\n\t"
  "v_mfma_f32_16x16x32_bf16 a[156:159], a[236:239], v[204:207], a[156:159]\n\t"
  "s_waitcnt lgkmcnt(5)\n\t"
  "ds_read_b64_tr_b16 a[226:227], v32 offset:26464\n\t"
  "v_fma_f32 v2, v2, v48, v50\n\t"
  "v_mfma_f32_16x16x32_bf16 a[160:163], a[240:243], v[200:203], a[160:163]\n\t"
  "ds_read_b64_tr_b16 a[228:229], v32 offset:24704\n\t"
  "ds_read_b64_tr_b16 a[230:231], v32 offset:26496\n\t"
  "v_mfma_f32_16x16x32_bf16 a[164:167], a[240:243], v[204:207], a[164:167]\n\t"
  "v_fma_f32 v1, v1, v48, v50\n\t"
  "v_lshlrev_b32 v4, 16, v210\n\t"
  "v_mfma_f32_16x16x32_bf16 a[168:171], a[244:247], v[200:203], a[168:171]\n\t"
  "ds_read_b64_tr_b16 a[232:233], v32 offset:24736\n\t"
  "ds_read_b64_tr_b16 a[234:235], v32 offset:26528\n\t"
  "v_mfma_f32_16x16x32_bf16 a[172:175], a[244:247], v[204:207], a[172:175]\n\t"
  "s_waitcnt lgkmcnt(6)\n\t"
  "v_cvt_pk_bf16_f32 v209, v2, v1\n\t"
  "v_and_b32 v3, s83, v210\n\t"
  "v_mfma_f32_16x16x32_bf16 a[176:179], a[248:251], v[200:203], a[176:179]\n\t"
  "s_waitcnt vmcnt(10)\n\t"
  "ds_read_b64_tr_b16 a[236:237], v32 offset:24768\n\t"
  "ds_read_b64_tr_b16 a[238:239], v32 offset:26560\n\t"
  "v_mfma_f32_16x16x32_bf16 a[180:183], a[248:251], v[204:207], a[180:183]\n\t"
  "s_barrier\n\t"
  "buffer_load_dword v47, v30, s[44:47], s31 offen offset:0\n\t"
  "v_mfma_f32_16x16x32_bf16 a[184:187], a[252:255], v[200:203], a[184:187]\n\t"
  "ds_read_b64_tr_b16 a[240:241], v32 offset:32768\n\t"
  "v_fma_f32 v4, v4, v48, v50\n\t"
  "v_mfma_f32_16x16x32_bf16 a[188:191], a[252:255], v[204:207], a[188:191]\n\t"
  "s_waitcnt lgkmcnt(5)\n\t"
  "ds_read_b64_tr_b16 a[242:243], v32 offset:34560\n\t"
  "v_fma_f32 v3, v3, v48, v50\n\t"
  "v_mfma_f32_16x16x32_bf16 a[192:195], a[224:227], v[200:203], a[192:195]\n\t"
  "ds_read_b64_tr_b16 a[244:245], v32 offset:32800\n\t"
  "ds_read_b64_tr_b16 a[246:247], v32 offset:34592\n\t"
  "v_mfma_f32_16x16x32_bf16 a[196:199], a[224:227], v[204:207], a[196:199]\n\t"
  "buffer_load_dword v47, v29, s[48:51], s29 offen offset:0\n\t"
  "v_mfma_f32_16x16x32_bf16 a[200:203], a[228:231], v[200:203], a[200:203]\n\t"
  "s_add_u32 s29, s29, 0xe00\n\t"
  "ds_read_b64_tr_b16 a[248:249], v32 offset:32832\n\t"
  "v_cvt_pk_bf16_f32 v210, v4, v3\n\t"
  "v_mfma_f32_16x16x32_bf16 a[204:207], a[228:231], v[204:207], a[204:207]\n\t"
  "s_waitcnt lgkmcnt(5)\n\t"
  "ds_read_b64_tr_b16 a[250:251], v32 offset:34624\n\t"
  "v_lshlrev_b32 v6, 16, v211\n\t"
  "v_mfma_f32_16x16x32_bf16 a[208:211], a[232:235], v[200:203], a[208:211]\n\t"
  "ds_read_b64_tr_b16 a[252:253], v32 offset:32864\n\t"
  "ds_read_b64_tr_b16 a[254:255], v32 offset:34656\n\t"
  "v_mfma_f32_16x16x32_bf16 a[212:215], a[232:235], v[204:207], a[212:215]\n\t"
  "buffer_load_dword v47, v29, s[48:51], s29 offen offset:0\n\t"
  "v_mfma_f32_16x16x32_bf16 a[216:219], a[236:239], v[200:203], a[216:219]\n\t"
  "s_add_u32 s29, s29, 0xe00\n\t"
  "ds_read_b64_tr_b16 a[224:225], v32 offset:32896\n\t"
  "v_and_b32 v5, s83, v211\n\t"
  "v_mfma_f32_16x16x32_bf16 a[220:223], a[236:239], v[204:207], a[220:223]\n\t"
  "s_waitcnt lgkmcnt(5)\n\t"
  "ds_read_b64_tr_b16 a[226:227], v32 offset:34688\n\t"
  "v_fma_f32 v6, v6, v48, v50\n\t"
  "v_fma_f32 v5, v5, v48, v50\n\t"
  "v_cvt_pk_bf16_f32 v211, v6, v5\n\t"
  "s_nop 1\n\t"
  "v_mfma_f32_16x16x32_bf16 a[112:115], a[240:243], v[208:211], a[112:115]\n\t"
  "s_lshl_b32 s40, s80, 3\n\t"
  "ds_read_b64_tr_b16 a[228:229], v32 offset:32928\n\t"
  "ds_read_b64_tr_b16 a[230:231], v32 offset:34720\n\t"
  "v_lshlrev_b32 v2, 16, v212\n\t"
  "v_and_b32 v1, s83, v212\n\t"
  "v_fma_f32 v2, v2, v49, v51\n\t"
  "v_fma_f32 v1, v1, v49, v51\n\t"
  "v_lshlrev_b32 v4, 16, v213\n\t"
  "v_cvt_pk_bf16_f32 v212, v2, v1\n\t"
  "v_and_b32 v3, s83, v213\n\t"
  "v_fma_f32 v4, v4, v49, v51\n\t"
  "v_fma_f32 v3, v3, v49, v51\n\t"
  "v_lshlrev_b32 v6, 16, v214\n\t"
  "s_nop 0\n\t"
  "s_cbranch_execz TRAMP_%=\n\t"
  "v_cvt_pk_bf16_f32 v213, v4, v3\n\t"
  "v_and_b32 v5, s83, v214\n\t"
  "v_fma_f32 v6, v6, v49, v51\n\t"
  "v_fma_f32 v5, v5, v49, v51\n\t"
  "v_lshlrev_b32 v2, 16, v215\n\t"
  "v_cvt_pk_bf16_f32 v214, v6, v5\n\t"
  "v_and_b32 v1, s83, v215\n\t"
  "v_fma_f32 v2, v2, v49, v51\n\t"
  "v_fma_f32 v1, v1, v49, v51\n\t"
  "v_cvt_pk_bf16_f32 v215, v2, v1\n\t"
  "s_nop 1\n\t"
  "v_mfma_f32_16x16x32_bf16 a[116:119], a[240:243], v[212:215], a[116:119]\n\t"
  "s_lshr_b32 s41, s72, 1\n\t"
  "v_lshlrev_b32 v4, 16, v216\n\t"
  "v_and_b32 v3, s83, v216\n\t"
  "v_mfma_f32_16x16x32_bf16 a[120:123], a[244:247], v[208:211], a[120:123]\n\t"
  "s_lshl_b32 s41, s41, 8\n\t"
  "ds_read_b64_tr_b16 a[232:233], v32 offset:32960\n\t"
  "ds_read_b64_tr_b16 a[234:235], v32 offset:34752\n\t"
  "v_mfma_f32_16x16x32_bf16 a[124:127], a[244:247], v[212:215], a[124:127]\n\t"
  "s_waitcnt lgkmcnt(6)\n\t"
  "v_fma_f32 v4, v4, v48, v50\n\t"
  "v_fma_f32 v3, v3, v48, v50\n\t"
  "v_mfma_f32_16x16x32_bf16 a[128:131], a[248:251], v[208:211], a[128:131]\n\t"
  "s_add_u32 s41, s41, 0x10000\n\t"
  "ds_read_b64_tr_b16 a[236:237], v32 offset:40960\n\t"
  "ds_read_b64_tr_b16 a[238:239], v32 offset:42752\n\t"
  "s_add_u32 s40, s40, s41\n\t"
  "s_cmp_eq_u32 s11, 0\n\t"
  "s_cselect_b32 s40, s40, 0x7f000000\n\t"
  "s_lshl_b32 s80, s80, 13\n\t"
  "s_add_u32 s80, s80, s76\n\t"
  "s_lshl_b32 s32, s11, 11\n\t"
  "s_add_u32 s80, s80, s32\n\t"
  "s_waitcnt vmcnt(55)\n\t"
  "v_readfirstlane_b32 s89, v122\n\t"
  "v_readfirstlane_b32 s90, v123\n\t"
  "s_nop 0\n\t"
  "s_mul_i32 s30, s89, 0x9e3779b1\n\t"
  "s_add_u32 s30, s30, s90\n\t"
  "s_add_u32 s30, s30, 0x632be5ab\n\t"
  "s_mul_i32 s31, s90, 0x85ebca77\n\t"
  "s_xor_b32 s89, s89, 0x27d4eb2f\n\t"
  "s_add_u32 s31, s31, s89\n\t"
  "s_and_b32 s74, s72, 1\n\t"
  "s_mul_i32 s74, s74, 0\n\t"
  "s_add_u32 s74, s74, s11\n\t"
  "s_lshr_b32 s74, s74, 1\n\t"
  "s_mul_i32 s74, s74, 0x40000\n\t"
  "s_add_u32 m0, s84, 0x18000\n\t"
  "v_mfma_f32_16x16x32_bf16 a[132:135], a[248:251], v[212:215], a[132:135]\n\t"
  "s_waitcnt vmcnt(10)\n\t"
  "buffer_load_dwordx4 v16, s[92:95], s74 offen offset:0 lds\n\t"
  "v_mfma_f32_16x16x32_bf16 a[136:139], a[252:255], v[208:211], a[136:139]\n\t"
  "ds_read_b64_tr_b16 a[240:241], v32 offset:40992\n\t"
  "v_cvt_pk_bf16_f32 v216, v4, v3\n\t"
  "v_mfma_f32_16x16x32_bf16 a[140:143], a[252:255], v[212:215], a[140:143]\n\t"
  "s_waitcnt lgkmcnt(5)\n\t"
  "ds_read_b64_tr_b16 a[242:243], v32 offset:42784\n\t"
  "ds_read_b64 v[104:105], v25 offset:0\n\t"
  "v_mfma_f32_16x16x32_bf16 a[144:147], a[224:227], v[208:211], a[144:147]\n\t"
  "ds_read_b64_tr_b16 a[244:245], v32 offset:41024\n\t"
  "ds_read_b64_tr_b16 a[246:247], v32 offset:42816\n\t"
  "v_mfma_f32_16x16x32_bf16 a[148:151], a[224:227], v[212:215], a[148:151]\n\t"
  "buffer_load_dwordx4 v17, s[92:95], s74 offen offset:1024 lds\n\t"
  "v_mfma_f32_16x16x32_bf16 a[152:155], a[228:231], v[208:211], a[152:155]\n\t"
  "ds_read_b64_tr_b16 a[248:249], v32 offset:41056\n\t"
  "v_lshlrev_b32 v6, 16, v217\n\t"
  "v_mfma_f32_16x16x32_bf16 a[156:159], a[228:231], v[212:215], a[156:159]\n\t"
  "s_waitcnt lgkmcnt(6)\n\t"
  "ds_read_b64_tr_b16 a[250:251], v32 offset:42848\n\t"
  "ds_read_b64 v[106:107], v26 offset:0\n\t"
  "v_mfma_f32_16x16x32_bf16 a[160:163], a[232:235], v[208:211], a[160:163]\n\t"
  "ds_read_b64_tr_b16 a[252:253], v32 offset:41088\n\t"
  "ds_read_b64_tr_b16 a[254:255], v32 offset:42880\n\t"
  "v_mfma_f32_16x16x32_bf16 a[164:167], a[232:235], v[212:215], a[164:167]\n\t"
  "buffer_load_dwordx4 v18, s[92:95], s74 offen offset:2048 lds\n\t"
  "v_mfma_f32_16x16x32_bf16 a[168:171], a[236:239], v[208:211], a[168:171]\n\t"
  "ds_read_b64_tr_b16 a[224:225], v32 offset:41120\n\t"
  "v_and_b32 v5, s83, v217\n\t"
  "v_mfma_f32_16x16x32_bf16 a[172:175], a[236:239], v[212:215], a[172:175]\n\t"
  "s_waitcnt lgkmcnt(6)\n\t"
  "ds_read_b64_tr_b16 a[226:227], v32 offset:42912\n\t"
  "ds_read_b64 v[108:109], v25 offset:1024\n\t"
  "ds_read_b64 v[110:111], v26 offset:1024\n\t"
  "s_waitcnt lgkmcnt(0)\n\t"
  "v_mfma_f32_16x16x32_bf16 a[176:179], a[240:243], v[208:211], a[176:179]\n\t"
  "ds_read_b64_tr_b16 a[228:229], v32 offset:41152\n\t"
  "ds_read_b64_tr_b16 a[230:231], v32 offset:42944\n\t"
  "v_mfma_f32_16x16x32_bf16 a[180:183], a[240:243], v[212:215], a[180:183]\n\t"
  "buffer_load_dwordx4 v19, s[92:95], s74 offen offset:3072 lds\n\t"
  "v_mfma_f32_16x16x32_bf16 a[184:187], a[244:247], v[208:211], a[184:187]\n\t"
  "s_add_u32 m0, s84, 0x19000\n\t"
  "ds_read_b64_tr_b16 a[232:233], v32 offset:49152\n\t"
  "v_fma_f32 v6, v6, v48, v50\n\t"
  "v_mfma_f32_16x16x32_bf16 a[188:191], a[244:247], v[212:215], a[188:191]\n\t"
  "s_waitcnt lgkmcnt(7)\n\t"
  "ds_read_b64_tr_b16 a[234:235], v32 offset:50944\n\t"
  "v_fma_f32 v5, v5, v48, v50\n\t"
  "v_mfma_f32_16x16x32_bf16 a[192:195], a[248:251], v[208:211], a[192:195]\n\t"
  "ds_read_b64_tr_b16 a[236:237], v32 offset:49184\n\t"
  "ds_read_b64_tr_b16 a[238:239], v32 offset:50976\n\t"
  "v_mfma_f32_16x16x32_bf16 a[196:199], a[248:251], v[212:215], a[196:199]\n\t"
  "buffer_load_dwordx4 v36, s[92:95], s74 offen offset:0 lds\n\t"
  "v_mfma_f32_16x16x32_bf16 a[200:203], a[252:255], v[208:211], a[200:203]\n\t"
  "ds_read_b64_tr_b16 a[240:241], v32 offset:49216\n\t"
  "v_cvt_pk_bf16_f32 v217, v6, v5\n\t"
  "v_mfma_f32_16x16x32_bf16 a[204:207], a[252:255], v[212:215], a[204:207]\n\t"
  "s_waitcnt lgkmcnt(5)\n\t"
  "ds_read_b64_tr_b16 a[242:243], v32 offset:51008\n\t"
  "v_lshlrev_b32 v2, 16, v218\n\t"
  "v_mfma_f32_16x16x32_bf16 a[208:211], a[224:227], v[208:211], a[208:211]\n\t"
  "ds_read_b64_tr_b16 a[244:245], v32 offset:49248\n\t"
  "ds_read_b64_tr_b16 a[246:247], v32 offset:51040\n\t"
  "v_mfma_f32_16x16x32_bf16 a[212:215], a[224:227], v[212:215], a[212:215]\n\t"
  "buffer_store_dwordx4 v[104:107], v40, s[56:59], s80 offen offset:0 sc1\n\t"
  "v_mfma_f32_16x16x32_bf16 a[216:219], a[228:231], v[208:211], a[216:219]\n\t"
  "ds_read_b64_tr_b16 a[248:249], v32 offset:49280\n\t"
  "v_and_b32 v1, s83, v218\n\t"
  "v_mfma_f32_16x16x32_bf16 a[220:223], a[228:231], v[212:215], a[220:223]\n\t"
  "s_waitcnt lgkmcnt(5)\n\t"
  "ds_read_b64_tr_b16 a[250:251], v32 offset:51072\n\t"
  "v_fma_f32 v2, v2, v48, v50\n\t"
  "v_fma_f32 v1, v1, v48, v50\n\t"
  "v_lshlrev_b32 v4, 16, v219\n\t"
  "v_cvt_pk_bf16_f32 v218, v2, v1\n\t"
  "v_and_b32 v3, s83, v219\n\t"
  "v_fma_f32 v4, v4, v48, v50\n\t"
  "v_fma_f32 v3, v3, v48, v50\n\t"
  "v_cvt_pk_bf16_f32 v219, v4, v3\n\t"
  "s_nop 1\n\t"
  "v_mfma_f32_16x16x32_bf16 a[112:115], a[232:235], v[216:219], a[112:115]\n\t"
  "ds_read_b64_tr_b16 a[252:253], v32 offset:49312\n\t"
  "ds_read_b64_tr_b16 a[254:255], v32 offset:51104\n\t"
  "v_lshlrev_b32 v6, 16, v220\n\t"
  "v_and_b32 v5, s83, v220\n\t"
  "v_fma_f32 v6, v6, v49, v51\n\t"
  "v_fma_f32 v5, v5, v49, v51\n\t"
  "v_lshlrev_b32 v2, 16, v221\n\t"
  "v_cvt_pk_bf16_f32 v220, v6, v5\n\t"
  "v_and_b32 v1, s83, v221\n\t"
  "v_fma_f32 v2, v2, v49, v51\n\t"
  "v_fma_f32 v1, v1, v49, v51\n\t"
  "v_lshlrev_b32 v4, 16, v222\n\t"
  "v_cvt_pk_bf16_f32 v221, v2, v1\n\t"
  "v_and_b32 v3, s83, v222\n\t"
  "v_fma_f32 v4, v4, v49, v51\n\t"
  "v_fma_f32 v3, v3, v49, v51\n\t"
  "v_lshlrev_b32 v6, 16, v223\n\t"
  "v_cvt_pk_bf16_f32 v222, v4, v3\n\t"
  "v_and_b32 v5, s83, v223\n\t"
  "v_fma_f32 v6, v6, v49, v51\n\t"
  "v_fma_f32 v5, v5, v49, v51\n\t"
  "v_cvt_pk_bf16_f32 v223, v6, v5\n\t"
  "s_nop 1\n\t"
  "v_mfma_f32_16x16x32_bf16 a[116:119], a[232:235], v[220:223], a[116:119]\n\t"
  "buffer_load_dwordx4 v37, s[92:95], s74 offen offset:1024 lds\n\t"
  "v_mfma_f32_16x16x32_bf16 a[120:123], a[236:239], v[216:219], a[120:123]\n\t"
  "ds_read_b64_tr_b16 a[224:225], v32 offset:49344\n\t"
  "v_lshlrev_b32 v2, 16, v224\n\t"
  "v_mfma_f32_16x16x32_bf16 a[124:127], a[236:239], v[220:223], a[124:127]\n\t"
  "s_waitcnt lgkmcnt(5)\n\t"
  "ds_read_b64_tr_b16 a[226:227], v32 offset:51136\n\t"
  "v_and_b32 v1, s83, v224\n\t"
  "v_mfma_f32_16x16x32_bf16 a[128:131], a[240:243], v[216:219], a[128:131]\n\t"
  "ds_read_b64_tr_b16 a[228:229], v32 offset:57344\n\t"
  "ds_read_b64_tr_b16 a[230:231], v32 offset:59136\n\t"
  "v_mfma_f32_16x16x32_bf16 a[132:135], a[240:243], v[220:223], a[132:135]\n\t"
  "buffer_load_dwordx4 v38, s[92:95], s74 offen offset:2048 lds\n\t"
  "v_mfma_f32_16x16x32_bf16 a[136:139], a[244:247], v[216:219], a[136:139]\n\t"
  "ds_read_b64_tr_b16 a[232:233], v32 offset:57376\n\t"
  "v_fma_f32 v2, v2, v48, v50\n\t"
  "v_mfma_f32_16x16x32_bf16 a[140:143], a[244:247], v[220:223], a[140:143]\n\t"
  "s_waitcnt lgkmcnt(5)\n\t"
  "ds_read_b64_tr_b16 a[234:235], v32 offset:59168\n\t"
  "v_fma_f32 v1, v1, v48, v50\n\t"
  "v_mfma_f32_16x16x32_bf16 a[144:147], a[248:251], v[216:219], a[144:147]\n\t"
  "ds_read_b64_tr_b16 a[236:237], v32 offset:57408\n\t"
  "ds_read_b64_tr_b16 a[238:239], v32 offset:59200\n\t"
  "v_mfma_f32_16x16x32_bf16 a[148:151], a[248:251], v[220:223], a[148:151]\n\t"
  "buffer_load_dwordx4 v39, s[92:95], s74 offen offset:3072 lds\n\t"
  "v_mfma_f32_16x16x32_bf16 a[152:155], a[252:255], v[216:219], a[152:155]\n\t"
  "s_add_u32 s74, s74, 0x80000\n\t"
  "ds_read_b64_tr_b16 a[240:241], v32 offset:57440\n\t"
  "v_cvt_pk_bf16_f32 v224, v2, v1\n\t"
  "v_mfma_f32_16x16x32_bf16 a[156:159], a[252:255], v[220:223], a[156:159]\n\t"
  "s_waitcnt lgkmcnt(5)\n\t"
  "ds_read_b64_tr_b16 a[242:243], v32 offset:59232\n\t"
  "v_lshlrev_b32 v4, 16, v225\n\t"
  "v_mfma_f32_16x16x32_bf16 a[160:163], a[224:227], v[216:219], a[160:163]\n\t"
  "ds_read_b64_tr_b16 a[244:245], v32 offset:57472\n\t"
  "ds_read_b64_tr_b16 a[246:247], v32 offset:59264\n\t"
  "v_mfma_f32_16x16x32_bf16 a[164:167], a[224:227], v[220:223], a[164:167]\n\t"
  "v_and_b32 v3, s83, v225\n\t"
  "v_fma_f32 v4, v4, v48, v50\n\t"
  "v_mfma_f32_16x16x32_bf16 a[168:171], a[228:231], v[216:219], a[168:171]\n\t"
  "ds_read_b64_tr_b16 a[248:249], v32 offset:57504\n\t"
  "ds_read_b64_tr_b16 a[250:251], v32 offset:59296\n\t"
  "v_mfma_f32_16x16x32_bf16 a[172:175], a[228:231], v[220:223], a[172:175]\n\t"
  "s_waitcnt lgkmcnt(6)\n\t"
  "v_fma_f32 v3, v3, v48, v50\n\t"
  "v_lshlrev_b32 v6, 16, v226\n\t"
  "v_mfma_f32_16x16x32_bf16 a[176:179], a[232:235], v[216:219], a[176:179]\n\t"
  "s_waitcnt vmcnt(12)\n\t"
  "ds_read_b64_tr_b16 a[252:253], v32 offset:57536\n\t"
  "ds_read_b64_tr_b16 a[254:255], v32 offset:59328\n\t"
  "v_mfma_f32_16x16x32_bf16 a[180:183], a[232:235], v[220:223], a[180:183]\n\t"
  "s_barrier\n\t"
  "buffer_store_dwordx4 v[108:111], v40, s[56:59], s80 offen offset:1024 sc1\n\t"
  "v_mfma_f32_16x16x32_bf16 a[184:187], a[236:239], v[216:219], a[184:187]\n\t"
  "ds_read_b64_tr_b16 a[224:225], v33 offset:0\n\t"
  "v_cvt_pk_bf16_f32 v225, v4, v3\n\t"
  "v_mfma_f32_16x16x32_bf16 a[188:191], a[236:239], v[220:223], a[188:191]\n\t"
  "s_waitcnt lgkmcnt(5)\n\t"
  "ds_read_b64_tr_b16 a[226:227], v33 offset:1792\n\t"
  "v_and_b32 v5, s83, v226\n\t"
  "v_mfma_f32_16x16x32_bf16 a[192:195], a[240:243], v[216:219], a[192:195]\n\t"
  "ds_read_b64_tr_b16 a[228:229], v33 offset:32\n\t"
  "ds_read_b64_tr_b16 a[230:231], v33 offset:1824\n\t"
  "v_mfma_f32_16x16x32_bf16 a[196:199], a[240:243], v[220:223], a[196:199]\n\t"
  "global_load_dwordx4 v[12:15], v40, s[96:97] offset:2048\n\t"
  "v_mfma_f32_16x16x32_bf16 a[200:203], a[244:247], v[216:219], a[200:203]\n\t"
  "ds_read_b64_tr_b16 a[232:233], v33 offset:64\n\t"
  "ds_read_b64_tr_b16 v[96:97], v22 offset:0\n\t"
  "v_mfma_f32_16x16x32_bf16 a[204:207], a[244:247], v[220:223], a[204:207]\n\t"
  "s_waitcnt lgkmcnt(6)\n\t"
  "ds_read_b64_tr_b16 a[234:235], v33 offset:1856\n\t"
  "ds_read_b64_tr_b16 v[98:99], v22 offset:1792\n\t"
  "v_mfma_f32_16x16x32_bf16 a[208:211], a[248:251], v[216:219], a[208:211]\n\t"
  "ds_read_b64_tr_b16 a[236:237], v33 offset:96\n\t"
  "ds_read_b64_tr_b16 a[238:239], v33 offset:1888\n\t"
  "v_mfma_f32_16x16x32_bf16 a[212:215], a[248:251], v[220:223], a[212:215]\n\t"
  "global_load_dwordx2 v[10:11], v40, s[96:97] offset:3072\n\t"
  "v_mfma_f32_16x16x32_bf16 a[216:219], a[252:255], v[216:219], a[216:219]\n\t"
  "ds_read_b64_tr_b16 a[240:241], v33 offset:128\n\t"
  "ds_read_b64_tr_b16 v[100:101], v22 offset:32\n\t"
  "v_mfma_f32_16x16x32_bf16 a[220:223], a[252:255], v[220:223], a[220:223]\n\t"
  "s_waitcnt lgkmcnt(8)\n\t"
  "ds_read_b64_tr_b16 a[242:243], v33 offset:1920\n\t"
  "ds_read_b64_tr_b16 v[102:103], v22 offset:1824\n\t"
  "v_fma_f32 v6, v6, v48, v50\n\t"
  "v_fma_f32 v5, v5, v48, v50\n\t"
  "v_lshlrev_b32 v2, 16, v227\n\t"
  "v_cvt_pk_bf16_f32 v226, v6, v5\n\t"
  "v_and_b32 v1, s83, v227\n\t"
  "v_fma_f32 v2, v2, v48, v50\n\t"
  "v_fma_f32 v1, v1, v48, v50\n\t"
  "v_cvt_pk_bf16_f32 v227, v2, v1\n\t"
  "s_nop 1\n\t"
  "v_mfma_f32_16x16x32_bf16 a[112:115], a[224:227], v[224:227], a[112:115]\n\t"
  "ds_read_b64_tr_b16 a[244:245], v33 offset:160\n\t"
  "ds_read_b64_tr_b16 a[246:247], v33 offset:1952\n\t"
  "v_lshlrev_b32 v4, 16, v228\n\t"
  "v_and_b32 v3, s83, v228\n\t"
  "v_fma_f32 v4, v4, v49, v51\n\t"
  "v_fma_f32 v3, v3, v49, v51\n\t"
  "v_lshlrev_b32 v6, 16, v229\n\t"
  "v_cvt_pk_bf16_f32 v228, v4, v3\n\t"
  "v_and_b32 v5, s83, v229\n\t"
  "v_fma_f32 v6, v6, v49, v51\n\t"
  "v_fma_f32 v5, v5, v49, v51\n\t"
  "v_lshlrev_b32 v2, 16, v230\n\t"
  "v_cvt_pk_bf16_f32 v229, v6, v5\n\t"
  "v_and_b32 v1, s83, v230\n\t"
  "v_fma_f32 v2, v2, v49, v51\n\t"
  "v_fma_f32 v1, v1, v49, v51\n\t"
  "v_lshlrev_b32 v4, 16, v231\n\t"
  "v_cvt_pk_bf16_f32 v230, v2, v1\n\t"
  "v_and_b32 v3, s83, v231\n\t"
  "v_fma_f32 v4, v4, v49, v51\n\t"
  "v_fma_f32 v3, v3, v49, v51\n\t"
  "v_cvt_pk_bf16_f32 v231, v4, v3\n\t"
  "s_nop 1\n\t"
  "v_mfma_f32_16x16x32_bf16 a[116:119], a[224:227], v[228:231], a[116:119]\n\t"
  "v_lshlrev_b32 v6, 16, v232\n\t"
  "v_and_b32 v5, s83, v232\n\t"
  "v_mfma_f32_16x16x32_bf16 a[120:123], a[228:231], v[224:227], a[120:123]\n\t"
  "s_add_u32 m0, s84, 0\n\t"
  "ds_read_b64_tr_b16 a[248:249], v33 offset:192\n\t"
  "ds_read_b64_tr_b16 a[250:251], v33 offset:1984\n\t"
  "v_mfma_f32_16x16x32_bf16 a[124:127], a[228:231], v[228:231], a[124:127]\n\t"
  "s_waitcnt lgkmcnt(8)\n\t"
  "v_fma_f32 v6, v6, v48, v50\n\t"
  "v_fma_f32 v5, v5, v48, v50\n\t"
  "v_mfma_f32_16x16x32_bf16 a[128:131], a[232:235], v[224:227], a[128:131]\n\t"
  "s_waitcnt lgkmcnt(4)\n\t"
  "ds_read_b64_tr_b16 a[252:253], v33 offset:8192\n\t"
  "ds_read_b64_tr_b16 a[254:255], v33 offset:9984\n\t"
  "v_mfma_f32_16x16x32_bf16 a[132:135], a[232:235], v[228:231], a[132:135]\n\t"
  "buffer_load_dwordx4 v16, s[92:95], s74 offen offset:0 lds\n\t"
  "v_mfma_f32_16x16x32_bf16 a[136:139], a[236:239], v[224:227], a[136:139]\n\t"
  "ds_read_b64_tr_b16 a[224:225], v33 offset:8224\n\t"
  "v_cvt_pk_bf16_f32 v232, v6, v5\n\t"
  "v_mfma_f32_16x16x32_bf16 a[140:143], a[236:239], v[228:231], a[140:143]\n\t"
  "s_waitcnt lgkmcnt(5)\n\t"
  "ds_read_b64_tr_b16 a[226:227], v33 offset:10016\n\t"
  "v_lshlrev_b32 v2, 16, v233\n\t"
  "v_mfma_f32_16x16x32_bf16 a[144:147], a[240:243], v[224:227], a[144:147]\n\t"
  "ds_read_b64_tr_b16 a[228:229], v33 offset:8256\n\t"
  "ds_read_b64_tr_b16 a[230:231], v33 offset:10048\n\t"
  "v_mfma_f32_16x16x32_bf16 a[148:151], a[240:243], v[228:231], a[148:151]\n\t"
  "buffer_load_dwordx4 v17, s[92:95], s74 offen offset:1024 lds\n\t"
  "v_mfma_f32_16x16x32_bf16 a[152:155], a[244:247], v[224:227], a[152:155]\n\t"
  "ds_read_b64_tr_b16 a[232:233], v33 offset:8288\n\t"
  "v_and_b32 v1, s83, v233\n\t"
  "v_mfma_f32_16x16x32_bf16 a[156:159], a[244:247], v[228:231], a[156:159]\n\t"
  "s_waitcnt lgkmcnt(5)\n\t"
  "ds_read_b64_tr_b16 a[234:235], v33 offset:10080\n\t"
  "v_fma_f32 v2, v2, v48, v50\n\t"
  "v_mfma_f32_16x16x32_bf16 a[160:163], a[248:251], v[224:227], a[160:163]\n\t"
  "ds_read_b64_tr_b16 a[236:237], v33 offset:8320\n\t"
  "ds_read_b64_tr_b16 a[238:239], v33 offset:10112\n\t"
  "v_mfma_f32_16x16x32_bf16 a[164:167], a[248:251], v[228:231], a[164:167]\n\t"
  "buffer_load_dwordx4 v18, s[92:95], s74 offen offset:2048 lds\n\t"
  "v_mfma_f32_16x16x32_bf16 a[168:171], a[252:255], v[224:227], a[168:171]\n\t"
  "ds_read_b64_tr_b16 a[240:241], v33 offset:8352\n\t"
  "v_fma_f32 v1, v1, v48, v50\n\t"
  "v_mfma_f32_16x16x32_bf16 a[172:175], a[252:255], v[228:231], a[172:175]\n\t"
  "s_waitcnt lgkmcnt(5)\n\t"
  "ds_read_b64_tr_b16 a[242:243], v33 offset:10144\n\t"
  "v_cvt_pk_bf16_f32 v233, v2, v1\n\t"
  "v_mfma_f32_16x16x32_bf16 a[176:179], a[224:227], v[224:227], a[176:179]\n\t"
  "ds_read_b64_tr_b16 a[244:245], v33 offset:8384\n\t"
  "ds_read_b64_tr_b16 a[246:247], v33 offset:10176\n\t"
  "v_mfma_f32_16x16x32_bf16 a[180:183], a[224:227], v[228:231], a[180:183]\n\t"
  "buffer_load_dwordx4 v19, s[92:95], s74 offen offset:3072 lds\n\t"
  "v_mfma_f32_16x16x32_bf16 a[184:187], a[228:231], v[224:227], a[184:187]\n\t"
  "s_add_u32 m0, s84, 0x1000\n\t"
  "ds_read_b64_tr_b16 a[248:249], v33 offset:16384\n\t"
  "v_lshlrev_b32 v4, 16, v234\n\t"
  "v_mfma_f32_16x16x32_bf16 a[188:191], a[228:231], v[228:231], a[188:191]\n\t"
  "s_waitcnt lgkmcnt(5)\n\t"
  "ds_read_b64_tr_b16 a[250:251], v33 offset:18176\n\t"
  "v_and_b32 v3, s83, v234\n\t"
  "v_mfma_f32_16x16x32_bf16 a[192:195], a[232:235], v[224:227], a[192:195]\n\t"
  "ds_read_b64_tr_b16 a[252:253], v33 offset:16416\n\t"
  "ds_read_b64_tr_b16 a[254:255], v33 offset:18208\n\t"
  "v_mfma_f32_16x16x32_bf16 a[196:199], a[232:235], v[228:231], a[196:199]\n\t"
  "buffer_load_dwordx4 v36, s[92:95], s74 offen offset:0 lds\n\t"
  "v_mfma_f32_16x16x32_bf16 a[200:203], a[236:239], v[224:227], a[200:203]\n\t"
  "ds_read_b64_tr_b16 a[224:225], v33 offset:16448\n\t"
  "v_fma_f32 v4, v4, v48, v50\n\t"
  "v_mfma_f32_16x16x32_bf16 a[204:207], a[236:239], v[228:231], a[204:207]\n\t"
  "s_waitcnt lgkmcnt(5)\n\t"
  "ds_read_b64_tr_b16 a[226:227], v33 offset:18240\n\t"
  "v_fma_f32 v3, v3, v48, v50\n\t"
  "v_mfma_f32_16x16x32_bf16 a[208:211], a[240:243], v[224:227], a[208:211]\n\t"
  "ds_read_b64_tr_b16 a[228:229], v33 offset:16480\n\t"
  "ds_read_b64_tr_b16 a[230:231], v33 offset:18272\n\t"
  "v_mfma_f32_16x16x32_bf16 a[212:215], a[240:243], v[228:231], a[212:215]\n\t"
  "buffer_load_dwordx4 v37, s[92:95], s74 offen offset:1024 lds\n\t"
  "v_mfma_f32_16x16x32_bf16 a[216:219], a[244:247], v[224:227], a[216:219]\n\t"
  "ds_read_b64_tr_b16 a[232:233], v33 offset:16512\n\t"
  "v_cvt_pk_bf16_f32 v234, v4, v3\n\t"
  "v_mfma_f32_16x16x32_bf16 a[220:223], a[244:247], v[228:231], a[220:223]\n\t"
  "s_waitcnt lgkmcnt(5)\n\t"
  "ds_read_b64_tr_b16 a[234:235], v33 offset:18304\n\t"
  "v_lshlrev_b32 v6, 16, v235\n\t"
  "v_and_b32 v5, s83, v235\n\t"
  "v_fma_f32 v6, v6, v48, v50\n\t"
  "v_fma_f32 v5, v5, v48, v50\n\t"
  "v_cvt_pk_bf16_f32 v235, v6, v5\n\t"
  "s_nop 1\n\t"
  "v_mfma_f32_16x16x32_bf16 a[112:115], a[248:251], v[232:235], a[112:115]\n\t"
  "ds_read_b64_tr_b16 a[236:237], v33 offset:16544\n\t"
  "ds_read_b64_tr_b16 a[238:239], v33 offset:18336\n\t"
  "v_lshlrev_b32 v2, 16, v236\n\t"
  "v_and_b32 v1, s83, v236\n\t"
  "v_fma_f32 v2, v2, v49, v51\n\t"
  "v_fma_f32 v1, v1, v49, v51\n\t"
  "v_lshlrev_b32 v4, 16, v237\n\t"
  "v_cvt_pk_bf16_f32 v236, v2, v1\n\t"
  "v_and_b32 v3, s83, v237\n\t"
  "v_fma_f32 v4, v4, v49, v51\n\t"
  "v_fma_f32 v3, v3, v49, v51\n\t"
  "v_lshlrev_b32 v6, 16, v238\n\t"
  "v_cvt_pk_bf16_f32 v237, v4, v3\n\t"
  "v_and_b32 v5, s83, v238\n\t"
  "v_fma_f32 v6, v6, v49, v51\n\t"
  "v_fma_f32 v5, v5, v49, v51\n\t"
  "v_lshlrev_b32 v2, 16, v239\n\t"
  "v_cvt_pk_bf16_f32 v238, v6, v5\n\t"
  "v_and_b32 v1, s83, v239\n\t"
  "v_fma_f32 v2, v2, v49, v51\n\t"
  "v_fma_f32 v1, v1, v49, v51\n\t"
  "v_cvt_pk_bf16_f32 v239, v2, v1\n\t"
  "s_nop 1\n\t"
  "v_mfma_f32_16x16x32_bf16 a[116:119], a[248:251], v[236:239], a[116:119]\n\t"
  "buffer_load_dwordx4 v38, s[92:95], s74 offen offset:2048 lds\n\t"
  "v_mfma_f32_16x16x32_bf16 a[120:123], a[252:255], v[232:235], a[120:123]\n\t"
  "ds_read_b64_tr_b16 a[240:241], v33 offset:16576\n\t"
  "v_mov_b32 v8, s30\n\t"
  "v_mfma_f32_16x16x32_bf16 a[124:127], a[252:255], v[236:239], a[124:127]\n\t"
  "s_waitcnt lgkmcnt(5)\n\t"
  "ds_read_b64_tr_b16 a[242:243], v33 offset:18368\n\t"
  "v_mov_b32 v9, s31\n\t"
  "v_mfma_f32_16x16x32_bf16 a[128:131], a[224:227], v[232:235], a[128:131]\n\t"
  "ds_read_b64_tr_b16 a[244:245], v33 offset:24576\n\t"
  "ds_read_b64_tr_b16 a[246:247], v33 offset:26368\n\t"
  "v_mfma_f32_16x16x32_bf16 a[132:135], a[224:227], v[236:239], a[132:135]\n\t"
  "buffer_load_dwordx4 v39, s[92:95], s74 offen offset:3072 lds\n\t"
  "v_mfma_f32_16x16x32_bf16 a[136:139], a[228:231], v[232:235], a[136:139]\n\t"
  "s_add_u32 s74, s74, 0x80000\n\t"
  "ds_read_b64_tr_b16 a[248:249], v33 offset:24608\n\t"
  "v_mfma_f32_16x16x32_bf16 a[140:143], a[228:231], v[236:239], a[140:143]\n\t"
  "s_waitcnt lgkmcnt(5)\n\t"
  "ds_read_b64_tr_b16 a[250:251], v33 offset:26400\n\t"
  "v_mfma_f32_16x16x32_bf16 a[144:147], a[232:235], v[232:235], a[144:147]\n\t"
  "ds_read_b64_tr_b16 a[252:253], v33 offset:24640\n\t"
  "ds_read_b64_tr_b16 a[254:255], v33 offset:26432\n\t"
  "v_mfma_f32_16x16x32_bf16 a[148:151], a[232:235], v[236:239], a[148:151]\n\t"
  "v_mfma_f32_16x16x32_bf16 a[152:155], a[236:239], v[232:235], a[152:155]\n\t"
  "ds_read_b64_tr_b16 a[224:225], v33 offset:24672\n\t"
  "ds_read_b64_tr_b16 a[226:227], v33 offset:26464\n\t"
  "v_mfma_f32_16x16x32_bf16 a[156:159], a[236:239], v[236:239], a[156:159]\n\t"
  "s_waitcnt lgkmcnt(6)\n\t"
  "v_mfma_f32_16x16x32_bf16 a[160:163], a[240:243], v[232:235], a[160:163]\n\t"
  "ds_read_b64_tr_b16 a[228:229], v33 offset:24704\n\t"
  "ds_read_b64_tr_b16 a[230:231], v33 offset:26496\n\t"
  "v_mfma_f32_16x16x32_bf16 a[164:167], a[240:243], v[236:239], a[164:167]\n\t"
  "global_load_dwordx4 v[112:115], v40, s[96:97] offset:0\n\t"
  "v_mfma_f32_16x16x32_bf16 a[168:171], a[244:247], v[232:235], a[168:171]\n\t"
  "ds_read_b64_tr_b16 a[232:233], v33 offset:24736\n\t"
  "v_mfma_f32_16x16x32_bf16 a[172:175], a[244:247], v[236:239], a[172:175]\n\t"
  "s_waitcnt lgkmcnt(5)\n\t"
  "ds_read_b64_tr_b16 a[234:235], v33 offset:26528\n\t"
  "v_mfma_f32_16x16x32_bf16 a[176:179], a[248:251], v[232:235], a[176:179]\n\t"
  "s_waitcnt vmcnt(12)\n\t"
  "ds_read_b64_tr_b16 a[236:237], v33 offset:24768\n\t"
  "ds_read_b64_tr_b16 a[238:239], v33 offset:26560\n\t"
  "v_mfma_f32_16x16x32_bf16 a[180:183], a[248:251], v[236:239], a[180:183]\n\t"
  "s_barrier\n\t"
  "global_load_dwordx4 v[116:119], v40, s[96:97] offset:1024\n\t"
  "v_mfma_f32_16x16x32_bf16 a[184:187], a[252:255], v[232:235], a[184:187]\n\t"
  "ds_read_b64_tr_b16 a[240:241], v33 offset:32768\n\t"
  "v_mfma_f32_16x16x32_bf16 a[188:191], a[252:255], v[236:239], a[188:191]\n\t"
  "s_waitcnt lgkmcnt(5)\n\t"
  "ds_read_b64_tr_b16 a[242:243], v33 offset:34560\n\t"
  "v_mfma_f32_16x16x32_bf16 a[192:195], a[224:227], v[232:235], a[192:195]\n\t"
  "ds_read_b64_tr_b16 a[244:245], v33 offset:32800\n\t"
  "ds_read_b64_tr_b16 a[246:247], v33 offset:34592\n\t"
  "v_mfma_f32_16x16x32_bf16 a[196:199], a[224:227], v[236:239], a[196:199]\n\t"
  "buffer_store_dwordx4 v[96:99], v40, s[56:59], s52 offen offset:0 sc1\n\t"
  "v_mfma_f32_16x16x32_bf16 a[200:203], a[228:231], v[232:235], a[200:203]\n\t"
  "ds_read_b64_tr_b16 a[248:249], v33 offset:32832\n\t"
  "v_mfma_f32_16x16x32_bf16 a[204:207], a[228:231], v[236:239], a[204:207]\n\t"
  "s_waitcnt lgkmcnt(5)\n\t"
  "ds_read_b64_tr_b16 a[250:251], v33 offset:34624\n\t"
  "v_mfma_f32_16x16x32_bf16 a[208:211], a[232:235], v[232:235], a[208:211]\n\t"
  "ds_read_b64_tr_b16 a[252:253], v33 offset:32864\n\t"
  "ds_read_b64_tr_b16 a[254:255], v33 offset:34656\n\t"
  "v_mfma_f32_16x16x32_bf16 a[212:215], a[232:235], v[236:239], a[212:215]\n\t"
  "buffer_store_dwordx4 v[100:103], v23, s[56:59], s52 offen offset:1024 sc1\n\t"
  "v_mfma_f32_16x16x32_bf16 a[216:219], a[236:239], v[232:235], a[216:219]\n\t"
  "ds_read_b64_tr_b16 a[224:225], v33 offset:32896\n\t"
  "v_mfma_f32_16x16x32_bf16 a[220:223], a[236:239], v[236:239], a[220:223]\n\t"
  "s_waitcnt lgkmcnt(5)\n\t"
  "ds_read_b64_tr_b16 a[226:227], v33 offset:34688\n\t"
  "v_mfma_f32_16x16x32_bf16 a[0:3], a[240:243], v[128:131], 0\n\t"
  "ds_read_b64_tr_b16 a[228:229], v33 offset:32928\n\t"
  "ds_read_b64_tr_b16 a[230:231], v33 offset:34720\n\t"
  "v_mfma_f32_16x16x32_bf16 a[4:7], a[240:243], v[132:135], 0\n\t"
  "v_accvgpr_read_b32 v120, a112\n\t"
  "v_accvgpr_read_b32 v121, a168\n\t"
  "v_mfma_f32_16x16x32_bf16 a[8:11], a[244:247], v[128:131], 0\n\t"
  "s_add_u32 m0, s84, 0x8000\n\t"
  "ds_read_b64_tr_b16 a[232:233], v33 offset:32960\n\t"
  "ds_read_b64_tr_b16 a[234:235], v33 offset:34752\n\t"
  "v_mfma_f32_16x16x32_bf16 a[12:15], a[244:247], v[132:135], 0\n\t"
  "s_waitcnt lgkmcnt(6)\n\t"
  "v_accvgpr_read_b32 v122, a113\n\t"
  "v_accvgpr_read_b32 v123, a169\n\t"
  "v_mfma_f32_16x16x32_bf16 a[16:19], a[248:251], v[128:131], 0\n\t"
  "ds_read_b64_tr_b16 a[236:237], v35 offset:39936\n\t"
  "ds_read_b64_tr_b16 a[238:239], v35 offset:42240\n\t"
  "v_mfma_f32_16x16x32_bf16 a[20:23], a[248:251], v[132:135], 0\n\t"
  "buffer_load_dwordx4 v16, s[92:95], s74 offen offset:0 lds\n\t"
  "v_mfma_f32_16x16x32_bf16 a[24:27], a[252:255], v[128:131], 0\n\t"
  "ds_read_b64_tr_b16 a[240:241], v35 offset:39968\n\t"
  "v_mul_f32 v120, v120, v121\n\t"
  "v_mfma_f32_16x16x32_bf16 a[28:31], a[252:255], v[132:135], 0\n\t"
  "s_waitcnt lgkmcnt(5)\n\t"
  "ds_read_b64_tr_b16 a[242:243], v35 offset:42272\n\t"
  "v_mul_f32 v122, v122, v123\n\t"
  "v_mfma_f32_16x16x32_bf16 a[32:35], a[224:227], v[128:131], 0\n\t"
  "ds_read_b64_tr_b16 a[244:245], v35 offset:40000\n\t"
  "ds_read_b64_tr_b16 a[246:247], v35 offset:42304\n\t"
  "v_mfma_f32_16x16x32_bf16 a[36:39], a[224:227], v[132:135], 0\n\t"
  "buffer_load_dwordx4 v17, s[92:95], s74 offen offset:1024 lds\n\t"
  "v_mfma_f32_16x16x32_bf16 a[40:43], a[228:231], v[128:131], 0\n\t"
  "ds_read_b64_tr_b16 a[248:249], v35 offset:40032\n\t"
  "v_cvt_pk_bf16_f32 v120, v120, v122\n\t"
  "v_mfma_f32_16x16x32_bf16 a[44:47], a[228:231], v[132:135], 0\n\t"
  "s_waitcnt lgkmcnt(5)\n\t"
  "ds_read_b64_tr_b16 a[250:251], v35 offset:42336\n\t"
  "v_accvgpr_write_b32 a168, v120\n\t"
  "v_mfma_f32_16x16x32_bf16 a[48:51], a[232:235], v[128:131], 0\n\t"
  "ds_read_b64_tr_b16 a[252:253], v35 offset:40064\n\t"
  "ds_read_b64_tr_b16 a[254:255], v35 offset:42368\n\t"
  "v_mfma_f32_16x16x32_bf16 a[52:55], a[232:235], v[132:135], 0\n\t"
  "buffer_load_dwordx4 v18, s[92:95], s74 offen offset:2048 lds\n\t"
  "v_mfma_f32_16x16x32_bf16 a[56:59], a[236:239], v[128:131], 0\n\t"
  "ds_read_b64_tr_b16 a[224:225], v35 offset:40096\n\t"
  "v_accvgpr_read_b32 v124, a114\n\t"
  "v_mfma_f32_16x16x32_bf16 a[60:63], a[236:239], v[132:135], 0\n\t"
  "s_waitcnt lgkmcnt(5)\n\t"
  "ds_read_b64_tr_b16 a[226:227], v35 offset:42400\n\t"
  "v_accvgpr_read_b32 v125, a170\n\t"
  "v_mfma_f32_16x16x32_bf16 v[64:67], a[240:243], v[128:131], 0\n\t"
  "ds_read_b64_tr_b16 a[228:229], v35 offset:40128\n\t"
  "ds_read_b64_tr_b16 a[230:231], v35 offset:42432\n\t"
  "v_mfma_f32_16x16x32_bf16 v[68:71], a[240:243], v[132:135], 0\n\t"
  "buffer_load_dwordx4 v19, s[92:95], s74 offen offset:3072 lds\n\t"
  "v_mfma_f32_16x16x32_bf16 v[72:75], a[244:247], v[128:131], 0\n\t"
  "s_add_u32 m0, s84, 0x9000\n\t"
  "ds_read_b64_tr_b16 a[232:233], v35 offset:40160\n\t"
  "v_accvgpr_read_b32 v126, a115\n\t"
  "v_mfma_f32_16x16x32_bf16 v[76:79], a[244:247], v[132:135], 0\n\t"
  "s_waitcnt lgkmcnt(5)\n\t"
  "ds_read_b64_tr_b16 a[234:235], v35 offset:42464\n\t"
  "v_accvgpr_read_b32 v127, a171\n\t"
  "v_mfma_f32_16x16x32_bf16 v[80:83], a[248:251], v[128:131], 0\n\t"
  "ds_read_b64_tr_b16 a[236:237], v35 offset:40192\n\t"
  "ds_read_b64_tr_b16 a[238:239], v35 offset:42496\n\t"
  "v_mfma_f32_16x16x32_bf16 v[84:87], a[248:251], v[132:135], 0\n\t"
  "buffer_load_dwordx4 v36, s[92:95], s74 offen offset:0 lds\n\t"
  "v_mfma_f32_16x16x32_bf16 v[88:91], a[252:255], v[128:131], 0\n\t"
  "ds_read_b64_tr_b16 a[240:241], v33 offset:49152\n\t"
  "v_mul_f32 v124, v124, v125\n\t"
  "v_mfma_f32_16x16x32_bf16 v[92:95], a[252:255], v[132:135], 0\n\t"
  "s_waitcnt lgkmcnt(5)\n\t"
  "ds_read_b64_tr_b16 a[242:243], v33 offset:50944\n\t"
  "v_mul_f32 v126, v126, v127\n\t"
  "v_mfma_f32_16x16x32_bf16 v[96:99], a[224:227], v[128:131], 0\n\t"
  "ds_read_b64_tr_b16 a[244:245], v33 offset:49184\n\t"
  "ds_read_b64_tr_b16 a[246:247], v33 offset:50976\n\t"
  "v_mfma_f32_16x16x32_bf16 v[100:103], a[224:227], v[132:135], 0\n\t"
  "buffer_load_dwordx4 v37, s[92:95], s74 offen offset:1024 lds\n\t"
  "v_mfma_f32_16x16x32_bf16 v[104:107], a[228:231], v[128:131], 0\n\t"
  "ds_read_b64_tr_b16 a[248:249], v33 offset:49216\n\t"
  "v_cvt_pk_bf16_f32 v124, v124, v126\n\t"
  "v_mfma_f32_16x16x32_bf16 v[108:111], a[228:231], v[132:135], 0\n\t"
  "s_waitcnt lgkmcnt(5)\n\t"
  "ds_read_b64_tr_b16 a[250:251], v33 offset:51008\n\t"
  "v_accvgpr_write_b32 a169, v124\n\t"
  "v_mfma_f32_16x16x32_bf16 v[240:243], a[232:235], v[128:131], 0\n\t"
  "ds_read_b64_tr_b16 a[252:253], v33 offset:49248\n\t"
  "ds_read_b64_tr_b16 a[254:255], v33 offset:51040\n\t"
  "v_mfma_f32_16x16x32_bf16 v[244:247], a[232:235], v[132:135], 0\n\t"
  "buffer_load_dwordx4 v38, s[92:95], s74 offen offset:2048 lds\n\t"
  "v_mfma_f32_16x16x32_bf16 v[248:251], a[236:239], v[128:131], 0\n\t"
  "ds_read_b64_tr_b16 a[224:225], v33 offset:49280\n\t"
  "v_accvgpr_read_b32 v120, a120\n\t"
  "v_mfma_f32_16x16x32_bf16 v[252:255], a[236:239], v[132:135], 0\n\t"
  "s_waitcnt lgkmcnt(5)\n\t"
  "ds_read_b64_tr_b16 a[226:227], v33 offset:51072\n\t"
  "v_accvgpr_read_b32 v121, a176\n\t"
  "v_mfma_f32_16x16x32_bf16 a[0:3], a[240:243], v[136:139], a[0:3]\n\t"
  "ds_read_b64_tr_b16 a[228:229], v33 offset:49312\n\t"
  "ds_read_b64_tr_b16 a[230:231], v33 offset:51104\n\t"
  "v_mfma_f32_16x16x32_bf16 a[4:7], a[240:243], v[140:143], a[4:7]\n\t"
  "buffer_load_dwordx4 v39, s[92:95], s74 offen offset:3072 lds\n\t"
  "v_mfma_f32_16x16x32_bf16 a[8:11], a[244:247], v[136:139], a[8:11]\n\t"
  "s_add_u32 s74, s74, 0x80000\n\t"
  "ds_read_b64_tr_b16 a[232:233], v33 offset:49344\n\t"
  "v_accvgpr_read_b32 v122, a121\n\t"
  "v_mfma_f32_16x16x32_bf16 a[12:15], a[244:247], v[140:143], a[12:15]\n\t"
  "s_waitcnt lgkmcnt(5)\n\t"
  "ds_read_b64_tr_b16 a[234:235], v33 offset:51136\n\t"
  "v_accvgpr_read_b32 v123, a177\n\t"
  "v_mfma_f32_16x16x32_bf16 a[16:19], a[248:251], v[136:139], a[16:19]\n\t"
  "ds_read_b64_tr_b16 a[236:237], v35 offset:56320\n\t"
  "ds_read_b64_tr_b16 a[238:239], v35 offset:58624\n\t"
  "v_mfma_f32_16x16x32_bf16 a[20:23], a[248:251], v[140:143], a[20:23]\n\t"
  "v_mul_f32 v120, v120, v121\n\t"
  "v_mul_f32 v122, v122, v123\n\t"
  "s_cbranch_execz TRAMP_%=\n\t"
  "v_mfma_f32_16x16x32_bf16 a[24:27], a[252:255], v[136:139], a[24:27]\n\t"
  "ds_read_b64_tr_b16 a[240:241], v35 offset:56352\n\t"
  "ds_read_b64_tr_b16 a[242:243], v35 offset:58656\n\t"
  "v_mfma_f32_16x16x32_bf16 a[28:31], a[252:255], v[140:143], a[28:31]\n\t"
  "s_waitcnt lgkmcnt(6)\n\t"
  "v_cvt_pk_bf16_f32 v120, v120, v122\n\t"
  "v_accvgpr_read_b32 v124, a122\n\t"
  "v_mfma_f32_16x16x32_bf16 a[32:35], a[224:227], v[136:139], a[32:35]\n\t"
  "ds_read_b64_tr_b16 a[244:245], v35 offset:56384\n\t"
  "ds_read_b64_tr_b16 a[246:247], v35 offset:58688\n\t"
  "v_mfma_f32_16x16x32_bf16 a[36:39], a[224:227], v[140:143], a[36:39]\n\t"
  "v_accvgpr_write_b32 a170, v120\n\t"
  "v_accvgpr_read_b32 v125, a178\n\t"
  "v_mfma_f32_16x16x32_bf16 a[40:43], a[228:231], v[136:139], a[40:43]\n\t"
  "ds_read_b64_tr_b16 a[248:249], v35 offset:56416\n\t"
  "ds_read_b64_tr_b16 a[250:251], v35 offset:58720\n\t"
  "v_mfma_f32_16x16x32_bf16 a[44:47], a[228:231], v[140:143], a[44:47]\n\t"
  "s_waitcnt lgkmcnt(6)\n\t"
  "v_accvgpr_read_b32 v126, a123\n\t"
  "v_accvgpr_read_b32 v127, a179\n\t"
  "v_mfma_f32_16x16x32_bf16 a[48:51], a[232:235], v[136:139], a[48:51]\n\t"
  "ds_read_b64_tr_b16 a[252:253], v35 offset:56448\n\t"
  "ds_read_b64_tr_b16 a[254:255], v35 offset:58752\n\t"
  "v_mfma_f32_16x16x32_bf16 a[52:55], a[232:235], v[140:143], a[52:55]\n\t"
  "v_mul_f32 v124, v124, v125\n\t"
  "v_mul_f32 v126, v126, v127\n\t"
  "v_mfma_f32_16x16x32_bf16 a[56:59], a[236:239], v[136:139], a[56:59]\n\t"
  "s_add_u32 m0, s84, 0x20000\n\t"
  "ds_read_b64_tr_b16 a[224:225], v35 offset:56480\n\t"
  "ds_read_b64_tr_b16 a[226:227], v35 offset:58784\n\t"
  "v_mfma_f32_16x16x32_bf16 a[60:63], a[236:239], v[140:143], a[60:63]\n\t"
  "s_waitcnt lgkmcnt(6)\n\t"
  "v_cvt_pk_bf16_f32 v124, v124, v126\n\t"
  "v_accvgpr_read_b32 v120, a116\n\t"
  "v_mfma_f32_16x16x32_bf16 v[64:67], a[240:243], v[136:139], v[64:67]\n\t"
  "s_mov_b32 s32, 0x1000\n\t"
  "ds_read_b64_tr_b16 a[228:229], v35 offset:56512\n\t"
  "ds_read_b64_tr_b16 a[230:231], v35 offset:58816\n\t"
  "v_mfma_f32_16x16x32_bf16 v[68:71], a[240:243], v[140:143], v[68:71]\n\t"
  "buffer_load_dwordx4 v27, s[44:47], s32 offen offset:0 lds\n\t"
  "v_mfma_f32_16x16x32_bf16 v[72:75], a[244:247], v[136:139], v[72:75]\n\t"
  "s_add_u32 m0, s84, 0x20000\n\t"
  "ds_read_b64_tr_b16 a[232:233], v35 offset:56544\n\t"
  "v_accvgpr_write_b32 a171, v124\n\t"
  "v_mfma_f32_16x16x32_bf16 v[76:79], a[244:247], v[140:143], v[76:79]\n\t"
  "s_waitcnt lgkmcnt(5)\n\t"
  "ds_read_b64_tr_b16 a[234:235], v35 offset:58848\n\t"
  "v_accvgpr_read_b32 v121, a172\n\t"
  "v_mfma_f32_16x16x32_bf16 v[80:83], a[248:251], v[136:139], v[80:83]\n\t"
  "s_mov_b32 s33, 0x2c00\n\t"
  "ds_read_b64_tr_b16 a[236:237], v35 offset:56576\n\t"
  "ds_read_b64_tr_b16 a[238:239], v35 offset:58880\n\t"
  "v_mfma_f32_16x16x32_bf16 v[84:87], a[248:251], v[140:143], v[84:87]\n\t"
  "s_waitcnt vmcnt(13)\n\t"
  "buffer_load_dwordx4 v27, s[44:47], s33 offen offset:1024 lds\n\t"
  "v_mfma_f32_16x16x32_bf16 v[88:91], a[252:255], v[136:139], v[88:91]\n\t"
  "s_add_u32 m0, s84, 0x20000\n\t"
  "v_accvgpr_read_b32 v122, a117\n\t"
  "v_accvgpr_read_b32 v123, a173\n\t"
  "v_mfma_f32_16x16x32_bf16 v[92:95], a[252:255], v[140:143], v[92:95]\n\t"
  "s_waitcnt lgkmcnt(4)\n\t"
  "v_mul_f32 v120, v120, v121\n\t"
  "v_mul_f32 v122, v122, v123\n\t"
  "v_mfma_f32_16x16x32_bf16 v[96:99], a[224:227], v[136:139], v[96:99]\n\t"
  "s_mov_b32 s32, 0x840\n\t"
  "v_cvt_pk_bf16_f32 v120, v120, v122\n\t"
  "v_accvgpr_read_b32 v124, a118\n\t"
  "v_mfma_f32_16x16x32_bf16 v[100:103], a[224:227], v[140:143], v[100:103]\n\t"
  "s_barrier\n\t"
  "buffer_load_dwordx4 v27, s[44:47], s32 offen offset:2048 lds\n\t"
  "v_mfma_f32_16x16x32_bf16 v[104:107], a[228:231], v[136:139], v[104:107]\n\t"
  "s_add_u32 m0, s84, 0x20000\n\t"
  "ds_read_b64_tr_b16 a[240:241], v32 offset:0\n\t"
  "v_accvgpr_write_b32 a172, v120\n\t"
  "v_mfma_f32_16x16x32_bf16 v[108:111], a[228:231], v[140:143], v[108:111]\n\t"
  "s_waitcnt lgkmcnt(1)\n\t"
  "ds_read_b64_tr_b16 a[242:243], v32 offset:1792\n\t"
  "ds_read_b64_tr_b16 a[244:245], v32 offset:32\n\t"
  "buffer_store_dwordx2 v[8:9], v12, s[56:59], s40 offen offset:0 sc1\n\t"
  "v_mfma_f32_16x16x32_bf16 v[240:243], a[232:235], v[136:139], v[240:243]\n\t"
  "s_mov_b32 s33, 0x2440\n\t"
  "ds_read_b64_tr_b16 a[246:247], v32 offset:1824\n\t"
  "ds_read_b64_tr_b16 a[248:249], v32 offset:64\n\t"
  "v_mfma_f32_16x16x32_bf16 v[244:247], a[232:235], v[140:143], v[244:247]\n\t"
  "buffer_load_dwordx4 v27, s[44:47], s33 offen offset:3072 lds\n\t"
  "v_mfma_f32_16x16x32_bf16 v[248:251], a[236:239], v[136:139], v[248:251]\n\t"
  "ds_read_b64_tr_b16 a[250:251], v32 offset:1856\n\t"
  "v_accvgpr_read_b32 v125, a174\n\t"
  "v_mfma_f32_16x16x32_bf16 v[252:255], a[236:239], v[140:143], v[252:255]\n\t"
  "s_waitcnt lgkmcnt(2)\n\t"
  "ds_read_b64_tr_b16 a[252:253], v32 offset:96\n\t"
  "ds_read_b64_tr_b16 a[254:255], v32 offset:1888\n\t"
  "v_mfma_f32_16x16x32_bf16 a[0:3], a[240:243], v[144:147], a[0:3]\n\t"
  "ds_read_b64_tr_b16 a[224:225], v32 offset:128\n\t"
  "ds_read_b64_tr_b16 a[226:227], v32 offset:1920\n\t"
  "v_mfma_f32_16x16x32_bf16 a[4:7], a[240:243], v[148:151], a[4:7]\n\t"
  "ds_read_b64_tr_b16 a[228:229], v32 offset:160\n\t"
  "ds_read_b64_tr_b16 a[230:231], v32 offset:1952\n\t"
  "v_mfma_f32_16x16x32_bf16 a[8:11], a[244:247], v[144:147], a[8:11]\n\t"
  "s_add_u32 m0, s84, 0x10000\n\t"
  "ds_read_b64_tr_b16 a[232:233], v32 offset:192\n\t"
  "ds_read_b64_tr_b16 a[234:235], v32 offset:1984\n\t"
  "v_mfma_f32_16x16x32_bf16 a[12:15], a[244:247], v[148:151], a[12:15]\n\t"
  "s_waitcnt lgkmcnt(6)\n\t"
  "v_accvgpr_read_b32 v126, a119\n\t"
  "v_accvgpr_read_b32 v127, a175\n\t"
  "v_mfma_f32_16x16x32_bf16 a[16:19], a[248:251], v[144:147], a[16:19]\n\t"
  "ds_read_b64_tr_b16 a[236:237], v34 offset:7168\n\t"
  "ds_read_b64_tr_b16 a[238:239], v34 offset:9472\n\t"
  "v_mfma_f32_16x16x32_bf16 a[20:23], a[248:251], v[148:151], a[20:23]\n\t"
  "buffer_load_dwordx4 v16, s[92:95], s74 offen offset:0 lds\n\t"
  "v_mfma_f32_16x16x32_bf16 a[24:27], a[252:255], v[144:147], a[24:27]\n\t"
  "ds_read_b64_tr_b16 a[240:241], v34 offset:7200\n\t"
  "v_mul_f32 v124, v124, v125\n\t"
  "v_mfma_f32_16x16x32_bf16 a[28:31], a[252:255], v[148:151], a[28:31]\n\t"
  "s_waitcnt lgkmcnt(5)\n\t"
  "ds_read_b64_tr_b16 a[242:243], v34 offset:9504\n\t"
  "v_mul_f32 v126, v126, v127\n\t"
  "v_mfma_f32_16x16x32_bf16 a[32:35], a[224:227], v[144:147], a[32:35]\n\t"
  "ds_read_b64_tr_b16 a[244:245], v34 offset:7232\n\t"
  "ds_read_b64_tr_b16 a[246:247], v34 offset:9536\n\t"
  "v_mfma_f32_16x16x32_bf16 a[36:39], a[224:227], v[148:151], a[36:39]\n\t"
  "buffer_load_dwordx4 v17, s[92:95], s74 offen offset:1024 lds\n\t"
  "v_mfma_f32_16x16x32_bf16 a[40:43], a[228:231], v[144:147], a[40:43]\n\t"
  "ds_read_b64_tr_b16 a[248:249], v34 offset:7264\n\t"
  "v_cvt_pk_bf16_f32 v124, v124, v126\n\t"
  "v_mfma_f32_16x16x32_bf16 a[44:47], a[228:231], v[148:151], a[44:47]\n\t"
  "s_waitcnt lgkmcnt(5)\n\t"
  "ds_read_b64_tr_b16 a[250:251], v34 offset:9568\n\t"
  "v_accvgpr_write_b32 a173, v124\n\t"
  "v_mfma_f32_16x16x32_bf16 a[48:51], a[232:235], v[144:147], a[48:51]\n\t"
  "ds_read_b64_tr_b16 a[252:253], v34 offset:7296\n\t"
  "ds_read_b64_tr_b16 a[254:255], v34 offset:9600\n\t"
  "v_mfma_f32_16x16x32_bf16 a[52:55], a[232:235], v[148:151], a[52:55]\n\t"
  "buffer_load_dwordx4 v18, s[92:95], s74 offen offset:2048 lds\n\t"
  "v_mfma_f32_16x16x32_bf16 a[56:59], a[236:239], v[144:147], a[56:59]\n\t"
  "ds_read_b64_tr_b16 a[224:225], v34 offset:7328\n\t"
  "v_accvgpr_read_b32 v120, a124\n\t"
  "v_mfma_f32_16x16x32_bf16 a[60:63], a[236:239], v[148:151], a[60:63]\n\t"
  "s_waitcnt lgkmcnt(5)\n\t"
  "ds_read_b64_tr_b16 a[226:227], v34 offset:9632\n\t"
  "v_accvgpr_read_b32 v121, a180\n\t"
  "v_mfma_f32_16x16x32_bf16 v[64:67], a[240:243], v[144:147], v[64:67]\n\t"
  "ds_read_b64_tr_b16 a[228:229], v34 offset:7360\n\t"
  "ds_read_b64_tr_b16 a[230:231], v34 offset:9664\n\t"
  "v_mfma_f32_16x16x32_bf16 v[68:71], a[240:243], v[148:151], v[68:71]\n\t"
  "buffer_load_dwordx4 v19, s[92:95], s74 offen offset:3072 lds\n\t"
  "v_mfma_f32_16x16x32_bf16 v[72:75], a[244:247], v[144:147], v[72:75]\n\t"
  "s_add_u32 m0, s84, 0x11000\n\t"
  "ds_read_b64_tr_b16 a[232:233], v34 offset:7392\n\t"
  "v_accvgpr_read_b32 v122, a125\n\t"
  "v_mfma_f32_16x16x32_bf16 v[76:79], a[244:247], v[148:151], v[76:79]\n\t"
  "s_waitcnt lgkmcnt(5)\n\t"
  "ds_read_b64_tr_b16 a[234:235], v34 offset:9696\n\t"
  "v_accvgpr_read_b32 v123, a181\n\t"
  "v_mfma_f32_16x16x32_bf16 v[80:83], a[248:251], v[144:147], v[80:83]\n\t"
  "ds_read_b64_tr_b16 a[236:237], v34 offset:7424\n\t"
  "ds_read_b64_tr_b16 a[238:239], v34 offset:9728\n\t"
  "v_mfma_f32_16x16x32_bf16 v[84:87], a[248:251], v[148:151], v[84:87]\n\t"
  "buffer_load_dwordx4 v36, s[92:95], s74 offen offset:0 lds\n\t"
  "v_mfma_f32_16x16x32_bf16 v[88:91], a[252:255], v[144:147], v[88:91]\n\t"
  "ds_read_b64_tr_b16 a[240:241], v32 offset:16384\n\t"
  "v_mul_f32 v120, v120, v121\n\t"
  "v_mfma_f32_16x16x32_bf16 v[92:95], a[252:255], v[148:151], v[92:95]\n\t"
  "s_waitcnt lgkmcnt(5)\n\t"
  "ds_read_b64_tr_b16 a[242:243], v32 offset:18176\n\t"
  "v_mul_f32 v122, v122, v123\n\t"
  "v_mfma_f32_16x16x32_bf16 v[96:99], a[224:227], v[144:147], v[96:99]\n\t"
  "ds_read_b64_tr_b16 a[244:245], v32 offset:16416\n\t"
  "ds_read_b64_tr_b16 a[246:247], v32 offset:18208\n\t"
  "v_mfma_f32_16x16x32_bf16 v[100:103], a[224:227], v[148:151], v[100:103]\n\t"
  "buffer_load_dwordx4 v37, s[92:95], s74 offen offset:1024 lds\n\t"
  "v_mfma_f32_16x16x32_bf16 v[104:107], a[228:231], v[144:147], v[104:107]\n\t"
  "ds_read_b64_tr_b16 a[248:249], v32 offset:16448\n\t"
  "v_cvt_pk_bf16_f32 v120, v120, v122\n\t"
  "v_mfma_f32_16x16x32_bf16 v[108:111], a[228:231], v[148:151], v[108:111]\n\t"
  "s_waitcnt lgkmcnt(5)\n\t"
  "ds_read_b64_tr_b16 a[250:251], v32 offset:18240\n\t"
  "v_accvgpr_write_b32 a174, v120\n\t"
  "v_mfma_f32_16x16x32_bf16 v[240:243], a[232:235], v[144:147], v[240:243]\n\t"
  "ds_read_b64_tr_b16 a[252:253], v32 offset:16480\n\t"
  "ds_read_b64_tr_b16 a[254:255], v32 offset:18272\n\t"
  "v_mfma_f32_16x16x32_bf16 v[244:247], a[232:235], v[148:151], v[244:247]\n\t"
  "buffer_load_dwordx4 v38, s[92:95], s74 offen offset:2048 lds\n\t"
  "v_mfma_f32_16x16x32_bf16 v[248:251], a[236:239], v[144:147], v[248:251]\n\t"
  "ds_read_b64_tr_b16 a[224:225], v32 offset:16512\n\t"
  "v_accvgpr_read_b32 v124, a126\n\t"
  "v_mfma_f32_16x16x32_bf16 v[252:255], a[236:239], v[148:151], v[252:255]\n\t"
  "s_waitcnt lgkmcnt(5)\n\t"
  "ds_read_b64_tr_b16 a[226:227], v32 offset:18304\n\t"
  "v_accvgpr_read_b32 v125, a182\n\t"
  "v_mfma_f32_16x16x32_bf16 a[0:3], a[240:243], v[152:155], a[0:3]\n\t"
  "ds_read_b64_tr_b16 a[228:229], v32 offset:16544\n\t"
  "ds_read_b64_tr_b16 a[230:231], v32 offset:18336\n\t"
  "v_mfma_f32_16x16x32_bf16 a[4:7], a[240:243], v[156:159], a[4:7]\n\t"
  "buffer_load_dwordx4 v39, s[92:95], s74 offen offset:3072 lds\n\t"
  "v_mfma_f32_16x16x32_bf16 a[8:11], a[244:247], v[152:155], a[8:11]\n\t"
  "s_add_u32 s74, s74, 0x80000\n\t"
  "ds_read_b64_tr_b16 a[232:233], v32 offset:16576\n\t"
  "v_accvgpr_read_b32 v126, a127\n\t"
  "v_mfma_f32_16x16x32_bf16 a[12:15], a[244:247], v[156:159], a[12:15]\n\t"
  "s_waitcnt lgkmcnt(5)\n\t"
  "ds_read_b64_tr_b16 a[234:235], v32 offset:18368\n\t"
  "v_accvgpr_read_b32 v127, a183\n\t"
  "v_mfma_f32_16x16x32_bf16 a[16:19], a[248:251], v[152:155], a[16:19]\n\t"
  "ds_read_b64_tr_b16 a[236:237], v34 offset:23552\n\t"
  "ds_read_b64_tr_b16 a[238:239], v34 offset:25856\n\t"
  "v_mfma_f32_16x16x32_bf16 a[20:23], a[248:251], v[156:159], a[20:23]\n\t"
  "v_mul_f32 v124, v124, v125\n\t"
  "v_mul_f32 v126, v126, v127\n\t"
  "v_mfma_f32_16x16x32_bf16 a[24:27], a[252:255], v[152:155], a[24:27]\n\t"
  "ds_read_b64_tr_b16 a[240:241], v34 offset:23584\n\t"
  "ds_read_b64_tr_b16 a[242:243], v34 offset:25888\n\t"
  "v_mfma_f32_16x16x32_bf16 a[28:31], a[252:255], v[156:159], a[28:31]\n\t"
  "s_waitcnt lgkmcnt(6)\n\t"
  "v_cvt_pk_bf16_f32 v124, v124, v126\n\t"
  "v_accvgpr_read_b32 v120, a128\n\t"
  "v_mfma_f32_16x16x32_bf16 a[32:35], a[224:227], v[152:155], a[32:35]\n\t"
  "ds_read_b64_tr_b16 a[244:245], v34 offset:23616\n\t"
  "ds_read_b64_tr_b16 a[246:247], v34 offset:25920\n\t"
  "v_mfma_f32_16x16x32_bf16 a[36:39], a[224:227], v[156:159], a[36:39]\n\t"
  "v_accvgpr_write_b32 a175, v124\n\t"
  "v_accvgpr_read_b32 v121, a184\n\t"
  "v_mfma_f32_16x16x32_bf16 a[40:43], a[228:231], v[152:155], a[40:43]\n\t"
  "ds_read_b64_tr_b16 a[248:249], v34 offset:23648\n\t"
  "ds_read_b64_tr_b16 a[250:251], v34 offset:25952\n\t"
  "v_mfma_f32_16x16x32_bf16 a[44:47], a[228:231], v[156:159], a[44:47]\n\t"
  "s_waitcnt lgkmcnt(6)\n\t"
  "v_accvgpr_read_b32 v122, a129\n\t"
  "v_accvgpr_read_b32 v123, a185\n\t"
  "v_mfma_f32_16x16x32_bf16 a[48:51], a[232:235], v[152:155], a[48:51]\n\t"
  "ds_read_b64_tr_b16 a[252:253], v34 offset:23680\n\t"
  "ds_read_b64_tr_b16 a[254:255], v34 offset:25984\n\t"
  "v_mfma_f32_16x16x32_bf16 a[52:55], a[232:235], v[156:159], a[52:55]\n\t"
  "v_mul_f32 v120, v120, v121\n\t"
  "v_mul_f32 v122, v122, v123\n\t"
  "v_mfma_f32_16x16x32_bf16 a[56:59], a[236:239], v[152:155], a[56:59]\n\t"
  "s_add_u32 m0, s84, 0x21000\n\t"
  "ds_read_b64_tr_b16 a[224:225], v34 offset:23712\n\t"
  "ds_read_b64_tr_b16 a[226:227], v34 offset:26016\n\t"
  "v_mfma_f32_16x16x32_bf16 a[60:63], a[236:239], v[156:159], a[60:63]\n\t"
  "s_waitcnt lgkmcnt(6)\n\t"
  "v_cvt_pk_bf16_f32 v120, v120, v122\n\t"
  "v_accvgpr_read_b32 v124, a130\n\t"
  "v_mfma_f32_16x16x32_bf16 v[64:67], a[240:243], v[152:155], v[64:67]\n\t"
  "s_mov_b32 s32, 0x1080\n\t"
  "ds_read_b64_tr_b16 a[228:229], v34 offset:23744\n\t"
  "ds_read_b64_tr_b16 a[230:231], v34 offset:26048\n\t"
  "v_mfma_f32_16x16x32_bf16 v[68:71], a[240:243], v[156:159], v[68:71]\n\t"
  "buffer_load_dwordx4 v27, s[44:47], s32 offen offset:0 lds\n\t"
  "v_mfma_f32_16x16x32_bf16 v[72:75], a[244:247], v[152:155], v[72:75]\n\t"
  "s_add_u32 m0, s84, 0x21000\n\t"
  "ds_read_b64_tr_b16 a[232:233], v34 offset:23776\n\t"
  "v_accvgpr_write_b32 a184, v120\n\t"
  "v_mfma_f32_16x16x32_bf16 v[76:79], a[244:247], v[156:159], v[76:79]\n\t"
  "s_waitcnt lgkmcnt(5)\n\t"
  "ds_read_b64_tr_b16 a[234:235], v34 offset:26080\n\t"
  "v_accvgpr_read_b32 v125, a186\n\t"
  "v_mfma_f32_16x16x32_bf16 v[80:83], a[248:251], v[152:155], v[80:83]\n\t"
  "s_mov_b32 s33, 0x2c80\n\t"
  "ds_read_b64_tr_b16 a[236:237], v34 offset:23808\n\t"
  "ds_read_b64_tr_b16 a[238:239], v34 offset:26112\n\t"
  "v_mfma_f32_16x16x32_bf16 v[84:87], a[248:251], v[156:159], v[84:87]\n\t"
  "s_waitcnt vmcnt(14)\n\t"
  "buffer_load_dwordx4 v27, s[44:47], s33 offen offset:1024 lds\n\t"
  "v_mfma_f32_16x16x32_bf16 v[88:91], a[252:255], v[152:155], v[88:91]\n\t"
  "s_add_u32 m0, s84, 0x21000\n\t"
  "buffer_store_dwordx2 v[8:9], v12, s[56:59], s53 offen offset:0 sc1\n\t"
  "v_mfma_f32_16x16x32_bf16 v[92:95], a[252:255], v[156:159], v[92:95]\n\t"
  "s_waitcnt lgkmcnt(4)\n\t"
  "v_accvgpr_read_b32 v126, a131\n\t"
  "v_accvgpr_read_b32 v127, a187\n\t"
  "v_mfma_f32_16x16x32_bf16 v[96:99], a[224:227], v[152:155], v[96:99]\n\t"
  "s_mov_b32 s32, 0x8c0\n\t"
  "v_mul_f32 v124, v124, v125\n\t"
  "v_mul_f32 v126, v126, v127\n\t"
  "v_mfma_f32_16x16x32_bf16 v[100:103], a[224:227], v[156:159], v[100:103]\n\t"
  "s_barrier\n\t"
  "buffer_load_dwordx4 v27, s[44:47], s32 offen offset:2048 lds\n\t"
  "v_mfma_f32_16x16x32_bf16 v[104:107], a[228:231], v[152:155], v[104:107]\n\t"
  "s_add_u32 m0, s84, 0x21000\n\t"
  "ds_read_b64_tr_b16 a[240:241], v32 offset:32768\n\t"
  "v_cvt_pk_bf16_f32 v124, v124, v126\n\t"
  "v_mfma_f32_16x16x32_bf16 v[108:111], a[228:231], v[156:159], v[108:111]\n\t"
  "s_waitcnt lgkmcnt(1)\n\t"
  "ds_read_b64_tr_b16 a[242:243], v32 offset:34560\n\t"
  "ds_read_b64_tr_b16 a[244:245], v32 offset:32800\n\t"
  "v_mfma_f32_16x16x32_bf16 v[240:243], a[232:235], v[152:155], v[240:243]\n\t"
  "s_mov_b32 s33, 0x24c0\n\t"
  "ds_read_b64_tr_b16 a[246:247], v32 offset:34592\n\t"
  "ds_read_b64_tr_b16 a[248:249], v32 offset:32832\n\t"
  "v_mfma_f32_16x16x32_bf16 v[244:247], a[232:235], v[156:159], v[244:247]\n\t"
  "buffer_load_dwordx4 v27, s[44:47], s33 offen offset:3072 lds\n\t"
  "v_mfma_f32_16x16x32_bf16 v[248:251], a[236:239], v[152:155], v[248:251]\n\t"
  "ds_read_b64_tr_b16 a[250:251], v32 offset:34624\n\t"
  "v_accvgpr_write_b32 a185, v124\n\t"
  "v_mfma_f32_16x16x32_bf16 v[252:255], a[236:239], v[156:159], v[252:255]\n\t"
  "s_waitcnt lgkmcnt(2)\n\t"
  "ds_read_b64_tr_b16 a[252:253], v32 offset:32864\n\t"
  "ds_read_b64_tr_b16 a[254:255], v32 offset:34656\n\t"
  "v_mfma_f32_16x16x32_bf16 a[0:3], a[240:243], v[160:163], a[0:3]\n\t"
  "ds_read_b64_tr_b16 a[224:225], v32 offset:32896\n\t"
  "ds_read_b64_tr_b16 a[226:227], v32 offset:34688\n\t"
  "v_mfma_f32_16x16x32_bf16 a[4:7], a[240:243], v[164:167], a[4:7]\n\t"
  "ds_read_b64_tr_b16 a[228:229], v32 offset:32928\n\t"
  "ds_read_b64_tr_b16 a[230:231], v32 offset:34720\n\t"
  "v_mfma_f32_16x16x32_bf16 a[8:11], a[244:247], v[160:163], a[8:11]\n\t"
  "ds_read_b64_tr_b16 a[232:233], v32 offset:32960\n\t"
  "ds_read_b64_tr_b16 a[234:235], v32 offset:34752\n\t"
  "v_mfma_f32_16x16x32_bf16 a[12:15], a[244:247], v[164:167], a[12:15]\n\t"
  "s_waitcnt lgkmcnt(6)\n\t"
  "v_accvgpr_read_b32 v120, a136\n\t"
  "v_accvgpr_read_b32 v121, a192\n\t"
  "v_mfma_f32_16x16x32_bf16 a[16:19], a[248:251], v[160:163], a[16:19]\n\t"
  "ds_read_b64_tr_b16 a[236:237], v34 offset:39936\n\t"
  "ds_read_b64_tr_b16 a[238:239], v34 offset:42240\n\t"
  "v_mfma_f32_16x16x32_bf16 a[20:23], a[248:251], v[164:167], a[20:23]\n\t"
  "buffer_store_dwordx4 a[168:171], v40, s[56:59], s85 offen offset:0 sc1\n\t"
  "v_mfma_f32_16x16x32_bf16 a[24:27], a[252:255], v[160:163], a[24:27]\n\t"
  "s_add_u32 m0, s84, 0x18000\n\t"
  "ds_read_b64_tr_b16 a[240:241], v34 offset:39968\n\t"
  "v_accvgpr_read_b32 v122, a137\n\t"
  "v_mfma_f32_16x16x32_bf16 a[28:31], a[252:255], v[164:167], a[28:31]\n\t"
  "s_waitcnt lgkmcnt(5)\n\t"
  "ds_read_b64_tr_b16 a[242:243], v34 offset:42272\n\t"
  "v_accvgpr_read_b32 v123, a193\n\t"
  "v_mfma_f32_16x16x32_bf16 a[32:35], a[224:227], v[160:163], a[32:35]\n\t"
  "ds_read_b64_tr_b16 a[244:245], v34 offset:40000\n\t"
  "ds_read_b64_tr_b16 a[246:247], v34 offset:42304\n\t"
  "v_mfma_f32_16x16x32_bf16 a[36:39], a[224:227], v[164:167], a[36:39]\n\t"
  "buffer_load_dwordx4 v16, s[92:95], s74 offen offset:0 lds\n\t"
  "v_mfma_f32_16x16x32_bf16 a[40:43], a[228:231], v[160:163], a[40:43]\n\t"
  "ds_read_b64_tr_b16 a[248:249], v34 offset:40032\n\t"
  "v_mul_f32 v120, v120, v121\n\t"
  "v_mfma_f32_16x16x32_bf16 a[44:47], a[228:231], v[164:167], a[44:47]\n\t"
  "s_waitcnt lgkmcnt(5)\n\t"
  "ds_read_b64_tr_b16 a[250:251], v34 offset:42336\n\t"
  "v_mul_f32 v122, v122, v123\n\t"
  "v_mfma_f32_16x16x32_bf16 a[48:51], a[232:235], v[160:163], a[48:51]\n\t"
  "ds_read_b64_tr_b16 a[252:253], v34 offset:40064\n\t"
  "ds_read_b64_tr_b16 a[254:255], v34 offset:42368\n\t"
  "v_mfma_f32_16x16x32_bf16 a[52:55], a[232:235], v[164:167], a[52:55]\n\t"
  "buffer_load_dwordx4 v17, s[92:95], s74 offen offset:1024 lds\n\t"
  "v_mfma_f32_16x16x32_bf16 a[56:59], a[236:239], v[160:163], a[56:59]\n\t"
  "ds_read_b64_tr_b16 a[224:225], v34 offset:40096\n\t"
  "v_cvt_pk_bf16_f32 v120, v120, v122\n\t"
  "v_mfma_f32_16x16x32_bf16 a[60:63], a[236:239], v[164:167], a[60:63]\n\t"
  "s_waitcnt lgkmcnt(5)\n\t"
  "ds_read_b64_tr_b16 a[226:227], v34 offset:42400\n\t"
  "v_accvgpr_write_b32 a186, v120\n\t"
  "v_mfma_f32_16x16x32_bf16 v[64:67], a[240:243], v[160:163], v[64:67]\n\t"
  "ds_read_b64_tr_b16 a[228:229], v34 offset:40128\n\t"
  "ds_read_b64_tr_b16 a[230:231], v34 offset:42432\n\t"
  "v_mfma_f32_16x16x32_bf16 v[68:71], a[240:243], v[164:167], v[68:71]\n\t"
  "buffer_load_dwordx4 v18, s[92:95], s74 offen offset:2048 lds\n\t"
  "v_mfma_f32_16x16x32_bf16 v[72:75], a[244:247], v[160:163], v[72:75]\n\t"
  "ds_read_b64_tr_b16 a[232:233], v34 offset:40160\n\t"
  "v_accvgpr_read_b32 v124, a138\n\t"
  "v_mfma_f32_16x16x32_bf16 v[76:79], a[244:247], v[164:167], v[76:79]\n\t"
  "s_waitcnt lgkmcnt(5)\n\t"
  "ds_read_b64_tr_b16 a[234:235], v34 offset:42464\n\t"
  "v_accvgpr_read_b32 v125, a194\n\t"
  "v_mfma_f32_16x16x32_bf16 v[80:83], a[248:251], v[160:163], v[80:83]\n\t"
  "ds_read_b64_tr_b16 a[236:237], v34 offset:40192\n\t"
  "ds_read_b64_tr_b16 a[238:239], v34 offset:42496\n\t"
  "v_mfma_f32_16x16x32_bf16 v[84:87], a[248:251], v[164:167], v[84:87]\n\t"
  "buffer_load_dwordx4 v19, s[92:95], s74 offen offset:3072 lds\n\t"
  "v_mfma_f32_16x16x32_bf16 v[88:91], a[252:255], v[160:163], v[88:91]\n\t"
  "ds_read_b64_tr_b16 a[240:241], v32 offset:49152\n\t"
  "v_accvgpr_read_b32 v126, a139\n\t"
  "v_mfma_f32_16x16x32_bf16 v[92:95], a[252:255], v[164:167], v[92:95]\n\t"
  "s_waitcnt lgkmcnt(5)\n\t"
  "ds_read_b64_tr_b16 a[242:243], v32 offset:50944\n\t"
  "v_accvgpr_read_b32 v127, a195\n\t"
  "v_mfma_f32_16x16x32_bf16 v[96:99], a[224:227], v[160:163], v[96:99]\n\t"
  "ds_read_b64_tr_b16 a[244:245], v32 offset:49184\n\t"
  "ds_read_b64_tr_b16 a[246:247], v32 offset:50976\n\t"
  "v_mfma_f32_16x16x32_bf16 v[100:103], a[224:227], v[164:167], v[100:103]\n\t"
  "v_mul_f32 v124, v124, v125\n\t"
  "v_mul_f32 v126, v126, v127\n\t"
  "v_mfma_f32_16x16x32_bf16 v[104:107], a[228:231], v[160:163], v[104:107]\n\t"
  "s_add_u32 m0, s84, 0x19000\n\t"
  "ds_read_b64_tr_b16 a[248:249], v32 offset:49216\n\t"
  "ds_read_b64_tr_b16 a[250:251], v32 offset:51008\n\t"
  "v_mfma_f32_16x16x32_bf16 v[108:111], a[228:231], v[164:167], v[108:111]\n\t"
  "s_waitcnt lgkmcnt(6)\n\t"
  "v_cvt_pk_bf16_f32 v124, v124, v126\n\t"
  "v_accvgpr_read_b32 v120, a132\n\t"
  "v_mfma_f32_16x16x32_bf16 v[240:243], a[232:235], v[160:163], v[240:243]\n\t"
  "ds_read_b64_tr_b16 a[252:253], v32 offset:49248\n\t"
  "ds_read_b64_tr_b16 a[254:255], v32 offset:51040\n\t"
  "v_mfma_f32_16x16x32_bf16 v[244:247], a[232:235], v[164:167], v[244:247]\n\t"
  "buffer_load_dwordx4 v36, s[92:95], s74 offen offset:0 lds\n\t"
  "v_mfma_f32_16x16x32_bf16 v[248:251], a[236:239], v[160:163], v[248:251]\n\t"
  "ds_read_b64_tr_b16 a[224:225], v32 offset:49280\n\t"
  "v_accvgpr_write_b32 a187, v124\n\t"
  "v_mfma_f32_16x16x32_bf16 v[252:255], a[236:239], v[164:167], v[252:255]\n\t"
  "s_waitcnt lgkmcnt(5)\n\t"
  "ds_read_b64_tr_b16 a[226:227], v32 offset:51072\n\t"
  "v_accvgpr_read_b32 v121, a188\n\t"
  "v_mfma_f32_16x16x32_bf16 a[0:3], a[240:243], v[168:171], a[0:3]\n\t"
  "ds_read_b64_tr_b16 a[228:229], v32 offset:49312\n\t"
  "ds_read_b64_tr_b16 a[230:231], v32 offset:51104\n\t"
  "v_mfma_f32_16x16x32_bf16 a[4:7], a[240:243], v[172:175], a[4:7]\n\t"
  "buffer_load_dwordx4 v37, s[92:95], s74 offen offset:1024 lds\n\t"
  "v_mfma_f32_16x16x32_bf16 a[8:11], a[244:247], v[168:171], a[8:11]\n\t"
  "ds_read_b64_tr_b16 a[232:233], v32 offset:49344\n\t"
  "v_accvgpr_read_b32 v122, a133\n\t"
  "v_mfma_f32_16x16x32_bf16 a[12:15], a[244:247], v[172:175], a[12:15]\n\t"
  "s_waitcnt lgkmcnt(5)\n\t"
  "ds_read_b64_tr_b16 a[234:235], v32 offset:51136\n\t"
  "v_accvgpr_read_b32 v123, a189\n\t"
  "v_mfma_f32_16x16x32_bf16 a[16:19], a[248:251], v[168:171], a[16:19]\n\t"
  "ds_read_b64_tr_b16 a[236:237], v34 offset:56320\n\t"
  "ds_read_b64_tr_b16 a[238:239], v34 offset:58624\n\t"
  "v_mfma_f32_16x16x32_bf16 a[20:23], a[248:251], v[172:175], a[20:23]\n\t"
  "buffer_store_dwordx4 a[172:175], v40, s[56:59], s85 offen offset:1024 sc1\n\t"
  "v_mfma_f32_16x16x32_bf16 a[24:27], a[252:255], v[168:171], a[24:27]\n\t"
  "ds_read_b64_tr_b16 a[240:241], v34 offset:56352\n\t"
  "v_mul_f32 v120, v120, v121\n\t"
  "v_mfma_f32_16x16x32_bf16 a[28:31], a[252:255], v[172:175], a[28:31]\n\t"
  "s_waitcnt lgkmcnt(5)\n\t"
  "ds_read_b64_tr_b16 a[242:243], v34 offset:58656\n\t"
  "v_mul_f32 v122, v122, v123\n\t"
  "v_mfma_f32_16x16x32_bf16 a[32:35], a[224:227], v[168:171], a[32:35]\n\t"
  "ds_read_b64_tr_b16 a[244:245], v34 offset:56384\n\t"
  "ds_read_b64_tr_b16 a[246:247], v34 offset:58688\n\t"
  "v_mfma_f32_16x16x32_bf16 a[36:39], a[224:227], v[172:175], a[36:39]\n\t"
  "buffer_load_dwordx4 v38, s[92:95], s74 offen offset:2048 lds\n\t"
  "v_mfma_f32_16x16x32_bf16 a[40:43], a[228:231], v[168:171], a[40:43]\n\t"
  "ds_read_b64_tr_b16 a[248:249], v34 offset:56416\n\t"
  "v_cvt_pk_bf16_f32 v120, v120, v122\n\t"
  "v_mfma_f32_16x16x32_bf16 a[44:47], a[228:231], v[172:175], a[44:47]\n\t"
  "s_waitcnt lgkmcnt(5)\n\t"
  "ds_read_b64_tr_b16 a[250:251], v34 offset:58720\n\t"
  "v_accvgpr_write_b32 a188, v120\n\t"
  "v_mfma_f32_16x16x32_bf16 a[48:51], a[232:235], v[168:171], a[48:51]\n\t"
  "ds_read_b64_tr_b16 a[252:253], v34 offset:56448\n\t"
  "ds_read_b64_tr_b16 a[254:255], v34 offset:58752\n\t"
  "v_mfma_f32_16x16x32_bf16 a[52:55], a[232:235], v[172:175], a[52:55]\n\t"
  "buffer_load_dwordx4 v39, s[92:95], s74 offen offset:3072 lds\n\t"
  "v_mfma_f32_16x16x32_bf16 a[56:59], a[236:239], v[168:171], a[56:59]\n\t"
  "s_add_u32 s74, s74, 0x80000\n\t"
  "ds_read_b64_tr_b16 a[224:225], v34 offset:56480\n\t"
  "v_accvgpr_read_b32 v124, a134\n\t"
  "v_mfma_f32_16x16x32_bf16 a[60:63], a[236:239], v[172:175], a[60:63]\n\t"
  "s_waitcnt lgkmcnt(5)\n\t"
  "ds_read_b64_tr_b16 a[226:227], v34 offset:58784\n\t"
  "v_accvgpr_read_b32 v125, a190\n\t"
  "v_mfma_f32_16x16x32_bf16 v[64:67], a[240:243], v[168:171], v[64:67]\n\t"
  "ds_read_b64_tr_b16 a[228:229], v34 offset:56512\n\t"
  "ds_read_b64_tr_b16 a[230:231], v34 offset:58816\n\t"
  "v_mfma_f32_16x16x32_bf16 v[68:71], a[240:243], v[172:175], v[68:71]\n\t"
  "v_accvgpr_read_b32 v126, a135\n\t"
  "v_accvgpr_read_b32 v127, a191\n\t"
  "v_mfma_f32_16x16x32_bf16 v[72:75], a[244:247], v[168:171], v[72:75]\n\t"
  "ds_read_b64_tr_b16 a[232:233], v34 offset:56544\n\t"
  "ds_read_b64_tr_b16 a[234:235], v34 offset:58848\n\t"
  "v_mfma_f32_16x16x32_bf16 v[76:79], a[244:247], v[172:175], v[76:79]\n\t"
  "s_waitcnt lgkmcnt(6)\n\t"
  "v_mul_f32 v124, v124, v125\n\t"
  "v_mul_f32 v126, v126, v127\n\t"
  "v_mfma_f32_16x16x32_bf16 v[80:83], a[248:251], v[168:171], v[80:83]\n\t"
  "s_waitcnt vmcnt(15)\n\t"
  "ds_read_b64_tr_b16 a[236:237], v34 offset:56576\n\t"
  "ds_read_b64_tr_b16 a[238:239], v34 offset:58880\n\t"
  "v_mfma_f32_16x16x32_bf16 v[84:87], a[248:251], v[172:175], v[84:87]\n\t"
  "s_barrier\n\t"
  "v_cvt_pk_bf16_f32 v124, v124, v126\n\t"
  "v_accvgpr_read_b32 v120, a140\n\t"
  "v_mfma_f32_16x16x32_bf16 v[88:91], a[252:255], v[168:171], v[88:91]\n\t"
  "ds_read_b64_tr_b16 a[240:241], v33 offset:0\n\t"
  "ds_read_b64_tr_b16 a[242:243], v33 offset:1792\n\t"
  "v_mfma_f32_16x16x32_bf16 v[92:95], a[252:255], v[172:175], v[92:95]\n\t"
  "s_waitcnt lgkmcnt(6)\n\t"
  "v_accvgpr_write_b32 a189, v124\n\t"
  "v_accvgpr_read_b32 v121, a196\n\t"
  "v_mfma_f32_16x16x32_bf16 v[96:99], a[224:227], v[168:171], v[96:99]\n\t"
  "ds_read_b64_tr_b16 a[244:245], v33 offset:32\n\t"
  "ds_read_b64_tr_b16 a[246:247], v33 offset:1824\n\t"
  "v_mfma_f32_16x16x32_bf16 v[100:103], a[224:227], v[172:175], v[100:103]\n\t"
  "v_accvgpr_read_b32 v122, a141\n\t"
  "v_accvgpr_read_b32 v123, a197\n\t"
  "v_mfma_f32_16x16x32_bf16 v[104:107], a[228:231], v[168:171], v[104:107]\n\t"
  "ds_read_b64_tr_b16 a[248:249], v33 offset:64\n\t"
  "ds_read_b64_tr_b16 a[250:251], v33 offset:1856\n\t"
  "v_mfma_f32_16x16x32_bf16 v[108:111], a[228:231], v[172:175], v[108:111]\n\t"
  "s_waitcnt lgkmcnt(6)\n\t"
  "v_mul_f32 v120, v120, v121\n\t"
  "v_mul_f32 v122, v122, v123\n\t"
  "v_mfma_f32_16x16x32_bf16 v[240:243], a[232:235], v[168:171], v[240:243]\n\t"
  "ds_read_b64_tr_b16 a[252:253], v33 offset:96\n\t"
  "ds_read_b64_tr_b16 a[254:255], v33 offset:1888\n\t"
  "v_mfma_f32_16x16x32_bf16 v[244:247], a[232:235], v[172:175], v[244:247]\n\t"
  "v_cvt_pk_bf16_f32 v120, v120, v122\n\t"
  "v_accvgpr_read_b32 v124, a142\n\t"
  "v_mfma_f32_16x16x32_bf16 v[248:251], a[236:239], v[168:171], v[248:251]\n\t"
  "ds_read_b64_tr_b16 a[224:225], v33 offset:128\n\t"
  "ds_read_b64_tr_b16 a[226:227], v33 offset:1920\n\t"
  "v_mfma_f32_16x16x32_bf16 v[252:255], a[236:239], v[172:175], v[252:255]\n\t"
  "s_waitcnt lgkmcnt(6)\n\t"
  "v_accvgpr_write_b32 a190, v120\n\t"
  "v_accvgpr_read_b32 v125, a198\n\t"
  "v_mfma_f32_16x16x32_bf16 a[0:3], a[240:243], v[176:179], a[0:3]\n\t"
  "ds_read_b64_tr_b16 a[228:229], v33 offset:160\n\t"
  "ds_read_b64_tr_b16 a[230:231], v33 offset:1952\n\t"
  "v_mfma_f32_16x16x32_bf16 a[4:7], a[240:243], v[180:183], a[4:7]\n\t"
  "v_accvgpr_read_b32 v126, a143\n\t"
  "v_accvgpr_read_b32 v127, a199\n\t"
  "v_mfma_f32_16x16x32_bf16 a[8:11], a[244:247], v[176:179], a[8:11]\n\t"
  "ds_read_b64_tr_b16 a[232:233], v33 offset:192\n\t"
  "ds_read_b64_tr_b16 a[234:235], v33 offset:1984\n\t"
  "v_mfma_f32_16x16x32_bf16 a[12:15], a[244:247], v[180:183], a[12:15]\n\t"
  "s_waitcnt lgkmcnt(6)\n\t"
  "v_mul_f32 v124, v124, v125\n\t"
  "v_mul_f32 v126, v126, v127\n\t"
  "v_mfma_f32_16x16x32_bf16 a[16:19], a[248:251], v[176:179], a[16:19]\n\t"
  "s_waitcnt vmcnt(23)\n\t"
  "ds_read_b64_tr_b16 a[236:237], v35 offset:7168\n\t"
  "ds_read_b64_tr_b16 a[238:239], v35 offset:9472\n\t"
  "v_mfma_f32_16x16x32_bf16 a[20:23], a[248:251], v[180:183], a[20:23]\n\t"
  "s_waitcnt vmcnt(10)\n\t"
  "buffer_store_dwordx4 a[184:187], v40, s[56:59], s85 offen offset:2048 sc1\n\t"
  "v_mfma_f32_16x16x32_bf16 a[24:27], a[252:255], v[176:179], a[24:27]\n\t"
  "s_add_u32 m0, s84, 0\n\t"
  "ds_read_b64_tr_b16 a[240:241], v35 offset:7200\n\t"
  "ds_read_b128 a[64:67], v28 offset:0\n\t"
  "v_mfma_f32_16x16x32_bf16 a[28:31], a[252:255], v[180:183], a[28:31]\n\t"
  "s_waitcnt lgkmcnt(6)\n\t"
  "ds_read_b64_tr_b16 a[242:243], v35 offset:9504\n\t"
  "ds_read_b128 a[68:71], v28 offset:1024\n\t"
  "v_mfma_f32_16x16x32_bf16 a[32:35], a[224:227], v[176:179], a[32:35]\n\t"
  "ds_read_b64_tr_b16 a[244:245], v35 offset:7232\n\t"
  "ds_read_b64_tr_b16 a[246:247], v35 offset:9536\n\t"
  "v_mfma_f32_16x16x32_bf16 a[36:39], a[224:227], v[180:183], a[36:39]\n\t"
  "buffer_load_dwordx4 v16, s[92:95], s74 offen offset:0 lds\n\t"
  "ds_read_b128 a[72:75], v28 offset:2048\n\t"
  "ds_read_b128 a[76:79], v28 offset:3072\n\t"
  "ds_read_b128 a[80:83], v28 offset:4096\n\t"
  "ds_read_b128 a[84:87], v28 offset:5120\n\t"
  "ds_read_b128 a[88:91], v28 offset:6144\n\t"
  "ds_read_b128 a[92:95], v28 offset:7168\n\t"
  "v_mfma_f32_16x16x32_bf16 a[40:43], a[228:231], v[176:179], a[40:43]\n\t"
  "s_waitcnt lgkmcnt(4)\n\t"
  "ds_read_b64_tr_b16 a[248:249], v35 offset:7264\n\t"
  "v_cvt_pk_bf16_f32 v124, v124, v126\n\t"
  "v_mfma_f32_16x16x32_bf16 a[44:47], a[228:231], v[180:183], a[44:47]\n\t"
  "s_waitcnt lgkmcnt(13)\n\t"
  "ds_read_b64_tr_b16 a[250:251], v35 offset:9568\n\t"
  "v_accvgpr_write_b32 a191, v124\n\t"
  "s_nop 0\n\t"
  "s_cbranch_execz TRAMP_%=\n\t"
  "v_mfma_f32_16x16x32_bf16 a[48:51], a[232:235], v[176:179], a[48:51]\n\t"
  "s_waitcnt lgkmcnt(2)\n\t"
  "ds_read_b64_tr_b16 a[252:253], v35 offset:7296\n\t"
  "ds_read_b64_tr_b16 a[254:255], v35 offset:9600\n\t"
  "v_mfma_f32_16x16x32_bf16 a[52:55], a[232:235], v[180:183], a[52:55]\n\t"
  "buffer_load_dwordx4 v17, s[92:95], s74 offen offset:1024 lds\n\t"
  "v_mfma_f32_16x16x32_bf16 a[56:59], a[236:239], v[176:179], a[56:59]\n\t"
  "ds_read_b64_tr_b16 a[224:225], v35 offset:7328\n\t"
  "v_accvgpr_read_b32 v120, a144\n\t"
  "v_mfma_f32_16x16x32_bf16 a[60:63], a[236:239], v[180:183], a[60:63]\n\t"
  "s_waitcnt lgkmcnt(11)\n\t"
  "ds_read_b64_tr_b16 a[226:227], v35 offset:9632\n\t"
  "v_accvgpr_read_b32 v121, a200\n\t"
  "v_mfma_f32_16x16x32_bf16 v[64:67], a[240:243], v[176:179], v[64:67]\n\t"
  "ds_read_b64_tr_b16 a[228:229], v35 offset:7360\n\t"
  "ds_read_b64_tr_b16 a[230:231], v35 offset:9664\n\t"
  "v_mfma_f32_16x16x32_bf16 v[68:71], a[240:243], v[180:183], v[68:71]\n\t"
  "buffer_load_dwordx4 v18, s[92:95], s74 offen offset:2048 lds\n\t"
  "v_mfma_f32_16x16x32_bf16 v[72:75], a[244:247], v[176:179], v[72:75]\n\t"
  "ds_read_b64_tr_b16 a[232:233], v35 offset:7392\n\t"
  "v_accvgpr_read_b32 v122, a145\n\t"
  "v_mfma_f32_16x16x32_bf16 v[76:79], a[244:247], v[180:183], v[76:79]\n\t"
  "s_waitcnt lgkmcnt(5)\n\t"
  "ds_read_b64_tr_b16 a[234:235], v35 offset:9696\n\t"
  "v_accvgpr_read_b32 v123, a201\n\t"
  "v_mfma_f32_16x16x32_bf16 v[80:83], a[248:251], v[176:179], v[80:83]\n\t"
  "ds_read_b64_tr_b16 a[236:237], v35 offset:7424\n\t"
  "ds_read_b64_tr_b16 a[238:239], v35 offset:9728\n\t"
  "v_mfma_f32_16x16x32_bf16 v[84:87], a[248:251], v[180:183], v[84:87]\n\t"
  "buffer_load_dwordx4 v19, s[92:95], s74 offen offset:3072 lds\n\t"
  "v_mfma_f32_16x16x32_bf16 v[88:91], a[252:255], v[176:179], v[88:91]\n\t"
  "s_add_u32 m0, s84, 0x1000\n\t"
  "ds_read_b64_tr_b16 a[240:241], v33 offset:16384\n\t"
  "v_mul_f32 v120, v120, v121\n\t"
  "v_mfma_f32_16x16x32_bf16 v[92:95], a[252:255], v[180:183], v[92:95]\n\t"
  "s_waitcnt lgkmcnt(5)\n\t"
  "ds_read_b64_tr_b16 a[242:243], v33 offset:18176\n\t"
  "v_mul_f32 v122, v122, v123\n\t"
  "v_mfma_f32_16x16x32_bf16 v[96:99], a[224:227], v[176:179], v[96:99]\n\t"
  "ds_read_b64_tr_b16 a[244:245], v33 offset:16416\n\t"
  "ds_read_b64_tr_b16 a[246:247], v33 offset:18208\n\t"
  "v_mfma_f32_16x16x32_bf16 v[100:103], a[224:227], v[180:183], v[100:103]\n\t"
  "buffer_load_dwordx4 v36, s[92:95], s74 offen offset:0 lds\n\t"
  "v_mfma_f32_16x16x32_bf16 v[104:107], a[228:231], v[176:179], v[104:107]\n\t"
  "ds_read_b64_tr_b16 a[248:249], v33 offset:16448\n\t"
  "v_cvt_pk_bf16_f32 v120, v120, v122\n\t"
  "v_mfma_f32_16x16x32_bf16 v[108:111], a[228:231], v[180:183], v[108:111]\n\t"
  "s_waitcnt lgkmcnt(5)\n\t"
  "ds_read_b64_tr_b16 a[250:251], v33 offset:18240\n\t"
  "v_accvgpr_write_b32 a200, v120\n\t"
  "v_mfma_f32_16x16x32_bf16 v[240:243], a[232:235], v[176:179], v[240:243]\n\t"
  "ds_read_b64_tr_b16 a[252:253], v33 offset:16480\n\t"
  "ds_read_b64_tr_b16 a[254:255], v33 offset:18272\n\t"
  "v_mfma_f32_16x16x32_bf16 v[244:247], a[232:235], v[180:183], v[244:247]\n\t"
  "buffer_load_dwordx4 v37, s[92:95], s74 offen offset:1024 lds\n\t"
  "v_mfma_f32_16x16x32_bf16 v[248:251], a[236:239], v[176:179], v[248:251]\n\t"
  "ds_read_b64_tr_b16 a[224:225], v33 offset:16512\n\t"
  "v_accvgpr_read_b32 v124, a146\n\t"
  "v_mfma_f32_16x16x32_bf16 v[252:255], a[236:239], v[180:183], v[252:255]\n\t"
  "s_waitcnt lgkmcnt(5)\n\t"
  "ds_read_b64_tr_b16 a[226:227], v33 offset:18304\n\t"
  "v_accvgpr_read_b32 v125, a202\n\t"
  "v_mfma_f32_16x16x32_bf16 a[0:3], a[240:243], v[184:187], a[0:3]\n\t"
  "ds_read_b64_tr_b16 a[228:229], v33 offset:16544\n\t"
  "ds_read_b64_tr_b16 a[230:231], v33 offset:18336\n\t"
  "v_mfma_f32_16x16x32_bf16 a[4:7], a[240:243], v[188:191], a[4:7]\n\t"
  "buffer_load_dwordx4 v38, s[92:95], s74 offen offset:2048 lds\n\t"
  "v_mfma_f32_16x16x32_bf16 a[8:11], a[244:247], v[184:187], a[8:11]\n\t"
  "ds_read_b64_tr_b16 a[232:233], v33 offset:16576\n\t"
  "v_accvgpr_read_b32 v126, a147\n\t"
  "v_mfma_f32_16x16x32_bf16 a[12:15], a[244:247], v[188:191], a[12:15]\n\t"
  "s_waitcnt lgkmcnt(5)\n\t"
  "ds_read_b64_tr_b16 a[234:235], v33 offset:18368\n\t"
  "v_accvgpr_read_b32 v127, a203\n\t"
  "v_mfma_f32_16x16x32_bf16 a[16:19], a[248:251], v[184:187], a[16:19]\n\t"
  "ds_read_b64_tr_b16 a[236:237], v35 offset:23552\n\t"
  "ds_read_b64_tr_b16 a[238:239], v35 offset:25856\n\t"
  "v_mfma_f32_16x16x32_bf16 a[20:23], a[248:251], v[188:191], a[20:23]\n\t"
  "buffer_store_dwordx4 a[188:191], v40, s[56:59], s85 offen offset:3072 sc1\n\t"
  "v_mfma_f32_16x16x32_bf16 a[24:27], a[252:255], v[184:187], a[24:27]\n\t"
  "ds_read_b64_tr_b16 a[240:241], v35 offset:23584\n\t"
  "v_mul_f32 v124, v124, v125\n\t"
  "v_mfma_f32_16x16x32_bf16 a[28:31], a[252:255], v[188:191], a[28:31]\n\t"
  "s_waitcnt lgkmcnt(5)\n\t"
  "ds_read_b64_tr_b16 a[242:243], v35 offset:25888\n\t"
  "v_mul_f32 v126, v126, v127\n\t"
  "v_mfma_f32_16x16x32_bf16 a[32:35], a[224:227], v[184:187], a[32:35]\n\t"
  "ds_read_b64_tr_b16 a[244:245], v35 offset:23616\n\t"
  "ds_read_b64_tr_b16 a[246:247], v35 offset:25920\n\t"
  "v_mfma_f32_16x16x32_bf16 a[36:39], a[224:227], v[188:191], a[36:39]\n\t"
  "buffer_load_dwordx4 v39, s[92:95], s74 offen offset:3072 lds\n\t"
  "v_mfma_f32_16x16x32_bf16 a[40:43], a[228:231], v[184:187], a[40:43]\n\t"
  "s_add_u32 s74, s74, 0x80000\n\t"
  "ds_read_b64_tr_b16 a[248:249], v35 offset:23648\n\t"
  "v_cvt_pk_bf16_f32 v124, v124, v126\n\t"
  "v_mfma_f32_16x16x32_bf16 a[44:47], a[228:231], v[188:191], a[44:47]\n\t"
  "s_waitcnt lgkmcnt(5)\n\t"
  "ds_read_b64_tr_b16 a[250:251], v35 offset:25952\n\t"
  "v_accvgpr_write_b32 a201, v124\n\t"
  "v_mfma_f32_16x16x32_bf16 a[48:51], a[232:235], v[184:187], a[48:51]\n\t"
  "ds_read_b64_tr_b16 a[252:253], v35 offset:23680\n\t"
  "ds_read_b64_tr_b16 a[254:255], v35 offset:25984\n\t"
  "v_mfma_f32_16x16x32_bf16 a[52:55], a[232:235], v[188:191], a[52:55]\n\t"
  "v_accvgpr_read_b32 v120, a152\n\t"
  "v_accvgpr_read_b32 v121, a208\n\t"
  "v_mfma_f32_16x16x32_bf16 a[56:59], a[236:239], v[184:187], a[56:59]\n\t"
  "s_add_u32 m0, s84, 0x20000\n\t"
  "ds_read_b64_tr_b16 a[224:225], v35 offset:23712\n\t"
  "ds_read_b64_tr_b16 a[226:227], v35 offset:26016\n\t"
  "v_mfma_f32_16x16x32_bf16 a[60:63], a[236:239], v[188:191], a[60:63]\n\t"
  "s_waitcnt lgkmcnt(6)\n\t"
  "v_accvgpr_read_b32 v122, a153\n\t"
  "v_accvgpr_read_b32 v123, a209\n\t"
  "v_mfma_f32_16x16x32_bf16 v[64:67], a[240:243], v[184:187], v[64:67]\n\t"
  "s_mov_b32 s32, 0x1100\n\t"
  "ds_read_b64_tr_b16 a[228:229], v35 offset:23744\n\t"
  "ds_read_b64_tr_b16 a[230:231], v35 offset:26048\n\t"
  "v_mfma_f32_16x16x32_bf16 v[68:71], a[240:243], v[188:191], v[68:71]\n\t"
  "buffer_load_dwordx4 v27, s[44:47], s32 offen offset:0 lds\n\t"
  "v_mfma_f32_16x16x32_bf16 v[72:75], a[244:247], v[184:187], v[72:75]\n\t"
  "s_add_u32 m0, s84, 0x20000\n\t"
  "ds_read_b64_tr_b16 a[232:233], v35 offset:23776\n\t"
  "v_mul_f32 v120, v120, v121\n\t"
  "v_mfma_f32_16x16x32_bf16 v[76:79], a[244:247], v[188:191], v[76:79]\n\t"
  "s_waitcnt lgkmcnt(5)\n\t"
  "ds_read_b64_tr_b16 a[234:235], v35 offset:26080\n\t"
  "v_mul_f32 v122, v122, v123\n\t"
  "v_mfma_f32_16x16x32_bf16 v[80:83], a[248:251], v[184:187], v[80:83]\n\t"
  "s_mov_b32 s33, 0x2d00\n\t"
  "ds_read_b64_tr_b16 a[236:237], v35 offset:23808\n\t"
  "ds_read_b64_tr_b16 a[238:239], v35 offset:26112\n\t"
  "v_mfma_f32_16x16x32_bf16 v[84:87], a[248:251], v[188:191], v[84:87]\n\t"
  "s_waitcnt vmcnt(11)\n\t"
  "buffer_load_dwordx4 v27, s[44:47], s33 offen offset:1024 lds\n\t"
  "v_mfma_f32_16x16x32_bf16 v[88:91], a[252:255], v[184:187], v[88:91]\n\t"
  "s_add_u32 m0, s84, 0x20000\n\t"
  "v_cvt_pk_bf16_f32 v120, v120, v122\n\t"
  "v_accvgpr_read_b32 v124, a154\n\t"
  "v_mfma_f32_16x16x32_bf16 v[92:95], a[252:255], v[188:191], v[92:95]\n\t"
  "s_waitcnt lgkmcnt(4)\n\t"
  "v_accvgpr_write_b32 a202, v120\n\t"
  "v_accvgpr_read_b32 v125, a210\n\t"
  "v_mfma_f32_16x16x32_bf16 v[96:99], a[224:227], v[184:187], v[96:99]\n\t"
  "s_mov_b32 s32, 0x940\n\t"
  "v_accvgpr_read_b32 v126, a155\n\t"
  "v_accvgpr_read_b32 v127, a211\n\t"
  "v_mfma_f32_16x16x32_bf16 v[100:103], a[224:227], v[188:191], v[100:103]\n\t"
  "s_barrier\n\t"
  "buffer_load_dwordx4 v27, s[44:47], s32 offen offset:2048 lds\n\t"
  "v_mfma_f32_16x16x32_bf16 v[104:107], a[228:231], v[184:187], v[104:107]\n\t"
  "s_add_u32 m0, s84, 0x20000\n\t"
  "ds_read_b64_tr_b16 a[240:241], v33 offset:32768\n\t"
  "v_mul_f32 v124, v124, v125\n\t"
  "v_mfma_f32_16x16x32_bf16 v[108:111], a[228:231], v[188:191], v[108:111]\n\t"
  "s_waitcnt lgkmcnt(1)\n\t"
  "ds_read_b64_tr_b16 a[242:243], v33 offset:34560\n\t"
  "ds_read_b64_tr_b16 a[244:245], v33 offset:32800\n\t"
  "v_mfma_f32_16x16x32_bf16 v[240:243], a[232:235], v[184:187], v[240:243]\n\t"
  "s_mov_b32 s33, 0x2540\n\t"
  "ds_read_b64_tr_b16 a[246:247], v33 offset:34592\n\t"
  "ds_read_b64_tr_b16 a[248:249], v33 offset:32832\n\t"
  "v_mfma_f32_16x16x32_bf16 v[244:247], a[232:235], v[188:191], v[244:247]\n\t"
  "buffer_load_dwordx4 v27, s[44:47], s33 offen offset:3072 lds\n\t"
  "v_mfma_f32_16x16x32_bf16 v[248:251], a[236:239], v[184:187], v[248:251]\n\t"
  "ds_read_b64_tr_b16 a[250:251], v33 offset:34624\n\t"
  "v_mul_f32 v126, v126, v127\n\t"
  "v_mfma_f32_16x16x32_bf16 v[252:255], a[236:239], v[188:191], v[252:255]\n\t"
  "s_waitcnt lgkmcnt(2)\n\t"
  "ds_read_b64_tr_b16 a[252:253], v33 offset:32864\n\t"
  "ds_read_b64_tr_b16 a[254:255], v33 offset:34656\n\t"
  "v_mfma_f32_16x16x32_bf16 a[0:3], a[240:243], v[192:195], a[0:3]\n\t"
  "ds_read_b64_tr_b16 a[224:225], v33 offset:32896\n\t"
  "ds_read_b64_tr_b16 a[226:227], v33 offset:34688\n\t"
  "v_mfma_f32_16x16x32_bf16 a[4:7], a[240:243], v[196:199], a[4:7]\n\t"
  "ds_read_b64_tr_b16 a[228:229], v33 offset:32928\n\t"
  "ds_read_b64_tr_b16 a[230:231], v33 offset:34720\n\t"
  "v_mfma_f32_16x16x32_bf16 a[8:11], a[244:247], v[192:195], a[8:11]\n\t"
  "ds_read_b64_tr_b16 a[232:233], v33 offset:32960\n\t"
  "ds_read_b64_tr_b16 a[234:235], v33 offset:34752\n\t"
  "v_mfma_f32_16x16x32_bf16 a[12:15], a[244:247], v[196:199], a[12:15]\n\t"
  "s_waitcnt lgkmcnt(6)\n\t"
  "v_cvt_pk_bf16_f32 v124, v124, v126\n\t"
  "v_accvgpr_read_b32 v120, a148\n\t"
  "v_mfma_f32_16x16x32_bf16 a[16:19], a[248:251], v[192:195], a[16:19]\n\t"
  "ds_read_b64_tr_b16 a[236:237], v35 offset:39936\n\t"
  "ds_read_b64_tr_b16 a[238:239], v35 offset:42240\n\t"
  "v_mfma_f32_16x16x32_bf16 a[20:23], a[248:251], v[196:199], a[20:23]\n\t"
  "v_accvgpr_write_b32 a203, v124\n\t"
  "v_accvgpr_read_b32 v121, a204\n\t"
  "v_mfma_f32_16x16x32_bf16 a[24:27], a[252:255], v[192:195], a[24:27]\n\t"
  "s_add_u32 m0, s84, 0x8000\n\t"
  "buffer_store_dwordx4 a[200:203], v40, s[56:59], s88 offen offset:0 sc1\n\t"
  "v_mfma_f32_16x16x32_bf16 a[28:31], a[252:255], v[196:199], a[28:31]\n\t"
  "s_waitcnt lgkmcnt(4)\n\t"
  "ds_read_b64_tr_b16 a[240:241], v35 offset:39968\n\t"
  "ds_read_b64_tr_b16 a[242:243], v35 offset:42272\n\t"
  "v_mfma_f32_16x16x32_bf16 a[32:35], a[224:227], v[192:195], a[32:35]\n\t"
  "ds_read_b64_tr_b16 a[244:245], v35 offset:40000\n\t"
  "ds_read_b64_tr_b16 a[246:247], v35 offset:42304\n\t"
  "v_mfma_f32_16x16x32_bf16 a[36:39], a[224:227], v[196:199], a[36:39]\n\t"
  "buffer_load_dwordx4 v16, s[92:95], s74 offen offset:0 lds\n\t"
  "v_mfma_f32_16x16x32_bf16 a[40:43], a[228:231], v[192:195], a[40:43]\n\t"
  "ds_read_b64_tr_b16 a[248:249], v35 offset:40032\n\t"
  "v_accvgpr_read_b32 v122, a149\n\t"
  "v_mfma_f32_16x16x32_bf16 a[44:47], a[228:231], v[196:199], a[44:47]\n\t"
  "s_waitcnt lgkmcnt(5)\n\t"
  "ds_read_b64_tr_b16 a[250:251], v35 offset:42336\n\t"
  "v_accvgpr_read_b32 v123, a205\n\t"
  "buffer_load_dwordx2 v[48:49], v11, s[56:59], s41 offen offset:0 sc1\n\t"
  "v_mfma_f32_16x16x32_bf16 a[48:51], a[232:235], v[192:195], a[48:51]\n\t"
  "ds_read_b64_tr_b16 a[252:253], v35 offset:40064\n\t"
  "ds_read_b64_tr_b16 a[254:255], v35 offset:42368\n\t"
  "v_mfma_f32_16x16x32_bf16 a[52:55], a[232:235], v[196:199], a[52:55]\n\t"
  "buffer_load_dwordx4 v17, s[92:95], s74 offen offset:1024 lds\n\t"
  "v_mfma_f32_16x16x32_bf16 a[56:59], a[236:239], v[192:195], a[56:59]\n\t"
  "ds_read_b64_tr_b16 a[224:225], v35 offset:40096\n\t"
  "v_mul_f32 v120, v120, v121\n\t"
  "v_mfma_f32_16x16x32_bf16 a[60:63], a[236:239], v[196:199], a[60:63]\n\t"
  "s_waitcnt lgkmcnt(5)\n\t"
  "ds_read_b64_tr_b16 a[226:227], v35 offset:42400\n\t"
  "v_mul_f32 v122, v122, v123\n\t"
  "v_mfma_f32_16x16x32_bf16 v[64:67], a[240:243], v[192:195], v[64:67]\n\t"
  "ds_read_b64_tr_b16 a[228:229], v35 offset:40128\n\t"
  "ds_read_b64_tr_b16 a[230:231], v35 offset:42432\n\t"
  "v_mfma_f32_16x16x32_bf16 v[68:71], a[240:243], v[196:199], v[68:71]\n\t"
  "buffer_load_dwordx4 v18, s[92:95], s74 offen offset:2048 lds\n\t"
  "v_mfma_f32_16x16x32_bf16 v[72:75], a[244:247], v[192:195], v[72:75]\n\t"
  "ds_read_b64_tr_b16 a[232:233], v35 offset:40160\n\t"
  "v_cvt_pk_bf16_f32 v120, v120, v122\n\t"
  "v_mfma_f32_16x16x32_bf16 v[76:79], a[244:247], v[196:199], v[76:79]\n\t"
  "s_waitcnt lgkmcnt(5)\n\t"
  "ds_read_b64_tr_b16 a[234:235], v35 offset:42464\n\t"
  "v_accvgpr_write_b32 a204, v120\n\t"
  "v_mfma_f32_16x16x32_bf16 v[80:83], a[248:251], v[192:195], v[80:83]\n\t"
  "ds_read_b64_tr_b16 a[236:237], v35 offset:40192\n\t"
  "ds_read_b64_tr_b16 a[238:239], v35 offset:42496\n\t"
  "v_mfma_f32_16x16x32_bf16 v[84:87], a[248:251], v[196:199], v[84:87]\n\t"
  "buffer_load_dwordx4 v19, s[92:95], s74 offen offset:3072 lds\n\t"
  "v_mfma_f32_16x16x32_bf16 v[88:91], a[252:255], v[192:195], v[88:91]\n\t"
  "s_add_u32 m0, s84, 0x9000\n\t"
  "ds_read_b64_tr_b16 a[240:241], v33 offset:49152\n\t"
  "v_accvgpr_read_b32 v124, a150\n\t"
  "v_mfma_f32_16x16x32_bf16 v[92:95], a[252:255], v[196:199], v[92:95]\n\t"
  "s_waitcnt lgkmcnt(5)\n\t"
  "ds_read_b64_tr_b16 a[242:243], v33 offset:50944\n\t"
  "v_accvgpr_read_b32 v125, a206\n\t"
  "v_mfma_f32_16x16x32_bf16 v[96:99], a[224:227], v[192:195], v[96:99]\n\t"
  "ds_read_b64_tr_b16 a[244:245], v33 offset:49184\n\t"
  "ds_read_b64_tr_b16 a[246:247], v33 offset:50976\n\t"
  "v_mfma_f32_16x16x32_bf16 v[100:103], a[224:227], v[196:199], v[100:103]\n\t"
  "buffer_load_dwordx4 v36, s[92:95], s74 offen offset:0 lds\n\t"
  "v_mfma_f32_16x16x32_bf16 v[104:107], a[228:231], v[192:195], v[104:107]\n\t"
  "ds_read_b64_tr_b16 a[248:249], v33 offset:49216\n\t"
  "v_accvgpr_read_b32 v126, a151\n\t"
  "v_mfma_f32_16x16x32_bf16 v[108:111], a[228:231], v[196:199], v[108:111]\n\t"
  "s_waitcnt lgkmcnt(5)\n\t"
  "ds_read_b64_tr_b16 a[250:251], v33 offset:51008\n\t"
  "v_accvgpr_read_b32 v127, a207\n\t"
  "v_mfma_f32_16x16x32_bf16 v[240:243], a[232:235], v[192:195], v[240:243]\n\t"
  "ds_read_b64_tr_b16 a[252:253], v33 offset:49248\n\t"
  "ds_read_b64_tr_b16 a[254:255], v33 offset:51040\n\t"
  "v_mfma_f32_16x16x32_bf16 v[244:247], a[232:235], v[196:199], v[244:247]\n\t"
  "buffer_load_dwordx4 v37, s[92:95], s74 offen offset:1024 lds\n\t"
  "v_mfma_f32_16x16x32_bf16 v[248:251], a[236:239], v[192:195], v[248:251]\n\t"
  "ds_read_b64_tr_b16 a[224:225], v33 offset:49280\n\t"
  "v_mul_f32 v124, v124, v125\n\t"
  "v_mfma_f32_16x16x32_bf16 v[252:255], a[236:239], v[196:199], v[252:255]\n\t"
  "s_waitcnt lgkmcnt(5)\n\t"
  "ds_read_b64_tr_b16 a[226:227], v33 offset:51072\n\t"
  "v_mul_f32 v126, v126, v127\n\t"
  "v_mfma_f32_16x16x32_bf16 a[0:3], a[240:243], v[200:203], a[0:3]\n\t"
  "ds_read_b64_tr_b16 a[228:229], v33 offset:49312\n\t"
  "ds_read_b64_tr_b16 a[230:231], v33 offset:51104\n\t"
  "v_mfma_f32_16x16x32_bf16 a[4:7], a[240:243], v[204:207], a[4:7]\n\t"
  "buffer_load_dwordx4 v38, s[92:95], s74 offen offset:2048 lds\n\t"
  "v_mfma_f32_16x16x32_bf16 a[8:11], a[244:247], v[200:203], a[8:11]\n\t"
  "ds_read_b64_tr_b16 a[232:233], v33 offset:49344\n\t"
  "v_cvt_pk_bf16_f32 v124, v124, v126\n\t"
  "v_mfma_f32_16x16x32_bf16 a[12:15], a[244:247], v[204:207], a[12:15]\n\t"
  "s_waitcnt lgkmcnt(5)\n\t"
  "ds_read_b64_tr_b16 a[234:235], v33 offset:51136\n\t"
  "v_accvgpr_write_b32 a205, v124\n\t"
  "v_mfma_f32_16x16x32_bf16 a[16:19], a[248:251], v[200:203], a[16:19]\n\t"
  "ds_read_b64_tr_b16 a[236:237], v35 offset:56320\n\t"
  "ds_read_b64_tr_b16 a[238:239], v35 offset:58624\n\t"
  "v_mfma_f32_16x16x32_bf16 a[20:23], a[248:251], v[204:207], a[20:23]\n\t"
  "v_accvgpr_read_b32 v120, a156\n\t"
  "v_accvgpr_read_b32 v121, a212\n\t"
  "v_mfma_f32_16x16x32_bf16 a[24:27], a[252:255], v[200:203], a[24:27]\n\t"
  "ds_read_b64_tr_b16 a[240:241], v35 offset:56352\n\t"
  "v_accvgpr_read_b32 v122, a157\n\t"
  "v_mfma_f32_16x16x32_bf16 a[28:31], a[252:255], v[204:207], a[28:31]\n\t"
  "s_waitcnt lgkmcnt(5)\n\t"
  "ds_read_b64_tr_b16 a[242:243], v35 offset:58656\n\t"
  "v_accvgpr_read_b32 v123, a213\n\t"
  "v_mul_f32 v120, v120, v121\n\t"
  "v_mul_f32 v122, v122, v123\n\t"
  "v_accvgpr_read_b32 v124, a158\n\t"
  "v_cvt_pk_bf16_f32 v120, v120, v122\n\t"
  "v_accvgpr_read_b32 v125, a214\n\t"
  "v_accvgpr_write_b32 a206, v120\n\t"
  "v_accvgpr_read_b32 v126, a159\n\t"
  "v_accvgpr_read_b32 v127, a215\n\t"
  "v_mul_f32 v124, v124, v125\n\t"
  "v_mul_f32 v126, v126, v127\n\t"
  "v_cvt_pk_bf16_f32 v124, v124, v126\n\t"
  "v_accvgpr_write_b32 a207, v124\n\t"
  "buffer_store_dwordx4 a[204:207], v40, s[56:59], s88 offen offset:1024 sc1\n\t"
  "v_mfma_f32_16x16x32_bf16 a[32:35], a[224:227], v[200:203], a[32:35]\n\t"
  "ds_read_b64_tr_b16 a[244:245], v35 offset:56384\n\t"
  "ds_read_b64_tr_b16 a[246:247], v35 offset:58688\n\t"
  "v_mfma_f32_16x16x32_bf16 a[36:39], a[224:227], v[204:207], a[36:39]\n\t"
  "buffer_load_dwordx4 v39, s[92:95], s74 offen offset:3072 lds\n\t"
  "v_mfma_f32_16x16x32_bf16 a[40:43], a[228:231], v[200:203], a[40:43]\n\t"
  "s_add_u32 s74, s74, 0x80000\n\t"
  "ds_read_b64_tr_b16 a[248:249], v35 offset:56416\n\t"
  "v_accvgpr_read_b32 v120, a160\n\t"
  "v_mfma_f32_16x16x32_bf16 a[44:47], a[228:231], v[204:207], a[44:47]\n\t"
  "s_waitcnt lgkmcnt(5)\n\t"
  "ds_read_b64_tr_b16 a[250:251], v35 offset:58720\n\t"
  "v_accvgpr_read_b32 v121, a216\n\t"
  "v_mfma_f32_16x16x32_bf16 a[48:51], a[232:235], v[200:203], a[48:51]\n\t"
  "ds_read_b64_tr_b16 a[252:253], v35 offset:56448\n\t"
  "ds_read_b64_tr_b16 a[254:255], v35 offset:58752\n\t"
  "v_mfma_f32_16x16x32_bf16 a[52:55], a[232:235], v[204:207], a[52:55]\n\t"
  "v_accvgpr_read_b32 v122, a161\n\t"
  "v_accvgpr_read_b32 v123, a217\n\t"
  "v_mfma_f32_16x16x32_bf16 a[56:59], a[236:239], v[200:203], a[56:59]\n\t"
  "s_add_u32 m0, s84, 0x21000\n\t"
  "ds_read_b64_tr_b16 a[224:225], v35 offset:56480\n\t"
  "ds_read_b64_tr_b16 a[226:227], v35 offset:58784\n\t"
  "v_mfma_f32_16x16x32_bf16 a[60:63], a[236:239], v[204:207], a[60:63]\n\t"
  "s_waitcnt lgkmcnt(6)\n\t"
  "v_mul_f32 v120, v120, v121\n\t"
  "v_mul_f32 v122, v122, v123\n\t"
  "v_mfma_f32_16x16x32_bf16 v[64:67], a[240:243], v[200:203], v[64:67]\n\t"
  "s_mov_b32 s32, 0x1180\n\t"
  "ds_read_b64_tr_b16 a[228:229], v35 offset:56512\n\t"
  "ds_read_b64_tr_b16 a[230:231], v35 offset:58816\n\t"
  "v_mfma_f32_16x16x32_bf16 v[68:71], a[240:243], v[204:207], v[68:71]\n\t"
  "buffer_load_dwordx4 v27, s[44:47], s32 offen offset:0 lds\n\t"
  "v_mfma_f32_16x16x32_bf16 v[72:75], a[244:247], v[200:203], v[72:75]\n\t"
  "s_add_u32 m0, s84, 0x21000\n\t"
  "ds_read_b64_tr_b16 a[232:233], v35 offset:56544\n\t"
  "v_cvt_pk_bf16_f32 v120, v120, v122\n\t"
  "v_mfma_f32_16x16x32_bf16 v[76:79], a[244:247], v[204:207], v[76:79]\n\t"
  "s_waitcnt lgkmcnt(5)\n\t"
  "ds_read_b64_tr_b16 a[234:235], v35 offset:58848\n\t"
  "v_accvgpr_write_b32 a216, v120\n\t"
  "v_mfma_f32_16x16x32_bf16 v[80:83], a[248:251], v[200:203], v[80:83]\n\t"
  "s_mov_b32 s33, 0x2d80\n\t"
  "ds_read_b64_tr_b16 a[236:237], v35 offset:56576\n\t"
  "ds_read_b64_tr_b16 a[238:239], v35 offset:58880\n\t"
  "v_mfma_f32_16x16x32_bf16 v[84:87], a[248:251], v[204:207], v[84:87]\n\t"
  "s_waitcnt vmcnt(16)\n\t"
  "buffer_load_dwordx4 v27, s[44:47], s33 offen offset:1024 lds\n\t"
  "v_mfma_f32_16x16x32_bf16 v[88:91], a[252:255], v[200:203], v[88:91]\n\t"
  "s_add_u32 m0, s84, 0x21000\n\t"
  "v_accvgpr_read_b32 v124, a162\n\t"
  "v_accvgpr_read_b32 v125, a218\n\t"
  "v_mfma_f32_16x16x32_bf16 v[92:95], a[252:255], v[204:207], v[92:95]\n\t"
  "s_waitcnt lgkmcnt(4)\n\t"
  "v_accvgpr_read_b32 v126, a163\n\t"
  "v_accvgpr_read_b32 v127, a219\n\t"
  "v_mfma_f32_16x16x32_bf16 v[96:99], a[224:227], v[200:203], v[96:99]\n\t"
  "s_mov_b32 s32, 0x9c0\n\t"
  "v_mul_f32 v124, v124, v125\n\t"
  "v_mul_f32 v126, v126, v127\n\t"
  "v_mfma_f32_16x16x32_bf16 v[100:103], a[224:227], v[204:207], v[100:103]\n\t"
  "s_barrier\n\t"
  "buffer_load_dwordx4 v27, s[44:47], s32 offen offset:2048 lds\n\t"
  "v_mfma_f32_16x16x32_bf16 v[104:107], a[228:231], v[200:203], v[104:107]\n\t"
  "s_add_u32 m0, s84, 0x21000\n\t"
  "ds_read_b64_tr_b16 a[240:241], v32 offset:0\n\t"
  "v_cvt_pk_bf16_f32 v124, v124, v126\n\t"
  "v_mfma_f32_16x16x32_bf16 v[108:111], a[228:231], v[204:207], v[108:111]\n\t"
  "s_waitcnt lgkmcnt(1)\n\t"
  "ds_read_b64_tr_b16 a[242:243], v32 offset:1792\n\t"
  "ds_read_b64_tr_b16 a[244:245], v32 offset:32\n\t"
  "v_mfma_f32_16x16x32_bf16 v[240:243], a[232:235], v[200:203], v[240:243]\n\t"
  "s_mov_b32 s33, 0x25c0\n\t"
  "ds_read_b64_tr_b16 a[246:247], v32 offset:1824\n\t"
  "ds_read_b64_tr_b16 a[248:249], v32 offset:64\n\t"
  "v_mfma_f32_16x16x32_bf16 v[244:247], a[232:235], v[204:207], v[244:247]\n\t"
  "buffer_load_dwordx4 v27, s[44:47], s33 offen offset:3072 lds\n\t"
  "v_mfma_f32_16x16x32_bf16 v[248:251], a[236:239], v[200:203], v[248:251]\n\t"
  "ds_read_b64_tr_b16 a[250:251], v32 offset:1856\n\t"
  "v_accvgpr_write_b32 a217, v124\n\t"
  "v_mfma_f32_16x16x32_bf16 v[252:255], a[236:239], v[204:207], v[252:255]\n\t"
  "s_waitcnt lgkmcnt(2)\n\t"
  "ds_read_b64_tr_b16 a[252:253], v32 offset:96\n\t"
  "ds_read_b64_tr_b16 a[254:255], v32 offset:1888\n\t"
  "v_mfma_f32_16x16x32_bf16 a[0:3], a[240:243], v[208:211], a[0:3]\n\t"
  "ds_read_b64_tr_b16 a[224:225], v32 offset:128\n\t"
  "ds_read_b64_tr_b16 a[226:227], v32 offset:1920\n\t"
  "v_mfma_f32_16x16x32_bf16 a[4:7], a[240:243], v[212:215], a[4:7]\n\t"
  "ds_read_b64_tr_b16 a[228:229], v32 offset:160\n\t"
  "ds_read_b64_tr_b16 a[230:231], v32 offset:1952\n\t"
  "v_mfma_f32_16x16x32_bf16 a[8:11], a[244:247], v[208:211], a[8:11]\n\t"
  "ds_read_b64_tr_b16 a[232:233], v32 offset:192\n\t"
  "ds_read_b64_tr_b16 a[234:235], v32 offset:1984\n\t"
  "v_mfma_f32_16x16x32_bf16 a[12:15], a[244:247], v[212:215], a[12:15]\n\t"
  "s_waitcnt lgkmcnt(6)\n\t"
  "v_accvgpr_read_b32 v120, a164\n\t"
  "v_accvgpr_read_b32 v121, a220\n\t"
  "v_mfma_f32_16x16x32_bf16 a[16:19], a[248:251], v[208:211], a[16:19]\n\t"
  "ds_read_b64_tr_b16 a[236:237], v34 offset:7168\n\t"
  "ds_read_b64_tr_b16 a[238:239], v34 offset:9472\n\t"
  "v_mfma_f32_16x16x32_bf16 a[20:23], a[248:251], v[212:215], a[20:23]\n\t"
  "buffer_store_dwordx2 a[216:217], v40, s[56:59], s35 offen offset:0 sc1\n\t"
  "v_mfma_f32_16x16x32_bf16 a[24:27], a[252:255], v[208:211], a[24:27]\n\t"
  "ds_read_b64_tr_b16 a[240:241], v34 offset:7200\n\t"
  "v_accvgpr_read_b32 v122, a165\n\t"
  "v_mfma_f32_16x16x32_bf16 a[28:31], a[252:255], v[212:215], a[28:31]\n\t"
  "s_waitcnt lgkmcnt(5)\n\t"
  "ds_read_b64_tr_b16 a[242:243], v34 offset:9504\n\t"
  "v_accvgpr_read_b32 v123, a221\n\t"
  "v_mfma_f32_16x16x32_bf16 a[32:35], a[224:227], v[208:211], a[32:35]\n\t"
  "s_waitcnt vmcnt(13)\n\t"
  "ds_read_b64_tr_b16 a[244:245], v34 offset:7232\n\t"
  "ds_read_b64_tr_b16 a[246:247], v34 offset:9536\n\t"
  "v_cmp_ne_u64 vcc, v[48:49], v[8:9]\n\t"
  "s_nop 1\n\t"
  "s_cbranch_vccnz SPINK_%=\n\t"
  "SRETK_%=:\n\t"
  "s_add_u32 m0, s84, 0x10000\n\t"
  "v_mfma_f32_16x16x32_bf16 a[36:39], a[224:227], v[212:215], a[36:39]\n\t"
  "buffer_load_dwordx4 v112, s[48:51], s78 offen offset:0 lds\n\t"
  "v_mfma_f32_16x16x32_bf16 a[40:43], a[228:231], v[208:211], a[40:43]\n\t"
  "ds_read_b64_tr_b16 a[248:249], v34 offset:7264\n\t"
  "v_mul_f32 v120, v120, v121\n\t"
  "v_mfma_f32_16x16x32_bf16 a[44:47], a[228:231], v[212:215], a[44:47]\n\t"
  "s_waitcnt lgkmcnt(5)\n\t"
  "ds_read_b64_tr_b16 a[250:251], v34 offset:9568\n\t"
  "v_mul_f32 v122, v122, v123\n\t"
  "v_mfma_f32_16x16x32_bf16 a[48:51], a[232:235], v[208:211], a[48:51]\n\t"
  "ds_read_b64_tr_b16 a[252:253], v34 offset:7296\n\t"
  "ds_read_b64_tr_b16 a[254:255], v34 offset:9600\n\t"
  "v_mfma_f32_16x16x32_bf16 a[52:55], a[232:235], v[212:215], a[52:55]\n\t"
  "buffer_load_dwordx4 v113, s[48:51], s78 offen offset:1024 lds\n\t"
  "v_mfma_f32_16x16x32_bf16 a[56:59], a[236:239], v[208:211], a[56:59]\n\t"
  "ds_read_b64_tr_b16 a[224:225], v34 offset:7328\n\t"
  "v_cvt_pk_bf16_f32 v120, v120, v122\n\t"
  "v_mfma_f32_16x16x32_bf16 a[60:63], a[236:239], v[212:215], a[60:63]\n\t"
  "s_waitcnt lgkmcnt(5)\n\t"
  "ds_read_b64_tr_b16 a[226:227], v34 offset:9632\n\t"
  "v_accvgpr_write_b32 a220, v120\n\t"
  "v_mfma_f32_16x16x32_bf16 v[64:67], a[240:243], v[208:211], v[64:67]\n\t"
  "ds_read_b64_tr_b16 a[228:229], v34 offset:7360\n\t"
  "ds_read_b64_tr_b16 a[230:231], v34 offset:9664\n\t"
  "v_mfma_f32_16x16x32_bf16 v[68:71], a[240:243], v[212:215], v[68:71]\n\t"
  "buffer_load_dwordx4 v114, s[48:51], s78 offen offset:2048 lds\n\t"
  "v_mfma_f32_16x16x32_bf16 v[72:75], a[244:247], v[208:211], v[72:75]\n\t"
  "ds_read_b64_tr_b16 a[232:233], v34 offset:7392\n\t"
  "v_accvgpr_read_b32 v124, a166\n\t"
  "v_mfma_f32_16x16x32_bf16 v[76:79], a[244:247], v[212:215], v[76:79]\n\t"
  "s_waitcnt lgkmcnt(5)\n\t"
  "ds_read_b64_tr_b16 a[234:235], v34 offset:9696\n\t"
  "v_accvgpr_read_b32 v125, a222\n\t"
  "v_mfma_f32_16x16x32_bf16 v[80:83], a[248:251], v[208:211], v[80:83]\n\t"
  "ds_read_b64_tr_b16 a[236:237], v34 offset:7424\n\t"
  "ds_read_b64_tr_b16 a[238:239], v34 offset:9728\n\t"
  "v_mfma_f32_16x16x32_bf16 v[84:87], a[248:251], v[212:215], v[84:87]\n\t"
  "v_accvgpr_read_b32 v126, a167\n\t"
  "v_accvgpr_read_b32 v127, a223\n\t"
  "v_mfma_f32_16x16x32_bf16 v[88:91], a[252:255], v[208:211], v[88:91]\n\t"
  "ds_read_b64_tr_b16 a[240:241], v32 offset:16384\n\t"
  "ds_read_b64_tr_b16 a[242:243], v32 offset:18176\n\t"
  "v_mfma_f32_16x16x32_bf16 v[92:95], a[252:255], v[212:215], v[92:95]\n\t"
  "s_waitcnt lgkmcnt(6)\n\t"
  "v_mul_f32 v124, v124, v125\n\t"
  "v_mul_f32 v126, v126, v127\n\t"
  "v_mfma_f32_16x16x32_bf16 v[96:99], a[224:227], v[208:211], v[96:99]\n\t"
  "ds_read_b64_tr_b16 a[244:245], v32 offset:16416\n\t"
  "ds_read_b64_tr_b16 a[246:247], v32 offset:18208\n\t"
  "v_mfma_f32_16x16x32_bf16 v[100:103], a[224:227], v[212:215], v[100:103]\n\t"
  "buffer_load_dwordx4 v115, s[48:51], s78 offen offset:3072 lds\n\t"
  "v_mfma_f32_16x16x32_bf16 v[104:107], a[228:231], v[208:211], v[104:107]\n\t"
  "s_add_u32 m0, s84, 0x11000\n\t"
  "ds_read_b64_tr_b16 a[248:249], v32 offset:16448\n\t"
  "v_cvt_pk_bf16_f32 v124, v124, v126\n\t"
  "v_mfma_f32_16x16x32_bf16 v[108:111], a[228:231], v[212:215], v[108:111]\n\t"
  "s_waitcnt lgkmcnt(5)\n\t"
  "ds_read_b64_tr_b16 a[250:251], v32 offset:18240\n\t"
  "v_accvgpr_write_b32 a221, v124\n\t"
  "v_mfma_f32_16x16x32_bf16 v[240:243], a[232:235], v[208:211], v[240:243]\n\t"
  "ds_read_b64_tr_b16 a[252:253], v32 offset:16480\n\t"
  "ds_read_b64_tr_b16 a[254:255], v32 offset:18272\n\t"
  "v_mfma_f32_16x16x32_bf16 v[244:247], a[232:235], v[212:215], v[244:247]\n\t"
  "buffer_load_dwordx4 v116, s[48:51], s78 offen offset:0 lds\n\t"
  "v_mfma_f32_16x16x32_bf16 v[248:251], a[236:239], v[208:211], v[248:251]\n\t"
  "ds_read_b64_tr_b16 a[224:225], v32 offset:16512\n\t"
  "v_mfma_f32_16x16x32_bf16 v[252:255], a[236:239], v[212:215], v[252:255]\n\t"
  "s_waitcnt lgkmcnt(5)\n\t"
  "ds_read_b64_tr_b16 a[226:227], v32 offset:18304\n\t"
  "v_mfma_f32_16x16x32_bf16 a[0:3], a[240:243], v[216:219], a[0:3]\n\t"
  "ds_read_b64_tr_b16 a[228:229], v32 offset:16544\n\t"
  "ds_read_b64_tr_b16 a[230:231], v32 offset:18336\n\t"
  "v_mfma_f32_16x16x32_bf16 a[4:7], a[240:243], v[220:223], a[4:7]\n\t"
  "v_mfma_f32_16x16x32_bf16 a[8:11], a[244:247], v[216:219], a[8:11]\n\t"
  "ds_read_b64_tr_b16 a[232:233], v32 offset:16576\n\t"
  "ds_read_b64_tr_b16 a[234:235], v32 offset:18368\n\t"
  "v_mfma_f32_16x16x32_bf16 a[12:15], a[244:247], v[220:223], a[12:15]\n\t"
  "s_waitcnt lgkmcnt(6)\n\t"
  "v_mfma_f32_16x16x32_bf16 a[16:19], a[248:251], v[216:219], a[16:19]\n\t"
  "ds_read_b64_tr_b16 a[236:237], v34 offset:23552\n\t"
  "ds_read_b64_tr_b16 a[238:239], v34 offset:25856\n\t"
  "v_mfma_f32_16x16x32_bf16 a[20:23], a[248:251], v[220:223], a[20:23]\n\t"
  "buffer_store_dwordx2 a[220:221], v40, s[56:59], s35 offen offset:1024 sc1\n\t"
  "v_mfma_f32_16x16x32_bf16 a[24:27], a[252:255], v[216:219], a[24:27]\n\t"
  "ds_read_b64_tr_b16 a[240:241], v34 offset:23584\n\t"
  "v_mfma_f32_16x16x32_bf16 a[28:31], a[252:255], v[220:223], a[28:31]\n\t"
  "s_waitcnt lgkmcnt(5)\n\t"
  "ds_read_b64_tr_b16 a[242:243], v34 offset:25888\n\t"
  "v_mfma_f32_16x16x32_bf16 a[32:35], a[224:227], v[216:219], a[32:35]\n\t"
  "s_waitcnt vmcnt(22)\n\t"
  "ds_read_b64_tr_b16 a[244:245], v34 offset:23616\n\t"
  "ds_read_b64_tr_b16 a[246:247], v34 offset:25920\n\t"
  "v_mfma_f32_16x16x32_bf16 a[36:39], a[224:227], v[220:223], a[36:39]\n\t"
  "buffer_load_dwordx4 v117, s[48:51], s78 offen offset:1024 lds\n\t"
  "v_mfma_f32_16x16x32_bf16 a[40:43], a[228:231], v[216:219], a[40:43]\n\t"
  "ds_read_b64_tr_b16 a[248:249], v34 offset:23648\n\t"
  "v_mfma_f32_16x16x32_bf16 a[44:47], a[228:231], v[220:223], a[44:47]\n\t"
  "s_waitcnt lgkmcnt(5)\n\t"
  "ds_read_b64_tr_b16 a[250:251], v34 offset:25952\n\t"
  "ds_read_b128 a[96:99], v28 offset:0\n\t"
  "v_mfma_f32_16x16x32_bf16 a[48:51], a[232:235], v[216:219], a[48:51]\n\t"
  "ds_read_b64_tr_b16 a[252:253], v34 offset:23680\n\t"
  "ds_read_b64_tr_b16 a[254:255], v34 offset:25984\n\t"
  "v_mfma_f32_16x16x32_bf16 a[52:55], a[232:235], v[220:223], a[52:55]\n\t"
  "buffer_load_dwordx4 v118, s[48:51], s78 offen offset:2048 lds\n\t"
  "ds_read_b128 a[100:103], v28 offset:1024\n\t"
  "ds_read_b128 a[104:107], v28 offset:2048\n\t"
  "ds_read_b128 a[108:111], v28 offset:3072\n\t"
  "v_mfma_f32_16x16x32_bf16 a[56:59], a[236:239], v[216:219], a[56:59]\n\t"
  "s_waitcnt lgkmcnt(0)\n\t"
  "ds_read_b64_tr_b16 a[224:225], v34 offset:23712\n\t"
  "v_mfma_f32_16x16x32_bf16 a[60:63], a[236:239], v[220:223], a[60:63]\n\t"
  "s_waitcnt lgkmcnt(9)\n\t"
  "ds_read_b64_tr_b16 a[226:227], v34 offset:26016\n\t"
  "v_mfma_f32_16x16x32_bf16 v[64:67], a[240:243], v[216:219], v[64:67]\n\t"
  "s_add_u32 s78, s78, 0x1c000\n\t"
  "ds_read_b64_tr_b16 a[228:229], v34 offset:23744\n\t"
  "ds_read_b64_tr_b16 a[230:231], v34 offset:26048\n\t"
  "v_mfma_f32_16x16x32_bf16 v[68:71], a[240:243], v[220:223], v[68:71]\n\t"
  "v_mfma_f32_16x16x32_bf16 v[72:75], a[244:247], v[216:219], v[72:75]\n\t"
  "ds_read_b64_tr_b16 a[232:233], v34 offset:23776\n\t"
  "ds_read_b64_tr_b16 a[234:235], v34 offset:26080\n\t"
  "v_mfma_f32_16x16x32_bf16 v[76:79], a[244:247], v[220:223], v[76:79]\n\t"
  "s_waitcnt lgkmcnt(9)\n\t"
  "v_mfma_f32_16x16x32_bf16 v[80:83], a[248:251], v[216:219], v[80:83]\n\t"
  "s_cbranch_execz TRAMP_%=\n\t"
  "s_waitcnt vmcnt(13)\n\t"
  "ds_read_b64_tr_b16 a[236:237], v34 offset:23808\n\t"
  "ds_read_b64_tr_b16 a[238:239], v34 offset:26112\n\t"
  "v_mfma_f32_16x16x32_bf16 v[84:87], a[248:251], v[220:223], v[84:87]\n\t"
  "s_barrier\n\t"
  "v_mfma_f32_16x16x32_bf16 v[88:91], a[252:255], v[216:219], v[88:91]\n\t"
  "ds_read_b64_tr_b16 a[240:241], v32 offset:32768\n\t"
  "ds_read_b64_tr_b16 a[242:243], v32 offset:34560\n\t"
  "v_mfma_f32_16x16x32_bf16 v[92:95], a[252:255], v[220:223], v[92:95]\n\t"
  "s_waitcnt lgkmcnt(6)\n\t"
  "v_mfma_f32_16x16x32_bf16 v[96:99], a[224:227], v[216:219], v[96:99]\n\t"
  "ds_read_b64_tr_b16 a[244:245], v32 offset:49152\n\t"
  "ds_read_b64_tr_b16 a[246:247], v32 offset:50944\n\t"
  "v_mfma_f32_16x16x32_bf16 v[100:103], a[224:227], v[220:223], v[100:103]\n\t"
  "v_mfma_f32_16x16x32_bf16 v[104:107], a[228:231], v[216:219], v[104:107]\n\t"
  "ds_read_b64_tr_b16 a[248:249], v32 offset:32800\n\t"
  "ds_read_b64_tr_b16 a[250:251], v32 offset:34592\n\t"
  "v_mfma_f32_16x16x32_bf16 v[108:111], a[228:231], v[220:223], v[108:111]\n\t"
  "s_waitcnt lgkmcnt(6)\n\t"
  "v_mfma_f32_16x16x32_bf16 v[240:243], a[232:235], v[216:219], v[240:243]\n\t"
  "ds_read_b64_tr_b16 a[252:253], v32 offset:49184\n\t"
  "ds_read_b64_tr_b16 a[254:255], v32 offset:50976\n\t"
  "v_mfma_f32_16x16x32_bf16 v[244:247], a[232:235], v[220:223], v[244:247]\n\t"
  "v_mfma_f32_16x16x32_bf16 v[248:251], a[236:239], v[216:219], v[248:251]\n\t"
  "ds_read_b64_tr_b16 a[224:225], v32 offset:32832\n\t"
  "ds_read_b64_tr_b16 a[226:227], v32 offset:34624\n\t"
  "v_mfma_f32_16x16x32_bf16 v[252:255], a[236:239], v[220:223], v[252:255]\n\t"
  "s_waitcnt lgkmcnt(6)\n\t"
  "v_mfma_f32_16x16x32_bf16 a[0:3], a[240:243], v[224:227], a[0:3]\n\t"
  "ds_read_b64_tr_b16 a[228:229], v32 offset:49216\n\t"
  "ds_read_b64_tr_b16 a[230:231], v32 offset:51008\n\t"
  "v_mfma_f32_16x16x32_bf16 a[4:7], a[240:243], v[228:231], a[4:7]\n\t"
  "v_mfma_f32_16x16x32_bf16 a[0:3], a[244:247], v[232:235], a[0:3]\n\t"
  "s_add_u32 m0, s84, 0x18000\n\t"
  "ds_read_b64_tr_b16 a[232:233], v32 offset:32864\n\t"
  "ds_read_b64_tr_b16 a[234:235], v32 offset:34656\n\t"
  "v_mfma_f32_16x16x32_bf16 a[4:7], a[244:247], v[236:239], a[4:7]\n\t"
  "s_waitcnt lgkmcnt(6)\n\t"
  "v_mfma_f32_16x16x32_bf16 a[8:11], a[248:251], v[224:227], a[8:11]\n\t"
  "ds_read_b64_tr_b16 a[236:237], v32 offset:49248\n\t"
  "ds_read_b64_tr_b16 a[238:239], v32 offset:51040\n\t"
  "v_mfma_f32_16x16x32_bf16 a[12:15], a[248:251], v[228:231], a[12:15]\n\t"
  "buffer_load_dwordx4 v112, s[48:51], s78 offen offset:0 lds\n\t"
  "v_mfma_f32_16x16x32_bf16 a[8:11], a[252:255], v[232:235], a[8:11]\n\t"
  "ds_read_b64_tr_b16 a[240:241], v32 offset:32896\n\t"
  "v_accvgpr_read_b32 v120, a0\n\t"
  "v_mfma_f32_16x16x32_bf16 a[12:15], a[252:255], v[236:239], a[12:15]\n\t"
  "s_waitcnt lgkmcnt(5)\n\t"
  "ds_read_b64_tr_b16 a[242:243], v32 offset:34688\n\t"
  "v_accvgpr_read_b32 v121, a1\n\t"
  "v_mfma_f32_16x16x32_bf16 a[16:19], a[224:227], v[224:227], a[16:19]\n\t"
  "ds_read_b64_tr_b16 a[244:245], v32 offset:49280\n\t"
  "ds_read_b64_tr_b16 a[246:247], v32 offset:51072\n\t"
  "v_mfma_f32_16x16x32_bf16 a[20:23], a[224:227], v[228:231], a[20:23]\n\t"
  "buffer_load_dwordx4 v113, s[48:51], s78 offen offset:1024 lds\n\t"
  "v_mfma_f32_16x16x32_bf16 a[16:19], a[228:231], v[232:235], a[16:19]\n\t"
  "ds_read_b64_tr_b16 a[248:249], v32 offset:32928\n\t"
  "v_accvgpr_read_b32 v122, a2\n\t"
  "v_mfma_f32_16x16x32_bf16 a[20:23], a[228:231], v[236:239], a[20:23]\n\t"
  "s_waitcnt lgkmcnt(5)\n\t"
  "ds_read_b64_tr_b16 a[250:251], v32 offset:34720\n\t"
  "v_accvgpr_read_b32 v123, a3\n\t"
  "buffer_load_dwordx2 v[48:49], v24, s[56:59], s54 offen offset:0 sc1\n\t"
  "v_mfma_f32_16x16x32_bf16 a[24:27], a[232:235], v[224:227], a[24:27]\n\t"
  "ds_read_b64_tr_b16 a[252:253], v32 offset:49312\n\t"
  "ds_read_b64_tr_b16 a[254:255], v32 offset:51104\n\t"
  "v_mfma_f32_16x16x32_bf16 a[28:31], a[232:235], v[228:231], a[28:31]\n\t"
  "v_cvt_pk_bf16_f32 v184, v120, v121\n\t"
  "v_cvt_pk_bf16_f32 v185, v122, v123\n\t"
  "v_mfma_f32_16x16x32_bf16 a[24:27], a[236:239], v[232:235], a[24:27]\n\t"
  "ds_read_b64_tr_b16 a[224:225], v32 offset:32960\n\t"
  "ds_read_b64_tr_b16 a[226:227], v32 offset:34752\n\t"
  "v_mfma_f32_16x16x32_bf16 a[28:31], a[236:239], v[236:239], a[28:31]\n\t"
  "s_waitcnt lgkmcnt(6)\n\t"
  "v_accvgpr_read_b32 v120, a4\n\t"
  "v_accvgpr_read_b32 v121, a5\n\t"
  "v_mfma_f32_16x16x32_bf16 a[32:35], a[240:243], v[224:227], a[32:35]\n\t"
  "ds_read_b64_tr_b16 a[228:229], v32 offset:49344\n\t"
  "ds_read_b64_tr_b16 a[230:231], v32 offset:51136\n\t"
  "v_mfma_f32_16x16x32_bf16 a[36:39], a[240:243], v[228:231], a[36:39]\n\t"
  "buffer_load_dwordx4 v114, s[48:51], s78 offen offset:2048 lds\n\t"
  "v_mfma_f32_16x16x32_bf16 a[32:35], a[244:247], v[232:235], a[32:35]\n\t"
  "ds_read_b64_tr_b16 a[232:233], v34 offset:39936\n\t"
  "v_accvgpr_read_b32 v122, a6\n\t"
  "v_mfma_f32_16x16x32_bf16 a[36:39], a[244:247], v[236:239], a[36:39]\n\t"
  "s_waitcnt lgkmcnt(5)\n\t"
  "ds_read_b64_tr_b16 a[234:235], v34 offset:42240\n\t"
  "v_accvgpr_read_b32 v123, a7\n\t"
  "v_mfma_f32_16x16x32_bf16 a[40:43], a[248:251], v[224:227], a[40:43]\n\t"
  "ds_read_b64_tr_b16 a[236:237], v34 offset:56320\n\t"
  "ds_read_b64_tr_b16 a[238:239], v34 offset:58624\n\t"
  "v_mfma_f32_16x16x32_bf16 a[44:47], a[248:251], v[228:231], a[44:47]\n\t"
  "v_cvt_pk_bf16_f32 v186, v120, v121\n\t"
  "v_cvt_pk_bf16_f32 v187, v122, v123\n\t"
  "v_mfma_f32_16x16x32_bf16 a[40:43], a[252:255], v[232:235], a[40:43]\n\t"
  "ds_read_b64_tr_b16 a[240:241], v34 offset:39968\n\t"
  "ds_read_b64_tr_b16 a[242:243], v34 offset:42272\n\t"
  "v_mfma_f32_16x16x32_bf16 a[44:47], a[252:255], v[236:239], a[44:47]\n\t"
  "s_waitcnt lgkmcnt(6)\n\t"
  "v_accvgpr_read_b32 v124, a8\n\t"
  "v_accvgpr_read_b32 v125, a9\n\t"
  "v_mfma_f32_16x16x32_bf16 a[48:51], a[224:227], v[224:227], a[48:51]\n\t"
  "ds_read_b64_tr_b16 a[244:245], v34 offset:56352\n\t"
  "ds_read_b64_tr_b16 a[246:247], v34 offset:58656\n\t"
  "v_mfma_f32_16x16x32_bf16 a[52:55], a[224:227], v[228:231], a[52:55]\n\t"
  "buffer_load_dwordx4 v115, s[48:51], s78 offen offset:3072 lds\n\t"
  "v_mfma_f32_16x16x32_bf16 a[48:51], a[228:231], v[232:235], a[48:51]\n\t"
  "s_add_u32 m0, s84, 0x19000\n\t"
  "ds_read_b64_tr_b16 a[248:249], v34 offset:40000\n\t"
  "v_accvgpr_read_b32 v126, a10\n\t"
  "v_mfma_f32_16x16x32_bf16 a[52:55], a[228:231], v[236:239], a[52:55]\n\t"
  "s_waitcnt lgkmcnt(5)\n\t"
  "ds_read_b64_tr_b16 a[250:251], v34 offset:42304\n\t"
  "v_accvgpr_read_b32 v127, a11\n\t"
  "v_mfma_f32_16x16x32_bf16 a[56:59], a[232:235], v[224:227], a[56:59]\n\t"
  "ds_read_b64_tr_b16 a[252:253], v34 offset:56384\n\t"
  "ds_read_b64_tr_b16 a[254:255], v34 offset:58688\n\t"
  "v_mfma_f32_16x16x32_bf16 a[60:63], a[232:235], v[228:231], a[60:63]\n\t"
  "buffer_load_dwordx4 v116, s[48:51], s78 offen offset:0 lds\n\t"
  "v_mfma_f32_16x16x32_bf16 a[56:59], a[236:239], v[232:235], a[56:59]\n\t"
  "ds_read_b64_tr_b16 a[224:225], v34 offset:40032\n\t"
  "v_cvt_pk_bf16_f32 v188, v124, v125\n\t"
  "v_mfma_f32_16x16x32_bf16 a[60:63], a[236:239], v[236:239], a[60:63]\n\t"
  "s_waitcnt lgkmcnt(5)\n\t"
  "ds_read_b64_tr_b16 a[226:227], v34 offset:42336\n\t"
  "v_cvt_pk_bf16_f32 v189, v126, v127\n\t"
  "v_mfma_f32_16x16x32_bf16 v[64:67], a[240:243], v[224:227], v[64:67]\n\t"
  "ds_read_b64_tr_b16 a[228:229], v34 offset:56416\n\t"
  "ds_read_b64_tr_b16 a[230:231], v34 offset:58720\n\t"
  "v_mfma_f32_16x16x32_bf16 v[68:71], a[240:243], v[228:231], v[68:71]\n\t"
  "v_accvgpr_write_b32 a0, v184\n\t"
  "v_accvgpr_write_b32 a1, v185\n\t"
  "v_mfma_f32_16x16x32_bf16 v[64:67], a[244:247], v[232:235], v[64:67]\n\t"
  "ds_read_b64_tr_b16 a[232:233], v34 offset:40064\n\t"
  "ds_read_b64_tr_b16 a[234:235], v34 offset:42368\n\t"
  "v_mfma_f32_16x16x32_bf16 v[68:71], a[244:247], v[236:239], v[68:71]\n\t"
  "s_waitcnt lgkmcnt(6)\n\t"
  "v_accvgpr_write_b32 a2, v188\n\t"
  "v_accvgpr_write_b32 a3, v189\n\t"
  "v_mfma_f32_16x16x32_bf16 v[72:75], a[248:251], v[224:227], v[72:75]\n\t"
  "ds_read_b64_tr_b16 a[236:237], v34 offset:56448\n\t"
  "ds_read_b64_tr_b16 a[238:239], v34 offset:58752\n\t"
  "v_mfma_f32_16x16x32_bf16 v[76:79], a[248:251], v[228:231], v[76:79]\n\t"
  "buffer_load_dwordx4 v117, s[48:51], s78 offen offset:1024 lds\n\t"
  "v_mfma_f32_16x16x32_bf16 v[72:75], a[252:255], v[232:235], v[72:75]\n\t"
  "ds_read_b64_tr_b16 a[240:241], v34 offset:40096\n\t"
  "v_accvgpr_write_b32 a4, v186\n\t"
  "v_mfma_f32_16x16x32_bf16 v[76:79], a[252:255], v[236:239], v[76:79]\n\t"
  "s_waitcnt lgkmcnt(5)\n\t"
  "ds_read_b64_tr_b16 a[242:243], v34 offset:42400\n\t"
  "v_accvgpr_write_b32 a5, v187\n\t"
  "v_mfma_f32_16x16x32_bf16 v[80:83], a[224:227], v[224:227], v[80:83]\n\t"
  "ds_read_b64_tr_b16 a[244:245], v34 offset:56480\n\t"
  "ds_read_b64_tr_b16 a[246:247], v34 offset:58784\n\t"
  "v_mfma_f32_16x16x32_bf16 v[84:87], a[224:227], v[228:231], v[84:87]\n\t"
  "v_accvgpr_read_b32 v124, a12\n\t"
  "v_accvgpr_read_b32 v125, a13\n\t"
  "v_mfma_f32_16x16x32_bf16 v[80:83], a[228:231], v[232:235], v[80:83]\n\t"
  "ds_read_b64_tr_b16 a[248:249], v34 offset:40128\n\t"
  "ds_read_b64_tr_b16 a[250:251], v34 offset:42432\n\t"
  "v_mfma_f32_16x16x32_bf16 v[84:87], a[228:231], v[236:239], v[84:87]\n\t"
  "s_waitcnt lgkmcnt(6)\n\t"
  "v_accvgpr_read_b32 v126, a14\n\t"
  "v_accvgpr_read_b32 v127, a15\n\t"
  "v_mfma_f32_16x16x32_bf16 v[88:91], a[232:235], v[224:227], v[88:91]\n\t"
  "ds_read_b64_tr_b16 a[252:253], v34 offset:56512\n\t"
  "ds_read_b64_tr_b16 a[254:255], v34 offset:58816\n\t"
  "v_mfma_f32_16x16x32_bf16 v[92:95], a[232:235], v[228:231], v[92:95]\n\t"
  "buffer_load_dwordx4 v118, s[48:51], s78 offen offset:2048 lds\n\t"
  "v_mfma_f32_16x16x32_bf16 v[88:91], a[236:239], v[232:235], v[88:91]\n\t"
  "ds_read_b64_tr_b16 a[224:225], v34 offset:40160\n\t"
  "v_cvt_pk_bf16_f32 v190, v124, v125\n\t"
  "v_mfma_f32_16x16x32_bf16 v[92:95], a[236:239], v[236:239], v[92:95]\n\t"
  "s_waitcnt lgkmcnt(5)\n\t"
  "ds_read_b64_tr_b16 a[226:227], v34 offset:42464\n\t"
  "v_cvt_pk_bf16_f32 v191, v126, v127\n\t"
  "v_mfma_f32_16x16x32_bf16 v[96:99], a[240:243], v[224:227], v[96:99]\n\t"
  "ds_read_b64_tr_b16 a[228:229], v34 offset:56544\n\t"
  "ds_read_b64_tr_b16 a[230:231], v34 offset:58848\n\t"
  "v_mfma_f32_16x16x32_bf16 v[100:103], a[240:243], v[228:231], v[100:103]\n\t"
  "v_accvgpr_write_b32 a6, v190\n\t"
  "v_accvgpr_write_b32 a7, v191\n\t"
  "v_mfma_f32_16x16x32_bf16 v[96:99], a[244:247], v[232:235], v[96:99]\n\t"
  "ds_read_b64_tr_b16 a[232:233], v34 offset:40192\n\t"
  "ds_read_b64_tr_b16 a[234:235], v34 offset:42496\n\t"
  "v_mfma_f32_16x16x32_bf16 v[100:103], a[244:247], v[236:239], v[100:103]\n\t"
  "s_waitcnt lgkmcnt(6)\n\t"
  "v_accvgpr_read_b32 v124, a24\n\t"
  "v_accvgpr_read_b32 v125, a25\n\t"
  "v_mfma_f32_16x16x32_bf16 v[104:107], a[248:251], v[224:227], v[104:107]\n\t"
  "s_waitcnt vmcnt(8)\n\t"
  "ds_read_b64_tr_b16 a[236:237], v34 offset:56576\n\t"
  "ds_read_b64_tr_b16 a[238:239], v34 offset:58880\n\t"
  "v_mfma_f32_16x16x32_bf16 v[108:111], a[248:251], v[228:231], v[108:111]\n\t"
  "s_barrier\n\t"
  "buffer_store_dwordx2 v[8:9], v12, s[56:59], s12 offen offset:0 sc1\n\t"
  "v_mfma_f32_16x16x32_bf16 v[104:107], a[252:255], v[232:235], v[104:107]\n\t"
  "ds_read_b64_tr_b16 a[240:241], v33 offset:0\n\t"
  "ds_read_b64_tr_b16 a[242:243], v33 offset:1792\n\t"
  "v_mfma_f32_16x16x32_bf16 v[108:111], a[252:255], v[236:239], v[108:111]\n\t"
  "s_waitcnt lgkmcnt(6)\n\t"
  "v_cvt_pk_bf16_f32 v196, v124, v125\n\t"
  "v_accvgpr_read_b32 v124, a28\n\t"
  "v_mfma_f32_16x16x32_bf16 v[240:243], a[224:227], v[224:227], v[240:243]\n\t"
  "ds_read_b64_tr_b16 a[244:245], v33 offset:32\n\t"
  "ds_read_b64_tr_b16 a[246:247], v33 offset:1824\n\t"
  "v_mfma_f32_16x16x32_bf16 v[244:247], a[224:227], v[228:231], v[244:247]\n\t"
  "v_accvgpr_read_b32 v125, a29\n\t"
  "v_cvt_pk_bf16_f32 v80, v80, v81\n\t"
  "v_mfma_f32_16x16x32_bf16 v[240:243], a[228:231], v[232:235], v[240:243]\n\t"
  "ds_read_b64_tr_b16 a[248:249], v33 offset:64\n\t"
  "ds_read_b64_tr_b16 a[250:251], v33 offset:1856\n\t"
  "v_mfma_f32_16x16x32_bf16 v[244:247], a[228:231], v[236:239], v[244:247]\n\t"
  "s_waitcnt lgkmcnt(6)\n\t"
  "v_cvt_pk_bf16_f32 v198, v124, v125\n\t"
  "v_accvgpr_read_b32 v124, a40\n\t"
  "v_mfma_f32_16x16x32_bf16 v[248:251], a[232:235], v[224:227], v[248:251]\n\t"
  "ds_read_b64_tr_b16 a[252:253], v33 offset:96\n\t"
  "ds_read_b64_tr_b16 a[254:255], v33 offset:1888\n\t"
  "v_mfma_f32_16x16x32_bf16 v[252:255], a[232:235], v[228:231], v[252:255]\n\t"
  "v_accvgpr_read_b32 v125, a41\n\t"
  "v_cvt_pk_bf16_f32 v81, v82, v83\n\t"
  "v_mfma_f32_16x16x32_bf16 v[248:251], a[236:239], v[232:235], v[248:251]\n\t"
  "ds_read_b64_tr_b16 a[224:225], v33 offset:128\n\t"
  "ds_read_b64_tr_b16 a[226:227], v33 offset:1920\n\t"
  "v_mfma_f32_16x16x32_bf16 v[252:255], a[236:239], v[236:239], v[252:255]\n\t"
  "s_waitcnt lgkmcnt(6)\n\t"
  "v_accvgpr_write_b32 a40, v80\n\t"
  "v_accvgpr_write_b32 a41, v81\n\t"
  "v_mfma_f32_16x16x32_bf16 v[128:131], a[64:67], a[240:243], 0\n\t"
  "ds_read_b64_tr_b16 a[228:229], v33 offset:160\n\t"
  "ds_read_b64_tr_b16 a[230:231], v33 offset:1952\n\t"
  "v_mfma_f32_16x16x32_bf16 v[132:135], a[68:71], a[240:243], 0\n\t"
  "v_cvt_pk_bf16_f32 v204, v124, v125\n\t"
  "v_accvgpr_read_b32 v124, a44\n\t"
  "v_mfma_f32_16x16x32_bf16 v[136:139], a[64:67], a[244:247], 0\n\t"
  "s_add_u32 s76, s76, s84\n\t"
  "ds_read_b64_tr_b16 a[232:233], v33 offset:192\n\t"
  "ds_read_b64_tr_b16 a[234:235], v33 offset:1984\n\t"
  "v_mfma_f32_16x16x32_bf16 v[140:143], a[68:71], a[244:247], 0\n\t"
  "s_waitcnt lgkmcnt(6)\n\t"
  "v_accvgpr_read_b32 v125, a45\n\t"
  "v_cvt_pk_bf16_f32 v84, v84, v85\n\t"
  "v_mfma_f32_16x16x32_bf16 v[144:147], a[64:67], a[248:251], 0\n\t"
  "s_add_u32 m0, s84, 0\n\t"
  "ds_read_b64_tr_b16 a[236:237], v33 offset:8192\n\t"
  "ds_read_b64_tr_b16 a[238:239], v33 offset:9984\n\t"
  "v_mfma_f32_16x16x32_bf16 v[148:151], a[68:71], a[248:251], 0\n\t"
  "buffer_load_dwordx4 v40, s[56:59], s76 offen offset:0 sc1 lds\n\t"
  "v_mfma_f32_16x16x32_bf16 v[152:155], a[64:67], a[252:255], 0\n\t"
  "ds_read_b64_tr_b16 a[240:241], v33 offset:8224\n\t"
  "v_cvt_pk_bf16_f32 v85, v86, v87\n\t"
  "v_mfma_f32_16x16x32_bf16 v[156:159], a[68:71], a[252:255], 0\n\t"
  "s_waitcnt lgkmcnt(5)\n\t"
  "ds_read_b64_tr_b16 a[242:243], v33 offset:10016\n\t"
  "v_accvgpr_write_b32 a44, v84\n\t"
  "v_mfma_f32_16x16x32_bf16 v[160:163], a[64:67], a[224:227], 0\n\t"
  "s_waitcnt vmcnt(7)\n\t"
  "ds_read_b64_tr_b16 a[244:245], v33 offset:8256\n\t"
  "ds_read_b64_tr_b16 a[246:247], v33 offset:10048\n\t"
  "v_mfma_f32_16x16x32_bf16 v[164:167], a[68:71], a[224:227], 0\n\t"
  "buffer_load_dwordx4 v40, s[56:59], s76 offen offset:1024 sc1 lds\n\t"
  "v_mfma_f32_16x16x32_bf16 v[168:171], a[64:67], a[228:231], 0\n\t"
  "ds_read_b64_tr_b16 a[248:249], v33 offset:8288\n\t"
  "v_cmp_ne_u64 vcc, v[48:49], v[8:9]\n\t"
  "v_mfma_f32_16x16x32_bf16 v[172:175], a[68:71], a[228:231], 0\n\t"
  "ds_read_b64_tr_b16 a[250:251], v33 offset:10080\n\t"
  "s_waitcnt lgkmcnt(6)\n\t"
  "v_mfma_f32_16x16x32_bf16 v[176:179], a[64:67], a[232:235], 0\n\t"
  "s_cbranch_vccnz SPINW_%=\n\t"
  "SRETW_%=:\n\t"
  "v_mfma_f32_16x16x32_bf16 v[180:183], a[68:71], a[232:235], 0\n\t"
  "buffer_load_dwordx4 v40, s[56:59], s76 offen offset:2048 sc1 lds\n\t"
  "v_mfma_f32_16x16x32_bf16 v[128:131], a[72:75], a[236:239], v[128:131]\n\t"
  "ds_read_b64_tr_b16 a[252:253], v33 offset:8320\n\t"
  "v_accvgpr_write_b32 a45, v85\n\t"
  "v_mfma_f32_16x16x32_bf16 v[132:135], a[76:79], a[236:239], v[132:135]\n\t"
  "s_waitcnt lgkmcnt(3)\n\t"
  "ds_read_b64_tr_b16 a[254:255], v33 offset:10112\n\t"
  "ds_read_b64_tr_b16 a[224:225], v33 offset:8352\n\t"
  "v_mfma_f32_16x16x32_bf16 v[136:139], a[72:75], a[240:243], v[136:139]\n\t"
  "ds_read_b64_tr_b16 a[226:227], v33 offset:10144\n\t"
  "ds_read_b64_tr_b16 a[228:229], v33 offset:8384\n\t"
  "v_mfma_f32_16x16x32_bf16 v[140:143], a[76:79], a[240:243], v[140:143]\n\t"
  "buffer_load_dwordx4 v40, s[56:59], s76 offen offset:3072 sc1 lds\n\t"
  "v_mfma_f32_16x16x32_bf16 v[144:147], a[72:75], a[244:247], v[144:147]\n\t"
  "s_add_u32 s76, s76, 0x1000\n\t"
  "ds_read_b64_tr_b16 a[230:231], v33 offset:10176\n\t"
  "v_accvgpr_read_b32 v126, a26\n\t"
  "v_mfma_f32_16x16x32_bf16 v[148:151], a[76:79], a[244:247], v[148:151]\n\t"
  "s_waitcnt lgkmcnt(4)\n\t"
  "ds_read_b64_tr_b16 a[232:233], v33 offset:16384\n\t"
  "ds_read_b64_tr_b16 a[234:235], v33 offset:18176\n\t"
  "v_mfma_f32_16x16x32_bf16 v[152:155], a[72:75], a[248:251], v[152:155]\n\t"
  "ds_read_b64_tr_b16 a[236:237], v33 offset:16416\n\t"
  "ds_read_b64_tr_b16 a[238:239], v33 offset:18208\n\t"
  "v_mfma_f32_16x16x32_bf16 v[156:159], a[76:79], a[248:251], v[156:159]\n\t"
  "v_accvgpr_read_b32 v127, a27\n\t"
  "v_cvt_pk_bf16_f32 v88, v88, v89\n\t"
  "v_mfma_f32_16x16x32_bf16 v[160:163], a[72:75], a[252:255], v[160:163]\n\t"
  "s_add_u32 m0, s84, 0x1000\n\t"
  "ds_read_b64_tr_b16 a[240:241], v33 offset:16448\n\t"
  "ds_read_b64_tr_b16 a[242:243], v33 offset:18240\n\t"
  "v_mfma_f32_16x16x32_bf16 v[164:167], a[76:79], a[252:255], v[164:167]\n\t"
  "s_waitcnt lgkmcnt(6)\n\t"
  "v_cvt_pk_bf16_f32 v197, v126, v127\n\t"
  "v_accvgpr_read_b32 v126, a30\n\t"
  "v_mfma_f32_16x16x32_bf16 v[168:171], a[72:75], a[224:227], v[168:171]\n\t"
  "ds_read_b64_tr_b16 a[244:245], v33 offset:16480\n\t"
  "ds_read_b64_tr_b16 a[246:247], v33 offset:18272\n\t"
  "v_mfma_f32_16x16x32_bf16 v[172:175], a[76:79], a[224:227], v[172:175]\n\t"
  "buffer_load_dwordx4 v40, s[56:59], s76 offen offset:0 sc1 lds\n\t"
  "v_mfma_f32_16x16x32_bf16 v[176:179], a[72:75], a[228:231], v[176:179]\n\t"
  "ds_read_b64_tr_b16 a[248:249], v33 offset:16512\n\t"
  "v_accvgpr_read_b32 v127, a31\n\t"
  "v_mfma_f32_16x16x32_bf16 v[180:183], a[76:79], a[228:231], v[180:183]\n\t"
  "s_waitcnt lgkmcnt(5)\n\t"
  "ds_read_b64_tr_b16 a[250:251], v33 offset:18304\n\t"
  "v_cvt_pk_bf16_f32 v199, v126, v127\n\t"
  "v_mfma_f32_16x16x32_bf16 v[128:131], a[80:83], a[232:235], v[128:131]\n\t"
  "ds_read_b64_tr_b16 a[252:253], v33 offset:16544\n\t"
  "ds_read_b64_tr_b16 a[254:255], v33 offset:18336\n\t"
  "v_mfma_f32_16x16x32_bf16 v[132:135], a[84:87], a[232:235], v[132:135]\n\t"
  "buffer_load_dwordx4 v40, s[56:59], s76 offen offset:1024 sc1 lds\n\t"
  "v_mfma_f32_16x16x32_bf16 v[136:139], a[80:83], a[236:239], v[136:139]\n\t"
  "ds_read_b64_tr_b16 a[224:225], v33 offset:16576\n\t"
  "v_accvgpr_read_b32 v126, a42\n\t"
  "v_mfma_f32_16x16x32_bf16 v[140:143], a[84:87], a[236:239], v[140:143]\n\t"
  "s_waitcnt lgkmcnt(5)\n\t"
  "ds_read_b64_tr_b16 a[226:227], v33 offset:18368\n\t"
  "v_accvgpr_read_b32 v127, a43\n\t"
  "v_mfma_f32_16x16x32_bf16 v[144:147], a[80:83], a[240:243], v[144:147]\n\t"
  "s_waitcnt vmcnt(24)\n\t"
  "ds_read_b64_tr_b16 a[228:229], v33 offset:24576\n\t"
  "ds_read_b64_tr_b16 a[230:231], v33 offset:26368\n\t"
  "v_mfma_f32_16x16x32_bf16 v[148:151], a[84:87], a[240:243], v[148:151]\n\t"
  "buffer_load_dwordx4 v40, s[56:59], s76 offen offset:2048 sc1 lds\n\t"
  "v_mfma_f32_16x16x32_bf16 v[152:155], a[80:83], a[244:247], v[152:155]\n\t"
  "ds_read_b64_tr_b16 a[232:233], v33 offset:24608\n\t"
  "v_cvt_pk_bf16_f32 v89, v90, v91\n\t"
  "v_mfma_f32_16x16x32_bf16 v[156:159], a[84:87], a[244:247], v[156:159]\n\t"
  "s_waitcnt lgkmcnt(5)\n\t"
  "ds_read_b64_tr_b16 a[234:235], v33 offset:26400\n\t"
  "ds_read_b128 a[64:67], v28 offset:4096\n\t"
  "v_mfma_f32_16x16x32_bf16 v[160:163], a[80:83], a[248:251], v[160:163]\n\t"
  "ds_read_b64_tr_b16 a[236:237], v33 offset:24640\n\t"
  "ds_read_b64_tr_b16 a[238:239], v33 offset:26432\n\t"
  "v_mfma_f32_16x16x32_bf16 v[164:167], a[84:87], a[248:251], v[164:167]\n\t"
  "buffer_load_dwordx4 v40, s[56:59], s76 offen offset:3072 sc1 lds\n\t"
  "ds_read_b128 a[68:71], v28 offset:5120\n\t"
  "ds_read_b128 a[72:75], v28 offset:6144\n\t"
  "ds_read_b128 a[76:79], v28 offset:7168\n\t"
  "v_mfma_f32_16x16x32_bf16 v[168:171], a[80:83], a[252:255], v[168:171]\n\t"
  "s_waitcnt lgkmcnt(0)\n\t"
  "ds_read_b64_tr_b16 a[240:241], v33 offset:24672\n\t"
  "v_accvgpr_write_b32 a42, v88\n\t"
  "v_mfma_f32_16x16x32_bf16 v[172:175], a[84:87], a[252:255], v[172:175]\n\t"
  "s_waitcnt lgkmcnt(9)\n\t"
  "ds_read_b64_tr_b16 a[242:243], v33 offset:26464\n\t"
  "v_accvgpr_write_b32 a43, v89\n\t"
  "v_mfma_f32_16x16x32_bf16 v[176:179], a[80:83], a[224:227], v[176:179]\n\t"
  "s_add_u32 s76, s76, 0x7000\n\t"
  "ds_read_b64_tr_b16 a[244:245], v33 offset:24704\n\t"
  "ds_read_b64_tr_b16 a[246:247], v33 offset:26496\n\t"
  "v_mfma_f32_16x16x32_bf16 v[180:183], a[84:87], a[224:227], v[180:183]\n\t"
  "v_cvt_pk_bf16_f32 v205, v126, v127\n\t"
  "v_accvgpr_read_b32 v126, a46\n\t"
  "v_mfma_f32_16x16x32_bf16 v[128:131], a[88:91], a[228:231], v[128:131]\n\t"
  "ds_read_b64_tr_b16 a[248:249], v33 offset:24736\n\t"
  "ds_read_b64_tr_b16 a[250:251], v33 offset:26528\n\t"
  "v_mfma_f32_16x16x32_bf16 v[132:135], a[92:95], a[228:231], v[132:135]\n\t"
  "s_waitcnt lgkmcnt(9)\n\t"
  "v_accvgpr_read_b32 v127, a47\n\t"
  "v_cvt_pk_bf16_f32 v92, v92, v93\n\t"
  "v_mfma_f32_16x16x32_bf16 v[136:139], a[88:91], a[232:235], v[136:139]\n\t"
  "s_waitcnt vmcnt(9)\n\t"
  "ds_read_b64_tr_b16 a[252:253], v33 offset:24768\n\t"
  "ds_read_b64_tr_b16 a[254:255], v33 offset:26560\n\t"
  "v_mfma_f32_16x16x32_bf16 v[140:143], a[92:95], a[232:235], v[140:143]\n\t"
  "s_barrier\n\t"
  "v_cvt_pk_bf16_f32 v93, v94, v95\n\t"
  "v_accvgpr_write_b32 a46, v92\n\t"
  "v_mfma_f32_16x16x32_bf16 v[144:147], a[88:91], a[236:239], v[144:147]\n\t"
  "ds_read_b64_tr_b16 a[224:225], v33 offset:32768\n\t"
  "ds_read_b64_tr_b16 a[226:227], v33 offset:34560\n\t"
  "v_mfma_f32_16x16x32_bf16 v[148:151], a[92:95], a[236:239], v[148:151]\n\t"
  "s_waitcnt lgkmcnt(6)\n\t"
  "v_accvgpr_write_b32 a47, v93\n\t"
  "v_accvgpr_read_b32 v120, a16\n\t"
  "v_mfma_f32_16x16x32_bf16 v[152:155], a[88:91], a[240:243], v[152:155]\n\t"
  "ds_read_b64_tr_b16 a[228:229], v33 offset:40960\n\t"
  "ds_read_b64_tr_b16 a[230:231], v33 offset:42752\n\t"
  "v_mfma_f32_16x16x32_bf16 v[156:159], a[92:95], a[240:243], v[156:159]\n\t"
  "buffer_load_dwordx2 v[48:49], v14, s[56:59], s14 offen offset:0 sc1\n\t"
  "v_mfma_f32_16x16x32_bf16 v[160:163], a[88:91], a[244:247], v[160:163]\n\t"
  "ds_read_b64_tr_b16 a[232:233], v33 offset:49152\n\t"
  "ds_read_b64_tr_b16 a[234:235], v33 offset:50944\n\t"
  "v_mfma_f32_16x16x32_bf16 v[164:167], a[92:95], a[244:247], v[164:167]\n\t"
  "s_waitcnt lgkmcnt(6)\n\t"
  "v_accvgpr_read_b32 v121, a17\n\t"
  "v_accvgpr_read_b32 v122, a18\n\t"
  "v_mfma_f32_16x16x32_bf16 v[168:171], a[88:91], a[248:251], v[168:171]\n\t"
  "ds_read_b64_tr_b16 a[236:237], v33 offset:57344\n\t"
  "ds_read_b64_tr_b16 a[238:239], v33 offset:59136\n\t"
  "v_mfma_f32_16x16x32_bf16 v[172:175], a[92:95], a[248:251], v[172:175]\n\t"
  "v_accvgpr_read_b32 v123, a19\n\t"
  "v_cvt_pk_bf16_f32 v192, v120, v121\n\t"
  "v_mfma_f32_16x16x32_bf16 v[176:179], a[88:91], a[252:255], v[176:179]\n\t"
  "ds_read_b64_tr_b16 a[240:241], v33 offset:32800\n\t"
  "ds_read_b64_tr_b16 a[242:243], v33 offset:34592\n\t"
  "v_mfma_f32_16x16x32_bf16 v[180:183], a[92:95], a[252:255], v[180:183]\n\t"
  "s_waitcnt lgkmcnt(6)\n\t"
  "v_cvt_pk_bf16_f32 v193, v122, v123\n\t"
  "v_accvgpr_read_b32 v120, a20\n\t"
  "v_mfma_f32_16x16x32_bf16 v[128:131], a[96:99], a[224:227], v[128:131]\n\t"
  "ds_read_b64_tr_b16 a[244:245], v33 offset:40992\n\t"
  "ds_read_b64_tr_b16 a[246:247], v33 offset:42784\n\t"
  "v_mfma_f32_16x16x32_bf16 v[132:135], a[100:103], a[224:227], v[132:135]\n\t"
  "v_accvgpr_read_b32 v121, a21\n\t"
  "v_accvgpr_read_b32 v122, a22\n\t"
  "v_mfma_f32_16x16x32_bf16 v[128:131], a[104:107], a[228:231], v[128:131]\n\t"
  "s_add_u32 m0, s84, 0x8000\n\t"
  "ds_read_b64_tr_b16 a[248:249], v33 offset:49184\n\t"
  "ds_read_b64_tr_b16 a[250:251], v33 offset:50976\n\t"
  "v_mfma_f32_16x16x32_bf16 v[132:135], a[108:111], a[228:231], v[132:135]\n\t"
  "s_waitcnt lgkmcnt(6)\n\t"
  "v_accvgpr_read_b32 v123, a23\n\t"
  "v_cvt_pk_bf16_f32 v194, v120, v121\n\t"
  "v_mfma_f32_16x16x32_bf16 v[128:131], a[64:67], a[232:235], v[128:131]\n\t"
  "ds_read_b64_tr_b16 a[252:253], v33 offset:57376\n\t"
  "ds_read_b64_tr_b16 a[254:255], v33 offset:59168\n\t"
  "v_mfma_f32_16x16x32_bf16 v[132:135], a[68:71], a[232:235], v[132:135]\n\t"
  "buffer_load_dwordx4 v40, s[56:59], s76 offen offset:0 sc1 lds\n\t"
  "v_mfma_f32_16x16x32_bf16 v[128:131], a[72:75], a[236:239], v[128:131]\n\t"
  "ds_read_b64_tr_b16 a[224:225], v33 offset:32832\n\t"
  "v_cvt_pk_bf16_f32 v195, v122, v123\n\t"
  "v_mfma_f32_16x16x32_bf16 v[132:135], a[76:79], a[236:239], v[132:135]\n\t"
  "s_waitcnt lgkmcnt(5)\n\t"
  "ds_read_b64_tr_b16 a[226:227], v33 offset:34624\n\t"
  "v_accvgpr_write_b32 a8, v192\n\t"
  "v_mfma_f32_16x16x32_bf16 v[136:139], a[96:99], a[240:243], v[136:139]\n\t"
  "ds_read_b64_tr_b16 a[228:229], v33 offset:41024\n\t"
  "ds_read_b64_tr_b16 a[230:231], v33 offset:42816\n\t"
  "v_mfma_f32_16x16x32_bf16 v[140:143], a[100:103], a[240:243], v[140:143]\n\t"
  "buffer_load_dwordx4 v40, s[56:59], s76 offen offset:1024 sc1 lds\n\t"
  "v_mfma_f32_16x16x32_bf16 v[136:139], a[104:107], a[244:247], v[136:139]\n\t"
  "ds_read_b64_tr_b16 a[232:233], v33 offset:49216\n\t"
  "v_cvt_pk_bf16_f32 v184, v128, v129\n\t"
  "v_mfma_f32_16x16x32_bf16 v[140:143], a[108:111], a[244:247], v[140:143]\n\t"
  "s_waitcnt lgkmcnt(5)\n\t"
  "ds_read_b64_tr_b16 a[234:235], v33 offset:51008\n\t"
  "v_cvt_pk_bf16_f32 v185, v130, v131\n\t"
  "v_mfma_f32_16x16x32_bf16 v[136:139], a[64:67], a[248:251], v[136:139]\n\t"
  "ds_read_b64_tr_b16 a[236:237], v33 offset:57408\n\t"
  "ds_read_b64_tr_b16 a[238:239], v33 offset:59200\n\t"
  "v_mfma_f32_16x16x32_bf16 v[140:143], a[68:71], a[248:251], v[140:143]\n\t"
  "buffer_load_dwordx4 v40, s[56:59], s76 offen offset:2048 sc1 lds\n\t"
  "v_mfma_f32_16x16x32_bf16 v[136:139], a[72:75], a[252:255], v[136:139]\n\t"
  "ds_read_b64_tr_b16 a[240:241], v33 offset:32864\n\t"
  "v_cvt_pk_bf16_f32 v186, v132, v133\n\t"
  "v_mfma_f32_16x16x32_bf16 v[140:143], a[76:79], a[252:255], v[140:143]\n\t"
  "s_waitcnt lgkmcnt(5)\n\t"
  "ds_read_b64_tr_b16 a[242:243], v33 offset:34656\n\t"
  "v_cvt_pk_bf16_f32 v187, v134, v135\n\t"
  "v_mfma_f32_16x16x32_bf16 v[144:147], a[96:99], a[224:227], v[144:147]\n\t"
  "ds_read_b64_tr_b16 a[244:245], v33 offset:41056\n\t"
  "ds_read_b64_tr_b16 a[246:247], v33 offset:42848\n\t"
  "v_mfma_f32_16x16x32_bf16 v[148:151], a[100:103], a[224:227], v[148:151]\n\t"
  "buffer_store_dwordx4 v[184:187], v40, s[56:59], s20 offen offset:0 sc1\n\t"
  "v_mfma_f32_16x16x32_bf16 v[144:147], a[104:107], a[228:231], v[144:147]\n\t"
  "ds_read_b64_tr_b16 a[248:249], v33 offset:49248\n\t"
  "v_cvt_pk_bf16_f32 v188, v136, v137\n\t"
  "v_mfma_f32_16x16x32_bf16 v[148:151], a[108:111], a[228:231], v[148:151]\n\t"
  "s_waitcnt lgkmcnt(5)\n\t"
  "ds_read_b64_tr_b16 a[250:251], v33 offset:51040\n\t"
  "v_cvt_pk_bf16_f32 v189, v138, v139\n\t"
  "v_mfma_f32_16x16x32_bf16 v[144:147], a[64:67], a[232:235], v[144:147]\n\t"
  "ds_read_b64_tr_b16 a[252:253], v33 offset:57440\n\t"
  "ds_read_b64_tr_b16 a[254:255], v33 offset:59232\n\t"
  "v_mfma_f32_16x16x32_bf16 v[148:151], a[68:71], a[232:235], v[148:151]\n\t"
  "buffer_load_dwordx4 v40, s[56:59], s76 offen offset:3072 sc1 lds\n\t"
  "v_mfma_f32_16x16x32_bf16 v[144:147], a[72:75], a[236:239], v[144:147]\n\t"
  "s_add_u32 s76, s76, 0x1000\n\t"
  "ds_read_b64_tr_b16 a[224:225], v33 offset:32896\n\t"
  "v_cvt_pk_bf16_f32 v190, v140, v141\n\t"
  "v_mfma_f32_16x16x32_bf16 v[148:151], a[76:79], a[236:239], v[148:151]\n\t"
  "s_waitcnt lgkmcnt(5)\n\t"
  "ds_read_b64_tr_b16 a[226:227], v33 offset:34688\n\t"
  "v_cvt_pk_bf16_f32 v191, v142, v143\n\t"
  "v_mfma_f32_16x16x32_bf16 v[152:155], a[96:99], a[240:243], v[152:155]\n\t"
  "s_add_u32 m0, s84, 0x9000\n\t"
  "ds_read_b64_tr_b16 a[228:229], v33 offset:41088\n\t"
  "ds_read_b64_tr_b16 a[230:231], v33 offset:42880\n\t"
  "v_mfma_f32_16x16x32_bf16 v[156:159], a[100:103], a[240:243], v[156:159]\n\t"
  "buffer_load_dwordx4 v40, s[56:59], s76 offen offset:0 sc1 lds\n\t"
  "v_mfma_f32_16x16x32_bf16 v[152:155], a[104:107], a[244:247], v[152:155]\n\t"
  "ds_read_b64_tr_b16 a[232:233], v33 offset:49280\n\t"
  "v_accvgpr_write_b32 a9, v193\n\t"
  "v_mfma_f32_16x16x32_bf16 v[156:159], a[108:111], a[244:247], v[156:159]\n\t"
  "s_waitcnt lgkmcnt(5)\n\t"
  "ds_read_b64_tr_b16 a[234:235], v33 offset:51072\n\t"
  "v_accvgpr_write_b32 a10, v196\n\t"
  "v_mfma_f32_16x16x32_bf16 v[152:155], a[64:67], a[248:251], v[152:155]\n\t"
  "ds_read_b64_tr_b16 a[236:237], v33 offset:57472\n\t"
  "ds_read_b64_tr_b16 a[238:239], v33 offset:59264\n\t"
  "v_mfma_f32_16x16x32_bf16 v[156:159], a[68:71], a[248:251], v[156:159]\n\t"
  "buffer_load_dwordx4 v40, s[56:59], s76 offen offset:1024 sc1 lds\n\t"
  "v_mfma_f32_16x16x32_bf16 v[152:155], a[72:75], a[252:255], v[152:155]\n\t"
  "ds_read_b64_tr_b16 a[240:241], v33 offset:32928\n\t"
  "v_accvgpr_write_b32 a11, v197\n\t"
  "v_mfma_f32_16x16x32_bf16 v[156:159], a[76:79], a[252:255], v[156:159]\n\t"
  "s_waitcnt lgkmcnt(5)\n\t"
  "ds_read_b64_tr_b16 a[242:243], v33 offset:34720\n\t"
  "v_accvgpr_write_b32 a12, v194\n\t"
  "v_mfma_f32_16x16x32_bf16 v[160:163], a[96:99], a[224:227], v[160:163]\n\t"
  "ds_read_b64_tr_b16 a[244:245], v33 offset:41120\n\t"
  "ds_read_b64_tr_b16 a[246:247], v33 offset:42912\n\t"
  "v_mfma_f32_16x16x32_bf16 v[164:167], a[100:103], a[224:227], v[164:167]\n\t"
  "buffer_load_dwordx4 v40, s[56:59], s76 offen offset:2048 sc1 lds\n\t"
  "v_mfma_f32_16x16x32_bf16 v[160:163], a[104:107], a[228:231], v[160:163]\n\t"
  "ds_read_b64_tr_b16 a[248:249], v33 offset:49312\n\t"
  "v_accvgpr_write_b32 a13, v195\n\t"
  "v_mfma_f32_16x16x32_bf16 v[164:167], a[108:111], a[228:231], v[164:167]\n\t"
  "s_waitcnt lgkmcnt(5)\n\t"
  "ds_read_b64_tr_b16 a[250:251], v33 offset:51104\n\t"
  "v_accvgpr_write_b32 a14, v198\n\t"
  "v_mfma_f32_16x16x32_bf16 v[160:163], a[64:67], a[232:235], v[160:163]\n\t"
  "ds_read_b64_tr_b16 a[252:253], v33 offset:57504\n\t"
  "ds_read_b64_tr_b16 a[254:255], v33 offset:59296\n\t"
  "v_mfma_f32_16x16x32_bf16 v[164:167], a[68:71], a[232:235], v[164:167]\n\t"
  "buffer_store_dwordx4 v[188:191], v40, s[56:59], s20 offen offset:1024 sc1\n\t"
  "v_mfma_f32_16x16x32_bf16 v[160:163], a[72:75], a[236:239], v[160:163]\n\t"
  "ds_read_b64_tr_b16 a[224:225], v33 offset:32960\n\t"
  "v_accvgpr_write_b32 a15, v199\n\t"
  "v_mfma_f32_16x16x32_bf16 v[164:167], a[76:79], a[236:239], v[164:167]\n\t"
  "s_waitcnt lgkmcnt(5)\n\t"
  "ds_read_b64_tr_b16 a[226:227], v33 offset:34752\n\t"
  "v_cvt_pk_bf16_f32 v192, v144, v145\n\t"
  "v_mfma_f32_16x16x32_bf16 v[168:171], a[96:99], a[240:243], v[168:171]\n\t"
  "ds_read_b64_tr_b16 a[228:229], v33 offset:41152\n\t"
  "s_nop 0\n\t"
  "s_cbranch_execz TRAMP_%=\n\t"
  "ds_read_b64_tr_b16 a[230:231], v33 offset:42944\n\t"
  "v_mfma_f32_16x16x32_bf16 v[172:175], a[100:103], a[240:243], v[172:175]\n\t"
  "buffer_load_dwordx4 v40, s[56:59], s76 offen offset:3072 sc1 lds\n\t"
  "v_mfma_f32_16x16x32_bf16 v[168:171], a[104:107], a[244:247], v[168:171]\n\t"
  "s_add_u32 s76, s76, 0x7000\n\t"
  "ds_read_b64_tr_b16 a[232:233], v33 offset:49344\n\t"
  "v_cvt_pk_bf16_f32 v193, v146, v147\n\t"
  "v_mfma_f32_16x16x32_bf16 v[172:175], a[108:111], a[244:247], v[172:175]\n\t"
  "s_waitcnt lgkmcnt(5)\n\t"
  "ds_read_b64_tr_b16 a[234:235], v33 offset:51136\n\t"
  "v_cvt_pk_bf16_f32 v194, v148, v149\n\t"
  "v_mfma_f32_16x16x32_bf16 v[168:171], a[64:67], a[248:251], v[168:171]\n\t"
  "s_waitcnt vmcnt(11)\n\t"
  "ds_read_b64_tr_b16 a[236:237], v33 offset:57536\n\t"
  "ds_read_b64_tr_b16 a[238:239], v33 offset:59328\n\t"
  "v_mfma_f32_16x16x32_bf16 v[172:175], a[68:71], a[248:251], v[172:175]\n\t"
  "s_barrier\n\t"
  "v_cvt_pk_bf16_f32 v195, v150, v151\n\t"
  "v_accvgpr_read_b32 v120, a32\n\t"
  "v_mfma_f32_16x16x32_bf16 v[168:171], a[72:75], a[252:255], v[168:171]\n\t"
  "ds_read_b128 a[240:243], v40 offset:0\n\t"
  "v_accvgpr_read_b32 v121, a33\n\t"
  "v_mfma_f32_16x16x32_bf16 v[172:175], a[76:79], a[252:255], v[172:175]\n\t"
  "s_waitcnt lgkmcnt(5)\n\t"
  "v_accvgpr_read_b32 v122, a34\n\t"
  "v_accvgpr_read_b32 v123, a35\n\t"
  "v_mfma_f32_16x16x32_bf16 v[176:179], a[96:99], a[224:227], v[176:179]\n\t"
  "s_waitcnt vmcnt(10)\n\t"
  "ds_read_b128 a[244:247], v40 offset:8192\n\t"
  "v_cmp_ne_u64 vcc, v[48:49], v[8:9]\n\t"
  "v_mfma_f32_16x16x32_bf16 v[180:183], a[100:103], a[224:227], v[180:183]\n\t"
  "v_cvt_pk_bf16_f32 v200, v120, v121\n\t"
  "v_cvt_pk_bf16_f32 v201, v122, v123\n\t"
  "v_mfma_f32_16x16x32_bf16 v[176:179], a[104:107], a[228:231], v[176:179]\n\t"
  "s_cbranch_vccnz SPINH_%=\n\t"
  "SRETH_%=:\n\t"
  "v_mfma_f32_16x16x32_bf16 v[180:183], a[108:111], a[228:231], v[180:183]\n\t"
  "s_waitcnt lgkmcnt(2)\n\t"
  "ds_read_b128 a[248:251], v40 offset:1024\n\t"
  "v_accvgpr_read_b32 v120, a36\n\t"
  "v_mfma_f32_16x16x32_bf16 v[176:179], a[64:67], a[232:235], v[176:179]\n\t"
  "ds_read_b128 a[252:255], v40 offset:9216\n\t"
  "v_accvgpr_read_b32 v121, a37\n\t"
  "v_mfma_f32_16x16x32_bf16 v[180:183], a[68:71], a[232:235], v[180:183]\n\t"
  "v_accvgpr_read_b32 v122, a38\n\t"
  "v_accvgpr_read_b32 v123, a39\n\t"
  "v_mfma_f32_16x16x32_bf16 v[176:179], a[72:75], a[236:239], v[176:179]\n\t"
  "ds_read_b128 a[224:227], v40 offset:2048\n\t"
  "v_cvt_pk_bf16_f32 v202, v120, v121\n\t"
  "v_mfma_f32_16x16x32_bf16 v[180:183], a[76:79], a[236:239], v[180:183]\n\t"
  "s_waitcnt lgkmcnt(3)\n\t"
  "v_cvt_pk_bf16_f32 v203, v122, v123\n\t"
  "v_cvt_pk_bf16_f32 v206, v124, v125\n\t"
  "v_mfma_f32_16x16x32_bf16 v[80:83], a[240:243], a[0:3], 0\n\t"
  "ds_read_b128 a[228:231], v40 offset:10240\n\t"
  "v_cvt_pk_bf16_f32 v207, v126, v127\n\t"
  "v_mfma_f32_16x16x32_bf16 v[84:87], a[240:243], a[4:7], 0\n\t"
  "v_accvgpr_write_b32 a16, v200\n\t"
  "v_accvgpr_write_b32 a17, v201\n\t"
  "v_mfma_f32_16x16x32_bf16 v[88:91], a[244:247], a[0:3], 0\n\t"
  "ds_read_b128 a[232:235], v40 offset:3072\n\t"
  "v_accvgpr_write_b32 a18, v204\n\t"
  "v_mfma_f32_16x16x32_bf16 v[92:95], a[244:247], a[4:7], 0\n\t"
  "s_waitcnt lgkmcnt(3)\n\t"
  "buffer_store_dwordx4 v[192:195], v40, s[56:59], s20 offen offset:2048 sc1\n\t"
  "v_mfma_f32_16x16x32_bf16 v[80:83], a[248:251], a[8:11], v[80:83]\n\t"
  "s_add_u32 m0, s84, 0x10000\n\t"
  "ds_read_b128 a[236:239], v40 offset:11264\n\t"
  "v_accvgpr_write_b32 a19, v205\n\t"
  "v_mfma_f32_16x16x32_bf16 v[84:87], a[248:251], a[12:15], v[84:87]\n\t"
  "buffer_load_dwordx4 a[88:91], v40, s[56:59], s18 offen offset:0 sc1\n\t"
  "v_mfma_f32_16x16x32_bf16 v[88:91], a[252:255], a[8:11], v[88:91]\n\t"
  "ds_read_b128 a[240:243], v40 offset:4096\n\t"
  "v_accvgpr_write_b32 a20, v202\n\t"
  "v_mfma_f32_16x16x32_bf16 v[92:95], a[252:255], a[12:15], v[92:95]\n\t"
  "s_waitcnt lgkmcnt(3)\n\t"
  "buffer_load_dwordx4 v40, s[56:59], s76 offen offset:0 sc1 lds\n\t"
  "v_accvgpr_write_b32 a21, v203\n\t"
  "v_accvgpr_write_b32 a22, v206\n\t"
  "v_accvgpr_write_b32 a23, v207\n\t"
  "s_nop 1\n\t"
  "v_mfma_f32_16x16x32_bf16 v[80:83], a[224:227], a[16:19], v[80:83]\n\t"
  "ds_read_b128 a[244:247], v40 offset:12288\n\t"
  "v_accvgpr_read_b32 v120, a48\n\t"
  "v_mfma_f32_16x16x32_bf16 v[84:87], a[224:227], a[20:23], v[84:87]\n\t"
  "buffer_load_dwordx4 v40, s[56:59], s76 offen offset:1024 sc1 lds\n\t"
  "v_mfma_f32_16x16x32_bf16 v[88:91], a[228:231], a[16:19], v[88:91]\n\t"
  "ds_read_b128 a[248:251], v40 offset:5120\n\t"
  "v_accvgpr_read_b32 v121, a49\n\t"
  "v_mfma_f32_16x16x32_bf16 v[92:95], a[228:231], a[20:23], v[92:95]\n\t"
  "s_waitcnt lgkmcnt(3)\n\t"
  "buffer_load_dwordx4 a[92:95], v40, s[56:59], s18 offen offset:1024 sc1\n\t"
  "v_accvgpr_read_b32 v122, a50\n\t"
  "v_accvgpr_read_b32 v123, a51\n\t"
  "v_cvt_pk_bf16_f32 v208, v120, v121\n\t"
  "v_cvt_pk_bf16_f32 v209, v122, v123\n\t"
  "v_accvgpr_read_b32 v120, a52\n\t"
  "v_accvgpr_read_b32 v121, a53\n\t"
  "v_accvgpr_read_b32 v122, a54\n\t"
  "v_accvgpr_read_b32 v123, a55\n\t"
  "v_cvt_pk_bf16_f32 v210, v120, v121\n\t"
  "v_cvt_pk_bf16_f32 v211, v122, v123\n\t"
  "v_accvgpr_read_b32 v124, a56\n\t"
  "v_accvgpr_read_b32 v125, a57\n\t"
  "v_accvgpr_read_b32 v126, a58\n\t"
  "v_accvgpr_read_b32 v127, a59\n\t"
  "v_cvt_pk_bf16_f32 v212, v124, v125\n\t"
  "v_cvt_pk_bf16_f32 v213, v126, v127\n\t"
  "v_accvgpr_read_b32 v124, a60\n\t"
  "v_accvgpr_read_b32 v125, a61\n\t"
  "v_accvgpr_read_b32 v126, a62\n\t"
  "v_accvgpr_read_b32 v127, a63\n\t"
  "v_cvt_pk_bf16_f32 v214, v124, v125\n\t"
  "v_cvt_pk_bf16_f32 v215, v126, v127\n\t"
  "v_accvgpr_write_b32 a24, v208\n\t"
  "v_accvgpr_write_b32 a25, v209\n\t"
  "v_accvgpr_write_b32 a26, v212\n\t"
  "v_accvgpr_write_b32 a27, v213\n\t"
  "v_accvgpr_write_b32 a28, v210\n\t"
  "v_accvgpr_write_b32 a29, v211\n\t"
  "v_accvgpr_write_b32 a30, v214\n\t"
  "v_accvgpr_write_b32 a31, v215\n\t"
  "s_nop 1\n\t"
  "v_mfma_f32_16x16x32_bf16 v[80:83], a[232:235], a[24:27], v[80:83]\n\t"
  "s_add_u32 s18, s18, 0x800\n\t"
  "ds_read_b128 a[252:255], v40 offset:13312\n\t"
  "v_cvt_pk_bf16_f32 v64, v64, v65\n\t"
  "v_mfma_f32_16x16x32_bf16 v[84:87], a[232:235], a[28:31], v[84:87]\n\t"
  "v_cvt_pk_bf16_f32 v65, v66, v67\n\t"
  "v_cvt_pk_bf16_f32 v68, v68, v69\n\t"
  "v_mfma_f32_16x16x32_bf16 v[88:91], a[236:239], a[24:27], v[88:91]\n\t"
  "ds_read_b128 a[224:227], v40 offset:6144\n\t"
  "v_cvt_pk_bf16_f32 v69, v70, v71\n\t"
  "v_mfma_f32_16x16x32_bf16 v[92:95], a[236:239], a[28:31], v[92:95]\n\t"
  "s_waitcnt lgkmcnt(3)\n\t"
  "buffer_load_dwordx4 v40, s[56:59], s76 offen offset:2048 sc1 lds\n\t"
  "v_accvgpr_write_b32 a32, v64\n\t"
  "v_accvgpr_write_b32 a33, v65\n\t"
  "v_accvgpr_write_b32 a36, v68\n\t"
  "v_accvgpr_write_b32 a37, v69\n\t"
  "v_cvt_pk_bf16_f32 v72, v72, v73\n\t"
  "v_cvt_pk_bf16_f32 v73, v74, v75\n\t"
  "v_cvt_pk_bf16_f32 v76, v76, v77\n\t"
  "v_cvt_pk_bf16_f32 v77, v78, v79\n\t"
  "v_accvgpr_write_b32 a34, v72\n\t"
  "v_accvgpr_write_b32 a35, v73\n\t"
  "v_accvgpr_write_b32 a38, v76\n\t"
  "v_accvgpr_write_b32 a39, v77\n\t"
  "s_nop 1\n\t"
  "v_mfma_f32_16x16x32_bf16 v[80:83], a[240:243], a[32:35], v[80:83]\n\t"
  "ds_read_b128 a[228:231], v40 offset:14336\n\t"
  "v_cvt_pk_bf16_f32 v96, v96, v97\n\t"
  "v_mfma_f32_16x16x32_bf16 v[84:87], a[240:243], a[36:39], v[84:87]\n\t"
  "v_cvt_pk_bf16_f32 v97, v98, v99\n\t"
  "v_cvt_pk_bf16_f32 v100, v100, v101\n\t"
  "v_mfma_f32_16x16x32_bf16 v[88:91], a[244:247], a[32:35], v[88:91]\n\t"
  "ds_read_b128 a[232:235], v40 offset:7168\n\t"
  "v_cvt_pk_bf16_f32 v101, v102, v103\n\t"
  "v_mfma_f32_16x16x32_bf16 v[92:95], a[244:247], a[36:39], v[92:95]\n\t"
  "s_waitcnt lgkmcnt(3)\n\t"
  "buffer_load_dwordx4 a[168:171], v40, s[56:59], s79 offen offset:0 sc1\n\t"
  "v_mfma_f32_16x16x32_bf16 v[80:83], a[248:251], a[40:43], v[80:83]\n\t"
  "ds_read_b128 a[236:239], v40 offset:15360\n\t"
  "v_accvgpr_write_b32 a48, v96\n\t"
  "v_mfma_f32_16x16x32_bf16 v[84:87], a[248:251], a[44:47], v[84:87]\n\t"
  "v_accvgpr_write_b32 a49, v97\n\t"
  "v_accvgpr_write_b32 a52, v100\n\t"
  "v_mfma_f32_16x16x32_bf16 v[88:91], a[252:255], a[40:43], v[88:91]\n\t"
  "ds_read_b128 a[240:243], v40 offset:16384\n\t"
  "v_accvgpr_write_b32 a53, v101\n\t"
  "v_mfma_f32_16x16x32_bf16 v[92:95], a[252:255], a[44:47], v[92:95]\n\t"
  "s_waitcnt lgkmcnt(3)\n\t"
  "buffer_load_dwordx4 v40, s[56:59], s76 offen offset:3072 sc1 lds\n\t"
  "v_cvt_pk_bf16_f32 v104, v104, v105\n\t"
  "v_cvt_pk_bf16_f32 v105, v106, v107\n\t"
  "v_cvt_pk_bf16_f32 v108, v108, v109\n\t"
  "v_cvt_pk_bf16_f32 v109, v110, v111\n\t"
  "v_accvgpr_write_b32 a50, v104\n\t"
  "v_accvgpr_write_b32 a51, v105\n\t"
  "v_accvgpr_write_b32 a54, v108\n\t"
  "v_accvgpr_write_b32 a55, v109\n\t"
  "v_cvt_pk_bf16_f32 v196, v152, v153\n\t"
  "v_cvt_pk_bf16_f32 v197, v154, v155\n\t"
  "v_cvt_pk_bf16_f32 v198, v156, v157\n\t"
  "v_cvt_pk_bf16_f32 v199, v158, v159\n\t"
  "buffer_store_dwordx4 v[196:199], v40, s[56:59], s20 offen offset:3072 sc1\n\t"
  "v_mfma_f32_16x16x32_bf16 v[80:83], a[224:227], a[48:51], v[80:83]\n\t"
  "s_add_u32 s76, s76, 0x1000\n\t"
  "ds_read_b128 a[244:247], v40 offset:17408\n\t"
  "v_cvt_pk_bf16_f32 v240, v240, v241\n\t"
  "v_mfma_f32_16x16x32_bf16 v[84:87], a[224:227], a[52:55], v[84:87]\n\t"
  "v_cvt_pk_bf16_f32 v241, v242, v243\n\t"
  "v_cvt_pk_bf16_f32 v244, v244, v245\n\t"
  "v_mfma_f32_16x16x32_bf16 v[88:91], a[228:231], a[48:51], v[88:91]\n\t"
  "s_add_u32 m0, s84, 0x11000\n\t"
  "ds_read_b128 a[248:251], v40 offset:18432\n\t"
  "v_cvt_pk_bf16_f32 v245, v246, v247\n\t"
  "v_mfma_f32_16x16x32_bf16 v[92:95], a[228:231], a[52:55], v[92:95]\n\t"
  "s_waitcnt lgkmcnt(3)\n\t"
  "buffer_load_dwordx4 a[172:175], v40, s[56:59], s79 offen offset:1024 sc1\n\t"
  "v_accvgpr_write_b32 a56, v240\n\t"
  "v_accvgpr_write_b32 a57, v241\n\t"
  "v_accvgpr_write_b32 a60, v244\n\t"
  "v_accvgpr_write_b32 a61, v245\n\t"
  "v_cvt_pk_bf16_f32 v248, v248, v249\n\t"
  "v_cvt_pk_bf16_f32 v249, v250, v251\n\t"
  "v_cvt_pk_bf16_f32 v252, v252, v253\n\t"
  "v_cvt_pk_bf16_f32 v253, v254, v255\n\t"
  "v_accvgpr_write_b32 a58, v248\n\t"
  "v_accvgpr_write_b32 a59, v249\n\t"
  "v_accvgpr_write_b32 a62, v252\n\t"
  "v_accvgpr_write_b32 a63, v253\n\t"
  "s_nop 1\n\t"
  "v_mfma_f32_16x16x32_bf16 v[80:83], a[232:235], a[56:59], v[80:83]\n\t"
  "ds_read_b128 a[252:255], v40 offset:19456\n\t"
  "v_cvt_pk_bf16_f32 v200, v160, v161\n\t"
  "v_mfma_f32_16x16x32_bf16 v[84:87], a[232:235], a[60:63], v[84:87]\n\t"
  "buffer_load_dwordx4 v40, s[56:59], s76 offen offset:0 sc1 lds\n\t"
  "v_mfma_f32_16x16x32_bf16 v[88:91], a[236:239], a[56:59], v[88:91]\n\t"
  "ds_read_b128 a[224:227], v40 offset:20480\n\t"
  "v_cvt_pk_bf16_f32 v201, v162, v163\n\t"
  "v_mfma_f32_16x16x32_bf16 v[92:95], a[236:239], a[60:63], v[92:95]\n\t"
  "s_waitcnt lgkmcnt(3)\n\t"
  "v_cvt_pk_bf16_f32 v202, v164, v165\n\t"
  "v_cvt_pk_bf16_f32 v203, v166, v167\n\t"
  "v_mfma_f32_16x16x32_bf16 v[96:99], a[240:243], a[0:3], 0\n\t"
  "ds_read_b128 a[228:231], v40 offset:21504\n\t"
  "v_cvt_pk_bf16_f32 v204, v168, v169\n\t"
  "v_mfma_f32_16x16x32_bf16 v[100:103], a[240:243], a[4:7], 0\n\t"
  "buffer_load_dwordx4 v40, s[56:59], s76 offen offset:1024 sc1 lds\n\t"
  "v_mfma_f32_16x16x32_bf16 v[96:99], a[244:247], a[8:11], v[96:99]\n\t"
  "ds_read_b128 a[232:235], v40 offset:22528\n\t"
  "v_cvt_pk_bf16_f32 v205, v170, v171\n\t"
  "v_mfma_f32_16x16x32_bf16 v[100:103], a[244:247], a[12:15], v[100:103]\n\t"
  "s_waitcnt lgkmcnt(3)\n\t"
  "buffer_load_dwordx4 a[176:179], v40, s[56:59], s79 offen offset:2048 sc1\n\t"
  "v_mfma_f32_16x16x32_bf16 v[96:99], a[248:251], a[16:19], v[96:99]\n\t"
  "ds_read_b128 a[236:239], v40 offset:23552\n\t"
  "v_cvt_pk_bf16_f32 v206, v172, v173\n\t"
  "v_mfma_f32_16x16x32_bf16 v[100:103], a[248:251], a[20:23], v[100:103]\n\t"
  "v_cvt_pk_bf16_f32 v207, v174, v175\n\t"
  "v_max3_f32 v1, v80, v81, v82\n\t"
  "v_mfma_f32_16x16x32_bf16 v[96:99], a[252:255], a[24:27], v[96:99]\n\t"
  "ds_read_b128 a[240:243], v40 offset:24576\n\t"
  "v_max3_f32 v1, v1, v83, v88\n\t"
  "v_mfma_f32_16x16x32_bf16 v[100:103], a[252:255], a[28:31], v[100:103]\n\t"
  "s_waitcnt lgkmcnt(3)\n\t"
  "buffer_load_dwordx4 v40, s[56:59], s76 offen offset:2048 sc1 lds\n\t"
  "v_mfma_f32_16x16x32_bf16 v[96:99], a[224:227], a[32:35], v[96:99]\n\t"
  "ds_read_b128 a[244:247], v40 offset:25600\n\t"
  "v_max3_f32 v1, v1, v89, v90\n\t"
  "v_mfma_f32_16x16x32_bf16 v[100:103], a[224:227], a[36:39], v[100:103]\n\t"
  "buffer_store_dwordx4 v[200:203], v40, s[56:59], s27 offen offset:0 sc1\n\t"
  "v_mfma_f32_16x16x32_bf16 v[96:99], a[228:231], a[40:43], v[96:99]\n\t"
  "ds_read_b128 a[248:251], v40 offset:26624\n\t"
  "v_max_f32 v1, v1, v91\n\t"
  "v_mfma_f32_16x16x32_bf16 v[100:103], a[228:231], a[44:47], v[100:103]\n\t"
  "s_waitcnt lgkmcnt(3)\n\t"
  "buffer_load_dwordx4 a[180:183], v40, s[56:59], s79 offen offset:3072 sc1\n\t"
  "v_mfma_f32_16x16x32_bf16 v[96:99], a[232:235], a[48:51], v[96:99]\n\t"
  "s_add_u32 s79, s79, 0x1000\n\t"
  "ds_read_b128 a[252:255], v40 offset:27648\n\t"
  "v_mov_b32 v2, v1\n\t"
  "v_mfma_f32_16x16x32_bf16 v[100:103], a[232:235], a[52:55], v[100:103]\n\t"
  "v_max3_f32 v3, v84, v85, v86\n\t"
  "v_cvt_pk_bf16_f32 v208, v176, v177\n\t"
  "v_mfma_f32_16x16x32_bf16 v[96:99], a[236:239], a[56:59], v[96:99]\n\t"
  "ds_read_b128 a[224:227], v40 offset:28672\n\t"
  "ds_read_b128 a[228:231], v40 offset:29696\n\t"
  "v_mfma_f32_16x16x32_bf16 v[100:103], a[236:239], a[60:63], v[100:103]\n\t"
  "s_waitcnt lgkmcnt(4)\n\t"
  "buffer_load_dwordx4 v40, s[56:59], s76 offen offset:3072 sc1 lds\n\t"
  "v_mfma_f32_16x16x32_bf16 v[104:107], a[240:243], a[0:3], 0\n\t"
  "s_add_u32 s76, s76, 0x7000\n\t"
  "v_permlane32_swap_b32 v1, v2\n\t"
  "v_mfma_f32_16x16x32_bf16 v[108:111], a[240:243], a[4:7], 0\n\t"
  "v_max_f32 v1, v1, v2\n\t"
  "v_max3_f32 v3, v3, v87, v92\n\t"
  "v_mfma_f32_16x16x32_bf16 v[104:107], a[244:247], a[8:11], v[104:107]\n\t"
  "ds_read_b128 a[232:235], v40 offset:30720\n\t"
  "v_mov_b32 v2, v1\n\t"
  "v_mfma_f32_16x16x32_bf16 v[108:111], a[244:247], a[12:15], v[108:111]\n\t"
  "s_waitcnt lgkmcnt(3)\n\t"
  "buffer_load_dwordx4 a[184:187], v40, s[56:59], s79 offen offset:0 sc1\n\t"
  "v_mfma_f32_16x16x32_bf16 v[104:107], a[248:251], a[16:19], v[104:107]\n\t"
  "s_waitcnt vmcnt(18)\n\t"
  "ds_read_b128 a[236:239], v40 offset:31744\n\t"
  "v_max3_f32 v3, v3, v93, v94\n\t"
  "v_mfma_f32_16x16x32_bf16 v[108:111], a[248:251], a[20:23], v[108:111]\n\t"
  "s_barrier\n\t"
  "v_permlane16_swap_b32 v1, v2\n\t"
  "v_mfma_f32_16x16x32_bf16 v[104:107], a[252:255], a[24:27], v[104:107]\n\t"
  "ds_read_b128 a[240:243], v40 offset:32768\n\t"
  "v_max_f32 v1, v1, v2\n\t"
  "v_mfma_f32_16x16x32_bf16 v[108:111], a[252:255], a[28:31], v[108:111]\n\t"
  "s_waitcnt lgkmcnt(3)\n\t"
  "v_sub_f32 v112, 0, v1\n\t"
  "v_sub_f32 v113, 0, v1\n\t"
  "v_mfma_f32_16x16x32_bf16 v[104:107], a[224:227], a[32:35], v[104:107]\n\t"
  "ds_read_b128 a[244:247], v40 offset:33792\n\t"
  "v_sub_f32 v114, 0, v1\n\t"
  "v_mfma_f32_16x16x32_bf16 v[108:111], a[224:227], a[36:39], v[108:111]\n\t"
  "v_sub_f32 v115, 0, v1\n\t"
  "v_sub_f32 v80, v80, v1\n\t"
  "v_mfma_f32_16x16x32_bf16 v[104:107], a[228:231], a[40:43], v[104:107]\n\t"
  "ds_read_b128 a[248:251], v40 offset:34816\n\t"
  "v_sub_f32 v81, v81, v1\n\t"
  "v_mfma_f32_16x16x32_bf16 v[108:111], a[228:231], a[44:47], v[108:111]\n\t"
  "s_waitcnt lgkmcnt(3)\n\t"
  "buffer_load_dwordx4 a[188:191], v40, s[56:59], s79 offen offset:1024 sc1\n\t"
  "v_mfma_f32_16x16x32_bf16 v[104:107], a[232:235], a[48:51], v[104:107]\n\t"
  "v_exp_f32 v80, v80\n\t"
  "v_mfma_f32_16x16x32_bf16 v[108:111], a[232:235], a[52:55], v[108:111]\n\t"
  "buffer_store_dwordx4 v[204:207], v40, s[56:59], s27 offen offset:1024 sc1\n\t"
  "v_mfma_f32_16x16x32_bf16 v[104:107], a[236:239], a[56:59], v[104:107]\n\t"
  "v_sub_f32 v82, v82, v1\n\t"
  "v_sub_f32 v83, v83, v1\n\t"
  "v_mfma_f32_16x16x32_bf16 v[108:111], a[236:239], a[60:63], v[108:111]\n\t"
  "s_waitcnt lgkmcnt(1)\n\t"
  "v_exp_f32 v81, v81\n\t"
  "v_exp_f32 v82, v82\n\t"
  "v_cvt_pk_bf16_f32 v128, v80, v81\n\t"
  "v_exp_f32 v83, v83\n\t"
  "s_nop 0\n\t"
  "v_cvt_pk_bf16_f32 v129, v82, v83\n\t"
  "v_mfma_f32_16x16x32_bf16 v[80:83], a[240:243], a[0:3], v[112:115]\n\t"
  "ds_read_b128 a[252:255], v40 offset:35840\n\t"
  "v_max_f32 v3, v3, v95\n\t"
  "v_mov_b32 v4, v3\n\t"
  "s_nop 1\n\t"
  "v_permlane32_swap_b32 v3, v4\n\t"
  "v_max_f32 v3, v3, v4\n\t"
  "v_mov_b32 v4, v3\n\t"
  "s_nop 1\n\t"
  "v_permlane16_swap_b32 v3, v4\n\t"
  "v_max_f32 v3, v3, v4\n\t"
  "v_sub_f32 v116, 0, v3\n\t"
  "v_sub_f32 v117, 0, v3\n\t"
  "v_sub_f32 v118, 0, v3\n\t"
  "v_sub_f32 v119, 0, v3\n\t"
  "v_sub_f32 v84, v84, v3\n\t"
  "v_sub_f32 v85, v85, v3\n\t"
  "v_sub_f32 v86, v86, v3\n\t"
  "v_sub_f32 v87, v87, v3\n\t"
  "v_exp_f32 v84, v84\n\t"
  "v_exp_f32 v85, v85\n\t"
  "v_exp_f32 v86, v86\n\t"
  "v_cvt_pk_bf16_f32 v132, v84, v85\n\t"
  "v_exp_f32 v87, v87\n\t"
  "s_nop 0\n\t"
  "v_cvt_pk_bf16_f32 v133, v86, v87\n\t"
  "v_mfma_f32_16x16x32_bf16 v[84:87], a[240:243], a[4:7], v[116:119]\n\t"
  "v_sub_f32 v88, v88, v1\n\t"
  "v_sub_f32 v89, v89, v1\n\t"
  "v_mfma_f32_16x16x32_bf16 v[80:83], a[244:247], a[8:11], v[80:83]\n\t"
  "ds_read_b128 a[224:227], v40 offset:36864\n\t"
  "v_sub_f32 v90, v90, v1\n\t"
  "v_mfma_f32_16x16x32_bf16 v[84:87], a[244:247], a[12:15], v[84:87]\n\t"
  "s_waitcnt lgkmcnt(1)\n\t"
  "buffer_load_dwordx4 a[192:195], v40, s[56:59], s79 offen offset:2048 sc1\n\t"
  "v_mfma_f32_16x16x32_bf16 v[80:83], a[248:251], a[16:19], v[80:83]\n\t"
  "s_add_u32 s79, s79, 0xc00\n\t"
  "v_sub_f32 v91, v91, v1\n\t"
  "v_exp_f32 v88, v88\n\t"
  "v_mfma_f32_16x16x32_bf16 v[84:87], a[248:251], a[20:23], v[84:87]\n\t"
  "s_add_u32 m0, s84, 0x18000\n\t"
  "buffer_load_dwordx4 a[96:99], v40, s[56:59], s18 offen offset:0 sc1\n\t"
  "v_exp_f32 v89, v89\n\t"
  "v_exp_f32 v90, v90\n\t"
  "v_cvt_pk_bf16_f32 v130, v88, v89\n\t"
  "v_exp_f32 v91, v91\n\t"
  "s_nop 0\n\t"
  "v_cvt_pk_bf16_f32 v131, v90, v91\n\t"
  "s_nop 1\n\t"
  "v_mfma_f32_16x16x32_bf16 v[56:59], v[52:55], v[128:131], 0\n\t"
  "ds_read_b128 a[228:231], v40 offset:37888\n\t"
  "v_sub_f32 v92, v92, v3\n\t"
  "v_sub_f32 v93, v93, v3\n\t"
  "v_sub_f32 v94, v94, v3\n\t"
  "v_sub_f32 v95, v95, v3\n\t"
  "v_exp_f32 v92, v92\n\t"
  "v_exp_f32 v93, v93\n\t"
  "v_exp_f32 v94, v94\n\t"
  "v_cvt_pk_bf16_f32 v134, v92, v93\n\t"
  "v_exp_f32 v95, v95\n\t"
  "s_nop 0\n\t"
  "v_cvt_pk_bf16_f32 v135, v94, v95\n\t"
  "s_nop 1\n\t"
  "v_mfma_f32_16x16x32_bf16 v[60:63], v[52:55], v[132:135], 0\n\t"
  "s_waitcnt vmcnt(12)\n\t"
  "buffer_load_dwordx4 v40, s[56:59], s76 offen offset:0 sc1 lds\n\t"
  "v_mfma_f32_16x16x32_bf16 a[112:115], a[168:171], a[88:91], 0\n\t"
  "ds_read_b128 a[232:235], v40 offset:38912\n\t"
  "v_cvt_pk_bf16_f32 v209, v178, v179\n\t"
  "v_mfma_f32_16x16x32_bf16 a[116:119], a[168:171], a[92:95], 0\n\t"
  "v_cvt_pk_bf16_f32 v210, v180, v181\n\t"
  "v_cvt_pk_bf16_f32 v211, v182, v183\n\t"
  "v_mfma_f32_16x16x32_bf16 v[80:83], a[252:255], a[24:27], v[80:83]\n\t"
  "v_add_f32 v96, v96, v112\n\t"
  "v_add_f32 v97, v97, v112\n\t"
  "v_mfma_f32_16x16x32_bf16 v[84:87], a[252:255], a[28:31], v[84:87]\n\t"
  "s_waitcnt lgkmcnt(1)\n\t"
  "buffer_load_dwordx4 a[100:103], v40, s[56:59], s18 offen offset:1024 sc1\n\t"
  "v_mfma_f32_16x16x32_bf16 v[80:83], a[224:227], a[32:35], v[80:83]\n\t"
  "s_add_u32 s18, s18, 0x800\n\t"
  "ds_read_b128 a[236:239], v40 offset:39936\n\t"
  "v_add_f32 v98, v98, v112\n\t"
  "v_mfma_f32_16x16x32_bf16 v[84:87], a[224:227], a[36:39], v[84:87]\n\t"
  "buffer_load_dwordx4 v40, s[56:59], s76 offen offset:1024 sc1 lds\n\t"
  "v_mfma_f32_16x16x32_bf16 a[120:123], a[172:175], a[88:91], 0\n\t"
  "ds_read_b128 a[240:243], v40 offset:40960\n\t"
  "v_add_f32 v99, v99, v112\n\t"
  "v_mfma_f32_16x16x32_bf16 a[124:127], a[172:175], a[92:95], 0\n\t"
  "v_add_f32 v100, v100, v116\n\t"
  "v_add_f32 v101, v101, v116\n\t"
  "v_mfma_f32_16x16x32_bf16 v[80:83], a[228:231], a[40:43], v[80:83]\n\t"
  "v_add_f32 v102, v102, v116\n\t"
  "v_add_f32 v103, v103, v116\n\t"
  "v_mfma_f32_16x16x32_bf16 v[84:87], a[228:231], a[44:47], v[84:87]\n\t"
  "s_waitcnt lgkmcnt(1)\n\t"
  "buffer_store_dwordx4 v[208:211], v40, s[56:59], s27 offen offset:2048 sc1\n\t"
  "v_mfma_f32_16x16x32_bf16 v[80:83], a[232:235], a[48:51], v[80:83]\n\t"
  "ds_read_b128 a[244:247], v40 offset:41984\n\t"
  "v_add_f32 v104, v104, v112\n\t"
  "v_mfma_f32_16x16x32_bf16 v[84:87], a[232:235], a[52:55], v[84:87]\n\t"
  "s_waitcnt vmcnt(10)\n\t"
  "buffer_load_dwordx4 a[196:199], v40, s[56:59], s79 offen offset:0 sc1\n\t"
  "v_mfma_f32_16x16x32_bf16 a[128:131], a[176:179], a[88:91], 0\n\t"
  "ds_read_b128 a[248:251], v40 offset:43008\n\t"
  "v_add_f32 v105, v105, v112\n\t"
  "v_mfma_f32_16x16x32_bf16 a[132:135], a[176:179], a[92:95], 0\n\t"
  "v_add_f32 v106, v106, v112\n\t"
  "v_add_f32 v107, v107, v112\n\t"
  "v_mfma_f32_16x16x32_bf16 v[80:83], a[236:239], a[56:59], v[80:83]\n\t"
  "v_add_f32 v108, v108, v116\n\t"
  "v_add_f32 v109, v109, v116\n\t"
  "v_mfma_f32_16x16x32_bf16 v[84:87], a[236:239], a[60:63], v[84:87]\n\t"
  "s_waitcnt lgkmcnt(1)\n\t"
  "buffer_load_dwordx4 v40, s[56:59], s76 offen offset:2048 sc1 lds\n\t"
  "v_add_f32 v110, v110, v116\n\t"
  "v_add_f32 v111, v111, v116\n\t"
  "v_max3_f32 v45, v96, v97, v98\n\t"
  "v_max3_f32 v45, v45, v99, v100\n\t"
  "v_max3_f32 v45, v45, v101, v102\n\t"
  "v_max3_f32 v45, v45, v103, v104\n\t"
  "v_max3_f32 v45, v45, v105, v106\n\t"
  "v_max3_f32 v45, v45, v107, v108\n\t"
  "v_max3_f32 v45, v45, v109, v110\n\t"
  "v_max_f32 v45, v45, v111\n\t"
  "v_cmp_lt_f32 vcc, s82, v45\n\t"
  "s_nop 1\n\t"
  "s_cbranch_vccnz STUBT1_%=\n\t"
  "RETT1_%=:\n\t"
  "v_mfma_f32_16x16x32_bf16 v[88:91], a[240:243], a[0:3], v[112:115]\n\t"
  "ds_read_b128 a[252:255], v40 offset:44032\n\t"
  "v_max3_f32 v45, v80, v81, v82\n\t"
  "v_mfma_f32_16x16x32_bf16 v[92:95], a[240:243], a[4:7], v[116:119]\n\t"
  "v_exp_f32 v96, v96\n\t"
  "v_mfma_f32_16x16x32_bf16 a[136:139], a[180:183], a[88:91], 0\n\t"
  "ds_read_b128 a[224:227], v40 offset:45056\n\t"
  "v_max3_f32 v45, v45, v83, v84\n\t"
  "v_mfma_f32_16x16x32_bf16 a[140:143], a[180:183], a[92:95], 0\n\t"
  "buffer_load_dwordx4 a[200:203], v40, s[56:59], s79 offen offset:1024 sc1\n\t"
  "v_mfma_f32_16x16x32_bf16 v[88:91], a[244:247], a[8:11], v[88:91]\n\t"
  "v_exp_f32 v97, v97\n\t"
  "v_mfma_f32_16x16x32_bf16 v[92:95], a[244:247], a[12:15], v[92:95]\n\t"
  "s_waitcnt lgkmcnt(1)\n\t"
  "v_exp_f32 v98, v98\n\t"
  "v_mfma_f32_16x16x32_bf16 v[88:91], a[248:251], a[16:19], v[88:91]\n\t"
  "ds_read_b128 a[228:231], v40 offset:46080\n\t"
  "ds_read_b128 a[232:235], v40 offset:47104\n\t"
  "v_mfma_f32_16x16x32_bf16 v[92:95], a[248:251], a[20:23], v[92:95]\n\t"
  "s_waitcnt vmcnt(8)\n\t"
  "buffer_load_dwordx4 v40, s[56:59], s76 offen offset:3072 sc1 lds\n\t"
  "v_mfma_f32_16x16x32_bf16 a[144:147], a[184:187], a[88:91], 0\n\t"
  "s_add_u32 s76, s76, 0x1000\n\t"
  "v_exp_f32 v99, v99\n\t"
  "v_mfma_f32_16x16x32_bf16 a[148:151], a[184:187], a[92:95], 0\n\t"
  "v_exp_f32 v100, v100\n\t"
  "v_mfma_f32_16x16x32_bf16 v[88:91], a[252:255], a[24:27], v[88:91]\n\t"
  "v_exp_f32 v101, v101\n\t"
  "v_mfma_f32_16x16x32_bf16 v[92:95], a[252:255], a[28:31], v[92:95]\n\t"
  "s_waitcnt lgkmcnt(1)\n\t"
  "buffer_load_dwordx4 a[204:207], v40, s[56:59], s79 offen offset:2048 sc1\n\t"
  "v_mfma_f32_16x16x32_bf16 v[88:91], a[224:227], a[32:35], v[88:91]\n\t"
  "s_add_u32 m0, s84, 0x19000\n\t"
  "ds_read_b128 a[236:239], v40 offset:48128\n\t"
  "ds_read_b128 a[240:243], v40 offset:49152\n\t"
  "v_mfma_f32_16x16x32_bf16 v[92:95], a[224:227], a[36:39], v[92:95]\n\t"
  "v_exp_f32 v102, v102\n\t"
  "v_mfma_f32_16x16x32_bf16 a[152:155], a[188:191], a[88:91], 0\n\t"
  "v_exp_f32 v103, v103\n\t"
  "v_mfma_f32_16x16x32_bf16 a[156:159], a[188:191], a[92:95], 0\n\t"
  "buffer_load_dwordx4 v40, s[56:59], s76 offen offset:0 sc1 lds\n\t"
  "v_mfma_f32_16x16x32_bf16 v[88:91], a[228:231], a[40:43], v[88:91]\n\t"
  "ds_read_b128 a[244:247], v40 offset:50176\n\t"
  "v_cvt_pk_bf16_f32 v136, v96, v97\n\t"
  "v_mfma_f32_16x16x32_bf16 v[92:95], a[228:231], a[44:47], v[92:95]\n\t"
  "s_waitcnt lgkmcnt(2)\n\t"
  "v_exp_f32 v104, v104\n\t"
  "v_mfma_f32_16x16x32_bf16 v[88:91], a[232:235], a[48:51], v[88:91]\n\t"
  "ds_read_b128 a[248:251], v40 offset:51200\n\t"
  "v_cvt_pk_bf16_f32 v137, v98, v99\n\t"
  "v_mfma_f32_16x16x32_bf16 v[92:95], a[232:235], a[52:55], v[92:95]\n\t"
  "buffer_load_dwordx4 a[208:211], v40, s[56:59], s79 offen offset:3072 sc1\n\t"
  "v_mfma_f32_16x16x32_bf16 a[160:163], a[192:195], a[88:91], 0\n\t"
  "s_add_u32 s79, s79, 0x1000\n\t"
  "v_exp_f32 v105, v105\n\t"
  "v_mfma_f32_16x16x32_bf16 a[164:167], a[192:195], a[92:95], 0\n\t"
  "v_exp_f32 v106, v106\n\t"
  "v_mfma_f32_16x16x32_bf16 v[88:91], a[236:239], a[56:59], v[88:91]\n\t"
  "ds_read_b128 a[252:255], v40 offset:52224\n\t"
  "v_cvt_pk_bf16_f32 v140, v100, v101\n\t"
  "v_mfma_f32_16x16x32_bf16 v[92:95], a[236:239], a[60:63], v[92:95]\n\t"
  "s_waitcnt lgkmcnt(2)\n\t"
  "buffer_load_dwordx4 v40, s[56:59], s76 offen offset:1024 sc1 lds\n\t"
  "v_mfma_f32_16x16x32_bf16 v[96:99], a[240:243], a[0:3], v[112:115]\n\t"
  "ds_read_b128 a[224:227], v40 offset:53248\n\t"
  "v_cvt_pk_bf16_f32 v141, v102, v103\n\t"
  "v_mfma_f32_16x16x32_bf16 v[100:103], a[240:243], a[4:7], v[116:119]\n\t"
  "v_exp_f32 v107, v107\n\t"
  "v_mfma_f32_16x16x32_bf16 v[96:99], a[244:247], a[8:11], v[96:99]\n\t"
  "ds_read_b128 a[228:231], v40 offset:54272\n\t"
  "v_cvt_pk_bf16_f32 v138, v104, v105\n\t"
  "v_mfma_f32_16x16x32_bf16 v[100:103], a[244:247], a[12:15], v[100:103]\n\t"
  "s_waitcnt lgkmcnt(2)\n\t"
  "buffer_load_dwordx4 a[212:215], v40, s[56:59], s79 offen offset:0 sc1\n\t"
  "v_mfma_f32_16x16x32_bf16 v[96:99], a[248:251], a[16:19], v[96:99]\n\t"
  "ds_read_b128 a[232:235], v40 offset:55296\n\t"
  "v_cvt_pk_bf16_f32 v139, v106, v107\n\t"
  "v_mfma_f32_16x16x32_bf16 v[100:103], a[248:251], a[20:23], v[100:103]\n\t"
  "v_exp_f32 v108, v108\n\t"
  "v_mfma_f32_16x16x32_bf16 v[56:59], v[52:55], v[136:139], v[56:59]\n\t"
  "ds_read_b128 a[236:239], v40 offset:56320\n\t"
  "v_exp_f32 v109, v109\n\t"
  "v_exp_f32 v110, v110\n\t"
  "v_cvt_pk_bf16_f32 v142, v108, v109\n\t"
  "v_exp_f32 v111, v111\n\t"
  "s_nop 0\n\t"
  "v_cvt_pk_bf16_f32 v143, v110, v111\n\t"
  "s_nop 1\n\t"
  "v_mfma_f32_16x16x32_bf16 v[60:63], v[52:55], v[140:143], v[60:63]\n\t"
  "buffer_load_dwordx4 v40, s[56:59], s76 offen offset:2048 sc1 lds\n\t"
  "v_mfma_f32_16x16x32_bf16 v[96:99], a[252:255], a[24:27], v[96:99]\n\t"
  "ds_read_b128 a[240:243], v40 offset:57344\n\t"
  "v_max3_f32 v45, v45, v85, v86\n\t"
  "v_mfma_f32_16x16x32_bf16 v[100:103], a[252:255], a[28:31], v[100:103]\n\t"
  "s_waitcnt lgkmcnt(3)\n\t"
  "v_max3_f32 v45, v45, v87, v88\n\t"
  "v_max3_f32 v45, v45, v89, v90\n\t"
  "v_mfma_f32_16x16x32_bf16 v[96:99], a[224:227], a[32:35], v[96:99]\n\t"
  "ds_read_b128 a[244:247], v40 offset:58368\n\t"
  "v_max3_f32 v45, v45, v91, v92\n\t"
  "v_mfma_f32_16x16x32_bf16 v[100:103], a[224:227], a[36:39], v[100:103]\n\t"
  "buffer_load_dwordx4 a[216:219], v40, s[56:59], s79 offen offset:1024 sc1\n\t"
  "v_mfma_f32_16x16x32_bf16 v[96:99], a[228:231], a[40:43], v[96:99]\n\t"
  "ds_read_b128 a[248:251], v40 offset:59392\n\t"
  "v_max3_f32 v45, v45, v93, v94\n\t"
  "v_mfma_f32_16x16x32_bf16 v[100:103], a[228:231], a[44:47], v[100:103]\n\t"
  "s_waitcnt lgkmcnt(3)\n\t"
  "v_max_f32 v45, v45, v95\n\t"
  "v_cmp_lt_f32 vcc, s82, v45\n\t"
  "v_mfma_f32_16x16x32_bf16 v[96:99], a[232:235], a[48:51], v[96:99]\n\t"
  "ds_read_b128 a[252:255], v40 offset:60416\n\t"
  "v_mfma_f32_16x16x32_bf16 v[100:103], a[232:235], a[52:55], v[100:103]\n\t"
  "s_cbranch_vccnz STUBT2_%=\n\t"
  "RETT2_%=:\n\t"
  "v_mfma_f32_16x16x32_bf16 v[96:99], a[236:239], a[56:59], v[96:99]\n\t"
  "buffer_load_dwordx4 v40, s[56:59], s76 offen offset:3072 sc1 lds\n\t"
  "v_mfma_f32_16x16x32_bf16 v[100:103], a[236:239], a[60:63], v[100:103]\n\t"
  "s_waitcnt lgkmcnt(2)\n\t"
  "ds_read_b128 a[224:227], v40 offset:61440\n\t"
  "v_exp_f32 v88, v88\n\t"
  "v_mfma_f32_16x16x32_bf16 v[104:107], a[240:243], a[0:3], v[112:115]\n\t"
  "s_add_u32 s76, s76, 0x7000\n\t"
  "ds_read_b128 a[228:231], v40 offset:62464\n\t"
  "ds_read_b128 a[232:235], v40 offset:63488\n\t"
  "v_mfma_f32_16x16x32_bf16 v[108:111], a[240:243], a[4:7], v[116:119]\n\t"
  "buffer_load_dwordx4 a[220:223], v40, s[56:59], s79 offen offset:2048 sc1\n\t"
  "v_mfma_f32_16x16x32_bf16 v[104:107], a[244:247], a[8:11], v[104:107]\n\t"
  "s_add_u32 s79, s79, 0xc00\n\t"
  "v_exp_f32 v80, v80\n\t"
  "v_mfma_f32_16x16x32_bf16 v[108:111], a[244:247], a[12:15], v[108:111]\n\t"
  "s_waitcnt lgkmcnt(3)\n\t"
  "v_exp_f32 v81, v81\n\t"
  "v_mfma_f32_16x16x32_bf16 v[104:107], a[248:251], a[16:19], v[104:107]\n\t"
  "s_waitcnt vmcnt(22)\n\t"
  "ds_read_b128 a[236:239], v40 offset:64512\n\t"
  "v_cvt_pk_bf16_f32 v144, v80, v81\n\t"
  "v_mfma_f32_16x16x32_bf16 v[108:111], a[248:251], a[20:23], v[108:111]\n\t"
  "s_barrier\n\t"
  "v_exp_f32 v82, v82\n\t"
  "s_nop 0\n\t"
  "s_cbranch_execz TRAMP_%=\n\t"
  "v_mfma_f32_16x16x32_bf16 v[104:107], a[252:255], a[24:27], v[104:107]\n\t"
  "ds_read_b128 a[240:243], v42 offset:0\n\t"
  "ds_read_b128 a[244:247], v42 offset:1024\n\t"
  "v_mfma_f32_16x16x32_bf16 v[108:111], a[252:255], a[28:31], v[108:111]\n\t"
  "s_waitcnt lgkmcnt(4)\n\t"
  "v_exp_f32 v83, v83\n\t"
  "v_mfma_f32_16x16x32_bf16 v[104:107], a[224:227], a[32:35], v[104:107]\n\t"
  "v_exp_f32 v84, v84\n\t"
  "v_mfma_f32_16x16x32_bf16 v[108:111], a[224:227], a[36:39], v[108:111]\n\t"
  "v_exp_f32 v85, v85\n\t"
  "v_mfma_f32_16x16x32_bf16 v[104:107], a[228:231], a[40:43], v[104:107]\n\t"
  "ds_read_b128 a[248:251], v42 offset:2048\n\t"
  "v_cvt_pk_bf16_f32 v145, v82, v83\n\t"
  "v_mfma_f32_16x16x32_bf16 v[108:111], a[228:231], a[44:47], v[108:111]\n\t"
  "s_waitcnt lgkmcnt(3)\n\t"
  "v_exp_f32 v86, v86\n\t"
  "v_mfma_f32_16x16x32_bf16 v[104:107], a[232:235], a[48:51], v[104:107]\n\t"
  "v_exp_f32 v87, v87\n\t"
  "v_mfma_f32_16x16x32_bf16 v[108:111], a[232:235], a[52:55], v[108:111]\n\t"
  "v_exp_f32 v89, v89\n\t"
  "v_mfma_f32_16x16x32_bf16 v[104:107], a[236:239], a[56:59], v[104:107]\n\t"
  "v_exp_f32 v90, v90\n\t"
  "v_mfma_f32_16x16x32_bf16 v[108:111], a[236:239], a[60:63], v[108:111]\n\t"
  "s_waitcnt lgkmcnt(1)\n\t"
  "v_cvt_pk_bf16_f32 v148, v84, v85\n\t"
  "v_cvt_pk_bf16_f32 v149, v86, v87\n\t"
  "v_mfma_f32_16x16x32_bf16 v[80:83], a[240:243], a[0:3], v[112:115]\n\t"
  "ds_read_b128 a[252:255], v42 offset:3072\n\t"
  "v_cvt_pk_bf16_f32 v146, v88, v89\n\t"
  "v_mfma_f32_16x16x32_bf16 v[84:87], a[240:243], a[4:7], v[116:119]\n\t"
  "v_exp_f32 v91, v91\n\t"
  "v_mfma_f32_16x16x32_bf16 v[80:83], a[244:247], a[8:11], v[80:83]\n\t"
  "s_add_u32 m0, s84, 0\n\t"
  "ds_read_b128 a[224:227], v42 offset:4096\n\t"
  "v_cvt_pk_bf16_f32 v147, v90, v91\n\t"
  "v_mfma_f32_16x16x32_bf16 v[84:87], a[244:247], a[12:15], v[84:87]\n\t"
  "s_waitcnt lgkmcnt(1)\n\t"
  "buffer_load_dwordx4 a[88:91], v40, s[56:59], s18 offen offset:0 sc1\n\t"
  "v_mfma_f32_16x16x32_bf16 v[80:83], a[248:251], a[16:19], v[80:83]\n\t"
  "v_exp_f32 v92, v92\n\t"
  "v_mfma_f32_16x16x32_bf16 v[84:87], a[248:251], a[20:23], v[84:87]\n\t"
  "buffer_load_dwordx4 v40, s[56:59], s76 offen offset:0 sc1 lds\n\t"
  "v_mfma_f32_16x16x32_bf16 v[56:59], v[52:55], v[144:147], v[56:59]\n\t"
  "ds_read_b128 a[228:231], v42 offset:5120\n\t"
  "v_exp_f32 v93, v93\n\t"
  "v_exp_f32 v94, v94\n\t"
  "v_cvt_pk_bf16_f32 v150, v92, v93\n\t"
  "v_exp_f32 v95, v95\n\t"
  "s_nop 0\n\t"
  "v_cvt_pk_bf16_f32 v151, v94, v95\n\t"
  "s_nop 1\n\t"
  "v_mfma_f32_16x16x32_bf16 v[60:63], v[52:55], v[148:151], v[60:63]\n\t"
  "s_waitcnt vmcnt(12)\n\t"
  "v_max3_f32 v45, v96, v97, v98\n\t"
  "v_max3_f32 v45, v45, v99, v100\n\t"
  "v_mfma_f32_16x16x32_bf16 a[112:115], a[196:199], a[96:99], a[112:115]\n\t"
  "ds_read_b128 a[232:235], v42 offset:6144\n\t"
  "v_max3_f32 v45, v45, v101, v102\n\t"
  "v_mfma_f32_16x16x32_bf16 a[116:119], a[196:199], a[100:103], a[116:119]\n\t"
  "v_max3_f32 v45, v45, v103, v104\n\t"
  "v_max3_f32 v45, v45, v105, v106\n\t"
  "v_mfma_f32_16x16x32_bf16 v[80:83], a[252:255], a[24:27], v[80:83]\n\t"
  "v_max3_f32 v45, v45, v107, v108\n\t"
  "v_max3_f32 v45, v45, v109, v110\n\t"
  "v_mfma_f32_16x16x32_bf16 v[84:87], a[252:255], a[28:31], v[84:87]\n\t"
  "s_waitcnt lgkmcnt(1)\n\t"
  "buffer_load_dwordx4 a[92:95], v40, s[56:59], s18 offen offset:1024 sc1\n\t"
  "v_mfma_f32_16x16x32_bf16 v[80:83], a[224:227], a[32:35], v[80:83]\n\t"
  "s_add_u32 s18, s18, 0x800\n\t"
  "ds_read_b128 a[236:239], v42 offset:7168\n\t"
  "v_max_f32 v45, v45, v111\n\t"
  "v_mfma_f32_16x16x32_bf16 v[84:87], a[224:227], a[36:39], v[84:87]\n\t"
  "buffer_load_dwordx4 v40, s[56:59], s76 offen offset:1024 sc1 lds\n\t"
  "v_mfma_f32_16x16x32_bf16 a[120:123], a[200:203], a[96:99], a[120:123]\n\t"
  "ds_read_b128 a[240:243], v42 offset:8192\n\t"
  "v_cmp_lt_f32 vcc, s82, v45\n\t"
  "v_mfma_f32_16x16x32_bf16 a[124:127], a[200:203], a[100:103], a[124:127]\n\t"
  "v_mfma_f32_16x16x32_bf16 v[80:83], a[228:231], a[40:43], v[80:83]\n\t"
  "s_cbranch_vccnz STUBT3_%=\n\t"
  "RETT3_%=:\n\t"
  "v_mfma_f32_16x16x32_bf16 v[84:87], a[228:231], a[44:47], v[84:87]\n\t"
  "s_waitcnt lgkmcnt(1)\n\t"
  "v_exp_f32 v96, v96\n\t"
  "v_mfma_f32_16x16x32_bf16 v[80:83], a[232:235], a[48:51], v[80:83]\n\t"
  "ds_read_b128 a[244:247], v42 offset:9216\n\t"
  "ds_read_b128 a[248:251], v42 offset:10240\n\t"
  "v_mfma_f32_16x16x32_bf16 v[84:87], a[232:235], a[52:55], v[84:87]\n\t"
  "s_waitcnt vmcnt(10)\n\t"
  "buffer_load_dwordx4 a[168:171], v40, s[56:59], s79 offen offset:0 sc1\n\t"
  "v_mfma_f32_16x16x32_bf16 a[128:131], a[204:207], a[96:99], a[128:131]\n\t"
  "v_exp_f32 v97, v97\n\t"
  "v_mfma_f32_16x16x32_bf16 a[132:135], a[204:207], a[100:103], a[132:135]\n\t"
  "buffer_load_dwordx4 v40, s[56:59], s76 offen offset:2048 sc1 lds\n\t"
  "v_mfma_f32_16x16x32_bf16 v[80:83], a[236:239], a[56:59], v[80:83]\n\t"
  "v_exp_f32 v98, v98\n\t"
  "v_mfma_f32_16x16x32_bf16 v[84:87], a[236:239], a[60:63], v[84:87]\n\t"
  "s_waitcnt lgkmcnt(1)\n\t"
  "v_exp_f32 v99, v99\n\t"
  "v_mfma_f32_16x16x32_bf16 v[88:91], a[240:243], a[0:3], v[112:115]\n\t"
  "ds_read_b128 a[252:255], v42 offset:11264\n\t"
  "ds_read_b128 a[224:227], v42 offset:12288\n\t"
  "v_mfma_f32_16x16x32_bf16 v[92:95], a[240:243], a[4:7], v[116:119]\n\t"
  "v_exp_f32 v100, v100\n\t"
  "v_mfma_f32_16x16x32_bf16 a[136:139], a[208:211], a[96:99], a[136:139]\n\t"
  "v_exp_f32 v101, v101\n\t"
  "v_mfma_f32_16x16x32_bf16 a[140:143], a[208:211], a[100:103], a[140:143]\n\t"
  "buffer_load_dwordx4 a[172:175], v40, s[56:59], s79 offen offset:1024 sc1\n\t"
  "v_mfma_f32_16x16x32_bf16 v[88:91], a[244:247], a[8:11], v[88:91]\n\t"
  "v_exp_f32 v102, v102\n\t"
  "v_mfma_f32_16x16x32_bf16 v[92:95], a[244:247], a[12:15], v[92:95]\n\t"
  "s_waitcnt lgkmcnt(1)\n\t"
  "buffer_load_dwordx4 v40, s[56:59], s76 offen offset:3072 sc1 lds\n\t"
  "v_mfma_f32_16x16x32_bf16 v[88:91], a[248:251], a[16:19], v[88:91]\n\t"
  "s_add_u32 s76, s76, 0x1000\n\t"
  "ds_read_b128 a[228:231], v42 offset:13312\n\t"
  "v_cvt_pk_bf16_f32 v152, v96, v97\n\t"
  "v_mfma_f32_16x16x32_bf16 v[92:95], a[248:251], a[20:23], v[92:95]\n\t"
  "s_waitcnt vmcnt(8)\n\t"
  "v_exp_f32 v103, v103\n\t"
  "v_mfma_f32_16x16x32_bf16 a[144:147], a[212:215], a[96:99], a[144:147]\n\t"
  "ds_read_b128 a[232:235], v42 offset:14336\n\t"
  "v_cvt_pk_bf16_f32 v153, v98, v99\n\t"
  "v_mfma_f32_16x16x32_bf16 a[148:151], a[212:215], a[100:103], a[148:151]\n\t"
  "v_exp_f32 v104, v104\n\t"
  "v_mfma_f32_16x16x32_bf16 v[88:91], a[252:255], a[24:27], v[88:91]\n\t"
  "s_add_u32 m0, s84, 0x1000\n\t"
  "v_exp_f32 v105, v105\n\t"
  "v_mfma_f32_16x16x32_bf16 v[92:95], a[252:255], a[28:31], v[92:95]\n\t"
  "s_waitcnt lgkmcnt(1)\n\t"
  "buffer_load_dwordx4 a[176:179], v40, s[56:59], s79 offen offset:2048 sc1\n\t"
  "v_mfma_f32_16x16x32_bf16 v[88:91], a[224:227], a[32:35], v[88:91]\n\t"
  "ds_read_b128 a[236:239], v42 offset:15360\n\t"
  "ds_read_b128 a[240:243], v42 offset:16384\n\t"
  "v_mfma_f32_16x16x32_bf16 v[92:95], a[224:227], a[36:39], v[92:95]\n\t"
  "buffer_load_dwordx4 v40, s[56:59], s76 offen offset:0 sc1 lds\n\t"
  "v_mfma_f32_16x16x32_bf16 a[152:155], a[216:219], a[96:99], a[152:155]\n\t"
  "v_exp_f32 v106, v106\n\t"
  "v_mfma_f32_16x16x32_bf16 a[156:159], a[216:219], a[100:103], a[156:159]\n\t"
  "v_exp_f32 v107, v107\n\t"
  "v_mfma_f32_16x16x32_bf16 v[88:91], a[228:231], a[40:43], v[88:91]\n\t"
  "ds_read_b128 a[244:247], v42 offset:17408\n\t"
  "ds_read_b128 a[248:251], v42 offset:18432\n\t"
  "v_mfma_f32_16x16x32_bf16 v[92:95], a[228:231], a[44:47], v[92:95]\n\t"
  "s_waitcnt lgkmcnt(3)\n\t"
  "v_exp_f32 v108, v108\n\t"
  "v_mfma_f32_16x16x32_bf16 v[88:91], a[232:235], a[48:51], v[88:91]\n\t"
  "v_exp_f32 v109, v109\n\t"
  "v_mfma_f32_16x16x32_bf16 v[92:95], a[232:235], a[52:55], v[92:95]\n\t"
  "buffer_load_dwordx4 a[180:183], v40, s[56:59], s79 offen offset:3072 sc1\n\t"
  "v_mfma_f32_16x16x32_bf16 a[160:163], a[220:223], a[96:99], a[160:163]\n\t"
  "s_add_u32 s79, s79, 0x1000\n\t"
  "v_exp_f32 v110, v110\n\t"
  "v_mfma_f32_16x16x32_bf16 a[164:167], a[220:223], a[100:103], a[164:167]\n\t"
  "buffer_load_dwordx4 v40, s[56:59], s76 offen offset:1024 sc1 lds\n\t"
  "v_mfma_f32_16x16x32_bf16 v[88:91], a[236:239], a[56:59], v[88:91]\n\t"
  "ds_read_b128 a[252:255], v42 offset:19456\n\t"
  "v_cvt_pk_bf16_f32 v156, v100, v101\n\t"
  "v_mfma_f32_16x16x32_bf16 v[92:95], a[236:239], a[60:63], v[92:95]\n\t"
  "s_waitcnt lgkmcnt(2)\n\t"
  "v_cvt_pk_bf16_f32 v157, v102, v103\n\t"
  "v_cvt_pk_bf16_f32 v154, v104, v105\n\t"
  "v_mfma_f32_16x16x32_bf16 v[96:99], a[240:243], a[0:3], v[112:115]\n\t"
  "ds_read_b128 a[224:227], v42 offset:20480\n\t"
  "v_cvt_pk_bf16_f32 v155, v106, v107\n\t"
  "v_mfma_f32_16x16x32_bf16 v[100:103], a[240:243], a[4:7], v[116:119]\n\t"
  "v_exp_f32 v111, v111\n\t"
  "v_mfma_f32_16x16x32_bf16 v[96:99], a[244:247], a[8:11], v[96:99]\n\t"
  "ds_read_b128 a[228:231], v42 offset:21504\n\t"
  "v_cvt_pk_bf16_f32 v158, v108, v109\n\t"
  "v_mfma_f32_16x16x32_bf16 v[100:103], a[244:247], a[12:15], v[100:103]\n\t"
  "s_waitcnt lgkmcnt(2)\n\t"
  "buffer_load_dwordx4 a[184:187], v40, s[56:59], s79 offen offset:0 sc1\n\t"
  "v_mfma_f32_16x16x32_bf16 v[96:99], a[248:251], a[16:19], v[96:99]\n\t"
  "ds_read_b128 a[232:235], v42 offset:22528\n\t"
  "v_cvt_pk_bf16_f32 v159, v110, v111\n\t"
  "v_mfma_f32_16x16x32_bf16 v[100:103], a[248:251], a[20:23], v[100:103]\n\t"
  "buffer_load_dwordx4 v40, s[56:59], s76 offen offset:2048 sc1 lds\n\t"
  "v_mfma_f32_16x16x32_bf16 v[56:59], v[52:55], v[152:155], v[56:59]\n\t"
  "ds_read_b128 a[236:239], v42 offset:23552\n\t"
  "v_max3_f32 v45, v80, v81, v82\n\t"
  "v_mfma_f32_16x16x32_bf16 v[60:63], v[52:55], v[156:159], v[60:63]\n\t"
  "v_max3_f32 v45, v45, v83, v84\n\t"
  "v_max3_f32 v45, v45, v85, v86\n\t"
  "v_mfma_f32_16x16x32_bf16 v[96:99], a[252:255], a[24:27], v[96:99]\n\t"
  "ds_read_b128 a[240:243], v42 offset:24576\n\t"
  "v_max3_f32 v45, v45, v87, v88\n\t"
  "v_mfma_f32_16x16x32_bf16 v[100:103], a[252:255], a[28:31], v[100:103]\n\t"
  "s_waitcnt lgkmcnt(3)\n\t"
  "v_max3_f32 v45, v45, v89, v90\n\t"
  "v_max3_f32 v45, v45, v91, v92\n\t"
  "v_mfma_f32_16x16x32_bf16 v[96:99], a[224:227], a[32:35], v[96:99]\n\t"
  "ds_read_b128 a[244:247], v42 offset:25600\n\t"
  "v_max3_f32 v45, v45, v93, v94\n\t"
  "v_mfma_f32_16x16x32_bf16 v[100:103], a[224:227], a[36:39], v[100:103]\n\t"
  "buffer_load_dwordx4 a[188:191], v40, s[56:59], s79 offen offset:1024 sc1\n\t"
  "v_mfma_f32_16x16x32_bf16 v[96:99], a[228:231], a[40:43], v[96:99]\n\t"
  "ds_read_b128 a[248:251], v42 offset:26624\n\t"
  "v_max_f32 v45, v45, v95\n\t"
  "v_mfma_f32_16x16x32_bf16 v[100:103], a[228:231], a[44:47], v[100:103]\n\t"
  "s_waitcnt lgkmcnt(3)\n\t"
  "buffer_load_dwordx4 v40, s[56:59], s76 offen offset:3072 sc1 lds\n\t"
  "v_mfma_f32_16x16x32_bf16 v[96:99], a[232:235], a[48:51], v[96:99]\n\t"
  "s_add_u32 s76, s76, 0x7000\n\t"
  "ds_read_b128 a[252:255], v42 offset:27648\n\t"
  "v_cmp_lt_f32 vcc, s82, v45\n\t"
  "v_mfma_f32_16x16x32_bf16 v[100:103], a[232:235], a[52:55], v[100:103]\n\t"
  "v_mfma_f32_16x16x32_bf16 v[96:99], a[236:239], a[56:59], v[96:99]\n\t"
  "s_cbranch_vccnz STUBT4_%=\n\t"
  "RETT4_%=:\n\t"
  "v_mfma_f32_16x16x32_bf16 v[100:103], a[236:239], a[60:63], v[100:103]\n\t"
  "s_waitcnt lgkmcnt(2)\n\t"
  "ds_read_b128 a[224:227], v42 offset:28672\n\t"
  "ds_read_b128 a[228:231], v42 offset:29696\n\t"
  "v_mfma_f32_16x16x32_bf16 v[104:107], a[240:243], a[0:3], v[112:115]\n\t"
  "v_exp_f32 v80, v80\n\t"
  "v_mfma_f32_16x16x32_bf16 v[108:111], a[240:243], a[4:7], v[116:119]\n\t"
  "buffer_load_dwordx4 a[192:195], v40, s[56:59], s79 offen offset:2048 sc1\n\t"
  "v_mfma_f32_16x16x32_bf16 v[104:107], a[244:247], a[8:11], v[104:107]\n\t"
  "s_add_u32 s79, s79, 0xc00\n\t"
  "ds_read_b128 a[232:235], v42 offset:30720\n\t"
  "ds_read_b128 a[236:239], v42 offset:31744\n\t"
  "v_mfma_f32_16x16x32_bf16 v[108:111], a[244:247], a[12:15], v[108:111]\n\t"
  "s_waitcnt lgkmcnt(4)\n\t"
  "v_exp_f32 v81, v81\n\t"
  "v_mfma_f32_16x16x32_bf16 v[104:107], a[248:251], a[16:19], v[104:107]\n\t"
  "s_waitcnt vmcnt(18)\n\t"
  "buffer_store_dwordx2 v[8:9], v12, s[56:59], s17 offen offset:0 sc1\n\t"
  "v_mfma_f32_16x16x32_bf16 v[108:111], a[248:251], a[20:23], v[108:111]\n\t"
  "s_barrier\n\t"
  "v_exp_f32 v82, v82\n\t"
  "v_mfma_f32_16x16x32_bf16 v[104:107], a[252:255], a[24:27], v[104:107]\n\t"
  "ds_read_b128 a[240:243], v42 offset:32768\n\t"
  "ds_read_b128 a[244:247], v42 offset:33792\n\t"
  "v_mfma_f32_16x16x32_bf16 v[108:111], a[252:255], a[28:31], v[108:111]\n\t"
  "s_waitcnt lgkmcnt(4)\n\t"
  "v_exp_f32 v83, v83\n\t"
  "v_mfma_f32_16x16x32_bf16 v[104:107], a[224:227], a[32:35], v[104:107]\n\t"
  "v_exp_f32 v84, v84\n\t"
  "v_mfma_f32_16x16x32_bf16 v[108:111], a[224:227], a[36:39], v[108:111]\n\t"
  "v_exp_f32 v85, v85\n\t"
  "v_mfma_f32_16x16x32_bf16 v[104:107], a[228:231], a[40:43], v[104:107]\n\t"
  "ds_read_b128 a[248:251], v42 offset:34816\n\t"
  "v_cvt_pk_bf16_f32 v160, v80, v81\n\t"
  "v_mfma_f32_16x16x32_bf16 v[108:111], a[228:231], a[44:47], v[108:111]\n\t"
  "s_waitcnt lgkmcnt(3)\n\t"
  "v_exp_f32 v86, v86\n\t"
  "v_mfma_f32_16x16x32_bf16 v[104:107], a[232:235], a[48:51], v[104:107]\n\t"
  "v_exp_f32 v87, v87\n\t"
  "v_mfma_f32_16x16x32_bf16 v[108:111], a[232:235], a[52:55], v[108:111]\n\t"
  "v_exp_f32 v88, v88\n\t"
  "v_mfma_f32_16x16x32_bf16 v[104:107], a[236:239], a[56:59], v[104:107]\n\t"
  "v_cvt_pk_bf16_f32 v161, v82, v83\n\t"
  "v_cvt_pk_bf16_f32 v164, v84, v85\n\t"
  "v_mfma_f32_16x16x32_bf16 v[108:111], a[236:239], a[60:63], v[108:111]\n\t"
  "s_waitcnt lgkmcnt(1)\n\t"
  "v_cvt_pk_bf16_f32 v165, v86, v87\n\t"
  "v_exp_f32 v89, v89\n\t"
  "v_mfma_f32_16x16x32_bf16 v[80:83], a[240:243], a[0:3], v[112:115]\n\t"
  "ds_read_b128 a[252:255], v42 offset:35840\n\t"
  "v_cvt_pk_bf16_f32 v162, v88, v89\n\t"
  "v_mfma_f32_16x16x32_bf16 v[84:87], a[240:243], a[4:7], v[116:119]\n\t"
  "v_exp_f32 v90, v90\n\t"
  "v_mfma_f32_16x16x32_bf16 v[80:83], a[244:247], a[8:11], v[80:83]\n\t"
  "s_add_u32 m0, s84, 0x8000\n\t"
  "ds_read_b128 a[224:227], v42 offset:36864\n\t"
  "v_exp_f32 v91, v91\n\t"
  "v_mfma_f32_16x16x32_bf16 v[84:87], a[244:247], a[12:15], v[84:87]\n\t"
  "s_waitcnt lgkmcnt(1)\n\t"
  "buffer_load_dwordx4 a[96:99], v40, s[56:59], s18 offen offset:0 sc1\n\t"
  "v_mfma_f32_16x16x32_bf16 v[80:83], a[248:251], a[16:19], v[80:83]\n\t"
  "v_cvt_pk_bf16_f32 v163, v90, v91\n\t"
  "v_exp_f32 v92, v92\n\t"
  "v_mfma_f32_16x16x32_bf16 v[84:87], a[248:251], a[20:23], v[84:87]\n\t"
  "buffer_load_dwordx4 v40, s[56:59], s76 offen offset:0 sc1 lds\n\t"
  "v_mfma_f32_16x16x32_bf16 v[56:59], v[52:55], v[160:163], v[56:59]\n\t"
  "ds_read_b128 a[228:231], v42 offset:37888\n\t"
  "v_exp_f32 v93, v93\n\t"
  "v_exp_f32 v94, v94\n\t"
  "v_cvt_pk_bf16_f32 v166, v92, v93\n\t"
  "v_exp_f32 v95, v95\n\t"
  "s_nop 0\n\t"
  "v_cvt_pk_bf16_f32 v167, v94, v95\n\t"
  "s_nop 1\n\t"
  "v_mfma_f32_16x16x32_bf16 v[60:63], v[52:55], v[164:167], v[60:63]\n\t"
  "s_waitcnt vmcnt(13)\n\t"
  "v_max3_f32 v45, v96, v97, v98\n\t"
  "v_max3_f32 v45, v45, v99, v100\n\t"
  "v_mfma_f32_16x16x32_bf16 a[112:115], a[168:171], a[88:91], a[112:115]\n\t"
  "ds_read_b128 a[232:235], v42 offset:38912\n\t"
  "v_max3_f32 v45, v45, v101, v102\n\t"
  "v_mfma_f32_16x16x32_bf16 a[116:119], a[168:171], a[92:95], a[116:119]\n\t"
  "v_max3_f32 v45, v45, v103, v104\n\t"
  "v_max3_f32 v45, v45, v105, v106\n\t"
  "v_mfma_f32_16x16x32_bf16 v[80:83], a[252:255], a[24:27], v[80:83]\n\t"
  "v_max3_f32 v45, v45, v107, v108\n\t"
  "v_max3_f32 v45, v45, v109, v110\n\t"
  "v_mfma_f32_16x16x32_bf16 v[84:87], a[252:255], a[28:31], v[84:87]\n\t"
  "s_waitcnt lgkmcnt(1)\n\t"
  "buffer_load_dwordx4 a[100:103], v40, s[56:59], s18 offen offset:1024 sc1\n\t"
  "v_mfma_f32_16x16x32_bf16 v[80:83], a[224:227], a[32:35], v[80:83]\n\t"
  "s_add_u32 s18, s18, 0x800\n\t"
  "ds_read_b128 a[236:239], v42 offset:39936\n\t"
  "v_max_f32 v45, v45, v111\n\t"
  "v_mfma_f32_16x16x32_bf16 v[84:87], a[224:227], a[36:39], v[84:87]\n\t"
  "buffer_load_dwordx4 v40, s[56:59], s76 offen offset:1024 sc1 lds\n\t"
  "v_mfma_f32_16x16x32_bf16 a[120:123], a[172:175], a[88:91], a[120:123]\n\t"
  "ds_read_b128 a[240:243], v42 offset:40960\n\t"
  "v_cmp_lt_f32 vcc, s82, v45\n\t"
  "v_mfma_f32_16x16x32_bf16 a[124:127], a[172:175], a[92:95], a[124:127]\n\t"
  "v_mfma_f32_16x16x32_bf16 v[80:83], a[228:231], a[40:43], v[80:83]\n\t"
  "s_cbranch_vccnz STUBT5_%=\n\t"
  "RETT5_%=:\n\t"
  "v_mfma_f32_16x16x32_bf16 v[84:87], a[228:231], a[44:47], v[84:87]\n\t"
  "s_waitcnt lgkmcnt(1)\n\t"
  "v_exp_f32 v96, v96\n\t"
  "v_mfma_f32_16x16x32_bf16 v[80:83], a[232:235], a[48:51], v[80:83]\n\t"
  "ds_read_b128 a[244:247], v42 offset:41984\n\t"
  "ds_read_b128 a[248:251], v42 offset:43008\n\t"
  "v_mfma_f32_16x16x32_bf16 v[84:87], a[232:235], a[52:55], v[84:87]\n\t"
  "s_waitcnt vmcnt(11)\n\t"
  "buffer_load_dwordx4 a[196:199], v40, s[56:59], s79 offen offset:0 sc1\n\t"
  "v_mfma_f32_16x16x32_bf16 a[128:131], a[176:179], a[88:91], a[128:131]\n\t"
  "v_exp_f32 v97, v97\n\t"
  "v_mfma_f32_16x16x32_bf16 a[132:135], a[176:179], a[92:95], a[132:135]\n\t"
  "buffer_load_dwordx4 v40, s[56:59], s76 offen offset:2048 sc1 lds\n\t"
  "v_mfma_f32_16x16x32_bf16 v[80:83], a[236:239], a[56:59], v[80:83]\n\t"
  "v_exp_f32 v98, v98\n\t"
  "v_mfma_f32_16x16x32_bf16 v[84:87], a[236:239], a[60:63], v[84:87]\n\t"
  "s_waitcnt lgkmcnt(1)\n\t"
  "v_exp_f32 v99, v99\n\t"
  "v_mfma_f32_16x16x32_bf16 v[88:91], a[240:243], a[0:3], v[112:115]\n\t"
  "ds_read_b128 a[252:255], v42 offset:44032\n\t"
  "ds_read_b128 a[224:227], v42 offset:45056\n\t"
  "v_mfma_f32_16x16x32_bf16 v[92:95], a[240:243], a[4:7], v[116:119]\n\t"
  "v_exp_f32 v100, v100\n\t"
  "v_mfma_f32_16x16x32_bf16 a[136:139], a[180:183], a[88:91], a[136:139]\n\t"
  "v_exp_f32 v101, v101\n\t"
  "v_mfma_f32_16x16x32_bf16 a[140:143], a[180:183], a[92:95], a[140:143]\n\t"
  "buffer_load_dwordx4 a[200:203], v40, s[56:59], s79 offen offset:1024 sc1\n\t"
  "v_mfma_f32_16x16x32_bf16 v[88:91], a[244:247], a[8:11], v[88:91]\n\t"
  "v_exp_f32 v102, v102\n\t"
  "v_mfma_f32_16x16x32_bf16 v[92:95], a[244:247], a[12:15], v[92:95]\n\t"
  "s_waitcnt lgkmcnt(1)\n\t"
  "buffer_load_dwordx4 v40, s[56:59], s76 offen offset:3072 sc1 lds\n\t"
  "v_mfma_f32_16x16x32_bf16 v[88:91], a[248:251], a[16:19], v[88:91]\n\t"
  "s_add_u32 s76, s76, 0x1000\n\t"
  "ds_read_b128 a[228:231], v42 offset:46080\n\t"
  "v_cvt_pk_bf16_f32 v168, v96, v97\n\t"
  "v_mfma_f32_16x16x32_bf16 v[92:95], a[248:251], a[20:23], v[92:95]\n\t"
  "s_waitcnt vmcnt(9)\n\t"
  "v_exp_f32 v103, v103\n\t"
  "v_mfma_f32_16x16x32_bf16 a[144:147], a[184:187], a[88:91], a[144:147]\n\t"
  "ds_read_b128 a[232:235], v42 offset:47104\n\t"
  "v_cvt_pk_bf16_f32 v169, v98, v99\n\t"
  "v_mfma_f32_16x16x32_bf16 a[148:151], a[184:187], a[92:95], a[148:151]\n\t"
  "v_exp_f32 v104, v104\n\t"
  "v_mfma_f32_16x16x32_bf16 v[88:91], a[252:255], a[24:27], v[88:91]\n\t"
  "s_add_u32 m0, s84, 0x9000\n\t"
  "v_exp_f32 v105, v105\n\t"
  "v_mfma_f32_16x16x32_bf16 v[92:95], a[252:255], a[28:31], v[92:95]\n\t"
  "s_waitcnt lgkmcnt(1)\n\t"
  "buffer_load_dwordx4 a[204:207], v40, s[56:59], s79 offen offset:2048 sc1\n\t"
  "v_mfma_f32_16x16x32_bf16 v[88:91], a[224:227], a[32:35], v[88:91]\n\t"
  "ds_read_b128 a[236:239], v42 offset:48128\n\t"
  "ds_read_b128 a[240:243], v42 offset:49152\n\t"
  "v_mfma_f32_16x16x32_bf16 v[92:95], a[224:227], a[36:39], v[92:95]\n\t"
  "buffer_load_dwordx4 v40, s[56:59], s76 offen offset:0 sc1 lds\n\t"
  "v_mfma_f32_16x16x32_bf16 a[152:155], a[188:191], a[88:91], a[152:155]\n\t"
  "v_exp_f32 v106, v106\n\t"
  "v_mfma_f32_16x16x32_bf16 a[156:159], a[188:191], a[92:95], a[156:159]\n\t"
  "v_exp_f32 v107, v107\n\t"
  "v_mfma_f32_16x16x32_bf16 v[88:91], a[228:231], a[40:43], v[88:91]\n\t"
  "ds_read_b128 a[244:247], v42 offset:50176\n\t"
  "ds_read_b128 a[248:251], v42 offset:51200\n\t"
  "v_mfma_f32_16x16x32_bf16 v[92:95], a[228:231], a[44:47], v[92:95]\n\t"
  "s_waitcnt lgkmcnt(3)\n\t"
  "v_exp_f32 v108, v108\n\t"
  "v_mfma_f32_16x16x32_bf16 v[88:91], a[232:235], a[48:51], v[88:91]\n\t"
  "v_exp_f32 v109, v109\n\t"
  "v_mfma_f32_16x16x32_bf16 v[92:95], a[232:235], a[52:55], v[92:95]\n\t"
  "buffer_load_dwordx4 a[208:211], v40, s[56:59], s79 offen offset:3072 sc1\n\t"
  "v_mfma_f32_16x16x32_bf16 a[160:163], a[192:195], a[88:91], a[160:163]\n\t"
  "s_add_u32 s79, s79, 0x1000\n\t"
  "v_exp_f32 v110, v110\n\t"
  "v_mfma_f32_16x16x32_bf16 a[164:167], a[192:195], a[92:95], a[164:167]\n\t"
  "buffer_load_dwordx4 v40, s[56:59], s76 offen offset:1024 sc1 lds\n\t"
  "v_mfma_f32_16x16x32_bf16 v[88:91], a[236:239], a[56:59], v[88:91]\n\t"
  "ds_read_b128 a[252:255], v42 offset:52224\n\t"
  "v_cvt_pk_bf16_f32 v172, v100, v101\n\t"
  "v_mfma_f32_16x16x32_bf16 v[92:95], a[236:239], a[60:63], v[92:95]\n\t"
  "s_waitcnt lgkmcnt(2)\n\t"
  "v_cvt_pk_bf16_f32 v173, v102, v103\n\t"
  "v_cvt_pk_bf16_f32 v170, v104, v105\n\t"
  "v_mfma_f32_16x16x32_bf16 v[96:99], a[240:243], a[0:3], v[112:115]\n\t"
  "ds_read_b128 a[224:227], v42 offset:53248\n\t"
  "v_cvt_pk_bf16_f32 v171, v106, v107\n\t"
  "v_mfma_f32_16x16x32_bf16 v[100:103], a[240:243], a[4:7], v[116:119]\n\t"
  "v_exp_f32 v111, v111\n\t"
  "v_mfma_f32_16x16x32_bf16 v[96:99], a[244:247], a[8:11], v[96:99]\n\t"
  "ds_read_b128 a[228:231], v42 offset:54272\n\t"
  "v_cvt_pk_bf16_f32 v174, v108, v109\n\t"
  "v_mfma_f32_16x16x32_bf16 v[100:103], a[244:247], a[12:15], v[100:103]\n\t"
  "s_waitcnt lgkmcnt(2)\n\t"
  "buffer_load_dwordx4 a[212:215], v40, s[56:59], s79 offen offset:0 sc1\n\t"
  "v_mfma_f32_16x16x32_bf16 v[96:99], a[248:251], a[16:19], v[96:99]\n\t"
  "ds_read_b128 a[232:235], v42 offset:55296\n\t"
  "v_cvt_pk_bf16_f32 v175, v110, v111\n\t"
  "v_mfma_f32_16x16x32_bf16 v[100:103], a[248:251], a[20:23], v[100:103]\n\t"
  "buffer_load_dwordx4 v40, s[56:59], s76 offen offset:2048 sc1 lds\n\t"
  "v_mfma_f32_16x16x32_bf16 v[56:59], v[52:55], v[168:171], v[56:59]\n\t"
  "ds_read_b128 a[236:239], v42 offset:56320\n\t"
  "v_max3_f32 v45, v80, v81, v82\n\t"
  "v_mfma_f32_16x16x32_bf16 v[60:63], v[52:55], v[172:175], v[60:63]\n\t"
  "v_max3_f32 v45, v45, v83, v84\n\t"
  "v_max3_f32 v45, v45, v85, v86\n\t"
  "v_mfma_f32_16x16x32_bf16 v[96:99], a[252:255], a[24:27], v[96:99]\n\t"
  "ds_read_b128 a[240:243], v42 offset:57344\n\t"
  "v_max3_f32 v45, v45, v87, v88\n\t"
  "v_mfma_f32_16x16x32_bf16 v[100:103], a[252:255], a[28:31], v[100:103]\n\t"
  "s_waitcnt lgkmcnt(3)\n\t"
  "v_max3_f32 v45, v45, v89, v90\n\t"
  "v_max3_f32 v45, v45, v91, v92\n\t"
  "v_mfma_f32_16x16x32_bf16 v[96:99], a[224:227], a[32:35], v[96:99]\n\t"
  "ds_read_b128 a[244:247], v42 offset:58368\n\t"
  "v_max3_f32 v45, v45, v93, v94\n\t"
  "v_mfma_f32_16x16x32_bf16 v[100:103], a[224:227], a[36:39], v[100:103]\n\t"
  "buffer_load_dwordx4 a[216:219], v40, s[56:59], s79 offen offset:1024 sc1\n\t"
  "v_mfma_f32_16x16x32_bf16 v[96:99], a[228:231], a[40:43], v[96:99]\n\t"
  "ds_read_b128 a[248:251], v42 offset:59392\n\t"
  "v_max_f32 v45, v45, v95\n\t"
  "v_mfma_f32_16x16x32_bf16 v[100:103], a[228:231], a[44:47], v[100:103]\n\t"
  "s_waitcnt lgkmcnt(3)\n\t"
  "buffer_load_dwordx4 v40, s[56:59], s76 offen offset:3072 sc1 lds\n\t"
  "v_mfma_f32_16x16x32_bf16 v[96:99], a[232:235], a[48:51], v[96:99]\n\t"
  "s_add_u32 s76, s76, 0x7000\n\t"
  "ds_read_b128 a[252:255], v42 offset:60416\n\t"
  "v_cmp_lt_f32 vcc, s82, v45\n\t"
  "v_mfma_f32_16x16x32_bf16 v[100:103], a[232:235], a[52:55], v[100:103]\n\t"
  "v_mfma_f32_16x16x32_bf16 v[96:99], a[236:239], a[56:59], v[96:99]\n\t"
  "s_cbranch_vccnz STUBT6_%=\n\t"
  "RETT6_%=:\n\t"
  "v_mfma_f32_16x16x32_bf16 v[100:103], a[236:239], a[60:63], v[100:103]\n\t"
  "s_waitcnt lgkmcnt(2)\n\t"
  "ds_read_b128 a[224:227], v42 offset:61440\n\t"
  "ds_read_b128 a[228:231], v42 offset:62464\n\t"
  "v_mfma_f32_16x16x32_bf16 v[104:107], a[240:243], a[0:3], v[112:115]\n\t"
  "v_exp_f32 v80, v80\n\t"
  "v_mfma_f32_16x16x32_bf16 v[108:111], a[240:243], a[4:7], v[116:119]\n\t"
  "buffer_load_dwordx4 a[220:223], v40, s[56:59], s79 offen offset:2048 sc1\n\t"
  "v_mfma_f32_16x16x32_bf16 v[104:107], a[244:247], a[8:11], v[104:107]\n\t"
  "s_add_u32 s79, s79, 0xc00\n\t"
  "ds_read_b128 a[232:235], v42 offset:63488\n\t"
  "ds_read_b128 a[236:239], v42 offset:64512\n\t"
  "v_mfma_f32_16x16x32_bf16 v[108:111], a[244:247], a[12:15], v[108:111]\n\t"
  "s_waitcnt lgkmcnt(4)\n\t"
  "v_exp_f32 v81, v81\n\t"
  "v_mfma_f32_16x16x32_bf16 v[104:107], a[248:251], a[16:19], v[104:107]\n\t"
  "s_waitcnt vmcnt(19)\n\t"
  "v_exp_f32 v82, v82\n\t"
  "v_mfma_f32_16x16x32_bf16 v[108:111], a[248:251], a[20:23], v[108:111]\n\t"
  "s_barrier\n\t"
  "v_exp_f32 v83, v83\n\t"
  "v_mfma_f32_16x16x32_bf16 v[104:107], a[252:255], a[24:27], v[104:107]\n\t"
  "ds_read_b128 a[240:243], v40 offset:0\n\t"
  "ds_read_b128 a[244:247], v40 offset:1024\n\t"
  "v_mfma_f32_16x16x32_bf16 v[108:111], a[252:255], a[28:31], v[108:111]\n\t"
  "s_waitcnt lgkmcnt(4)\n\t"
  "v_exp_f32 v84, v84\n\t"
  "v_mfma_f32_16x16x32_bf16 v[104:107], a[224:227], a[32:35], v[104:107]\n\t"
  "v_exp_f32 v85, v85\n\t"
  "v_mfma_f32_16x16x32_bf16 v[108:111], a[224:227], a[36:39], v[108:111]\n\t"
  "v_exp_f32 v86, v86\n\t"
  "v_mfma_f32_16x16x32_bf16 v[104:107], a[228:231], a[40:43], v[104:107]\n\t"
  "ds_read_b128 a[248:251], v40 offset:2048\n\t"
  "v_cvt_pk_bf16_f32 v176, v80, v81\n\t"
  "v_mfma_f32_16x16x32_bf16 v[108:111], a[228:231], a[44:47], v[108:111]\n\t"
  "s_waitcnt lgkmcnt(3)\n\t"
  "v_exp_f32 v87, v87\n\t"
  "v_mfma_f32_16x16x32_bf16 v[104:107], a[232:235], a[48:51], v[104:107]\n\t"
  "v_exp_f32 v88, v88\n\t"
  "v_mfma_f32_16x16x32_bf16 v[108:111], a[232:235], a[52:55], v[108:111]\n\t"
  "v_exp_f32 v89, v89\n\t"
  "v_mfma_f32_16x16x32_bf16 v[104:107], a[236:239], a[56:59], v[104:107]\n\t"
  "v_cvt_pk_bf16_f32 v177, v82, v83\n\t"
  "v_cvt_pk_bf16_f32 v180, v84, v85\n\t"
  "v_mfma_f32_16x16x32_bf16 v[108:111], a[236:239], a[60:63], v[108:111]\n\t"
  "s_waitcnt lgkmcnt(1)\n\t"
  "v_cvt_pk_bf16_f32 v181, v86, v87\n\t"
  "v_cvt_pk_bf16_f32 v178, v88, v89\n\t"
  "v_mfma_f32_16x16x32_bf16 v[80:83], a[240:243], a[0:3], v[112:115]\n\t"
  "ds_read_b128 a[252:255], v40 offset:3072\n\t"
  "v_exp_f32 v90, v90\n\t"
  "v_mfma_f32_16x16x32_bf16 v[84:87], a[240:243], a[4:7], v[116:119]\n\t"
  "v_exp_f32 v91, v91\n\t"
  "v_mfma_f32_16x16x32_bf16 v[80:83], a[244:247], a[8:11], v[80:83]\n\t"
  "s_add_u32 m0, s84, 0x10000\n\t"
  "ds_read_b128 a[224:227], v40 offset:4096\n\t"
  "v_cvt_pk_bf16_f32 v179, v90, v91\n\t"
  "v_mfma_f32_16x16x32_bf16 v[84:87], a[244:247], a[12:15], v[84:87]\n\t"
  "s_waitcnt lgkmcnt(1)\n\t"
  "buffer_load_dwordx4 a[88:91], v40, s[56:59], s18 offen offset:0 sc1\n\t"
  "v_mfma_f32_16x16x32_bf16 v[80:83], a[248:251], a[16:19], v[80:83]\n\t"
  "v_exp_f32 v92, v92\n\t"
  "v_mfma_f32_16x16x32_bf16 v[84:87], a[248:251], a[20:23], v[84:87]\n\t"
  "buffer_load_dwordx4 v40, s[56:59], s76 offen offset:0 sc1 lds\n\t"
  "v_mfma_f32_16x16x32_bf16 v[56:59], v[52:55], v[176:179], v[56:59]\n\t"
  "ds_read_b128 a[228:231], v40 offset:5120\n\t"
  "v_exp_f32 v93, v93\n\t"
  "v_exp_f32 v94, v94\n\t"
  "v_cvt_pk_bf16_f32 v182, v92, v93\n\t"
  "v_exp_f32 v95, v95\n\t"
  "s_nop 0\n\t"
  "v_cvt_pk_bf16_f32 v183, v94, v95\n\t"
  "s_nop 1\n\t"
  "v_mfma_f32_16x16x32_bf16 v[60:63], v[52:55], v[180:183], v[60:63]\n\t"
  "s_waitcnt vmcnt(12)\n\t"
  "v_max3_f32 v45, v96, v97, v98\n\t"
  "v_max3_f32 v45, v45, v99, v100\n\t"
  "v_mfma_f32_16x16x32_bf16 a[112:115], a[196:199], a[96:99], a[112:115]\n\t"
  "ds_read_b128 a[232:235], v40 offset:6144\n\t"
  "v_max3_f32 v45, v45, v101, v102\n\t"
  "v_mfma_f32_16x16x32_bf16 a[116:119], a[196:199], a[100:103], a[116:119]\n\t"
  "v_max3_f32 v45, v45, v103, v104\n\t"
  "v_max3_f32 v45, v45, v105, v106\n\t"
  "v_mfma_f32_16x16x32_bf16 v[80:83], a[252:255], a[24:27], v[80:83]\n\t"
  "v_max3_f32 v45, v45, v107, v108\n\t"
  "v_max3_f32 v45, v45, v109, v110\n\t"
  "v_mfma_f32_16x16x32_bf16 v[84:87], a[252:255], a[28:31], v[84:87]\n\t"
  "s_waitcnt lgkmcnt(1)\n\t"
  "buffer_load_dwordx4 a[92:95], v40, s[56:59], s18 offen offset:1024 sc1\n\t"
  "v_mfma_f32_16x16x32_bf16 v[80:83], a[224:227], a[32:35], v[80:83]\n\t"
  "s_add_u32 s18, s18, 0x800\n\t"
  "ds_read_b128 a[236:239], v40 offset:7168\n\t"
  "v_max_f32 v45, v45, v111\n\t"
  "v_mfma_f32_16x16x32_bf16 v[84:87], a[224:227], a[36:39], v[84:87]\n\t"
  "buffer_load_dwordx4 v40, s[56:59], s76 offen offset:1024 sc1 lds\n\t"
  "v_mfma_f32_16x16x32_bf16 a[120:123], a[200:203], a[96:99], a[120:123]\n\t"
  "ds_read_b128 a[240:243], v40 offset:8192\n\t"
  "v_cmp_lt_f32 vcc, s82, v45\n\t"
  "v_mfma_f32_16x16x32_bf16 a[124:127], a[200:203], a[100:103], a[124:127]\n\t"
  "v_mfma_f32_16x16x32_bf16 v[80:83], a[228:231], a[40:43], v[80:83]\n\t"
  "s_cbranch_vccnz STUBT7_%=\n\t"
  "RETT7_%=:\n\t"
  "v_mfma_f32_16x16x32_bf16 v[84:87], a[228:231], a[44:47], v[84:87]\n\t"
  "s_waitcnt lgkmcnt(1)\n\t"
  "v_exp_f32 v96, v96\n\t"
  "v_mfma_f32_16x16x32_bf16 v[80:83], a[232:235], a[48:51], v[80:83]\n\t"
  "ds_read_b128 a[244:247], v40 offset:9216\n\t"
  "ds_read_b128 a[248:251], v40 offset:10240\n\t"
  "v_mfma_f32_16x16x32_bf16 v[84:87], a[232:235], a[52:55], v[84:87]\n\t"
  "s_waitcnt vmcnt(10)\n\t"
  "buffer_load_dwordx4 a[168:171], v40, s[56:59], s79 offen offset:0 sc1\n\t"
  "v_mfma_f32_16x16x32_bf16 a[128:131], a[204:207], a[96:99], a[128:131]\n\t"
  "v_exp_f32 v97, v97\n\t"
  "v_mfma_f32_16x16x32_bf16 a[132:135], a[204:207], a[100:103], a[132:135]\n\t"
  "buffer_load_dwordx4 v40, s[56:59], s76 offen offset:2048 sc1 lds\n\t"
  "v_mfma_f32_16x16x32_bf16 v[80:83], a[236:239], a[56:59], v[80:83]\n\t"
  "v_exp_f32 v98, v98\n\t"
  "v_mfma_f32_16x16x32_bf16 v[84:87], a[236:239], a[60:63], v[84:87]\n\t"
  "s_waitcnt lgkmcnt(1)\n\t"
  "v_exp_f32 v99, v99\n\t"
  "v_mfma_f32_16x16x32_bf16 v[88:91], a[240:243], a[0:3], v[112:115]\n\t"
  "ds_read_b128 a[252:255], v40 offset:11264\n\t"
  "ds_read_b128 a[224:227], v40 offset:12288\n\t"
  "v_mfma_f32_16x16x32_bf16 v[92:95], a[240:243], a[4:7], v[116:119]\n\t"
  "v_exp_f32 v100, v100\n\t"
  "v_mfma_f32_16x16x32_bf16 a[136:139], a[208:211], a[96:99], a[136:139]\n\t"
  "v_exp_f32 v101, v101\n\t"
  "v_mfma_f32_16x16x32_bf16 a[140:143], a[208:211], a[100:103], a[140:143]\n\t"
  "buffer_load_dwordx4 a[172:175], v40, s[56:59], s79 offen offset:1024 sc1\n\t"
  "v_mfma_f32_16x16x32_bf16 v[88:91], a[244:247], a[8:11], v[88:91]\n\t"
  "v_exp_f32 v102, v102\n\t"
  "v_mfma_f32_16x16x32_bf16 v[92:95], a[244:247], a[12:15], v[92:95]\n\t"
  "s_waitcnt lgkmcnt(1)\n\t"
  "buffer_load_dwordx4 v40, s[56:59], s76 offen offset:3072 sc1 lds\n\t"
  "v_mfma_f32_16x16x32_bf16 v[88:91], a[248:251], a[16:19], v[88:91]\n\t"
  "s_add_u32 s76, s76, 0x1000\n\t"
  "ds_read_b128 a[228:231], v40 offset:13312\n\t"
  "v_cvt_pk_bf16_f32 v184, v96, v97\n\t"
  "v_mfma_f32_16x16x32_bf16 v[92:95], a[248:251], a[20:23], v[92:95]\n\t"
  "s_waitcnt vmcnt(8)\n\t"
  "v_exp_f32 v103, v103\n\t"
  "v_mfma_f32_16x16x32_bf16 a[144:147], a[212:215], a[96:99], a[144:147]\n\t"
  "ds_read_b128 a[232:235], v40 offset:14336\n\t"
  "v_cvt_pk_bf16_f32 v185, v98, v99\n\t"
  "v_mfma_f32_16x16x32_bf16 a[148:151], a[212:215], a[100:103], a[148:151]\n\t"
  "v_exp_f32 v104, v104\n\t"
  "v_mfma_f32_16x16x32_bf16 v[88:91], a[252:255], a[24:27], v[88:91]\n\t"
  "s_add_u32 m0, s84, 0x11000\n\t"
  "v_exp_f32 v105, v105\n\t"
  "v_mfma_f32_16x16x32_bf16 v[92:95], a[252:255], a[28:31], v[92:95]\n\t"
  "s_waitcnt lgkmcnt(1)\n\t"
  "buffer_load_dwordx4 a[176:179], v40, s[56:59], s79 offen offset:2048 sc1\n\t"
  "v_mfma_f32_16x16x32_bf16 v[88:91], a[224:227], a[32:35], v[88:91]\n\t"
  "ds_read_b128 a[236:239], v40 offset:15360\n\t"
  "ds_read_b128 a[240:243], v40 offset:16384\n\t"
  "v_mfma_f32_16x16x32_bf16 v[92:95], a[224:227], a[36:39], v[92:95]\n\t"
  "s_nop 0\n\t"
  "s_cbranch_execz TRAMP_%=\n\t"
  "buffer_load_dwordx4 v40, s[56:59], s76 offen offset:0 sc1 lds\n\t"
  "v_mfma_f32_16x16x32_bf16 a[152:155], a[216:219], a[96:99], a[152:155]\n\t"
  "v_exp_f32 v106, v106\n\t"
  "v_mfma_f32_16x16x32_bf16 a[156:159], a[216:219], a[100:103], a[156:159]\n\t"
  "v_exp_f32 v107, v107\n\t"
  "v_mfma_f32_16x16x32_bf16 v[88:91], a[228:231], a[40:43], v[88:91]\n\t"
  "ds_read_b128 a[244:247], v40 offset:17408\n\t"
  "ds_read_b128 a[248:251], v40 offset:18432\n\t"
  "v_mfma_f32_16x16x32_bf16 v[92:95], a[228:231], a[44:47], v[92:95]\n\t"
  "s_waitcnt lgkmcnt(3)\n\t"
  "v_exp_f32 v108, v108\n\t"
  "v_mfma_f32_16x16x32_bf16 v[88:91], a[232:235], a[48:51], v[88:91]\n\t"
  "v_exp_f32 v109, v109\n\t"
  "v_mfma_f32_16x16x32_bf16 v[92:95], a[232:235], a[52:55], v[92:95]\n\t"
  "buffer_load_dwordx4 a[180:183], v40, s[56:59], s79 offen offset:3072 sc1\n\t"
  "v_mfma_f32_16x16x32_bf16 a[160:163], a[220:223], a[96:99], a[160:163]\n\t"
  "s_add_u32 s79, s79, 0x1000\n\t"
  "v_exp_f32 v110, v110\n\t"
  "v_mfma_f32_16x16x32_bf16 a[164:167], a[220:223], a[100:103], a[164:167]\n\t"
  "buffer_load_dwordx4 v40, s[56:59], s76 offen offset:1024 sc1 lds\n\t"
  "v_mfma_f32_16x16x32_bf16 v[88:91], a[236:239], a[56:59], v[88:91]\n\t"
  "ds_read_b128 a[252:255], v40 offset:19456\n\t"
  "v_cvt_pk_bf16_f32 v188, v100, v101\n\t"
  "v_mfma_f32_16x16x32_bf16 v[92:95], a[236:239], a[60:63], v[92:95]\n\t"
  "s_waitcnt lgkmcnt(2)\n\t"
  "v_cvt_pk_bf16_f32 v189, v102, v103\n\t"
  "v_cvt_pk_bf16_f32 v186, v104, v105\n\t"
  "v_mfma_f32_16x16x32_bf16 v[96:99], a[240:243], a[0:3], v[112:115]\n\t"
  "ds_read_b128 a[224:227], v40 offset:20480\n\t"
  "v_cvt_pk_bf16_f32 v187, v106, v107\n\t"
  "v_mfma_f32_16x16x32_bf16 v[100:103], a[240:243], a[4:7], v[116:119]\n\t"
  "buffer_load_dwordx2 v[48:49], v15, s[56:59], s55 offen offset:0 sc1\n\t"
  "v_mfma_f32_16x16x32_bf16 v[96:99], a[244:247], a[8:11], v[96:99]\n\t"
  "ds_read_b128 a[228:231], v40 offset:21504\n\t"
  "v_cvt_pk_bf16_f32 v190, v108, v109\n\t"
  "v_mfma_f32_16x16x32_bf16 v[100:103], a[244:247], a[12:15], v[100:103]\n\t"
  "s_waitcnt lgkmcnt(2)\n\t"
  "buffer_load_dwordx4 a[184:187], v40, s[56:59], s79 offen offset:0 sc1\n\t"
  "v_mfma_f32_16x16x32_bf16 v[96:99], a[248:251], a[16:19], v[96:99]\n\t"
  "ds_read_b128 a[232:235], v40 offset:22528\n\t"
  "ds_read_b128 a[236:239], v40 offset:23552\n\t"
  "v_mfma_f32_16x16x32_bf16 v[100:103], a[248:251], a[20:23], v[100:103]\n\t"
  "buffer_load_dwordx4 v40, s[56:59], s76 offen offset:2048 sc1 lds\n\t"
  "v_mfma_f32_16x16x32_bf16 v[56:59], v[52:55], v[184:187], v[56:59]\n\t"
  "v_exp_f32 v111, v111\n\t"
  "s_nop 0\n\t"
  "v_cvt_pk_bf16_f32 v191, v110, v111\n\t"
  "s_nop 1\n\t"
  "v_mfma_f32_16x16x32_bf16 v[60:63], v[52:55], v[188:191], v[60:63]\n\t"
  "v_max3_f32 v45, v80, v81, v82\n\t"
  "v_max3_f32 v45, v45, v83, v84\n\t"
  "v_mfma_f32_16x16x32_bf16 v[96:99], a[252:255], a[24:27], v[96:99]\n\t"
  "ds_read_b128 a[240:243], v40 offset:24576\n\t"
  "v_max3_f32 v45, v45, v85, v86\n\t"
  "v_mfma_f32_16x16x32_bf16 v[100:103], a[252:255], a[28:31], v[100:103]\n\t"
  "s_waitcnt lgkmcnt(3)\n\t"
  "v_max3_f32 v45, v45, v87, v88\n\t"
  "v_max3_f32 v45, v45, v89, v90\n\t"
  "v_mfma_f32_16x16x32_bf16 v[96:99], a[224:227], a[32:35], v[96:99]\n\t"
  "ds_read_b128 a[244:247], v40 offset:25600\n\t"
  "v_max3_f32 v45, v45, v91, v92\n\t"
  "v_mfma_f32_16x16x32_bf16 v[100:103], a[224:227], a[36:39], v[100:103]\n\t"
  "buffer_load_dwordx4 a[188:191], v40, s[56:59], s79 offen offset:1024 sc1\n\t"
  "v_mfma_f32_16x16x32_bf16 v[96:99], a[228:231], a[40:43], v[96:99]\n\t"
  "ds_read_b128 a[248:251], v40 offset:26624\n\t"
  "v_max3_f32 v45, v45, v93, v94\n\t"
  "v_mfma_f32_16x16x32_bf16 v[100:103], a[228:231], a[44:47], v[100:103]\n\t"
  "s_waitcnt lgkmcnt(3)\n\t"
  "buffer_load_dwordx4 v40, s[56:59], s76 offen offset:3072 sc1 lds\n\t"
  "v_mfma_f32_16x16x32_bf16 v[96:99], a[232:235], a[48:51], v[96:99]\n\t"
  "s_add_u32 s76, s76, 0x7000\n\t"
  "ds_read_b128 a[252:255], v40 offset:27648\n\t"
  "v_max_f32 v45, v45, v95\n\t"
  "v_mfma_f32_16x16x32_bf16 v[100:103], a[232:235], a[52:55], v[100:103]\n\t"
  "v_cmp_lt_f32 vcc, s82, v45\n\t"
  "v_mfma_f32_16x16x32_bf16 v[96:99], a[236:239], a[56:59], v[96:99]\n\t"
  "ds_read_b128 a[224:227], v40 offset:28672\n\t"
  "v_mfma_f32_16x16x32_bf16 v[100:103], a[236:239], a[60:63], v[100:103]\n\t"
  "s_cbranch_vccnz STUBT8_%=\n\t"
  "RETT8_%=:\n\t"
  "s_waitcnt lgkmcnt(3)\n\t"
  "v_mfma_f32_16x16x32_bf16 v[104:107], a[240:243], a[0:3], v[112:115]\n\t"
  "ds_read_b128 a[228:231], v40 offset:29696\n\t"
  "ds_read_b128 a[232:235], v40 offset:30720\n\t"
  "v_mfma_f32_16x16x32_bf16 v[108:111], a[240:243], a[4:7], v[116:119]\n\t"
  "buffer_load_dwordx4 a[192:195], v40, s[56:59], s79 offen offset:2048 sc1\n\t"
  "v_mfma_f32_16x16x32_bf16 v[104:107], a[244:247], a[8:11], v[104:107]\n\t"
  "s_add_u32 s79, s79, 0xc00\n\t"
  "v_exp_f32 v80, v80\n\t"
  "v_mfma_f32_16x16x32_bf16 v[108:111], a[244:247], a[12:15], v[108:111]\n\t"
  "s_waitcnt lgkmcnt(3)\n\t"
  "v_exp_f32 v81, v81\n\t"
  "v_mfma_f32_16x16x32_bf16 v[104:107], a[248:251], a[16:19], v[104:107]\n\t"
  "s_waitcnt vmcnt(19)\n\t"
  "ds_read_b128 a[236:239], v40 offset:31744\n\t"
  "v_cvt_pk_bf16_f32 v192, v80, v81\n\t"
  "v_mfma_f32_16x16x32_bf16 v[108:111], a[248:251], a[20:23], v[108:111]\n\t"
  "s_barrier\n\t"
  "v_exp_f32 v82, v82\n\t"
  "v_mfma_f32_16x16x32_bf16 v[104:107], a[252:255], a[24:27], v[104:107]\n\t"
  "ds_read_b128 a[240:243], v40 offset:32768\n\t"
  "ds_read_b128 a[244:247], v40 offset:33792\n\t"
  "v_mfma_f32_16x16x32_bf16 v[108:111], a[252:255], a[28:31], v[108:111]\n\t"
  "s_waitcnt lgkmcnt(4)\n\t"
  "v_exp_f32 v83, v83\n\t"
  "v_mfma_f32_16x16x32_bf16 v[104:107], a[224:227], a[32:35], v[104:107]\n\t"
  "v_exp_f32 v84, v84\n\t"
  "v_mfma_f32_16x16x32_bf16 v[108:111], a[224:227], a[36:39], v[108:111]\n\t"
  "v_exp_f32 v85, v85\n\t"
  "v_mfma_f32_16x16x32_bf16 v[104:107], a[228:231], a[40:43], v[104:107]\n\t"
  "ds_read_b128 a[248:251], v40 offset:34816\n\t"
  "v_cvt_pk_bf16_f32 v193, v82, v83\n\t"
  "v_mfma_f32_16x16x32_bf16 v[108:111], a[228:231], a[44:47], v[108:111]\n\t"
  "s_waitcnt lgkmcnt(3)\n\t"
  "v_exp_f32 v86, v86\n\t"
  "v_mfma_f32_16x16x32_bf16 v[104:107], a[232:235], a[48:51], v[104:107]\n\t"
  "v_exp_f32 v87, v87\n\t"
  "v_mfma_f32_16x16x32_bf16 v[108:111], a[232:235], a[52:55], v[108:111]\n\t"
  "v_exp_f32 v88, v88\n\t"
  "v_mfma_f32_16x16x32_bf16 v[104:107], a[236:239], a[56:59], v[104:107]\n\t"
  "v_exp_f32 v89, v89\n\t"
  "v_mfma_f32_16x16x32_bf16 v[108:111], a[236:239], a[60:63], v[108:111]\n\t"
  "s_waitcnt lgkmcnt(1)\n\t"
  "v_cvt_pk_bf16_f32 v196, v84, v85\n\t"
  "v_cvt_pk_bf16_f32 v197, v86, v87\n\t"
  "v_mfma_f32_16x16x32_bf16 v[80:83], a[240:243], a[0:3], v[112:115]\n\t"
  "ds_read_b128 a[252:255], v40 offset:35840\n\t"
  "v_cvt_pk_bf16_f32 v194, v88, v89\n\t"
  "v_mfma_f32_16x16x32_bf16 v[84:87], a[240:243], a[4:7], v[116:119]\n\t"
  "v_exp_f32 v90, v90\n\t"
  "v_mfma_f32_16x16x32_bf16 v[80:83], a[244:247], a[8:11], v[80:83]\n\t"
  "s_add_u32 m0, s84, 0x18000\n\t"
  "ds_read_b128 a[224:227], v40 offset:36864\n\t"
  "v_exp_f32 v91, v91\n\t"
  "v_mfma_f32_16x16x32_bf16 v[84:87], a[244:247], a[12:15], v[84:87]\n\t"
  "s_waitcnt lgkmcnt(1)\n\t"
  "buffer_load_dwordx4 a[96:99], v40, s[56:59], s18 offen offset:0 sc1\n\t"
  "v_mfma_f32_16x16x32_bf16 v[80:83], a[248:251], a[16:19], v[80:83]\n\t"
  "v_cvt_pk_bf16_f32 v195, v90, v91\n\t"
  "v_exp_f32 v92, v92\n\t"
  "v_mfma_f32_16x16x32_bf16 v[84:87], a[248:251], a[20:23], v[84:87]\n\t"
  "buffer_load_dwordx4 v40, s[56:59], s76 offen offset:0 sc1 lds\n\t"
  "v_mfma_f32_16x16x32_bf16 v[56:59], v[52:55], v[192:195], v[56:59]\n\t"
  "ds_read_b128 a[228:231], v40 offset:37888\n\t"
  "v_exp_f32 v93, v93\n\t"
  "v_exp_f32 v94, v94\n\t"
  "v_cvt_pk_bf16_f32 v198, v92, v93\n\t"
  "v_exp_f32 v95, v95\n\t"
  "s_nop 0\n\t"
  "v_cvt_pk_bf16_f32 v199, v94, v95\n\t"
  "s_nop 1\n\t"
  "v_mfma_f32_16x16x32_bf16 v[60:63], v[52:55], v[196:199], v[60:63]\n\t"
  "s_waitcnt vmcnt(13)\n\t"
  "v_max3_f32 v45, v96, v97, v98\n\t"
  "v_max3_f32 v45, v45, v99, v100\n\t"
  "v_mfma_f32_16x16x32_bf16 a[112:115], a[168:171], a[88:91], a[112:115]\n\t"
  "ds_read_b128 a[232:235], v40 offset:38912\n\t"
  "v_max3_f32 v45, v45, v101, v102\n\t"
  "v_mfma_f32_16x16x32_bf16 a[116:119], a[168:171], a[92:95], a[116:119]\n\t"
  "v_max3_f32 v45, v45, v103, v104\n\t"
  "v_max3_f32 v45, v45, v105, v106\n\t"
  "v_mfma_f32_16x16x32_bf16 v[80:83], a[252:255], a[24:27], v[80:83]\n\t"
  "v_max3_f32 v45, v45, v107, v108\n\t"
  "v_max3_f32 v45, v45, v109, v110\n\t"
  "v_mfma_f32_16x16x32_bf16 v[84:87], a[252:255], a[28:31], v[84:87]\n\t"
  "s_waitcnt lgkmcnt(1)\n\t"
  "buffer_load_dwordx4 a[100:103], v40, s[56:59], s18 offen offset:1024 sc1\n\t"
  "v_mfma_f32_16x16x32_bf16 v[80:83], a[224:227], a[32:35], v[80:83]\n\t"
  "s_add_u32 s18, s18, 0x800\n\t"
  "ds_read_b128 a[236:239], v40 offset:39936\n\t"
  "v_max_f32 v45, v45, v111\n\t"
  "v_mfma_f32_16x16x32_bf16 v[84:87], a[224:227], a[36:39], v[84:87]\n\t"
  "buffer_load_dwordx4 v40, s[56:59], s76 offen offset:1024 sc1 lds\n\t"
  "v_mfma_f32_16x16x32_bf16 a[120:123], a[172:175], a[88:91], a[120:123]\n\t"
  "ds_read_b128 a[240:243], v40 offset:40960\n\t"
  "v_cmp_lt_f32 vcc, s82, v45\n\t"
  "v_mfma_f32_16x16x32_bf16 a[124:127], a[172:175], a[92:95], a[124:127]\n\t"
  "v_mfma_f32_16x16x32_bf16 v[80:83], a[228:231], a[40:43], v[80:83]\n\t"
  "s_cbranch_vccnz STUBT9_%=\n\t"
  "RETT9_%=:\n\t"
  "v_mfma_f32_16x16x32_bf16 v[84:87], a[228:231], a[44:47], v[84:87]\n\t"
  "s_waitcnt lgkmcnt(1)\n\t"
  "v_exp_f32 v96, v96\n\t"
  "v_mfma_f32_16x16x32_bf16 v[80:83], a[232:235], a[48:51], v[80:83]\n\t"
  "ds_read_b128 a[244:247], v40 offset:41984\n\t"
  "ds_read_b128 a[248:251], v40 offset:43008\n\t"
  "v_mfma_f32_16x16x32_bf16 v[84:87], a[232:235], a[52:55], v[84:87]\n\t"
  "s_waitcnt vmcnt(11)\n\t"
  "buffer_load_dwordx4 a[196:199], v40, s[56:59], s79 offen offset:0 sc1\n\t"
  "v_mfma_f32_16x16x32_bf16 a[128:131], a[176:179], a[88:91], a[128:131]\n\t"
  "v_exp_f32 v97, v97\n\t"
  "v_mfma_f32_16x16x32_bf16 a[132:135], a[176:179], a[92:95], a[132:135]\n\t"
  "buffer_load_dwordx4 v40, s[56:59], s76 offen offset:2048 sc1 lds\n\t"
  "v_mfma_f32_16x16x32_bf16 v[80:83], a[236:239], a[56:59], v[80:83]\n\t"
  "v_exp_f32 v98, v98\n\t"
  "v_mfma_f32_16x16x32_bf16 v[84:87], a[236:239], a[60:63], v[84:87]\n\t"
  "s_waitcnt lgkmcnt(1)\n\t"
  "v_exp_f32 v99, v99\n\t"
  "v_mfma_f32_16x16x32_bf16 v[88:91], a[240:243], a[0:3], v[112:115]\n\t"
  "ds_read_b128 a[252:255], v40 offset:44032\n\t"
  "ds_read_b128 a[224:227], v40 offset:45056\n\t"
  "v_mfma_f32_16x16x32_bf16 v[92:95], a[240:243], a[4:7], v[116:119]\n\t"
  "v_exp_f32 v100, v100\n\t"
  "v_mfma_f32_16x16x32_bf16 a[136:139], a[180:183], a[88:91], a[136:139]\n\t"
  "v_exp_f32 v101, v101\n\t"
  "v_mfma_f32_16x16x32_bf16 a[140:143], a[180:183], a[92:95], a[140:143]\n\t"
  "buffer_load_dwordx4 a[200:203], v40, s[56:59], s79 offen offset:1024 sc1\n\t"
  "v_mfma_f32_16x16x32_bf16 v[88:91], a[244:247], a[8:11], v[88:91]\n\t"
  "s_waitcnt vmcnt(12)\n\t"
  "v_exp_f32 v102, v102\n\t"
  "v_mfma_f32_16x16x32_bf16 v[92:95], a[244:247], a[12:15], v[92:95]\n\t"
  "s_waitcnt lgkmcnt(1)\n\t"
  "buffer_load_dwordx4 v40, s[56:59], s76 offen offset:3072 sc1 lds\n\t"
  "v_mfma_f32_16x16x32_bf16 v[88:91], a[248:251], a[16:19], v[88:91]\n\t"
  "ds_read_b128 a[228:231], v40 offset:46080\n\t"
  "v_cmp_ne_u64 vcc, v[48:49], v[8:9]\n\t"
  "v_mfma_f32_16x16x32_bf16 v[92:95], a[248:251], a[20:23], v[92:95]\n\t"
  "s_waitcnt vmcnt(8)\n\t"
  "v_cvt_pk_bf16_f32 v200, v96, v97\n\t"
  "v_mfma_f32_16x16x32_bf16 a[144:147], a[184:187], a[88:91], a[144:147]\n\t"
  "s_cbranch_vccnz SPINN_%=\n\t"
  "SRETN_%=:\n\t"
  "v_mfma_f32_16x16x32_bf16 a[148:151], a[184:187], a[92:95], a[148:151]\n\t"
  "s_add_u32 s76, s76, 0x1000\n\t"
  "ds_read_b128 a[232:235], v40 offset:47104\n\t"
  "ds_read_b128 a[236:239], v40 offset:48128\n\t"
  "v_mfma_f32_16x16x32_bf16 v[88:91], a[252:255], a[24:27], v[88:91]\n\t"
  "s_add_u32 m0, s84, 0x19000\n\t"
  "v_exp_f32 v103, v103\n\t"
  "v_mfma_f32_16x16x32_bf16 v[92:95], a[252:255], a[28:31], v[92:95]\n\t"
  "s_waitcnt lgkmcnt(2)\n\t"
  "buffer_load_dwordx4 a[204:207], v40, s[56:59], s79 offen offset:2048 sc1\n\t"
  "v_mfma_f32_16x16x32_bf16 v[88:91], a[224:227], a[32:35], v[88:91]\n\t"
  "v_exp_f32 v104, v104\n\t"
  "v_mfma_f32_16x16x32_bf16 v[92:95], a[224:227], a[36:39], v[92:95]\n\t"
  "buffer_load_dwordx4 v40, s[56:59], s76 offen offset:0 sc1 lds\n\t"
  "v_mfma_f32_16x16x32_bf16 a[152:155], a[188:191], a[88:91], a[152:155]\n\t"
  "ds_read_b128 a[240:243], v40 offset:49152\n\t"
  "v_cvt_pk_bf16_f32 v201, v98, v99\n\t"
  "v_mfma_f32_16x16x32_bf16 a[156:159], a[188:191], a[92:95], a[156:159]\n\t"
  "v_exp_f32 v105, v105\n\t"
  "v_mfma_f32_16x16x32_bf16 v[88:91], a[228:231], a[40:43], v[88:91]\n\t"
  "ds_read_b128 a[244:247], v40 offset:50176\n\t"
  "ds_read_b128 a[248:251], v40 offset:51200\n\t"
  "v_mfma_f32_16x16x32_bf16 v[92:95], a[228:231], a[44:47], v[92:95]\n\t"
  "s_waitcnt lgkmcnt(3)\n\t"
  "v_exp_f32 v106, v106\n\t"
  "v_mfma_f32_16x16x32_bf16 v[88:91], a[232:235], a[48:51], v[88:91]\n\t"
  "v_exp_f32 v107, v107\n\t"
  "v_mfma_f32_16x16x32_bf16 v[92:95], a[232:235], a[52:55], v[92:95]\n\t"
  "buffer_load_dwordx4 a[208:211], v40, s[56:59], s79 offen offset:3072 sc1\n\t"
  "v_mfma_f32_16x16x32_bf16 a[160:163], a[192:195], a[88:91], a[160:163]\n\t"
  "s_add_u32 s79, s79, 0x1000\n\t"
  "v_exp_f32 v108, v108\n\t"
  "v_mfma_f32_16x16x32_bf16 a[164:167], a[192:195], a[92:95], a[164:167]\n\t"
  "buffer_load_dwordx4 v40, s[56:59], s76 offen offset:1024 sc1 lds\n\t"
  "v_mfma_f32_16x16x32_bf16 v[88:91], a[236:239], a[56:59], v[88:91]\n\t"
  "ds_read_b128 a[252:255], v40 offset:52224\n\t"
  "v_cvt_pk_bf16_f32 v204, v100, v101\n\t"
  "v_mfma_f32_16x16x32_bf16 v[92:95], a[236:239], a[60:63], v[92:95]\n\t"
  "s_waitcnt lgkmcnt(2)\n\t"
  "v_cvt_pk_bf16_f32 v205, v102, v103\n\t"
  "v_cvt_pk_bf16_f32 v202, v104, v105\n\t"
  "v_mfma_f32_16x16x32_bf16 v[96:99], a[240:243], a[0:3], v[112:115]\n\t"
  "ds_read_b128 a[224:227], v40 offset:53248\n\t"
  "v_cvt_pk_bf16_f32 v203, v106, v107\n\t"
  "v_mfma_f32_16x16x32_bf16 v[100:103], a[240:243], a[4:7], v[116:119]\n\t"
  "v_exp_f32 v109, v109\n\t"
  "v_mfma_f32_16x16x32_bf16 v[96:99], a[244:247], a[8:11], v[96:99]\n\t"
  "ds_read_b128 a[228:231], v40 offset:54272\n\t"
  "v_cvt_pk_bf16_f32 v206, v108, v109\n\t"
  "v_mfma_f32_16x16x32_bf16 v[100:103], a[244:247], a[12:15], v[100:103]\n\t"
  "s_waitcnt lgkmcnt(2)\n\t"
  "buffer_load_dwordx4 a[212:215], v40, s[56:59], s79 offen offset:0 sc1\n\t"
  "v_mfma_f32_16x16x32_bf16 v[96:99], a[248:251], a[16:19], v[96:99]\n\t"
  "ds_read_b128 a[232:235], v40 offset:55296\n\t"
  "v_exp_f32 v110, v110\n\t"
  "v_mfma_f32_16x16x32_bf16 v[100:103], a[248:251], a[20:23], v[100:103]\n\t"
  "buffer_load_dwordx4 v40, s[56:59], s76 offen offset:2048 sc1 lds\n\t"
  "v_mfma_f32_16x16x32_bf16 v[56:59], v[52:55], v[200:203], v[56:59]\n\t"
  "ds_read_b128 a[236:239], v40 offset:56320\n\t"
  "v_exp_f32 v111, v111\n\t"
  "s_nop 0\n\t"
  "v_cvt_pk_bf16_f32 v207, v110, v111\n\t"
  "s_nop 1\n\t"
  "v_mfma_f32_16x16x32_bf16 v[60:63], v[52:55], v[204:207], v[60:63]\n\t"
  "v_max3_f32 v45, v80, v81, v82\n\t"
  "v_max3_f32 v45, v45, v83, v84\n\t"
  "v_mfma_f32_16x16x32_bf16 v[96:99], a[252:255], a[24:27], v[96:99]\n\t"
  "ds_read_b128 a[240:243], v40 offset:57344\n\t"
  "v_max3_f32 v45, v45, v85, v86\n\t"
  "v_mfma_f32_16x16x32_bf16 v[100:103], a[252:255], a[28:31], v[100:103]\n\t"
  "s_waitcnt lgkmcnt(3)\n\t"
  "v_max3_f32 v45, v45, v87, v88\n\t"
  "v_max3_f32 v45, v45, v89, v90\n\t"
  "v_mfma_f32_16x16x32_bf16 v[96:99], a[224:227], a[32:35], v[96:99]\n\t"
  "ds_read_b128 a[244:247], v40 offset:58368\n\t"
  "v_max3_f32 v45, v45, v91, v92\n\t"
  "v_mfma_f32_16x16x32_bf16 v[100:103], a[224:227], a[36:39], v[100:103]\n\t"
  "buffer_load_dwordx4 a[216:219], v40, s[56:59], s79 offen offset:1024 sc1\n\t"
  "v_mfma_f32_16x16x32_bf16 v[96:99], a[228:231], a[40:43], v[96:99]\n\t"
  "ds_read_b128 a[248:251], v40 offset:59392\n\t"
  "v_max3_f32 v45, v45, v93, v94\n\t"
  "v_mfma_f32_16x16x32_bf16 v[100:103], a[228:231], a[44:47], v[100:103]\n\t"
  "s_waitcnt lgkmcnt(3)\n\t"
  "buffer_load_dwordx4 v40, s[56:59], s76 offen offset:3072 sc1 lds\n\t"
  "v_mfma_f32_16x16x32_bf16 v[96:99], a[232:235], a[48:51], v[96:99]\n\t"
  "s_add_u32 s76, s76, 0x7000\n\t"
  "ds_read_b128 a[252:255], v40 offset:60416\n\t"
  "v_max_f32 v45, v45, v95\n\t"
  "v_mfma_f32_16x16x32_bf16 v[100:103], a[232:235], a[52:55], v[100:103]\n\t"
  "v_cmp_lt_f32 vcc, s82, v45\n\t"
  "v_mfma_f32_16x16x32_bf16 v[96:99], a[236:239], a[56:59], v[96:99]\n\t"
  "ds_read_b128 a[224:227], v40 offset:61440\n\t"
  "v_mfma_f32_16x16x32_bf16 v[100:103], a[236:239], a[60:63], v[100:103]\n\t"
  "s_cbranch_vccnz STUBT10_%=\n\t"
  "RETT10_%=:\n\t"
  "s_waitcnt lgkmcnt(3)\n\t"
  "v_mfma_f32_16x16x32_bf16 v[104:107], a[240:243], a[0:3], v[112:115]\n\t"
  "ds_read_b128 a[228:231], v40 offset:62464\n\t"
  "ds_read_b128 a[232:235], v40 offset:63488\n\t"
  "v_mfma_f32_16x16x32_bf16 v[108:111], a[240:243], a[4:7], v[116:119]\n\t"
  "buffer_load_dwordx4 a[220:223], v40, s[56:59], s79 offen offset:2048 sc1\n\t"
  "v_mfma_f32_16x16x32_bf16 v[104:107], a[244:247], a[8:11], v[104:107]\n\t"
  "s_add_u32 s79, s79, 0xc00\n\t"
  "v_exp_f32 v80, v80\n\t"
  "v_mfma_f32_16x16x32_bf16 v[108:111], a[244:247], a[12:15], v[108:111]\n\t"
  "s_waitcnt lgkmcnt(3)\n\t"
  "v_exp_f32 v81, v81\n\t"
  "v_mfma_f32_16x16x32_bf16 v[104:107], a[248:251], a[16:19], v[104:107]\n\t"
  "s_waitcnt vmcnt(18)\n\t"
  "ds_read_b128 a[236:239], v40 offset:64512\n\t"
  "v_cvt_pk_bf16_f32 v208, v80, v81\n\t"
  "v_mfma_f32_16x16x32_bf16 v[108:111], a[248:251], a[20:23], v[108:111]\n\t"
  "s_barrier\n\t"
  "v_exp_f32 v82, v82\n\t"
  "v_mfma_f32_16x16x32_bf16 v[104:107], a[252:255], a[24:27], v[104:107]\n\t"
  "ds_read_b128 a[240:243], v42 offset:0\n\t"
  "ds_read_b128 a[244:247], v42 offset:1024\n\t"
  "v_mfma_f32_16x16x32_bf16 v[108:111], a[252:255], a[28:31], v[108:111]\n\t"
  "s_waitcnt lgkmcnt(4)\n\t"
  "v_exp_f32 v83, v83\n\t"
  "v_mfma_f32_16x16x32_bf16 v[104:107], a[224:227], a[32:35], v[104:107]\n\t"
  "v_exp_f32 v84, v84\n\t"
  "v_mfma_f32_16x16x32_bf16 v[108:111], a[224:227], a[36:39], v[108:111]\n\t"
  "v_exp_f32 v85, v85\n\t"
  "v_mfma_f32_16x16x32_bf16 v[104:107], a[228:231], a[40:43], v[104:107]\n\t"
  "ds_read_b128 a[248:251], v42 offset:2048\n\t"
  "v_cvt_pk_bf16_f32 v209, v82, v83\n\t"
  "v_mfma_f32_16x16x32_bf16 v[108:111], a[228:231], a[44:47], v[108:111]\n\t"
  "s_waitcnt lgkmcnt(3)\n\t"
  "v_exp_f32 v86, v86\n\t"
  "v_mfma_f32_16x16x32_bf16 v[104:107], a[232:235], a[48:51], v[104:107]\n\t"
  "v_exp_f32 v87, v87\n\t"
  "v_mfma_f32_16x16x32_bf16 v[108:111], a[232:235], a[52:55], v[108:111]\n\t"
  "v_exp_f32 v88, v88\n\t"
  "v_mfma_f32_16x16x32_bf16 v[104:107], a[236:239], a[56:59], v[104:107]\n\t"
  "v_exp_f32 v89, v89\n\t"
  "v_mfma_f32_16x16x32_bf16 v[108:111], a[236:239], a[60:63], v[108:111]\n\t"
  "s_waitcnt lgkmcnt(1)\n\t"
  "v_cvt_pk_bf16_f32 v212, v84, v85\n\t"
  "v_cvt_pk_bf16_f32 v213, v86, v87\n\t"
  "v_mfma_f32_16x16x32_bf16 v[80:83], a[240:243], a[0:3], v[112:115]\n\t"
  "ds_read_b128 a[252:255], v42 offset:3072\n\t"
  "v_cvt_pk_bf16_f32 v210, v88, v89\n\t"
  "v_mfma_f32_16x16x32_bf16 v[84:87], a[240:243], a[4:7], v[116:119]\n\t"
  "v_exp_f32 v90, v90\n\t"
  "v_mfma_f32_16x16x32_bf16 v[80:83], a[244:247], a[8:11], v[80:83]\n\t"
  "s_add_u32 s29, s72, 0\n\t"
  "ds_read_b128 a[224:227], v42 offset:4096\n\t"
  "v_exp_f32 v91, v91\n\t"
  "v_mfma_f32_16x16x32_bf16 v[84:87], a[244:247], a[12:15], v[84:87]\n\t"
  "s_waitcnt lgkmcnt(1)\n\t"
  "buffer_load_dwordx4 a[88:91], v40, s[56:59], s18 offen offset:0 sc1\n\t"
  "v_mfma_f32_16x16x32_bf16 v[80:83], a[248:251], a[16:19], v[80:83]\n\t"
  "s_and_b32 s29, s29, 3\n\t"
  "s_mul_i32 s29, s29, 0x1c000\n\t"
  "s_add_u32 s29, s29, s77\n\t"
  "s_add_u32 m0, s87, 0\n\t"
  "v_mfma_f32_16x16x32_bf16 v[84:87], a[248:251], a[20:23], v[84:87]\n\t"
  "buffer_load_dwordx4 v40, s[56:59], s29 offen offset:0 sc1 lds\n\t"
  "v_cvt_pk_bf16_f32 v211, v90, v91\n\t"
  "s_nop 1\n\t"
  "v_mfma_f32_16x16x32_bf16 v[56:59], v[52:55], v[208:211], v[56:59]\n\t"
  "ds_read_b128 a[228:231], v42 offset:5120\n\t"
  "v_exp_f32 v92, v92\n\t"
  "v_exp_f32 v93, v93\n\t"
  "v_exp_f32 v94, v94\n\t"
  "v_cvt_pk_bf16_f32 v214, v92, v93\n\t"
  "v_exp_f32 v95, v95\n\t"
  "s_nop 0\n\t"
  "v_cvt_pk_bf16_f32 v215, v94, v95\n\t"
  "s_nop 1\n\t"
  "v_mfma_f32_16x16x32_bf16 v[60:63], v[52:55], v[212:215], v[60:63]\n\t"
  "s_waitcnt vmcnt(12)\n\t"
  "v_max3_f32 v45, v96, v97, v98\n\t"
  "v_max3_f32 v45, v45, v99, v100\n\t"
  "v_mfma_f32_16x16x32_bf16 a[112:115], a[196:199], a[96:99], a[112:115]\n\t"
  "ds_read_b128 a[232:235], v42 offset:6144\n\t"
  "v_max3_f32 v45, v45, v101, v102\n\t"
  "v_mfma_f32_16x16x32_bf16 a[116:119], a[196:199], a[100:103], a[116:119]\n\t"
  "v_max3_f32 v45, v45, v103, v104\n\t"
  "v_max3_f32 v45, v45, v105, v106\n\t"
  "v_mfma_f32_16x16x32_bf16 v[80:83], a[252:255], a[24:27], v[80:83]\n\t"
  "v_max3_f32 v45, v45, v107, v108\n\t"
  "v_max3_f32 v45, v45, v109, v110\n\t"
  "v_mfma_f32_16x16x32_bf16 v[84:87], a[252:255], a[28:31], v[84:87]\n\t"
  "s_waitcnt lgkmcnt(1)\n\t"
  "buffer_load_dwordx4 a[92:95], v40, s[56:59], s18 offen offset:1024 sc1\n\t"
  "v_mfma_f32_16x16x32_bf16 v[80:83], a[224:227], a[32:35], v[80:83]\n\t"
  "s_add_u32 s18, s18, 0x800\n\t"
  "ds_read_b128 a[236:239], v42 offset:7168\n\t"
  "v_max_f32 v45, v45, v111\n\t"
  "v_mfma_f32_16x16x32_bf16 v[84:87], a[224:227], a[36:39], v[84:87]\n\t"
  "buffer_load_dwordx4 v40, s[56:59], s29 offen offset:1024 sc1 lds\n\t"
  "v_mfma_f32_16x16x32_bf16 a[120:123], a[200:203], a[96:99], a[120:123]\n\t"
  "ds_read_b128 a[240:243], v42 offset:8192\n\t"
  "v_cmp_lt_f32 vcc, s82, v45\n\t"
  "v_mfma_f32_16x16x32_bf16 a[124:127], a[200:203], a[100:103], a[124:127]\n\t"
  "v_mfma_f32_16x16x32_bf16 v[80:83], a[228:231], a[40:43], v[80:83]\n\t"
  "s_cbranch_vccnz STUBT11_%=\n\t"
  "RETT11_%=:\n\t"
  "v_mfma_f32_16x16x32_bf16 v[84:87], a[228:231], a[44:47], v[84:87]\n\t"
  "s_waitcnt lgkmcnt(1)\n\t"
  "v_exp_f32 v96, v96\n\t"
  "v_mfma_f32_16x16x32_bf16 v[80:83], a[232:235], a[48:51], v[80:83]\n\t"
  "ds_read_b128 a[244:247], v42 offset:9216\n\t"
  "ds_read_b128 a[248:251], v42 offset:10240\n\t"
  "v_mfma_f32_16x16x32_bf16 v[84:87], a[232:235], a[52:55], v[84:87]\n\t"
  "s_waitcnt vmcnt(10)\n\t"
  "buffer_load_dwordx4 a[168:171], v40, s[56:59], s79 offen offset:0 sc1\n\t"
  "v_mfma_f32_16x16x32_bf16 a[128:131], a[204:207], a[96:99], a[128:131]\n\t"
  "v_exp_f32 v97, v97\n\t"
  "v_mfma_f32_16x16x32_bf16 a[132:135], a[204:207], a[100:103], a[132:135]\n\t"
  "v_exp_f32 v98, v98\n\t"
  "v_mfma_f32_16x16x32_bf16 v[80:83], a[236:239], a[56:59], v[80:83]\n\t"
  "v_exp_f32 v99, v99\n\t"
  "v_mfma_f32_16x16x32_bf16 v[84:87], a[236:239], a[60:63], v[84:87]\n\t"
  "s_waitcnt lgkmcnt(1)\n\t"
  "buffer_load_dwordx4 v40, s[56:59], s29 offen offset:2048 sc1 lds\n\t"
  "v_mfma_f32_16x16x32_bf16 v[88:91], a[240:243], a[0:3], v[112:115]\n\t"
  "ds_read_b128 a[252:255], v42 offset:11264\n\t"
  "v_cvt_pk_bf16_f32 v216, v96, v97\n\t"
  "v_mfma_f32_16x16x32_bf16 v[92:95], a[240:243], a[4:7], v[116:119]\n\t"
  "v_exp_f32 v100, v100\n\t"
  "v_mfma_f32_16x16x32_bf16 a[136:139], a[208:211], a[96:99], a[136:139]\n\t"
  "ds_read_b128 a[224:227], v42 offset:12288\n\t"
  "v_cvt_pk_bf16_f32 v217, v98, v99\n\t"
  "v_mfma_f32_16x16x32_bf16 a[140:143], a[208:211], a[100:103], a[140:143]\n\t"
  "buffer_load_dwordx4 a[172:175], v40, s[56:59], s79 offen offset:1024 sc1\n\t"
  "v_mfma_f32_16x16x32_bf16 v[88:91], a[244:247], a[8:11], v[88:91]\n\t"
  "v_exp_f32 v101, v101\n\t"
  "v_mfma_f32_16x16x32_bf16 v[92:95], a[244:247], a[12:15], v[92:95]\n\t"
  "s_waitcnt lgkmcnt(1)\n\t"
  "v_exp_f32 v102, v102\n\t"
  "v_mfma_f32_16x16x32_bf16 v[88:91], a[248:251], a[16:19], v[88:91]\n\t"
  "ds_read_b128 a[228:231], v42 offset:13312\n\t"
  "ds_read_b128 a[232:235], v42 offset:14336\n\t"
  "v_mfma_f32_16x16x32_bf16 v[92:95], a[248:251], a[20:23], v[92:95]\n\t"
  "s_waitcnt vmcnt(7)\n\t"
  "buffer_load_dwordx4 v40, s[56:59], s29 offen offset:3072 sc1 lds\n\t"
  "v_mfma_f32_16x16x32_bf16 a[144:147], a[212:215], a[96:99], a[144:147]\n\t"
  "s_add_u32 s29, s29, 0x1000\n\t"
  "v_exp_f32 v103, v103\n\t"
  "v_mfma_f32_16x16x32_bf16 a[148:151], a[212:215], a[100:103], a[148:151]\n\t"
  "v_exp_f32 v104, v104\n\t"
  "v_mfma_f32_16x16x32_bf16 v[88:91], a[252:255], a[24:27], v[88:91]\n\t"
  "v_exp_f32 v105, v105\n\t"
  "v_mfma_f32_16x16x32_bf16 v[92:95], a[252:255], a[28:31], v[92:95]\n\t"
  "s_waitcnt lgkmcnt(1)\n\t"
  "buffer_load_dwordx4 a[176:179], v40, s[56:59], s79 offen offset:2048 sc1\n\t"
  "v_mfma_f32_16x16x32_bf16 v[88:91], a[224:227], a[32:35], v[88:91]\n\t"
  "ds_read_b128 a[236:239], v42 offset:15360\n\t"
  "ds_read_b128 a[240:243], v42 offset:16384\n\t"
  "v_mfma_f32_16x16x32_bf16 v[92:95], a[224:227], a[36:39], v[92:95]\n\t"
  "v_exp_f32 v106, v106\n\t"
  "v_mfma_f32_16x16x32_bf16 a[152:155], a[216:219], a[96:99], a[152:155]\n\t"
  "s_add_u32 m0, s87, 0x1000\n\t"
  "v_exp_f32 v107, v107\n\t"
  "v_mfma_f32_16x16x32_bf16 a[156:159], a[216:219], a[100:103], a[156:159]\n\t"
  "v_exp_f32 v108, v108\n\t"
  "v_mfma_f32_16x16x32_bf16 v[88:91], a[228:231], a[40:43], v[88:91]\n\t"
  "ds_read_b128 a[244:247], v42 offset:17408\n\t"
  "ds_read_b128 a[248:251], v42 offset:18432\n\t"
  "v_mfma_f32_16x16x32_bf16 v[92:95], a[228:231], a[44:47], v[92:95]\n\t"
  "s_waitcnt lgkmcnt(3)\n\t"
  "buffer_load_dwordx4 v40, s[56:59], s29 offen offset:0 sc1 lds\n\t"
  "v_mfma_f32_16x16x32_bf16 v[88:91], a[232:235], a[48:51], v[88:91]\n\t"
  "v_exp_f32 v109, v109\n\t"
  "v_mfma_f32_16x16x32_bf16 v[92:95], a[232:235], a[52:55], v[92:95]\n\t"
  "buffer_load_dwordx4 a[180:183], v40, s[56:59], s79 offen offset:3072 sc1\n\t"
  "v_mfma_f32_16x16x32_bf16 a[160:163], a[220:223], a[96:99], a[160:163]\n\t"
  "s_add_u32 s79, s79, 0x1000\n\t"
  "v_exp_f32 v110, v110\n\t"
  "v_mfma_f32_16x16x32_bf16 a[164:167], a[220:223], a[100:103], a[164:167]\n\t"
  "v_exp_f32 v111, v111\n\t"
  "v_mfma_f32_16x16x32_bf16 v[88:91], a[236:239], a[56:59], v[88:91]\n\t"
  "ds_read_b128 a[252:255], v42 offset:19456\n\t"
  "v_cvt_pk_bf16_f32 v220, v100, v101\n\t"
  "v_mfma_f32_16x16x32_bf16 v[92:95], a[236:239], a[60:63], v[92:95]\n\t"
  "s_waitcnt lgkmcnt(2)\n\t"
  "v_cvt_pk_bf16_f32 v221, v102, v103\n\t"
  "v_cvt_pk_bf16_f32 v218, v104, v105\n\t"
  "v_mfma_f32_16x16x32_bf16 v[96:99], a[240:243], a[0:3], v[112:115]\n\t"
  "ds_read_b128 a[224:227], v42 offset:20480\n\t"
  "v_cvt_pk_bf16_f32 v219, v106, v107\n\t"
  "v_mfma_f32_16x16x32_bf16 v[100:103], a[240:243], a[4:7], v[116:119]\n\t"
  "buffer_load_dwordx4 v40, s[56:59], s29 offen offset:1024 sc1 lds\n\t"
  "v_mfma_f32_16x16x32_bf16 v[96:99], a[244:247], a[8:11], v[96:99]\n\t"
  "ds_read_b128 a[228:231], v42 offset:21504\n\t"
  "v_cvt_pk_bf16_f32 v222, v108, v109\n\t"
  "v_mfma_f32_16x16x32_bf16 v[100:103], a[244:247], a[12:15], v[100:103]\n\t"
  "s_waitcnt lgkmcnt(2)\n\t"
  "buffer_load_dwordx4 a[184:187], v40, s[56:59], s79 offen offset:0 sc1\n\t"
  "v_mfma_f32_16x16x32_bf16 v[96:99], a[248:251], a[16:19], v[96:99]\n\t"
  "ds_read_b128 a[232:235], v42 offset:22528\n\t"
  "v_cvt_pk_bf16_f32 v223, v110, v111\n\t"
  "v_mfma_f32_16x16x32_bf16 v[100:103], a[248:251], a[20:23], v[100:103]\n\t"
  "v_max3_f32 v45, v80, v81, v82\n\t"
  "v_max3_f32 v45, v45, v83, v84\n\t"
  "v_mfma_f32_16x16x32_bf16 v[56:59], v[52:55], v[216:219], v[56:59]\n\t"
  "ds_read_b128 a[236:239], v42 offset:23552\n\t"
  "v_max3_f32 v45, v45, v85, v86\n\t"
  "v_mfma_f32_16x16x32_bf16 v[60:63], v[52:55], v[220:223], v[60:63]\n\t"
  "v_max3_f32 v45, v45, v87, v88\n\t"
  "v_max3_f32 v45, v45, v89, v90\n\t"
  "v_mfma_f32_16x16x32_bf16 v[96:99], a[252:255], a[24:27], v[96:99]\n\t"
  "ds_read_b128 a[240:243], v42 offset:24576\n\t"
  "v_max3_f32 v45, v45, v91, v92\n\t"
  "v_mfma_f32_16x16x32_bf16 v[100:103], a[252:255], a[28:31], v[100:103]\n\t"
  "s_waitcnt lgkmcnt(3)\n\t"
  "v_max3_f32 v45, v45, v93, v94\n\t"
  "v_max_f32 v45, v45, v95\n\t"
  "v_mfma_f32_16x16x32_bf16 v[96:99], a[224:227], a[32:35], v[96:99]\n\t"
  "ds_read_b128 a[244:247], v42 offset:25600\n\t"
  "v_cmp_lt_f32 vcc, s82, v45\n\t"
  "v_mfma_f32_16x16x32_bf16 v[100:103], a[224:227], a[36:39], v[100:103]\n\t"
  "buffer_load_dwordx4 a[188:191], v40, s[56:59], s79 offen offset:1024 sc1\n\t"
  "v_mfma_f32_16x16x32_bf16 v[96:99], a[228:231], a[40:43], v[96:99]\n\t"
  "s_cbranch_vccnz STUBT12_%=\n\t"
  "RETT12_%=:\n\t"
  "v_mfma_f32_16x16x32_bf16 v[100:103], a[228:231], a[44:47], v[100:103]\n\t"
  "s_waitcnt lgkmcnt(2)\n\t"
  "buffer_load_dwordx4 v40, s[56:59], s29 offen offset:2048 sc1 lds\n\t"
  "v_mfma_f32_16x16x32_bf16 v[96:99], a[232:235], a[48:51], v[96:99]\n\t"
  "s_add_u32 s29, s29, 0x6000\n\t"
  "ds_read_b128 a[248:251], v42 offset:26624\n\t"
  "v_mfma_f32_16x16x32_bf16 v[100:103], a[232:235], a[52:55], v[100:103]\n\t"
  "ds_read_b128 a[252:255], v42 offset:27648\n\t"
  "ds_read_b128 a[224:227], v42 offset:28672\n\t"
  "v_mfma_f32_16x16x32_bf16 v[96:99], a[236:239], a[56:59], v[96:99]\n\t"
  "v_exp_f32 v80, v80\n\t"
  "v_mfma_f32_16x16x32_bf16 v[100:103], a[236:239], a[60:63], v[100:103]\n\t"
  "s_waitcnt lgkmcnt(3)\n\t"
  "v_exp_f32 v81, v81\n\t"
  "v_mfma_f32_16x16x32_bf16 v[104:107], a[240:243], a[0:3], v[112:115]\n\t"
  "ds_read_b128 a[228:231], v42 offset:29696\n\t"
  "ds_read_b128 a[232:235], v42 offset:30720\n\t"
  "v_mfma_f32_16x16x32_bf16 v[108:111], a[240:243], a[4:7], v[116:119]\n\t"
  "buffer_load_dwordx4 a[192:195], v40, s[56:59], s79 offen offset:2048 sc1\n\t"
  "v_mfma_f32_16x16x32_bf16 v[104:107], a[244:247], a[8:11], v[104:107]\n\t"
  "s_add_u32 s79, s79, 0xc00\n\t"
  "v_exp_f32 v82, v82\n\t"
  "v_mfma_f32_16x16x32_bf16 v[108:111], a[244:247], a[12:15], v[108:111]\n\t"
  "s_waitcnt lgkmcnt(3)\n\t"
  "v_exp_f32 v83, v83\n\t"
  "v_mfma_f32_16x16x32_bf16 v[104:107], a[248:251], a[16:19], v[104:107]\n\t"
  "s_waitcnt vmcnt(17)\n\t"
  "ds_read_b128 a[236:239], v42 offset:31744\n\t"
  "v_cvt_pk_bf16_f32 v224, v80, v81\n\t"
  "v_mfma_f32_16x16x32_bf16 v[108:111], a[248:251], a[20:23], v[108:111]\n\t"
  "s_barrier\n\t"
  "v_exp_f32 v84, v84\n\t"
  "v_mfma_f32_16x16x32_bf16 v[104:107], a[252:255], a[24:27], v[104:107]\n\t"
  "ds_read_b128 a[240:243], v42 offset:32768\n\t"
  "ds_read_b128 a[244:247], v42 offset:33792\n\t"
  "v_mfma_f32_16x16x32_bf16 v[108:111], a[252:255], a[28:31], v[108:111]\n\t"
  "s_waitcnt lgkmcnt(4)\n\t"
  "v_exp_f32 v85, v85\n\t"
  "v_mfma_f32_16x16x32_bf16 v[104:107], a[224:227], a[32:35], v[104:107]\n\t"
  "v_exp_f32 v86, v86\n\t"
  "v_mfma_f32_16x16x32_bf16 v[108:111], a[224:227], a[36:39], v[108:111]\n\t"
  "v_exp_f32 v87, v87\n\t"
  "v_mfma_f32_16x16x32_bf16 v[104:107], a[228:231], a[40:43], v[104:107]\n\t"
  "ds_read_b128 a[248:251], v42 offset:34816\n\t"
  "v_cvt_pk_bf16_f32 v225, v82, v83\n\t"
  "v_mfma_f32_16x16x32_bf16 v[108:111], a[228:231], a[44:47], v[108:111]\n\t"
  "s_waitcnt lgkmcnt(3)\n\t"
  "v_exp_f32 v88, v88\n\t"
  "v_mfma_f32_16x16x32_bf16 v[104:107], a[232:235], a[48:51], v[104:107]\n\t"
  "v_exp_f32 v89, v89\n\t"
  "v_mfma_f32_16x16x32_bf16 v[108:111], a[232:235], a[52:55], v[108:111]\n\t"
  "v_exp_f32 v90, v90\n\t"
  "v_mfma_f32_16x16x32_bf16 v[104:107], a[236:239], a[56:59], v[104:107]\n\t"
  "v_exp_f32 v91, v91\n\t"
  "v_mfma_f32_16x16x32_bf16 v[108:111], a[236:239], a[60:63], v[108:111]\n\t"
  "s_waitcnt lgkmcnt(1)\n\t"
  "v_cvt_pk_bf16_f32 v228, v84, v85\n\t"
  "v_cvt_pk_bf16_f32 v229, v86, v87\n\t"
  "v_mfma_f32_16x16x32_bf16 v[80:83], a[240:243], a[0:3], v[112:115]\n\t"
  "s_add_u32 m0, s87, 0x8000\n\t"
  "s_cbranch_execz TRAMP_%=\n\t"
  "ds_read_b128 a[252:255], v42 offset:35840\n\t"
  "v_cvt_pk_bf16_f32 v226, v88, v89\n\t"
  "v_mfma_f32_16x16x32_bf16 v[84:87], a[240:243], a[4:7], v[116:119]\n\t"
  "v_exp_f32 v92, v92\n\t"
  "v_mfma_f32_16x16x32_bf16 v[80:83], a[244:247], a[8:11], v[80:83]\n\t"
  "ds_read_b128 a[224:227], v42 offset:36864\n\t"
  "v_cvt_pk_bf16_f32 v227, v90, v91\n\t"
  "v_mfma_f32_16x16x32_bf16 v[84:87], a[244:247], a[12:15], v[84:87]\n\t"
  "s_waitcnt lgkmcnt(1)\n\t"
  "buffer_load_dwordx4 v40, s[56:59], s29 offen offset:0 sc1 lds\n\t"
  "v_mfma_f32_16x16x32_bf16 v[80:83], a[248:251], a[16:19], v[80:83]\n\t"
  "v_exp_f32 v93, v93\n\t"
  "v_mfma_f32_16x16x32_bf16 v[84:87], a[248:251], a[20:23], v[84:87]\n\t"
  "v_cvt_pk_bf16_f32 v230, v92, v93\n\t"
  "v_exp_f32 v94, v94\n\t"
  "v_mfma_f32_16x16x32_bf16 v[56:59], v[52:55], v[224:227], v[56:59]\n\t"
  "ds_read_b128 a[228:231], v42 offset:37888\n\t"
  "v_exp_f32 v95, v95\n\t"
  "s_nop 0\n\t"
  "v_cvt_pk_bf16_f32 v231, v94, v95\n\t"
  "s_nop 1\n\t"
  "v_mfma_f32_16x16x32_bf16 v[60:63], v[52:55], v[228:231], v[60:63]\n\t"
  "s_waitcnt vmcnt(10)\n\t"
  "buffer_load_dwordx4 a[96:99], v40, s[56:59], s18 offen offset:0 sc1\n\t"
  "v_mfma_f32_16x16x32_bf16 a[112:115], a[168:171], a[88:91], a[112:115]\n\t"
  "ds_read_b128 a[232:235], v42 offset:38912\n\t"
  "v_max3_f32 v45, v96, v97, v98\n\t"
  "v_mfma_f32_16x16x32_bf16 a[116:119], a[168:171], a[92:95], a[116:119]\n\t"
  "v_max3_f32 v45, v45, v99, v100\n\t"
  "v_max3_f32 v45, v45, v101, v102\n\t"
  "v_mfma_f32_16x16x32_bf16 v[80:83], a[252:255], a[24:27], v[80:83]\n\t"
  "v_max3_f32 v45, v45, v103, v104\n\t"
  "v_max3_f32 v45, v45, v105, v106\n\t"
  "v_mfma_f32_16x16x32_bf16 v[84:87], a[252:255], a[28:31], v[84:87]\n\t"
  "s_waitcnt lgkmcnt(1)\n\t"
  "buffer_load_dwordx4 v40, s[56:59], s29 offen offset:1024 sc1 lds\n\t"
  "v_mfma_f32_16x16x32_bf16 v[80:83], a[224:227], a[32:35], v[80:83]\n\t"
  "ds_read_b128 a[236:239], v42 offset:39936\n\t"
  "v_max3_f32 v45, v45, v107, v108\n\t"
  "v_mfma_f32_16x16x32_bf16 v[84:87], a[224:227], a[36:39], v[84:87]\n\t"
  "v_max3_f32 v45, v45, v109, v110\n\t"
  "v_max_f32 v45, v45, v111\n\t"
  "v_mfma_f32_16x16x32_bf16 a[120:123], a[172:175], a[88:91], a[120:123]\n\t"
  "ds_read_b128 a[240:243], v42 offset:40960\n\t"
  "v_cmp_lt_f32 vcc, s82, v45\n\t"
  "v_mfma_f32_16x16x32_bf16 a[124:127], a[172:175], a[92:95], a[124:127]\n\t"
  "buffer_load_dwordx4 a[100:103], v40, s[56:59], s18 offen offset:1024 sc1\n\t"
  "v_mfma_f32_16x16x32_bf16 v[80:83], a[228:231], a[40:43], v[80:83]\n\t"
  "s_cbranch_vccnz STUBT13_%=\n\t"
  "RETT13_%=:\n\t"
  "v_mfma_f32_16x16x32_bf16 v[84:87], a[228:231], a[44:47], v[84:87]\n\t"
  "s_waitcnt lgkmcnt(1)\n\t"
  "v_exp_f32 v96, v96\n\t"
  "v_mfma_f32_16x16x32_bf16 v[80:83], a[232:235], a[48:51], v[80:83]\n\t"
  "s_add_u32 s18, s18, 0x800\n\t"
  "ds_read_b128 a[244:247], v42 offset:41984\n\t"
  "ds_read_b128 a[248:251], v42 offset:43008\n\t"
  "v_mfma_f32_16x16x32_bf16 v[84:87], a[232:235], a[52:55], v[84:87]\n\t"
  "s_waitcnt vmcnt(9)\n\t"
  "buffer_load_dwordx4 v40, s[56:59], s29 offen offset:2048 sc1 lds\n\t"
  "v_mfma_f32_16x16x32_bf16 a[128:131], a[176:179], a[88:91], a[128:131]\n\t"
  "v_exp_f32 v97, v97\n\t"
  "v_mfma_f32_16x16x32_bf16 a[132:135], a[176:179], a[92:95], a[132:135]\n\t"
  "v_exp_f32 v98, v98\n\t"
  "v_mfma_f32_16x16x32_bf16 v[80:83], a[236:239], a[56:59], v[80:83]\n\t"
  "v_exp_f32 v99, v99\n\t"
  "v_mfma_f32_16x16x32_bf16 v[84:87], a[236:239], a[60:63], v[84:87]\n\t"
  "s_waitcnt lgkmcnt(1)\n\t"
  "buffer_load_dwordx4 a[196:199], v40, s[56:59], s79 offen offset:0 sc1\n\t"
  "v_mfma_f32_16x16x32_bf16 v[88:91], a[240:243], a[0:3], v[112:115]\n\t"
  "ds_read_b128 a[252:255], v42 offset:44032\n\t"
  "ds_read_b128 a[224:227], v42 offset:45056\n\t"
  "v_mfma_f32_16x16x32_bf16 v[92:95], a[240:243], a[4:7], v[116:119]\n\t"
  "v_exp_f32 v100, v100\n\t"
  "v_mfma_f32_16x16x32_bf16 a[136:139], a[180:183], a[88:91], a[136:139]\n\t"
  "v_exp_f32 v101, v101\n\t"
  "v_mfma_f32_16x16x32_bf16 a[140:143], a[180:183], a[92:95], a[140:143]\n\t"
  "v_exp_f32 v102, v102\n\t"
  "v_mfma_f32_16x16x32_bf16 v[88:91], a[244:247], a[8:11], v[88:91]\n\t"
  "v_exp_f32 v103, v103\n\t"
  "v_mfma_f32_16x16x32_bf16 v[92:95], a[244:247], a[12:15], v[92:95]\n\t"
  "s_waitcnt lgkmcnt(1)\n\t"
  "buffer_load_dwordx4 v40, s[56:59], s29 offen offset:3072 sc1 lds\n\t"
  "v_mfma_f32_16x16x32_bf16 v[88:91], a[248:251], a[16:19], v[88:91]\n\t"
  "s_add_u32 s29, s29, 0x1000\n\t"
  "ds_read_b128 a[228:231], v42 offset:46080\n\t"
  "v_cvt_pk_bf16_f32 v232, v96, v97\n\t"
  "v_mfma_f32_16x16x32_bf16 v[92:95], a[248:251], a[20:23], v[92:95]\n\t"
  "s_waitcnt vmcnt(7)\n\t"
  "buffer_load_dwordx4 a[200:203], v40, s[56:59], s79 offen offset:1024 sc1\n\t"
  "v_mfma_f32_16x16x32_bf16 a[144:147], a[184:187], a[88:91], a[144:147]\n\t"
  "ds_read_b128 a[232:235], v42 offset:47104\n\t"
  "v_cvt_pk_bf16_f32 v233, v98, v99\n\t"
  "v_mfma_f32_16x16x32_bf16 a[148:151], a[184:187], a[92:95], a[148:151]\n\t"
  "v_exp_f32 v104, v104\n\t"
  "v_mfma_f32_16x16x32_bf16 v[88:91], a[252:255], a[24:27], v[88:91]\n\t"
  "s_add_u32 m0, s87, 0x9000\n\t"
  "v_exp_f32 v105, v105\n\t"
  "v_mfma_f32_16x16x32_bf16 v[92:95], a[252:255], a[28:31], v[92:95]\n\t"
  "s_waitcnt lgkmcnt(1)\n\t"
  "v_exp_f32 v106, v106\n\t"
  "v_mfma_f32_16x16x32_bf16 v[88:91], a[224:227], a[32:35], v[88:91]\n\t"
  "ds_read_b128 a[236:239], v42 offset:48128\n\t"
  "ds_read_b128 a[240:243], v42 offset:49152\n\t"
  "v_mfma_f32_16x16x32_bf16 v[92:95], a[224:227], a[36:39], v[92:95]\n\t"
  "buffer_load_dwordx4 v40, s[56:59], s29 offen offset:0 sc1 lds\n\t"
  "v_mfma_f32_16x16x32_bf16 a[152:155], a[188:191], a[88:91], a[152:155]\n\t"
  "v_exp_f32 v107, v107\n\t"
  "v_mfma_f32_16x16x32_bf16 a[156:159], a[188:191], a[92:95], a[156:159]\n\t"
  "buffer_load_dwordx4 a[204:207], v40, s[56:59], s79 offen offset:2048 sc1\n\t"
  "v_mfma_f32_16x16x32_bf16 v[88:91], a[228:231], a[40:43], v[88:91]\n\t"
  "ds_read_b128 a[244:247], v42 offset:50176\n\t"
  "ds_read_b128 a[248:251], v42 offset:51200\n\t"
  "v_mfma_f32_16x16x32_bf16 v[92:95], a[228:231], a[44:47], v[92:95]\n\t"
  "s_waitcnt lgkmcnt(3)\n\t"
  "v_exp_f32 v108, v108\n\t"
  "v_mfma_f32_16x16x32_bf16 v[88:91], a[232:235], a[48:51], v[88:91]\n\t"
  "v_exp_f32 v109, v109\n\t"
  "v_mfma_f32_16x16x32_bf16 v[92:95], a[232:235], a[52:55], v[92:95]\n\t"
  "v_exp_f32 v110, v110\n\t"
  "v_mfma_f32_16x16x32_bf16 a[160:163], a[192:195], a[88:91], a[160:163]\n\t"
  "v_exp_f32 v111, v111\n\t"
  "v_mfma_f32_16x16x32_bf16 a[164:167], a[192:195], a[92:95], a[164:167]\n\t"
  "v_cvt_pk_bf16_f32 v236, v100, v101\n\t"
  "v_cvt_pk_bf16_f32 v237, v102, v103\n\t"
  "v_mfma_f32_16x16x32_bf16 v[88:91], a[236:239], a[56:59], v[88:91]\n\t"
  "ds_read_b128 a[252:255], v42 offset:52224\n\t"
  "v_cvt_pk_bf16_f32 v234, v104, v105\n\t"
  "v_mfma_f32_16x16x32_bf16 v[92:95], a[236:239], a[60:63], v[92:95]\n\t"
  "s_waitcnt lgkmcnt(2)\n\t"
  "buffer_load_dwordx4 a[208:211], v40, s[56:59], s79 offen offset:3072 sc1\n\t"
  "v_mfma_f32_16x16x32_bf16 v[96:99], a[240:243], a[0:3], v[112:115]\n\t"
  "s_add_u32 s79, s79, 0x1000\n\t"
  "ds_read_b128 a[224:227], v42 offset:53248\n\t"
  "v_cvt_pk_bf16_f32 v235, v106, v107\n\t"
  "v_mfma_f32_16x16x32_bf16 v[100:103], a[240:243], a[4:7], v[116:119]\n\t"
  "buffer_load_dwordx4 v40, s[56:59], s29 offen offset:1024 sc1 lds\n\t"
  "v_mfma_f32_16x16x32_bf16 v[96:99], a[244:247], a[8:11], v[96:99]\n\t"
  "ds_read_b128 a[228:231], v42 offset:54272\n\t"
  "v_cvt_pk_bf16_f32 v238, v108, v109\n\t"
  "v_mfma_f32_16x16x32_bf16 v[100:103], a[244:247], a[12:15], v[100:103]\n\t"
  "s_waitcnt lgkmcnt(2)\n\t"
  "v_cvt_pk_bf16_f32 v239, v110, v111\n\t"
  "v_max3_f32 v45, v80, v81, v82\n\t"
  "v_mfma_f32_16x16x32_bf16 v[96:99], a[248:251], a[16:19], v[96:99]\n\t"
  "ds_read_b128 a[232:235], v42 offset:55296\n\t"
  "v_max3_f32 v45, v45, v83, v84\n\t"
  "v_mfma_f32_16x16x32_bf16 v[100:103], a[248:251], a[20:23], v[100:103]\n\t"
  "v_max3_f32 v45, v45, v85, v86\n\t"
  "v_max3_f32 v45, v45, v87, v88\n\t"
  "v_mfma_f32_16x16x32_bf16 v[56:59], v[52:55], v[232:235], v[56:59]\n\t"
  "ds_read_b128 a[236:239], v42 offset:56320\n\t"
  "v_max3_f32 v45, v45, v89, v90\n\t"
  "v_mfma_f32_16x16x32_bf16 v[60:63], v[52:55], v[236:239], v[60:63]\n\t"
  "buffer_load_dwordx4 a[212:215], v40, s[56:59], s79 offen offset:0 sc1\n\t"
  "v_mfma_f32_16x16x32_bf16 v[96:99], a[252:255], a[24:27], v[96:99]\n\t"
  "ds_read_b128 a[240:243], v42 offset:57344\n\t"
  "v_max3_f32 v45, v45, v91, v92\n\t"
  "v_mfma_f32_16x16x32_bf16 v[100:103], a[252:255], a[28:31], v[100:103]\n\t"
  "s_waitcnt lgkmcnt(3)\n\t"
  "buffer_load_dwordx4 v40, s[56:59], s29 offen offset:2048 sc1 lds\n\t"
  "v_mfma_f32_16x16x32_bf16 v[96:99], a[224:227], a[32:35], v[96:99]\n\t"
  "s_add_u32 s29, s29, 0x6000\n\t"
  "ds_read_b128 a[244:247], v42 offset:58368\n\t"
  "v_max3_f32 v45, v45, v93, v94\n\t"
  "v_mfma_f32_16x16x32_bf16 v[100:103], a[224:227], a[36:39], v[100:103]\n\t"
  "v_max_f32 v45, v45, v95\n\t"
  "v_cmp_lt_f32 vcc, s82, v45\n\t"
  "v_mfma_f32_16x16x32_bf16 v[96:99], a[228:231], a[40:43], v[96:99]\n\t"
  "ds_read_b128 a[248:251], v42 offset:59392\n\t"
  "v_mfma_f32_16x16x32_bf16 v[100:103], a[228:231], a[44:47], v[100:103]\n\t"
  "s_cbranch_vccnz STUBT14_%=\n\t"
  "RETT14_%=:\n\t"
  "s_waitcnt lgkmcnt(3)\n\t"
  "v_mfma_f32_16x16x32_bf16 v[96:99], a[232:235], a[48:51], v[96:99]\n\t"
  "ds_read_b128 a[252:255], v42 offset:60416\n\t"
  "ds_read_b128 a[224:227], v42 offset:61440\n\t"
  "v_mfma_f32_16x16x32_bf16 v[100:103], a[232:235], a[52:55], v[100:103]\n\t"
  "buffer_load_dwordx4 a[216:219], v40, s[56:59], s79 offen offset:1024 sc1\n\t"
  "v_mfma_f32_16x16x32_bf16 v[96:99], a[236:239], a[56:59], v[96:99]\n\t"
  "v_exp_f32 v80, v80\n\t"
  "v_mfma_f32_16x16x32_bf16 v[100:103], a[236:239], a[60:63], v[100:103]\n\t"
  "s_waitcnt lgkmcnt(3)\n\t"
  "v_exp_f32 v81, v81\n\t"
  "v_mfma_f32_16x16x32_bf16 v[104:107], a[240:243], a[0:3], v[112:115]\n\t"
  "ds_read_b128 a[228:231], v42 offset:62464\n\t"
  "ds_read_b128 a[232:235], v42 offset:63488\n\t"
  "v_mfma_f32_16x16x32_bf16 v[108:111], a[240:243], a[4:7], v[116:119]\n\t"
  "v_exp_f32 v82, v82\n\t"
  "v_mfma_f32_16x16x32_bf16 v[104:107], a[244:247], a[8:11], v[104:107]\n\t"
  "v_exp_f32 v83, v83\n\t"
  "v_mfma_f32_16x16x32_bf16 v[108:111], a[244:247], a[12:15], v[108:111]\n\t"
  "s_waitcnt lgkmcnt(3)\n\t"
  "v_exp_f32 v88, v88\n\t"
  "v_mfma_f32_16x16x32_bf16 v[104:107], a[248:251], a[16:19], v[104:107]\n\t"
  "ds_read_b128 a[236:239], v42 offset:64512\n\t"
  "v_cvt_pk_bf16_f32 v240, v80, v81\n\t"
  "v_mfma_f32_16x16x32_bf16 v[108:111], a[248:251], a[20:23], v[108:111]\n\t"
  "s_waitcnt vmcnt(16)\n\t"
  "buffer_load_dwordx4 a[220:223], v40, s[56:59], s79 offen offset:2048 sc1\n\t"
  "v_mfma_f32_16x16x32_bf16 v[104:107], a[252:255], a[24:27], v[104:107]\n\t"
  "s_add_u32 s79, s79, 0xc00\n\t"
  "v_exp_f32 v89, v89\n\t"
  "v_mfma_f32_16x16x32_bf16 v[108:111], a[252:255], a[28:31], v[108:111]\n\t"
  "s_waitcnt lgkmcnt(2)\n\t"
  "v_exp_f32 v90, v90\n\t"
  "v_mfma_f32_16x16x32_bf16 v[104:107], a[224:227], a[32:35], v[104:107]\n\t"
  "s_barrier\n\t"
  "ds_read_b128 a[240:243], v40 offset:0\n\t"
  "ds_read_b128 a[244:247], v40 offset:1024\n\t"
  "v_mfma_f32_16x16x32_bf16 v[108:111], a[224:227], a[36:39], v[108:111]\n\t"
  "v_exp_f32 v91, v91\n\t"
  "v_mfma_f32_16x16x32_bf16 v[104:107], a[228:231], a[40:43], v[104:107]\n\t"
  "v_exp_f32 v84, v84\n\t"
  "v_mfma_f32_16x16x32_bf16 v[108:111], a[228:231], a[44:47], v[108:111]\n\t"
  "s_waitcnt lgkmcnt(2)\n\t"
  "v_exp_f32 v85, v85\n\t"
  "v_mfma_f32_16x16x32_bf16 v[104:107], a[232:235], a[48:51], v[104:107]\n\t"
  "ds_read_b128 a[248:251], v40 offset:2048\n\t"
  "v_cvt_pk_bf16_f32 v241, v82, v83\n\t"
  "v_mfma_f32_16x16x32_bf16 v[108:111], a[232:235], a[52:55], v[108:111]\n\t"
  "buffer_load_dwordx4 a[88:91], v40, s[56:59], s18 offen offset:0 sc1\n\t"
  "v_mfma_f32_16x16x32_bf16 v[104:107], a[236:239], a[56:59], v[104:107]\n\t"
  "ds_read_b128 a[252:255], v40 offset:3072\n\t"
  "v_cvt_pk_bf16_f32 v242, v88, v89\n\t"
  "v_mfma_f32_16x16x32_bf16 v[108:111], a[236:239], a[60:63], v[108:111]\n\t"
  "v_cvt_pk_bf16_f32 v243, v90, v91\n\t"
  "v_cvt_pk_bf16_f32 v244, v84, v85\n\t"
  "v_exp_f32 v86, v86\n\t"
  "v_exp_f32 v87, v87\n\t"
  "v_exp_f32 v92, v92\n\t"
  "v_cvt_pk_bf16_f32 v245, v86, v87\n\t"
  "v_exp_f32 v93, v93\n\t"
  "v_exp_f32 v94, v94\n\t"
  "v_cvt_pk_bf16_f32 v246, v92, v93\n\t"
  "v_exp_f32 v95, v95\n\t"
  "v_max3_f32 v45, v96, v97, v98\n\t"
  "v_cvt_pk_bf16_f32 v247, v94, v95\n\t"
  "v_max3_f32 v45, v45, v99, v100\n\t"
  "v_max3_f32 v45, v45, v101, v102\n\t"
  "v_max3_f32 v45, v45, v103, v104\n\t"
  "v_max3_f32 v45, v45, v105, v106\n\t"
  "v_max3_f32 v45, v45, v107, v108\n\t"
  "v_max3_f32 v45, v45, v109, v110\n\t"
  "v_max_f32 v45, v45, v111\n\t"
  "v_cmp_lt_f32 vcc, s82, v45\n\t"
  "s_nop 1\n\t"
  "s_cbranch_vccnz STUBT15_%=\n\t"
  "RETT15_%=:\n\t"
  "v_mfma_f32_16x16x32_bf16 v[56:59], v[52:55], v[240:243], v[56:59]\n\t"
  "s_add_u32 m0, s87, 0x10000\n\t"
  "ds_read_b128 a[224:227], v40 offset:4096\n\t"
  "v_mfma_f32_16x16x32_bf16 v[60:63], v[52:55], v[244:247], v[60:63]\n\t"
  "s_waitcnt lgkmcnt(3)\n\t"
  "v_exp_f32 v96, v96\n\t"
  "v_mfma_f32_16x16x32_bf16 a[0:3], a[240:243], v[128:131], 0\n\t"
  "v_exp_f32 v97, v97\n\t"
  "v_mfma_f32_16x16x32_bf16 a[4:7], a[240:243], v[132:135], 0\n\t"
  "buffer_load_dwordx4 v40, s[56:59], s29 offen offset:0 sc1 lds\n\t"
  "v_mfma_f32_16x16x32_bf16 a[8:11], a[244:247], v[128:131], 0\n\t"
  "ds_read_b128 a[228:231], v40 offset:5120\n\t"
  "v_cvt_pk_bf16_f32 v248, v96, v97\n\t"
  "v_mfma_f32_16x16x32_bf16 a[12:15], a[244:247], v[132:135], 0\n\t"
  "s_waitcnt lgkmcnt(2)\n\t"
  "buffer_load_dwordx4 a[92:95], v40, s[56:59], s18 offen offset:1024 sc1\n\t"
  "v_mfma_f32_16x16x32_bf16 a[16:19], a[248:251], v[128:131], 0\n\t"
  "s_add_u32 s18, s18, 0x800\n\t"
  "ds_read_b128 a[232:235], v40 offset:6144\n\t"
  "v_mfma_f32_16x16x32_bf16 a[20:23], a[248:251], v[132:135], 0\n\t"
  "v_exp_f32 v98, v98\n\t"
  "v_mfma_f32_16x16x32_bf16 a[24:27], a[252:255], v[128:131], 0\n\t"
  "v_exp_f32 v99, v99\n\t"
  "v_mfma_f32_16x16x32_bf16 a[28:31], a[252:255], v[132:135], 0\n\t"
  "s_waitcnt lgkmcnt(1)\n\t"
  "v_exp_f32 v104, v104\n\t"
  "v_mfma_f32_16x16x32_bf16 a[32:35], a[224:227], v[128:131], 0\n\t"
  "ds_read_b128 a[236:239], v40 offset:7168\n\t"
  "ds_read_b128 a[240:243], v40 offset:8192\n\t"
  "v_mfma_f32_16x16x32_bf16 a[36:39], a[224:227], v[132:135], 0\n\t"
  "s_waitcnt vmcnt(11)\n\t"
  "buffer_load_dwordx4 v40, s[56:59], s29 offen offset:1024 sc1 lds\n\t"
  "v_mfma_f32_16x16x32_bf16 a[112:115], a[196:199], a[96:99], a[112:115]\n\t"
  "v_exp_f32 v105, v105\n\t"
  "v_mfma_f32_16x16x32_bf16 a[116:119], a[196:199], a[100:103], a[116:119]\n\t"
  "buffer_load_dwordx4 a[168:171], v40, s[56:59], s79 offen offset:0 sc1\n\t"
  "v_mfma_f32_16x16x32_bf16 a[40:43], a[228:231], v[128:131], 0\n\t"
  "v_exp_f32 v106, v106\n\t"
  "v_mfma_f32_16x16x32_bf16 a[44:47], a[228:231], v[132:135], 0\n\t"
  "s_waitcnt lgkmcnt(1)\n\t"
  "v_exp_f32 v107, v107\n\t"
  "v_mfma_f32_16x16x32_bf16 a[48:51], a[232:235], v[128:131], 0\n\t"
  "ds_read_b128 a[244:247], v40 offset:9216\n\t"
  "ds_read_b128 a[248:251], v40 offset:10240\n\t"
  "v_mfma_f32_16x16x32_bf16 a[52:55], a[232:235], v[132:135], 0\n\t"
  "v_exp_f32 v100, v100\n\t"
  "v_mfma_f32_16x16x32_bf16 a[120:123], a[200:203], a[96:99], a[120:123]\n\t"
  "v_exp_f32 v101, v101\n\t"
  "v_mfma_f32_16x16x32_bf16 a[124:127], a[200:203], a[100:103], a[124:127]\n\t"
  "buffer_load_dwordx4 v40, s[56:59], s29 offen offset:2048 sc1 lds\n\t"
  "v_mfma_f32_16x16x32_bf16 a[0:3], a[236:239], v[136:139], a[0:3]\n\t"
  "v_exp_f32 v102, v102\n\t"
  "v_mfma_f32_16x16x32_bf16 a[4:7], a[236:239], v[140:143], a[4:7]\n\t"
  "s_waitcnt lgkmcnt(1)\n\t"
  "buffer_load_dwordx4 a[172:175], v40, s[56:59], s79 offen offset:1024 sc1\n\t"
  "v_mfma_f32_16x16x32_bf16 a[8:11], a[240:243], v[136:139], a[8:11]\n\t"
  "ds_read_b128 a[252:255], v40 offset:11264\n\t"
  "ds_read_b128 a[224:227], v40 offset:12288\n\t"
  "v_mfma_f32_16x16x32_bf16 a[12:15], a[240:243], v[140:143], a[12:15]\n\t"
  "s_waitcnt vmcnt(12)\n\t"
  "v_exp_f32 v103, v103\n\t"
  "v_mfma_f32_16x16x32_bf16 a[128:131], a[204:207], a[96:99], a[128:131]\n\t"
  "v_exp_f32 v108, v108\n\t"
  "v_mfma_f32_16x16x32_bf16 a[132:135], a[204:207], a[100:103], a[132:135]\n\t"
  "v_exp_f32 v109, v109\n\t"
  "v_mfma_f32_16x16x32_bf16 a[16:19], a[244:247], v[136:139], a[16:19]\n\t"
  "v_exp_f32 v110, v110\n\t"
  "v_mfma_f32_16x16x32_bf16 a[20:23], a[244:247], v[140:143], a[20:23]\n\t"
  "s_waitcnt lgkmcnt(1)\n\t"
  "buffer_load_dwordx4 v40, s[56:59], s29 offen offset:3072 sc1 lds\n\t"
  "v_mfma_f32_16x16x32_bf16 a[24:27], a[248:251], v[136:139], a[24:27]\n\t"
  "s_add_u32 s29, s29, 0x1000\n\t"
  "ds_read_b128 a[228:231], v40 offset:13312\n\t"
  "v_cvt_pk_bf16_f32 v249, v98, v99\n\t"
  "v_mfma_f32_16x16x32_bf16 a[28:31], a[248:251], v[140:143], a[28:31]\n\t"
  "buffer_load_dwordx4 a[176:179], v40, s[56:59], s79 offen offset:2048 sc1\n\t"
  "v_mfma_f32_16x16x32_bf16 a[136:139], a[208:211], a[96:99], a[136:139]\n\t"
  "ds_read_b128 a[232:235], v40 offset:14336\n\t"
  "v_cvt_pk_bf16_f32 v250, v104, v105\n\t"
  "v_mfma_f32_16x16x32_bf16 a[140:143], a[208:211], a[100:103], a[140:143]\n\t"
  "v_exp_f32 v111, v111\n\t"
  "v_mfma_f32_16x16x32_bf16 a[32:35], a[252:255], v[136:139], a[32:35]\n\t"
  "s_add_u32 m0, s87, 0x11000\n\t"
  "v_cvt_pk_bf16_f32 v251, v106, v107\n\t"
  "v_cvt_pk_bf16_f32 v252, v100, v101\n\t"
  "v_mfma_f32_16x16x32_bf16 a[36:39], a[252:255], v[140:143], a[36:39]\n\t"
  "s_waitcnt lgkmcnt(1)\n\t"
  "v_cvt_pk_bf16_f32 v253, v102, v103\n\t"
  "v_cvt_pk_bf16_f32 v254, v108, v109\n\t"
  "v_mfma_f32_16x16x32_bf16 a[40:43], a[224:227], v[136:139], a[40:43]\n\t"
  "ds_read_b128 a[236:239], v40 offset:15360\n\t"
  "v_cvt_pk_bf16_f32 v255, v110, v111\n\t"
  "v_mfma_f32_16x16x32_bf16 a[44:47], a[224:227], v[140:143], a[44:47]\n\t"
  "s_waitcnt vmcnt(9)\n\t"
  "buffer_load_dwordx4 v40, s[56:59], s29 offen offset:0 sc1 lds\n\t"
  "v_mfma_f32_16x16x32_bf16 a[144:147], a[212:215], a[96:99], a[144:147]\n\t"
  "ds_read_b128 a[240:243], v40 offset:16384\n\t"
  "v_mfma_f32_16x16x32_bf16 a[148:151], a[212:215], a[100:103], a[148:151]\n\t"
  "buffer_load_dwordx4 a[180:183], v40, s[56:59], s79 offen offset:3072 sc1\n\t"
  "v_mfma_f32_16x16x32_bf16 a[48:51], a[228:231], v[136:139], a[48:51]\n\t"
  "s_add_u32 s79, s79, 0x1000\n\t"
  "v_mfma_f32_16x16x32_bf16 a[52:55], a[228:231], v[140:143], a[52:55]\n\t"
  "s_waitcnt lgkmcnt(1)\n\t"
  "v_mfma_f32_16x16x32_bf16 a[0:3], a[232:235], v[144:147], a[0:3]\n\t"
  "ds_read_b128 a[244:247], v40 offset:17408\n\t"
  "v_mfma_f32_16x16x32_bf16 a[4:7], a[232:235], v[148:151], a[4:7]\n\t"
  "v_mfma_f32_16x16x32_bf16 a[152:155], a[216:219], a[96:99], a[152:155]\n\t"
  "ds_read_b128 a[248:251], v40 offset:18432\n\t"
  "v_mfma_f32_16x16x32_bf16 a[156:159], a[216:219], a[100:103], a[156:159]\n\t"
  "buffer_load_dwordx4 v40, s[56:59], s29 offen offset:1024 sc1 lds\n\t"
  "v_mfma_f32_16x16x32_bf16 a[8:11], a[236:239], v[144:147], a[8:11]\n\t"
  "ds_read_b128 a[252:255], v40 offset:19456\n\t"
  "v_mfma_f32_16x16x32_bf16 a[12:15], a[236:239], v[148:151], a[12:15]\n\t"
  "s_waitcnt lgkmcnt(2)\n\t"
  "buffer_load_dwordx4 a[184:187], v40, s[56:59], s79 offen offset:0 sc1\n\t"
  "v_mfma_f32_16x16x32_bf16 a[16:19], a[240:243], v[144:147], a[16:19]\n\t"
  "ds_read_b128 a[224:227], v40 offset:20480\n\t"
  "v_mfma_f32_16x16x32_bf16 a[20:23], a[240:243], v[148:151], a[20:23]\n\t"
  "v_mfma_f32_16x16x32_bf16 a[160:163], a[220:223], a[96:99], a[160:163]\n\t"
  "ds_read_b128 a[228:231], v40 offset:21504\n\t"
  "v_mfma_f32_16x16x32_bf16 a[164:167], a[220:223], a[100:103], a[164:167]\n\t"
  "v_mfma_f32_16x16x32_bf16 a[24:27], a[244:247], v[144:147], a[24:27]\n\t"
  "ds_read_b128 a[232:235], v40 offset:22528\n\t"
  "v_mfma_f32_16x16x32_bf16 a[28:31], a[244:247], v[148:151], a[28:31]\n\t"
  "s_waitcnt lgkmcnt(3)\n\t"
  "buffer_load_dwordx4 v40, s[56:59], s29 offen offset:2048 sc1 lds\n\t"
  "v_mfma_f32_16x16x32_bf16 a[32:35], a[248:251], v[144:147], a[32:35]\n\t"
  "s_add_u32 s29, s29, 0x6000\n\t"
  "ds_read_b128 a[236:239], v40 offset:23552\n\t"
  "v_mfma_f32_16x16x32_bf16 a[36:39], a[248:251], v[148:151], a[36:39]\n\t"
  "buffer_load_dwordx4 a[188:191], v40, s[56:59], s79 offen offset:1024 sc1\n\t"
  "v_mfma_f32_16x16x32_bf16 a[40:43], a[252:255], v[144:147], a[40:43]\n\t"
  "ds_read_b128 a[240:243], v40 offset:24576\n\t"
  "v_mfma_f32_16x16x32_bf16 a[44:47], a[252:255], v[148:151], a[44:47]\n\t"
  "s_waitcnt lgkmcnt(3)\n\t"
  "v_mfma_f32_16x16x32_bf16 a[48:51], a[224:227], v[144:147], a[48:51]\n\t"
  "ds_read_b128 a[244:247], v40 offset:25600\n\t"
  "v_mfma_f32_16x16x32_bf16 a[52:55], a[224:227], v[148:151], a[52:55]\n\t"
  "v_mfma_f32_16x16x32_bf16 a[0:3], a[228:231], v[152:155], a[0:3]\n\t"
  "ds_read_b128 a[248:251], v40 offset:26624\n\t"
  "v_mfma_f32_16x16x32_bf16 a[4:7], a[228:231], v[156:159], a[4:7]\n\t"
  "s_waitcnt lgkmcnt(3)\n\t"
  "v_mfma_f32_16x16x32_bf16 a[8:11], a[232:235], v[152:155], a[8:11]\n\t"
  "s_waitcnt vmcnt(17)\n\t"
  "ds_read_b128 a[252:255], v40 offset:27648\n\t"
  "v_mfma_f32_16x16x32_bf16 a[12:15], a[232:235], v[156:159], a[12:15]\n\t"
  "s_barrier\n\t"
  "buffer_load_dwordx4 a[192:195], v40, s[56:59], s79 offen offset:2048 sc1\n\t"
  "v_mfma_f32_16x16x32_bf16 a[16:19], a[236:239], v[152:155], a[16:19]\n\t"
  "s_add_u32 s79, s79, 0xc00\n\t"
  "ds_read_b128 a[224:227], v40 offset:32768\n\t"
  "v_mfma_f32_16x16x32_bf16 a[20:23], a[236:239], v[156:159], a[20:23]\n\t"
  "s_waitcnt lgkmcnt(3)\n\t"
  "v_mfma_f32_16x16x32_bf16 a[24:27], a[240:243], v[152:155], a[24:27]\n\t"
  "ds_read_b128 a[228:231], v40 offset:33792\n\t"
  "v_mfma_f32_16x16x32_bf16 a[28:31], a[240:243], v[156:159], a[28:31]\n\t"
  "v_mfma_f32_16x16x32_bf16 a[32:35], a[244:247], v[152:155], a[32:35]\n\t"
  "ds_read_b128 a[232:235], v40 offset:34816\n\t"
  "v_mfma_f32_16x16x32_bf16 a[36:39], a[244:247], v[156:159], a[36:39]\n\t"
  "s_waitcnt lgkmcnt(3)\n\t"
  "buffer_load_dwordx4 a[96:99], v40, s[56:59], s18 offen offset:0 sc1\n\t"
  "v_mfma_f32_16x16x32_bf16 a[40:43], a[248:251], v[152:155], a[40:43]\n\t"
  "ds_read_b128 a[236:239], v40 offset:35840\n\t"
  "v_mfma_f32_16x16x32_bf16 a[44:47], a[248:251], v[156:159], a[44:47]\n\t"
  "v_mfma_f32_16x16x32_bf16 a[48:51], a[252:255], v[152:155], a[48:51]\n\t"
  "ds_read_b128 a[240:243], v40 offset:36864\n\t"
  "v_mfma_f32_16x16x32_bf16 a[52:55], a[252:255], v[156:159], a[52:55]\n\t"
  "s_waitcnt lgkmcnt(3)\n\t"
  "v_mfma_f32_16x16x32_bf16 a[0:3], a[224:227], v[160:163], a[0:3]\n\t"
  "ds_read_b128 a[244:247], v40 offset:37888\n\t"
  "v_mfma_f32_16x16x32_bf16 a[4:7], a[224:227], v[164:167], a[4:7]\n\t"
  "v_mfma_f32_16x16x32_bf16 a[8:11], a[228:231], v[160:163], a[8:11]\n\t"
  "v_mfma_f32_16x16x32_bf16 a[12:15], a[228:231], v[164:167], a[12:15]\n\t"
  "s_waitcnt lgkmcnt(2)\n\t"
  "buffer_load_dwordx4 a[100:103], v40, s[56:59], s18 offen offset:1024 sc1\n\t"
  "v_mfma_f32_16x16x32_bf16 a[16:19], a[232:235], v[160:163], a[16:19]\n\t"
  "s_add_u32 s18, s18, 0x800\n\t"
  "ds_read_b128 a[248:251], v40 offset:38912\n\t"
  "s_add_u32 m0, s87, 0x18000\n\t"
  "v_mfma_f32_16x16x32_bf16 a[20:23], a[232:235], v[164:167], a[20:23]\n\t"
  "buffer_load_dwordx4 v40, s[56:59], s29 offen offset:0 sc1 lds\n\t"
  "v_mfma_f32_16x16x32_bf16 a[24:27], a[236:239], v[160:163], a[24:27]\n\t"
  "ds_read_b128 a[252:255], v40 offset:39936\n\t"
  "v_mfma_f32_16x16x32_bf16 a[28:31], a[236:239], v[164:167], a[28:31]\n\t"
  "s_waitcnt lgkmcnt(2)\n\t"
  "v_mfma_f32_16x16x32_bf16 a[32:35], a[240:243], v[160:163], a[32:35]\n\t"
  "v_mfma_f32_16x16x32_bf16 a[36:39], a[240:243], v[164:167], a[36:39]\n\t"
  "buffer_load_dwordx4 v40, s[56:59], s29 offen offset:1024 sc1 lds\n\t"
  "v_mfma_f32_16x16x32_bf16 a[40:43], a[244:247], v[160:163], a[40:43]\n\t"
  "ds_read_b128 a[224:227], v40 offset:40960\n\t"
  "v_mfma_f32_16x16x32_bf16 a[44:47], a[244:247], v[164:167], a[44:47]\n\t"
  "s_waitcnt vmcnt(13)\n\t"
  "buffer_load_dwordx4 a[196:199], v40, s[56:59], s79 offen offset:0 sc1\n\t"
  "v_mfma_f32_16x16x32_bf16 a[112:115], a[168:171], a[88:91], a[112:115]\n\t"
  "ds_read_b128 a[228:231], v40 offset:41984\n\t"
  "v_mfma_f32_16x16x32_bf16 a[116:119], a[168:171], a[92:95], a[116:119]\n\t"
  "s_waitcnt lgkmcnt(2)\n\t"
  "v_mfma_f32_16x16x32_bf16 a[48:51], a[248:251], v[160:163], a[48:51]\n\t"
  "v_mfma_f32_16x16x32_bf16 a[52:55], a[248:251], v[164:167], a[52:55]\n\t"
  "v_mfma_f32_16x16x32_bf16 a[0:3], a[252:255], v[168:171], a[0:3]\n\t"
  "ds_read_b128 a[232:235], v40 offset:43008\n\t"
  "v_mfma_f32_16x16x32_bf16 a[4:7], a[252:255], v[172:175], a[4:7]\n\t"
  "buffer_load_dwordx4 v40, s[56:59], s29 offen offset:2048 sc1 lds\n\t"
  "v_mfma_f32_16x16x32_bf16 a[120:123], a[172:175], a[88:91], a[120:123]\n\t"
  "ds_read_b128 a[236:239], v40 offset:44032\n\t"
  "v_mfma_f32_16x16x32_bf16 a[124:127], a[172:175], a[92:95], a[124:127]\n\t"
  "s_waitcnt lgkmcnt(2)\n\t"
  "buffer_load_dwordx4 a[200:203], v40, s[56:59], s79 offen offset:1024 sc1\n\t"
  "v_mfma_f32_16x16x32_bf16 a[8:11], a[224:227], v[168:171], a[8:11]\n\t"
  "v_mfma_f32_16x16x32_bf16 a[12:15], a[224:227], v[172:175], a[12:15]\n\t"
  "v_mfma_f32_16x16x32_bf16 a[16:19], a[228:231], v[168:171], a[16:19]\n\t"
  "ds_read_b128 a[240:243], v40 offset:45056\n\t"
  "v_mfma_f32_16x16x32_bf16 a[20:23], a[228:231], v[172:175], a[20:23]\n\t"
  "s_waitcnt vmcnt(12)\n\t"
  "v_mfma_f32_16x16x32_bf16 a[128:131], a[176:179], a[88:91], a[128:131]\n\t"
  "ds_read_b128 a[244:247], v40 offset:46080\n\t"
  "v_mfma_f32_16x16x32_bf16 a[132:135], a[176:179], a[92:95], a[132:135]\n\t"
  "s_waitcnt lgkmcnt(2)\n\t"
  "buffer_load_dwordx4 v40, s[56:59], s29 offen offset:3072 sc1 lds\n\t"
  "v_mfma_f32_16x16x32_bf16 a[24:27], a[232:235], v[168:171], a[24:27]\n\t"
  "s_add_u32 s29, s29, 0x1000\n\t"
  "v_mfma_f32_16x16x32_bf16 a[28:31], a[232:235], v[172:175], a[28:31]\n\t"
  "buffer_load_dwordx4 a[204:207], v40, s[56:59], s79 offen offset:2048 sc1\n\t"
  "v_mfma_f32_16x16x32_bf16 a[32:35], a[236:239], v[168:171], a[32:35]\n\t"
  "ds_read_b128 a[248:251], v40 offset:47104\n\t"
  "v_mfma_f32_16x16x32_bf16 a[36:39], a[236:239], v[172:175], a[36:39]\n\t"
  "v_mfma_f32_16x16x32_bf16 a[136:139], a[180:183], a[88:91], a[136:139]\n\t"
  "s_add_u32 m0, s87, 0x19000\n\t"
  "ds_read_b128 a[252:255], v40 offset:48128\n\t"
  "v_mfma_f32_16x16x32_bf16 a[140:143], a[180:183], a[92:95], a[140:143]\n\t"
  "s_waitcnt lgkmcnt(2)\n\t"
  "v_mfma_f32_16x16x32_bf16 a[40:43], a[240:243], v[168:171], a[40:43]\n\t"
  "v_mfma_f32_16x16x32_bf16 a[44:47], a[240:243], v[172:175], a[44:47]\n\t"
  "buffer_load_dwordx4 v40, s[56:59], s29 offen offset:0 sc1 lds\n\t"
  "v_mfma_f32_16x16x32_bf16 a[48:51], a[244:247], v[168:171], a[48:51]\n\t"
  "ds_read_b128 a[224:227], v40 offset:49152\n\t"
  "v_mfma_f32_16x16x32_bf16 a[52:55], a[244:247], v[172:175], a[52:55]\n\t"
  "s_waitcnt vmcnt(10)\n\t"
  "buffer_load_dwordx4 a[208:211], v40, s[56:59], s79 offen offset:3072 sc1\n\t"
  "v_mfma_f32_16x16x32_bf16 a[144:147], a[184:187], a[88:91], a[144:147]\n\t"
  "s_add_u32 s79, s79, 0x1000\n\t"
  "ds_read_b128 a[228:231], v40 offset:50176\n\t"
  "v_mfma_f32_16x16x32_bf16 a[148:151], a[184:187], a[92:95], a[148:151]\n\t"
  "s_waitcnt lgkmcnt(2)\n\t"
  "v_mfma_f32_16x16x32_bf16 a[0:3], a[248:251], v[176:179], a[0:3]\n\t"
  "v_mfma_f32_16x16x32_bf16 a[4:7], a[248:251], v[180:183], a[4:7]\n\t"
  "v_mfma_f32_16x16x32_bf16 a[8:11], a[252:255], v[176:179], a[8:11]\n\t"
  "ds_read_b128 a[232:235], v40 offset:51200\n\t"
  "v_mfma_f32_16x16x32_bf16 a[12:15], a[252:255], v[180:183], a[12:15]\n\t"
  "buffer_load_dwordx4 v40, s[56:59], s29 offen offset:1024 sc1 lds\n\t"
  "v_mfma_f32_16x16x32_bf16 a[152:155], a[188:191], a[88:91], a[152:155]\n\t"
  "ds_read_b128 a[236:239], v40 offset:52224\n\t"
  "v_mfma_f32_16x16x32_bf16 a[156:159], a[188:191], a[92:95], a[156:159]\n\t"
  "s_waitcnt lgkmcnt(2)\n\t"
  "buffer_load_dwordx4 a[212:215], v40, s[56:59], s79 offen offset:0 sc1\n\t"
  "v_mfma_f32_16x16x32_bf16 a[16:19], a[224:227], v[176:179], a[16:19]\n\t"
  "ds_read_b128 a[240:243], v40 offset:53248\n\t"
  "v_mfma_f32_16x16x32_bf16 a[20:23], a[224:227], v[180:183], a[20:23]\n\t"
  "v_mfma_f32_16x16x32_bf16 a[24:27], a[228:231], v[176:179], a[24:27]\n\t"
  "ds_read_b128 a[244:247], v40 offset:54272\n\t"
  "v_mfma_f32_16x16x32_bf16 a[28:31], a[228:231], v[180:183], a[28:31]\n\t"
  "v_mfma_f32_16x16x32_bf16 a[160:163], a[192:195], a[88:91], a[160:163]\n\t"
  "ds_read_b128 a[248:251], v40 offset:55296\n\t"
  "v_mfma_f32_16x16x32_bf16 a[164:167], a[192:195], a[92:95], a[164:167]\n\t"
  "s_waitcnt lgkmcnt(3)\n\t"
  "buffer_load_dwordx4 v40, s[56:59], s29 offen offset:2048 sc1 lds\n\t"
  "v_mfma_f32_16x16x32_bf16 a[32:35], a[232:235], v[176:179], a[32:35]\n\t"
  "s_add_u32 s29, s29, 0x6000\n\t"
  "ds_read_b128 a[252:255], v40 offset:56320\n\t"
  "v_mfma_f32_16x16x32_bf16 a[36:39], a[232:235], v[180:183], a[36:39]\n\t"
  "buffer_load_dwordx4 a[216:219], v40, s[56:59], s79 offen offset:1024 sc1\n\t"
  "v_mfma_f32_16x16x32_bf16 a[40:43], a[236:239], v[176:179], a[40:43]\n\t"
  "ds_read_b128 a[224:227], v40 offset:57344\n\t"
  "v_mfma_f32_16x16x32_bf16 a[44:47], a[236:239], v[180:183], a[44:47]\n\t"
  "s_waitcnt lgkmcnt(3)\n\t"
  "v_mfma_f32_16x16x32_bf16 a[48:51], a[240:243], v[176:179], a[48:51]\n\t"
  "ds_read_b128 a[228:231], v40 offset:58368\n\t"
  "v_mfma_f32_16x16x32_bf16 a[52:55], a[240:243], v[180:183], a[52:55]\n\t"
  "v_mfma_f32_16x16x32_bf16 a[0:3], a[244:247], v[184:187], a[0:3]\n\t"
  "ds_read_b128 a[232:235], v40 offset:59392\n\t"
  "v_mfma_f32_16x16x32_bf16 a[4:7], a[244:247], v[188:191], a[4:7]\n\t"
  "s_waitcnt lgkmcnt(3)\n\t"
  "v_mfma_f32_16x16x32_bf16 a[8:11], a[248:251], v[184:187], a[8:11]\n\t"
  "ds_read_b128 a[236:239], v40 offset:60416\n\t"
  "v_mfma_f32_16x16x32_bf16 a[12:15], a[248:251], v[188:191], a[12:15]\n\t"
  "s_waitcnt vmcnt(17)\n\t"
  "buffer_load_dwordx4 a[220:223], v40, s[56:59], s79 offen offset:2048 sc1\n\t"
  "v_mfma_f32_16x16x32_bf16 a[16:19], a[252:255], v[184:187], a[16:19]\n\t"
  "s_add_u32 s79, s79, 0xc00\n\t"
  "v_mfma_f32_16x16x32_bf16 a[20:23], a[252:255], v[188:191], a[20:23]\n\t"
  "s_waitcnt lgkmcnt(2)\n\t"
  "v_mfma_f32_16x16x32_bf16 a[24:27], a[224:227], v[184:187], a[24:27]\n\t"
  "s_barrier\n\t"
  "ds_read_b128 a[240:243], v42 offset:0\n\t"
  "v_mfma_f32_16x16x32_bf16 a[28:31], a[224:227], v[188:191], a[28:31]\n\t"
  "v_mfma_f32_16x16x32_bf16 a[32:35], a[228:231], v[184:187], a[32:35]\n\t"
  "ds_read_b128 a[244:247], v42 offset:1024\n\t"
  "v_mfma_f32_16x16x32_bf16 a[36:39], a[228:231], v[188:191], a[36:39]\n\t"
  "s_waitcnt lgkmcnt(2)\n\t"
  "v_mfma_f32_16x16x32_bf16 a[40:43], a[232:235], v[184:187], a[40:43]\n\t"
  "ds_read_b128 a[248:251], v42 offset:2048\n\t"
  "v_mfma_f32_16x16x32_bf16 a[44:47], a[232:235], v[188:191], a[44:47]\n\t"
  "buffer_load_dwordx4 a[88:91], v40, s[56:59], s18 offen offset:0 sc1\n\t"
  "v_mfma_f32_16x16x32_bf16 a[48:51], a[236:239], v[184:187], a[48:51]\n\t"
  "ds_read_b128 a[252:255], v42 offset:3072\n\t"
  "v_mfma_f32_16x16x32_bf16 a[52:55], a[236:239], v[188:191], a[52:55]\n\t"
  "v_mfma_f32_16x16x32_bf16 v[56:59], v[52:55], v[248:251], v[56:59]\n\t"
  "s_add_u32 s29, s72, 1\n\t"
  "ds_read_b128 a[224:227], v42 offset:4096\n\t"
  "v_mfma_f32_16x16x32_bf16 v[60:63], v[52:55], v[252:255], v[60:63]\n\t"
  "s_waitcnt lgkmcnt(3)\n\t"
  "v_mfma_f32_16x16x32_bf16 a[0:3], a[240:243], v[192:195], a[0:3]\n\t"
  "s_and_b32 s29, s29, 3\n\t"
  "s_mul_i32 s29, s29, 0x1c000\n\t"
  "s_add_u32 s29, s29, s77\n\t"
  "s_add_u32 m0, s87, 0\n\t"
  "v_mfma_f32_16x16x32_bf16 a[4:7], a[240:243], v[196:199], a[4:7]\n\t"
  "buffer_load_dwordx4 v40, s[56:59], s29 offen offset:0 sc1 lds\n\t"
  "v_mfma_f32_16x16x32_bf16 a[8:11], a[244:247], v[192:195], a[8:11]\n\t"
  "ds_read_b128 a[228:231], v42 offset:5120\n\t"
  "v_mfma_f32_16x16x32_bf16 a[12:15], a[244:247], v[196:199], a[12:15]\n\t"
  "s_waitcnt lgkmcnt(2)\n\t"
  "buffer_load_dwordx4 a[92:95], v40, s[56:59], s18 offen offset:1024 sc1\n\t"
  "v_mfma_f32_16x16x32_bf16 a[16:19], a[248:251], v[192:195], a[16:19]\n\t"
  "s_add_u32 s18, s18, 0x800\n\t"
  "ds_read_b128 a[232:235], v42 offset:6144\n\t"
  "v_mfma_f32_16x16x32_bf16 a[20:23], a[248:251], v[196:199], a[20:23]\n\t"
  "v_rcp_f32 v50, v56\n\t"
  "v_mfma_f32_16x16x32_bf16 a[24:27], a[252:255], v[192:195], a[24:27]\n\t"
  "v_rcp_f32 v51, v60\n\t"
  "s_nop 0\n\t"
  "s_cbranch_execz TRAMP_%=\n\t"
  "v_mfma_f32_16x16x32_bf16 a[28:31], a[252:255], v[196:199], a[28:31]\n\t"
  "s_waitcnt lgkmcnt(1)\n\t"
  "v_mul_f32 v50, 0x42f5fdf0, v50\n\t"
  "v_mul_f32 v51, 0x42f5fdf0, v51\n\t"
  "v_mfma_f32_16x16x32_bf16 a[32:35], a[224:227], v[192:195], a[32:35]\n\t"
  "ds_read_b128 a[236:239], v42 offset:7168\n\t"
  "v_mfma_f32_16x16x32_bf16 a[36:39], a[224:227], v[196:199], a[36:39]\n\t"
  "s_waitcnt vmcnt(12)\n\t"
  "buffer_load_dwordx4 v40, s[56:59], s29 offen offset:1024 sc1 lds\n\t"
  "v_mfma_f32_16x16x32_bf16 a[112:115], a[196:199], a[96:99], a[112:115]\n\t"
  "ds_read_b128 a[240:243], v42 offset:8192\n\t"
  "v_mfma_f32_16x16x32_bf16 a[116:119], a[196:199], a[100:103], a[116:119]\n\t"
  "buffer_load_dwordx4 a[168:171], v40, s[56:59], s79 offen offset:0 sc1\n\t"
  "v_mfma_f32_16x16x32_bf16 a[40:43], a[228:231], v[192:195], a[40:43]\n\t"
  "v_mfma_f32_16x16x32_bf16 a[44:47], a[228:231], v[196:199], a[44:47]\n\t"
  "s_waitcnt lgkmcnt(1)\n\t"
  "v_mfma_f32_16x16x32_bf16 a[48:51], a[232:235], v[192:195], a[48:51]\n\t"
  "ds_read_b128 a[244:247], v42 offset:9216\n\t"
  "v_mfma_f32_16x16x32_bf16 a[52:55], a[232:235], v[196:199], a[52:55]\n\t"
  "v_mfma_f32_16x16x32_bf16 a[120:123], a[200:203], a[96:99], a[120:123]\n\t"
  "ds_read_b128 a[248:251], v42 offset:10240\n\t"
  "v_mfma_f32_16x16x32_bf16 a[124:127], a[200:203], a[100:103], a[124:127]\n\t"
  "buffer_load_dwordx4 v40, s[56:59], s29 offen offset:2048 sc1 lds\n\t"
  "v_mfma_f32_16x16x32_bf16 a[0:3], a[236:239], v[200:203], a[0:3]\n\t"
  "v_mfma_f32_16x16x32_bf16 a[4:7], a[236:239], v[204:207], a[4:7]\n\t"
  "s_waitcnt lgkmcnt(1)\n\t"
  "buffer_load_dwordx4 a[172:175], v40, s[56:59], s79 offen offset:1024 sc1\n\t"
  "v_mfma_f32_16x16x32_bf16 a[8:11], a[240:243], v[200:203], a[8:11]\n\t"
  "ds_read_b128 a[252:255], v42 offset:11264\n\t"
  "v_mfma_f32_16x16x32_bf16 a[12:15], a[240:243], v[204:207], a[12:15]\n\t"
  "s_waitcnt vmcnt(12)\n\t"
  "v_mfma_f32_16x16x32_bf16 a[128:131], a[204:207], a[96:99], a[128:131]\n\t"
  "ds_read_b128 a[224:227], v42 offset:12288\n\t"
  "v_mfma_f32_16x16x32_bf16 a[132:135], a[204:207], a[100:103], a[132:135]\n\t"
  "v_mfma_f32_16x16x32_bf16 a[16:19], a[244:247], v[200:203], a[16:19]\n\t"
  "v_mfma_f32_16x16x32_bf16 a[20:23], a[244:247], v[204:207], a[20:23]\n\t"
  "s_waitcnt lgkmcnt(1)\n\t"
  "buffer_load_dwordx4 v40, s[56:59], s29 offen offset:3072 sc1 lds\n\t"
  "v_mfma_f32_16x16x32_bf16 a[24:27], a[248:251], v[200:203], a[24:27]\n\t"
  "s_add_u32 s29, s29, 0x1000\n\t"
  "ds_read_b128 a[228:231], v42 offset:13312\n\t"
  "v_mfma_f32_16x16x32_bf16 a[28:31], a[248:251], v[204:207], a[28:31]\n\t"
  "buffer_load_dwordx4 a[176:179], v40, s[56:59], s79 offen offset:2048 sc1\n\t"
  "v_mfma_f32_16x16x32_bf16 a[136:139], a[208:211], a[96:99], a[136:139]\n\t"
  "ds_read_b128 a[232:235], v42 offset:14336\n\t"
  "v_mfma_f32_16x16x32_bf16 a[140:143], a[208:211], a[100:103], a[140:143]\n\t"
  "v_mfma_f32_16x16x32_bf16 a[32:35], a[252:255], v[200:203], a[32:35]\n\t"
  "s_add_u32 m0, s87, 0x1000\n\t"
  "v_mfma_f32_16x16x32_bf16 a[36:39], a[252:255], v[204:207], a[36:39]\n\t"
  "s_waitcnt lgkmcnt(1)\n\t"
  "v_mfma_f32_16x16x32_bf16 a[40:43], a[224:227], v[200:203], a[40:43]\n\t"
  "ds_read_b128 a[236:239], v42 offset:15360\n\t"
  "v_mfma_f32_16x16x32_bf16 a[44:47], a[224:227], v[204:207], a[44:47]\n\t"
  "s_waitcnt vmcnt(9)\n\t"
  "buffer_load_dwordx4 v40, s[56:59], s29 offen offset:0 sc1 lds\n\t"
  "v_mfma_f32_16x16x32_bf16 a[144:147], a[212:215], a[96:99], a[144:147]\n\t"
  "ds_read_b128 a[240:243], v42 offset:16384\n\t"
  "v_mfma_f32_16x16x32_bf16 a[148:151], a[212:215], a[100:103], a[148:151]\n\t"
  "buffer_load_dwordx4 a[180:183], v40, s[56:59], s79 offen offset:3072 sc1\n\t"
  "v_mfma_f32_16x16x32_bf16 a[48:51], a[228:231], v[200:203], a[48:51]\n\t"
  "s_add_u32 s79, s79, 0x1000\n\t"
  "v_mfma_f32_16x16x32_bf16 a[52:55], a[228:231], v[204:207], a[52:55]\n\t"
  "s_waitcnt lgkmcnt(1)\n\t"
  "v_mfma_f32_16x16x32_bf16 a[0:3], a[232:235], v[208:211], a[0:3]\n\t"
  "ds_read_b128 a[244:247], v42 offset:17408\n\t"
  "v_mfma_f32_16x16x32_bf16 a[4:7], a[232:235], v[212:215], a[4:7]\n\t"
  "v_mfma_f32_16x16x32_bf16 a[152:155], a[216:219], a[96:99], a[152:155]\n\t"
  "ds_read_b128 a[248:251], v42 offset:18432\n\t"
  "v_mfma_f32_16x16x32_bf16 a[156:159], a[216:219], a[100:103], a[156:159]\n\t"
  "buffer_load_dwordx4 v40, s[56:59], s29 offen offset:1024 sc1 lds\n\t"
  "v_mfma_f32_16x16x32_bf16 a[8:11], a[236:239], v[208:211], a[8:11]\n\t"
  "ds_read_b128 a[252:255], v42 offset:19456\n\t"
  "v_mfma_f32_16x16x32_bf16 a[12:15], a[236:239], v[212:215], a[12:15]\n\t"
  "s_waitcnt lgkmcnt(2)\n\t"
  "buffer_load_dwordx4 a[184:187], v40, s[56:59], s79 offen offset:0 sc1\n\t"
  "v_mfma_f32_16x16x32_bf16 a[16:19], a[240:243], v[208:211], a[16:19]\n\t"
  "ds_read_b128 a[224:227], v42 offset:20480\n\t"
  "v_mfma_f32_16x16x32_bf16 a[20:23], a[240:243], v[212:215], a[20:23]\n\t"
  "v_mfma_f32_16x16x32_bf16 a[160:163], a[220:223], a[96:99], a[160:163]\n\t"
  "ds_read_b128 a[228:231], v42 offset:21504\n\t"
  "v_mfma_f32_16x16x32_bf16 a[164:167], a[220:223], a[100:103], a[164:167]\n\t"
  "v_mfma_f32_16x16x32_bf16 a[24:27], a[244:247], v[208:211], a[24:27]\n\t"
  "ds_read_b128 a[232:235], v42 offset:22528\n\t"
  "v_mfma_f32_16x16x32_bf16 a[28:31], a[244:247], v[212:215], a[28:31]\n\t"
  "s_waitcnt lgkmcnt(3)\n\t"
  "buffer_load_dwordx4 v40, s[56:59], s29 offen offset:2048 sc1 lds\n\t"
  "v_mfma_f32_16x16x32_bf16 a[32:35], a[248:251], v[208:211], a[32:35]\n\t"
  "s_add_u32 s29, s29, 0x6000\n\t"
  "ds_read_b128 a[236:239], v42 offset:23552\n\t"
  "v_mfma_f32_16x16x32_bf16 a[36:39], a[248:251], v[212:215], a[36:39]\n\t"
  "buffer_load_dwordx4 a[188:191], v40, s[56:59], s79 offen offset:1024 sc1\n\t"
  "v_mfma_f32_16x16x32_bf16 a[40:43], a[252:255], v[208:211], a[40:43]\n\t"
  "ds_read_b128 a[240:243], v42 offset:24576\n\t"
  "v_mfma_f32_16x16x32_bf16 a[44:47], a[252:255], v[212:215], a[44:47]\n\t"
  "s_waitcnt lgkmcnt(3)\n\t"
  "v_mfma_f32_16x16x32_bf16 a[48:51], a[224:227], v[208:211], a[48:51]\n\t"
  "ds_read_b128 a[244:247], v42 offset:25600\n\t"
  "v_mfma_f32_16x16x32_bf16 a[52:55], a[224:227], v[212:215], a[52:55]\n\t"
  "v_mfma_f32_16x16x32_bf16 a[0:3], a[228:231], v[216:219], a[0:3]\n\t"
  "ds_read_b128 a[248:251], v42 offset:26624\n\t"
  "v_mfma_f32_16x16x32_bf16 a[4:7], a[228:231], v[220:223], a[4:7]\n\t"
  "s_waitcnt lgkmcnt(3)\n\t"
  "v_mfma_f32_16x16x32_bf16 a[8:11], a[232:235], v[216:219], a[8:11]\n\t"
  "s_waitcnt vmcnt(17)\n\t"
  "ds_read_b128 a[252:255], v42 offset:27648\n\t"
  "v_mfma_f32_16x16x32_bf16 a[12:15], a[232:235], v[220:223], a[12:15]\n\t"
  "s_barrier\n\t"
  "buffer_load_dwordx4 a[192:195], v40, s[56:59], s79 offen offset:2048 sc1\n\t"
  "v_mfma_f32_16x16x32_bf16 a[16:19], a[236:239], v[216:219], a[16:19]\n\t"
  "s_add_u32 s79, s79, 0xc00\n\t"
  "ds_read_b128 a[224:227], v42 offset:32768\n\t"
  "v_mfma_f32_16x16x32_bf16 a[20:23], a[236:239], v[220:223], a[20:23]\n\t"
  "s_waitcnt lgkmcnt(3)\n\t"
  "v_mfma_f32_16x16x32_bf16 a[24:27], a[240:243], v[216:219], a[24:27]\n\t"
  "ds_read_b128 a[228:231], v42 offset:39936\n\t"
  "v_mfma_f32_16x16x32_bf16 a[28:31], a[240:243], v[220:223], a[28:31]\n\t"
  "v_mfma_f32_16x16x32_bf16 a[32:35], a[244:247], v[216:219], a[32:35]\n\t"
  "ds_read_b128 a[232:235], v42 offset:47104\n\t"
  "v_mfma_f32_16x16x32_bf16 a[36:39], a[244:247], v[220:223], a[36:39]\n\t"
  "s_waitcnt lgkmcnt(3)\n\t"
  "buffer_load_dwordx4 a[96:99], v40, s[56:59], s18 offen offset:0 sc1\n\t"
  "v_mfma_f32_16x16x32_bf16 a[40:43], a[248:251], v[216:219], a[40:43]\n\t"
  "ds_read_b128 a[236:239], v42 offset:54272\n\t"
  "v_mfma_f32_16x16x32_bf16 a[44:47], a[248:251], v[220:223], a[44:47]\n\t"
  "v_mfma_f32_16x16x32_bf16 a[48:51], a[252:255], v[216:219], a[48:51]\n\t"
  "ds_read_b128 a[240:243], v42 offset:33792\n\t"
  "v_mfma_f32_16x16x32_bf16 a[52:55], a[252:255], v[220:223], a[52:55]\n\t"
  "s_waitcnt lgkmcnt(3)\n\t"
  "v_mfma_f32_16x16x32_bf16 a[0:3], a[224:227], v[224:227], a[0:3]\n\t"
  "ds_read_b128 a[244:247], v42 offset:40960\n\t"
  "v_mfma_f32_16x16x32_bf16 a[4:7], a[224:227], v[228:231], a[4:7]\n\t"
  "v_mfma_f32_16x16x32_bf16 a[0:3], a[228:231], v[232:235], a[0:3]\n\t"
  "v_mfma_f32_16x16x32_bf16 a[4:7], a[228:231], v[236:239], a[4:7]\n\t"
  "s_waitcnt lgkmcnt(2)\n\t"
  "buffer_load_dwordx4 a[100:103], v40, s[56:59], s18 offen offset:1024 sc1\n\t"
  "v_mfma_f32_16x16x32_bf16 a[0:3], a[232:235], v[240:243], a[0:3]\n\t"
  "s_add_u32 s18, s18, 0x800\n\t"
  "ds_read_b128 a[248:251], v42 offset:48128\n\t"
  "s_add_u32 m0, s87, 0x8000\n\t"
  "v_mfma_f32_16x16x32_bf16 a[4:7], a[232:235], v[244:247], a[4:7]\n\t"
  "buffer_load_dwordx4 v40, s[56:59], s29 offen offset:0 sc1 lds\n\t"
  "v_mfma_f32_16x16x32_bf16 a[0:3], a[236:239], v[248:251], a[0:3]\n\t"
  "ds_read_b128 a[252:255], v42 offset:55296\n\t"
  "v_mfma_f32_16x16x32_bf16 a[4:7], a[236:239], v[252:255], a[4:7]\n\t"
  "s_waitcnt lgkmcnt(2)\n\t"
  "v_mfma_f32_16x16x32_bf16 a[8:11], a[240:243], v[224:227], a[8:11]\n\t"
  "v_mfma_f32_16x16x32_bf16 a[12:15], a[240:243], v[228:231], a[12:15]\n\t"
  "buffer_load_dwordx4 v40, s[56:59], s29 offen offset:1024 sc1 lds\n\t"
  "v_mfma_f32_16x16x32_bf16 a[8:11], a[244:247], v[232:235], a[8:11]\n\t"
  "ds_read_b128 a[224:227], v42 offset:34816\n\t"
  "v_accvgpr_read_b32 v56, a0\n\t"
  "v_mfma_f32_16x16x32_bf16 a[12:15], a[244:247], v[236:239], a[12:15]\n\t"
  "s_waitcnt vmcnt(13)\n\t"
  "buffer_load_dwordx4 a[196:199], v40, s[56:59], s79 offen offset:0 sc1\n\t"
  "v_mfma_f32_16x16x32_bf16 a[112:115], a[168:171], a[88:91], a[112:115]\n\t"
  "ds_read_b128 a[228:231], v42 offset:41984\n\t"
  "v_accvgpr_read_b32 v57, a1\n\t"
  "v_mfma_f32_16x16x32_bf16 a[116:119], a[168:171], a[92:95], a[116:119]\n\t"
  "s_waitcnt lgkmcnt(2)\n\t"
  "v_accvgpr_read_b32 v58, a2\n\t"
  "v_accvgpr_read_b32 v59, a3\n\t"
  "v_mfma_f32_16x16x32_bf16 a[8:11], a[248:251], v[240:243], a[8:11]\n\t"
  "v_accvgpr_read_b32 v60, a4\n\t"
  "v_accvgpr_read_b32 v61, a5\n\t"
  "v_mfma_f32_16x16x32_bf16 a[12:15], a[248:251], v[244:247], a[12:15]\n\t"
  "v_accvgpr_read_b32 v62, a6\n\t"
  "v_accvgpr_read_b32 v63, a7\n\t"
  "v_mfma_f32_16x16x32_bf16 a[8:11], a[252:255], v[248:251], a[8:11]\n\t"
  "ds_read_b128 a[232:235], v42 offset:49152\n\t"
  "v_mul_f32 v56, v56, v50\n\t"
  "v_mfma_f32_16x16x32_bf16 a[12:15], a[252:255], v[252:255], a[12:15]\n\t"
  "buffer_load_dwordx4 v40, s[56:59], s29 offen offset:2048 sc1 lds\n\t"
  "v_mfma_f32_16x16x32_bf16 a[120:123], a[172:175], a[88:91], a[120:123]\n\t"
  "ds_read_b128 a[236:239], v42 offset:56320\n\t"
  "v_mul_f32 v57, v57, v50\n\t"
  "v_mfma_f32_16x16x32_bf16 a[124:127], a[172:175], a[92:95], a[124:127]\n\t"
  "s_waitcnt lgkmcnt(2)\n\t"
  "buffer_load_dwordx4 a[200:203], v40, s[56:59], s79 offen offset:1024 sc1\n\t"
  "v_mfma_f32_16x16x32_bf16 a[16:19], a[224:227], v[224:227], a[16:19]\n\t"
  "v_mul_f32 v58, v58, v50\n\t"
  "v_mul_f32 v59, v59, v50\n\t"
  "v_mfma_f32_16x16x32_bf16 a[20:23], a[224:227], v[228:231], a[20:23]\n\t"
  "v_mul_f32 v60, v60, v51\n\t"
  "v_mul_f32 v61, v61, v51\n\t"
  "v_mfma_f32_16x16x32_bf16 a[16:19], a[228:231], v[232:235], a[16:19]\n\t"
  "ds_read_b128 a[240:243], v42 offset:35840\n\t"
  "v_mul_f32 v62, v62, v51\n\t"
  "v_mfma_f32_16x16x32_bf16 a[20:23], a[228:231], v[236:239], a[20:23]\n\t"
  "s_waitcnt vmcnt(12)\n\t"
  "v_mul_f32 v63, v63, v51\n\t"
  "v_cvt_pk_bf16_f32 v56, v56, v57\n\t"
  "v_mfma_f32_16x16x32_bf16 a[128:131], a[176:179], a[88:91], a[128:131]\n\t"
  "ds_read_b128 a[244:247], v42 offset:43008\n\t"
  "v_cvt_pk_bf16_f32 v57, v58, v59\n\t"
  "v_mfma_f32_16x16x32_bf16 a[132:135], a[176:179], a[92:95], a[132:135]\n\t"
  "s_waitcnt lgkmcnt(2)\n\t"
  "buffer_load_dwordx4 v40, s[56:59], s29 offen offset:3072 sc1 lds\n\t"
  "v_mfma_f32_16x16x32_bf16 a[16:19], a[232:235], v[240:243], a[16:19]\n\t"
  "s_add_u32 s29, s29, 0x1000\n\t"
  "v_cvt_pk_bf16_f32 v58, v60, v61\n\t"
  "v_cvt_pk_bf16_f32 v59, v62, v63\n\t"
  "v_mfma_f32_16x16x32_bf16 a[20:23], a[232:235], v[244:247], a[20:23]\n\t"
  "buffer_load_dwordx4 a[204:207], v40, s[56:59], s79 offen offset:2048 sc1\n\t"
  "v_mfma_f32_16x16x32_bf16 a[16:19], a[236:239], v[248:251], a[16:19]\n\t"
  "s_add_u32 m0, s87, 0x9000\n\t"
  "ds_read_b128 a[248:251], v42 offset:50176\n\t"
  "v_accvgpr_write_b32 a0, v56\n\t"
  "v_mfma_f32_16x16x32_bf16 a[20:23], a[236:239], v[252:255], a[20:23]\n\t"
  "v_accvgpr_write_b32 a1, v57\n\t"
  "v_accvgpr_write_b32 a2, v58\n\t"
  "v_mfma_f32_16x16x32_bf16 a[136:139], a[180:183], a[88:91], a[136:139]\n\t"
  "ds_read_b128 a[252:255], v42 offset:57344\n\t"
  "v_accvgpr_write_b32 a3, v59\n\t"
  "v_mfma_f32_16x16x32_bf16 a[140:143], a[180:183], a[92:95], a[140:143]\n\t"
  "s_waitcnt lgkmcnt(2)\n\t"
  "buffer_load_dwordx4 v40, s[56:59], s29 offen offset:0 sc1 lds\n\t"
  "v_mfma_f32_16x16x32_bf16 a[24:27], a[240:243], v[224:227], a[24:27]\n\t"
  "v_accvgpr_read_b32 v120, a8\n\t"
  "v_accvgpr_read_b32 v121, a9\n\t"
  "v_mfma_f32_16x16x32_bf16 a[28:31], a[240:243], v[228:231], a[28:31]\n\t"
  "v_accvgpr_read_b32 v122, a10\n\t"
  "v_accvgpr_read_b32 v123, a11\n\t"
  "v_mfma_f32_16x16x32_bf16 a[24:27], a[244:247], v[232:235], a[24:27]\n\t"
  "ds_read_b128 a[224:227], v42 offset:36864\n\t"
  "v_accvgpr_read_b32 v124, a12\n\t"
  "v_mfma_f32_16x16x32_bf16 a[28:31], a[244:247], v[236:239], a[28:31]\n\t"
  "s_waitcnt vmcnt(10)\n\t"
  "buffer_load_dwordx4 a[208:211], v40, s[56:59], s79 offen offset:3072 sc1\n\t"
  "v_mfma_f32_16x16x32_bf16 a[144:147], a[184:187], a[88:91], a[144:147]\n\t"
  "s_add_u32 s79, s79, 0x1000\n\t"
  "ds_read_b128 a[228:231], v42 offset:44032\n\t"
  "v_accvgpr_read_b32 v125, a13\n\t"
  "v_mfma_f32_16x16x32_bf16 a[148:151], a[184:187], a[92:95], a[148:151]\n\t"
  "s_waitcnt lgkmcnt(2)\n\t"
  "v_accvgpr_read_b32 v126, a14\n\t"
  "v_accvgpr_read_b32 v127, a15\n\t"
  "v_mfma_f32_16x16x32_bf16 a[24:27], a[248:251], v[240:243], a[24:27]\n\t"
  "s_add_u32 s28, s72, 0\n\t"
  "s_and_b32 s28, s28, 3\n\t"
  "s_mul_i32 s28, s28, 0x1c000\n\t"
  "s_add_u32 s28, s28, s86\n\t"
  "s_add_u32 s76, s28, 0x1000\n\t"
  "v_mfma_f32_16x16x32_bf16 a[28:31], a[248:251], v[244:247], a[28:31]\n\t"
  "buffer_store_dwordx4 a[0:3], v40, s[56:59], s28 offen offset:0 sc1\n\t"
  "v_mfma_f32_16x16x32_bf16 a[24:27], a[252:255], v[248:251], a[24:27]\n\t"
  "ds_read_b128 a[232:235], v42 offset:51200\n\t"
  "v_mul_f32 v120, v120, v50\n\t"
  "v_mfma_f32_16x16x32_bf16 a[28:31], a[252:255], v[252:255], a[28:31]\n\t"
  "buffer_load_dwordx4 v40, s[56:59], s29 offen offset:1024 sc1 lds\n\t"
  "v_mfma_f32_16x16x32_bf16 a[152:155], a[188:191], a[88:91], a[152:155]\n\t"
  "ds_read_b128 a[236:239], v42 offset:58368\n\t"
  "v_mul_f32 v121, v121, v50\n\t"
  "v_mfma_f32_16x16x32_bf16 a[156:159], a[188:191], a[92:95], a[156:159]\n\t"
  "s_waitcnt lgkmcnt(2)\n\t"
  "buffer_load_dwordx4 a[212:215], v40, s[56:59], s79 offen offset:0 sc1\n\t"
  "v_mfma_f32_16x16x32_bf16 a[32:35], a[224:227], v[224:227], a[32:35]\n\t"
  "ds_read_b128 a[240:243], v42 offset:37888\n\t"
  "v_mul_f32 v122, v122, v50\n\t"
  "v_mfma_f32_16x16x32_bf16 a[36:39], a[224:227], v[228:231], a[36:39]\n\t"
  "v_mul_f32 v123, v123, v50\n\t"
  "v_mul_f32 v124, v124, v51\n\t"
  "v_mfma_f32_16x16x32_bf16 a[32:35], a[228:231], v[232:235], a[32:35]\n\t"
  "ds_read_b128 a[244:247], v42 offset:45056\n\t"
  "v_mul_f32 v125, v125, v51\n\t"
  "v_mfma_f32_16x16x32_bf16 a[36:39], a[228:231], v[236:239], a[36:39]\n\t"
  "v_mul_f32 v126, v126, v51\n\t"
  "v_mul_f32 v127, v127, v51\n\t"
  "v_mfma_f32_16x16x32_bf16 a[160:163], a[192:195], a[88:91], a[160:163]\n\t"
  "ds_read_b128 a[248:251], v42 offset:52224\n\t"
  "v_cvt_pk_bf16_f32 v120, v120, v121\n\t"
  "v_mfma_f32_16x16x32_bf16 a[164:167], a[192:195], a[92:95], a[164:167]\n\t"
  "s_waitcnt lgkmcnt(3)\n\t"
  "buffer_load_dwordx4 v40, s[56:59], s29 offen offset:2048 sc1 lds\n\t"
  "v_mfma_f32_16x16x32_bf16 a[32:35], a[232:235], v[240:243], a[32:35]\n\t"
  "s_add_u32 s29, s29, 0x6000\n\t"
  "ds_read_b128 a[252:255], v42 offset:59392\n\t"
  "v_cvt_pk_bf16_f32 v121, v122, v123\n\t"
  "v_mfma_f32_16x16x32_bf16 a[36:39], a[232:235], v[244:247], a[36:39]\n\t"
  "buffer_load_dwordx4 a[216:219], v40, s[56:59], s79 offen offset:1024 sc1\n\t"
  "v_mfma_f32_16x16x32_bf16 a[32:35], a[236:239], v[248:251], a[32:35]\n\t"
  "ds_read_b128 a[224:227], v42 offset:38912\n\t"
  "v_cvt_pk_bf16_f32 v122, v124, v125\n\t"
  "v_mfma_f32_16x16x32_bf16 a[36:39], a[236:239], v[252:255], a[36:39]\n\t"
  "s_waitcnt lgkmcnt(3)\n\t"
  "v_cvt_pk_bf16_f32 v123, v126, v127\n\t"
  "v_accvgpr_write_b32 a8, v120\n\t"
  "v_mfma_f32_16x16x32_bf16 a[40:43], a[240:243], v[224:227], a[40:43]\n\t"
  "ds_read_b128 a[228:231], v42 offset:46080\n\t"
  "v_accvgpr_write_b32 a9, v121\n\t"
  "v_mfma_f32_16x16x32_bf16 a[44:47], a[240:243], v[228:231], a[44:47]\n\t"
  "v_accvgpr_write_b32 a10, v122\n\t"
  "v_accvgpr_write_b32 a11, v123\n\t"
  "v_mfma_f32_16x16x32_bf16 a[40:43], a[244:247], v[232:235], a[40:43]\n\t"
  "ds_read_b128 a[232:235], v42 offset:53248\n\t"
  "v_accvgpr_read_b32 v56, a16\n\t"
  "v_mfma_f32_16x16x32_bf16 a[44:47], a[244:247], v[236:239], a[44:47]\n\t"
  "s_waitcnt lgkmcnt(3)\n\t"
  "v_accvgpr_read_b32 v57, a17\n\t"
  "v_accvgpr_read_b32 v58, a18\n\t"
  "v_mfma_f32_16x16x32_bf16 a[40:43], a[248:251], v[240:243], a[40:43]\n\t"
  "s_waitcnt vmcnt(18)\n\t"
  "ds_read_b128 a[236:239], v42 offset:60416\n\t"
  "v_accvgpr_read_b32 v59, a19\n\t"
  "v_mfma_f32_16x16x32_bf16 a[44:47], a[248:251], v[244:247], a[44:47]\n\t"
  "s_barrier\n\t"
  "buffer_load_dwordx4 a[220:223], v40, s[56:59], s79 offen offset:2048 sc1\n\t"
  "v_mfma_f32_16x16x32_bf16 a[40:43], a[252:255], v[248:251], a[40:43]\n\t"
  "s_add_u32 s79, s79, 0xc00\n\t"
  "ds_read_b128 a[240:243], v40 offset:0\n\t"
  "v_accvgpr_read_b32 v60, a20\n\t"
  "v_mfma_f32_16x16x32_bf16 a[44:47], a[252:255], v[252:255], a[44:47]\n\t"
  "s_waitcnt lgkmcnt(3)\n\t"
  "buffer_store_dwordx4 a[8:11], v40, s[56:59], s28 offen offset:1024 sc1\n\t"
  "v_mfma_f32_16x16x32_bf16 a[48:51], a[224:227], v[224:227], a[48:51]\n\t"
  "ds_read_b128 a[244:247], v40 offset:1024\n\t"
  "v_accvgpr_read_b32 v61, a21\n\t"
  "v_mfma_f32_16x16x32_bf16 a[52:55], a[224:227], v[228:231], a[52:55]\n\t"
  "v_accvgpr_read_b32 v62, a22\n\t"
  "v_accvgpr_read_b32 v63, a23\n\t"
  "v_mfma_f32_16x16x32_bf16 a[48:51], a[228:231], v[232:235], a[48:51]\n\t"
  "ds_read_b128 a[248:251], v40 offset:2048\n\t"
  "v_mul_f32 v56, v56, v50\n\t"
  "v_mfma_f32_16x16x32_bf16 a[52:55], a[228:231], v[236:239], a[52:55]\n\t"
  "s_waitcnt lgkmcnt(3)\n\t"
  "v_mul_f32 v57, v57, v50\n\t"
  "v_mul_f32 v58, v58, v50\n\t"
  "v_mfma_f32_16x16x32_bf16 a[48:51], a[232:235], v[240:243], a[48:51]\n\t"
  "ds_read_b128 a[252:255], v40 offset:3072\n\t"
  "v_mul_f32 v59, v59, v50\n\t"
  "v_mfma_f32_16x16x32_bf16 a[52:55], a[232:235], v[244:247], a[52:55]\n\t"
  "v_mul_f32 v60, v60, v51\n\t"
  "v_mul_f32 v61, v61, v51\n\t"
  "v_mfma_f32_16x16x32_bf16 a[48:51], a[236:239], v[248:251], a[48:51]\n\t"
  "ds_read_b128 a[224:227], v40 offset:4096\n\t"
  "v_mul_f32 v62, v62, v51\n\t"
  "v_mfma_f32_16x16x32_bf16 a[52:55], a[236:239], v[252:255], a[52:55]\n\t"
  "s_waitcnt lgkmcnt(3)\n\t"
  "v_mul_f32 v63, v63, v51\n\t"
  "v_cvt_pk_bf16_f32 v56, v56, v57\n\t"
  "v_mfma_f32_16x16x32_bf16 v[64:67], a[240:243], v[128:131], 0\n\t"
  "ds_read_b128 a[228:231], v40 offset:5120\n\t"
  "v_cvt_pk_bf16_f32 v57, v58, v59\n\t"
  "v_mfma_f32_16x16x32_bf16 v[68:71], a[240:243], v[132:135], 0\n\t"
  "v_cvt_pk_bf16_f32 v58, v60, v61\n\t"
  "v_cvt_pk_bf16_f32 v59, v62, v63\n\t"
  "v_mfma_f32_16x16x32_bf16 v[72:75], a[244:247], v[128:131], 0\n\t"
  "s_add_u32 m0, s87, 0x10000\n\t"
  "v_accvgpr_write_b32 a16, v56\n\t"
  "v_accvgpr_write_b32 a17, v57\n\t"
  "v_mfma_f32_16x16x32_bf16 v[76:79], a[244:247], v[132:135], 0\n\t"
  "s_waitcnt lgkmcnt(2)\n\t"
  "v_accvgpr_write_b32 a18, v58\n\t"
  "v_accvgpr_write_b32 a19, v59\n\t"
  "v_mfma_f32_16x16x32_bf16 v[80:83], a[248:251], v[128:131], 0\n\t"
  "ds_read_b128 a[232:235], v40 offset:6144\n\t"
  "v_accvgpr_read_b32 v120, a24\n\t"
  "v_mfma_f32_16x16x32_bf16 v[84:87], a[248:251], v[132:135], 0\n\t"
  "buffer_load_dwordx4 v40, s[56:59], s29 offen offset:0 sc1 lds\n\t"
  "v_mfma_f32_16x16x32_bf16 v[88:91], a[252:255], v[128:131], 0\n\t"
  "ds_read_b128 a[236:239], v40 offset:7168\n\t"
  "v_accvgpr_read_b32 v121, a25\n\t"
  "v_mfma_f32_16x16x32_bf16 v[92:95], a[252:255], v[132:135], 0\n\t"
  "s_waitcnt lgkmcnt(2)\n\t"
  "v_accvgpr_read_b32 v122, a26\n\t"
  "v_accvgpr_read_b32 v123, a27\n\t"
  "v_mfma_f32_16x16x32_bf16 v[96:99], a[224:227], v[128:131], 0\n\t"
  "v_accvgpr_read_b32 v124, a28\n\t"
  "v_accvgpr_read_b32 v125, a29\n\t"
  "v_mfma_f32_16x16x32_bf16 v[100:103], a[224:227], v[132:135], 0\n\t"
  "buffer_store_dwordx4 a[16:19], v40, s[56:59], s28 offen offset:2048 sc1\n\t"
  "v_mfma_f32_16x16x32_bf16 v[104:107], a[228:231], v[128:131], 0\n\t"
  "ds_read_b128 a[240:243], v40 offset:8192\n\t"
  "v_accvgpr_read_b32 v126, a30\n\t"
  "v_mfma_f32_16x16x32_bf16 v[108:111], a[228:231], v[132:135], 0\n\t"
  "s_waitcnt vmcnt(13)\n\t"
  "v_accvgpr_read_b32 v127, a31\n\t"
  "v_mul_f32 v120, v120, v50\n\t"
  "v_mfma_f32_16x16x32_bf16 a[112:115], a[196:199], a[96:99], a[112:115]\n\t"
  "ds_read_b128 a[244:247], v40 offset:9216\n\t"
  "v_mul_f32 v121, v121, v50\n\t"
  "v_mfma_f32_16x16x32_bf16 a[116:119], a[196:199], a[100:103], a[116:119]\n\t"
  "s_waitcnt lgkmcnt(2)\n\t"
  "v_mul_f32 v122, v122, v50\n\t"
  "v_mul_f32 v123, v123, v50\n\t"
  "v_mfma_f32_16x16x32_bf16 v[112:115], a[232:235], v[128:131], 0\n\t"
  "v_mul_f32 v124, v124, v51\n\t"
  "v_mul_f32 v125, v125, v51\n\t"
  "v_mfma_f32_16x16x32_bf16 v[116:119], a[232:235], v[132:135], 0\n\t"
  "buffer_load_dwordx4 v40, s[56:59], s29 offen offset:1024 sc1 lds\n\t"
  "v_mfma_f32_16x16x32_bf16 v[64:67], a[236:239], v[136:139], v[64:67]\n\t"
  "ds_read_b128 a[248:251], v40 offset:10240\n\t"
  "v_mul_f32 v126, v126, v51\n\t"
  "v_mfma_f32_16x16x32_bf16 v[68:71], a[236:239], v[140:143], v[68:71]\n\t"
  "v_mul_f32 v127, v127, v51\n\t"
  "v_cvt_pk_bf16_f32 v120, v120, v121\n\t"
  "v_mfma_f32_16x16x32_bf16 a[120:123], a[200:203], a[96:99], a[120:123]\n\t"
  "ds_read_b128 a[252:255], v40 offset:11264\n\t"
  "v_cvt_pk_bf16_f32 v121, v122, v123\n\t"
  "v_mfma_f32_16x16x32_bf16 a[124:127], a[200:203], a[100:103], a[124:127]\n\t"
  "s_waitcnt lgkmcnt(2)\n\t"
  "v_cvt_pk_bf16_f32 v122, v124, v125\n\t"
  "v_cvt_pk_bf16_f32 v123, v126, v127\n\t"
  "v_mfma_f32_16x16x32_bf16 v[72:75], a[240:243], v[136:139], v[72:75]\n\t"
  "v_accvgpr_write_b32 a24, v120\n\t"
  "v_accvgpr_write_b32 a25, v121\n\t"
  "v_mfma_f32_16x16x32_bf16 v[76:79], a[240:243], v[140:143], v[76:79]\n\t"
  "buffer_load_dwordx4 v40, s[56:59], s29 offen offset:2048 sc1 lds\n\t"
  "v_mfma_f32_16x16x32_bf16 v[80:83], a[244:247], v[136:139], v[80:83]\n\t"
  "ds_read_b128 a[224:227], v40 offset:12288\n\t"
  "v_accvgpr_write_b32 a26, v122\n\t"
  "v_mfma_f32_16x16x32_bf16 v[84:87], a[244:247], v[140:143], v[84:87]\n\t"
  "s_waitcnt vmcnt(11)\n\t"
  "v_accvgpr_write_b32 a27, v123\n\t"
  "v_accvgpr_read_b32 v56, a32\n\t"
  "v_mfma_f32_16x16x32_bf16 a[128:131], a[204:207], a[96:99], a[128:131]\n\t"
  "ds_read_b128 a[228:231], v40 offset:13312\n\t"
  "v_accvgpr_read_b32 v57, a33\n\t"
  "v_mfma_f32_16x16x32_bf16 a[132:135], a[204:207], a[100:103], a[132:135]\n\t"
  "s_waitcnt lgkmcnt(2)\n\t"
  "v_accvgpr_read_b32 v58, a34\n\t"
  "v_accvgpr_read_b32 v59, a35\n\t"
  "v_mfma_f32_16x16x32_bf16 v[88:91], a[248:251], v[136:139], v[88:91]\n\t"
  "v_accvgpr_read_b32 v60, a36\n\t"
  "v_accvgpr_read_b32 v61, a37\n\t"
  "v_mfma_f32_16x16x32_bf16 v[92:95], a[248:251], v[140:143], v[92:95]\n\t"
  "buffer_load_dwordx4 v40, s[56:59], s29 offen offset:3072 sc1 lds\n\t"
  "v_mfma_f32_16x16x32_bf16 v[96:99], a[252:255], v[136:139], v[96:99]\n\t"
  "s_add_u32 s29, s29, 0x1000\n\t"
  "ds_read_b128 a[232:235], v40 offset:14336\n\t"
  "v_accvgpr_read_b32 v62, a38\n\t"
  "v_mfma_f32_16x16x32_bf16 v[100:103], a[252:255], v[140:143], v[100:103]\n\t"
  "v_accvgpr_read_b32 v63, a39\n\t"
  "v_mul_f32 v56, v56, v50\n\t"
  "v_mfma_f32_16x16x32_bf16 a[136:139], a[208:211], a[96:99], a[136:139]\n\t"
  "ds_read_b128 a[236:239], v40 offset:15360\n\t"
  "v_mul_f32 v57, v57, v50\n\t"
  "v_mfma_f32_16x16x32_bf16 a[140:143], a[208:211], a[100:103], a[140:143]\n\t"
  "s_waitcnt lgkmcnt(2)\n\t"
  "v_mul_f32 v58, v58, v50\n\t"
  "v_mul_f32 v59, v59, v50\n\t"
  "v_mfma_f32_16x16x32_bf16 v[104:107], a[224:227], v[136:139], v[104:107]\n\t"
  "v_mul_f32 v60, v60, v51\n\t"
  "v_mul_f32 v61, v61, v51\n\t"
  "v_mfma_f32_16x16x32_bf16 v[108:111], a[224:227], v[140:143], v[108:111]\n\t"
  "buffer_store_dwordx4 a[24:27], v40, s[56:59], s28 offen offset:3072 sc1\n\t"
  "v_mfma_f32_16x16x32_bf16 v[112:115], a[228:231], v[136:139], v[112:115]\n\t"
  "ds_read_b128 a[240:243], v40 offset:16384\n\t"
  "v_mul_f32 v62, v62, v51\n\t"
  "v_mfma_f32_16x16x32_bf16 v[116:119], a[228:231], v[140:143], v[116:119]\n\t"
  "s_waitcnt vmcnt(7)\n\t"
  "v_mul_f32 v63, v63, v51\n\t"
  "v_cvt_pk_bf16_f32 v56, v56, v57\n\t"
  "v_mfma_f32_16x16x32_bf16 a[144:147], a[212:215], a[96:99], a[144:147]\n\t"
  "s_add_u32 m0, s87, 0x11000\n\t"
  "ds_read_b128 a[244:247], v40 offset:17408\n\t"
  "v_cvt_pk_bf16_f32 v57, v58, v59\n\t"
  "v_mfma_f32_16x16x32_bf16 a[148:151], a[212:215], a[100:103], a[148:151]\n\t"
  "s_waitcnt lgkmcnt(2)\n\t"
  "v_cvt_pk_bf16_f32 v58, v60, v61\n\t"
  "v_cvt_pk_bf16_f32 v59, v62, v63\n\t"
  "v_mfma_f32_16x16x32_bf16 v[64:67], a[232:235], v[144:147], v[64:67]\n\t"
  "v_accvgpr_write_b32 a32, v56\n\t"
  "v_accvgpr_write_b32 a33, v57\n\t"
  "v_mfma_f32_16x16x32_bf16 v[68:71], a[232:235], v[148:151], v[68:71]\n\t"
  "buffer_load_dwordx4 v40, s[56:59], s29 offen offset:0 sc1 lds\n\t"
  "v_mfma_f32_16x16x32_bf16 v[72:75], a[236:239], v[144:147], v[72:75]\n\t"
  "ds_read_b128 a[248:251], v40 offset:18432\n\t"
  "v_accvgpr_write_b32 a34, v58\n\t"
  "v_mfma_f32_16x16x32_bf16 v[76:79], a[236:239], v[148:151], v[76:79]\n\t"
  "v_accvgpr_write_b32 a35, v59\n\t"
  "v_accvgpr_read_b32 v120, a40\n\t"
  "v_mfma_f32_16x16x32_bf16 a[152:155], a[216:219], a[96:99], a[152:155]\n\t"
  "ds_read_b128 a[252:255], v40 offset:19456\n\t"
  "v_accvgpr_read_b32 v121, a41\n\t"
  "v_mfma_f32_16x16x32_bf16 a[156:159], a[216:219], a[100:103], a[156:159]\n\t"
  "s_waitcnt lgkmcnt(2)\n\t"
  "v_accvgpr_read_b32 v122, a42\n\t"
  "v_accvgpr_read_b32 v123, a43\n\t"
  "v_mfma_f32_16x16x32_bf16 v[80:83], a[240:243], v[144:147], v[80:83]\n\t"
  "ds_read_b128 a[224:227], v40 offset:20480\n\t"
  "v_accvgpr_read_b32 v124, a44\n\t"
  "v_mfma_f32_16x16x32_bf16 v[84:87], a[240:243], v[148:151], v[84:87]\n\t"
  "buffer_load_dwordx4 v40, s[56:59], s29 offen offset:1024 sc1 lds\n\t"
  "v_mfma_f32_16x16x32_bf16 v[88:91], a[244:247], v[144:147], v[88:91]\n\t"
  "ds_read_b128 a[228:231], v40 offset:21504\n\t"
  "v_accvgpr_read_b32 v125, a45\n\t"
  "v_mfma_f32_16x16x32_bf16 v[92:95], a[244:247], v[148:151], v[92:95]\n\t"
  "v_accvgpr_read_b32 v126, a46\n\t"
  "v_accvgpr_read_b32 v127, a47\n\t"
  "v_mfma_f32_16x16x32_bf16 a[160:163], a[220:223], a[96:99], a[160:163]\n\t"
  "ds_read_b128 a[232:235], v40 offset:22528\n\t"
  "v_mul_f32 v120, v120, v50\n\t"
  "v_mfma_f32_16x16x32_bf16 a[164:167], a[220:223], a[100:103], a[164:167]\n\t"
  "s_waitcnt lgkmcnt(3)\n\t"
  "v_mul_f32 v121, v121, v50\n\t"
  "v_mul_f32 v122, v122, v50\n\t"
  "v_mfma_f32_16x16x32_bf16 v[96:99], a[248:251], v[144:147], v[96:99]\n\t"
  "ds_read_b128 a[236:239], v40 offset:23552\n\t"
  "v_mul_f32 v123, v123, v50\n\t"
  "v_mfma_f32_16x16x32_bf16 v[100:103], a[248:251], v[148:151], v[100:103]\n\t"
  "buffer_store_dwordx4 a[32:35], v40, s[56:59], s76 offen offset:0 sc1\n\t"
  "v_mfma_f32_16x16x32_bf16 v[104:107], a[252:255], v[144:147], v[104:107]\n\t"
  "ds_read_b128 a[240:243], v40 offset:24576\n\t"
  "v_mul_f32 v124, v124, v51\n\t"
  "v_mfma_f32_16x16x32_bf16 v[108:111], a[252:255], v[148:151], v[108:111]\n\t"
  "s_waitcnt lgkmcnt(3)\n\t"
  "v_mul_f32 v125, v125, v51\n\t"
  "v_mul_f32 v126, v126, v51\n\t"
  "v_mfma_f32_16x16x32_bf16 v[112:115], a[224:227], v[144:147], v[112:115]\n\t"
  "ds_read_b128 a[244:247], v40 offset:25600\n\t"
  "v_mul_f32 v127, v127, v51\n\t"
  "v_mfma_f32_16x16x32_bf16 v[116:119], a[224:227], v[148:151], v[116:119]\n\t"
  "buffer_load_dwordx4 v40, s[56:59], s29 offen offset:2048 sc1 lds\n\t"
  "v_mfma_f32_16x16x32_bf16 v[64:67], a[228:231], v[152:155], v[64:67]\n\t"
  "s_add_u32 s29, s29, 0x6000\n\t"
  "ds_read_b128 a[248:251], v40 offset:26624\n\t"
  "v_cvt_pk_bf16_f32 v120, v120, v121\n\t"
  "v_mfma_f32_16x16x32_bf16 v[68:71], a[228:231], v[156:159], v[68:71]\n\t"
  "s_waitcnt lgkmcnt(3)\n\t"
  "v_cvt_pk_bf16_f32 v121, v122, v123\n\t"
  "v_cvt_pk_bf16_f32 v122, v124, v125\n\t"
  "v_mfma_f32_16x16x32_bf16 v[72:75], a[232:235], v[152:155], v[72:75]\n\t"
  "s_waitcnt vmcnt(13)\n\t"
  "ds_read_b128 a[252:255], v40 offset:27648\n\t"
  "v_cvt_pk_bf16_f32 v123, v126, v127\n\t"
  "v_mfma_f32_16x16x32_bf16 v[76:79], a[232:235], v[156:159], v[76:79]\n\t"
  "s_barrier\n\t"
  "v_accvgpr_write_b32 a40, v120\n\t"
  "v_accvgpr_write_b32 a41, v121\n\t"
  "v_mfma_f32_16x16x32_bf16 v[80:83], a[236:239], v[152:155], v[80:83]\n\t"
  "ds_read_b128 a[224:227], v40 offset:32768\n\t"
  "v_accvgpr_write_b32 a42, v122\n\t"
  "v_mfma_f32_16x16x32_bf16 v[84:87], a[236:239], v[156:159], v[84:87]\n\t"
  "s_waitcnt lgkmcnt(3)\n\t"
  "v_accvgpr_write_b32 a43, v123\n\t"
  "v_accvgpr_read_b32 v56, a48\n\t"
  "v_mfma_f32_16x16x32_bf16 v[88:91], a[240:243], v[152:155], v[88:91]\n\t"
  "ds_read_b128 a[228:231], v40 offset:33792\n\t"
  "v_accvgpr_read_b32 v57, a49\n\t"
  "v_mfma_f32_16x16x32_bf16 v[92:95], a[240:243], v[156:159], v[92:95]\n\t"
  "v_accvgpr_read_b32 v58, a50\n\t"
  "v_accvgpr_read_b32 v59, a51\n\t"
  "v_mfma_f32_16x16x32_bf16 v[96:99], a[244:247], v[152:155], v[96:99]\n\t"
  "ds_read_b128 a[232:235], v40 offset:34816\n\t"
  "v_accvgpr_read_b32 v60, a52\n\t"
  "v_mfma_f32_16x16x32_bf16 v[100:103], a[244:247], v[156:159], v[100:103]\n\t"
  "s_waitcnt lgkmcnt(3)\n\t"
  "v_accvgpr_read_b32 v61, a53\n\t"
  "v_accvgpr_read_b32 v62, a54\n\t"
  "v_mfma_f32_16x16x32_bf16 v[104:107], a[248:251], v[152:155], v[104:107]\n\t"
  "ds_read_b128 a[236:239], v40 offset:35840\n\t"
  "v_accvgpr_read_b32 v63, a55\n\t"
  "v_mfma_f32_16x16x32_bf16 v[108:111], a[248:251], v[156:159], v[108:111]\n\t"
  "buffer_store_dwordx4 a[40:43], v40, s[56:59], s76 offen offset:1024 sc1\n\t"
  "v_mfma_f32_16x16x32_bf16 v[112:115], a[252:255], v[152:155], v[112:115]\n\t"
  "ds_read_b128 a[240:243], v40 offset:36864\n\t"
  "v_mul_f32 v56, v56, v50\n\t"
  "v_mfma_f32_16x16x32_bf16 v[116:119], a[252:255], v[156:159], v[116:119]\n\t"
  "s_waitcnt lgkmcnt(3)\n\t"
  "v_mul_f32 v57, v57, v50\n\t"
  "v_mul_f32 v58, v58, v50\n\t"
  "v_mfma_f32_16x16x32_bf16 v[64:67], a[224:227], v[160:163], v[64:67]\n\t"
  "ds_read_b128 a[244:247], v40 offset:37888\n\t"
  "v_mul_f32 v59, v59, v50\n\t"
  "v_mfma_f32_16x16x32_bf16 v[68:71], a[224:227], v[164:167], v[68:71]\n\t"
  "v_mul_f32 v60, v60, v51\n\t"
  "v_mul_f32 v61, v61, v51\n\t"
  "v_mfma_f32_16x16x32_bf16 v[72:75], a[228:231], v[160:163], v[72:75]\n\t"
  "s_cbranch_execz TRAMP_%=\n\t"
  "s_add_u32 m0, s87, 0x18000\n\t"
  "ds_read_b128 a[248:251], v40 offset:38912\n\t"
  "v_mul_f32 v62, v62, v51\n\t"
  "v_mfma_f32_16x16x32_bf16 v[76:79], a[228:231], v[164:167], v[76:79]\n\t"
  "s_waitcnt lgkmcnt(3)\n\t"
  "v_mul_f32 v63, v63, v51\n\t"
  "v_cvt_pk_bf16_f32 v56, v56, v57\n\t"
  "v_mfma_f32_16x16x32_bf16 v[80:83], a[232:235], v[160:163], v[80:83]\n\t"
  "ds_read_b128 a[252:255], v40 offset:39936\n\t"
  "v_cvt_pk_bf16_f32 v57, v58, v59\n\t"
  "v_mfma_f32_16x16x32_bf16 v[84:87], a[232:235], v[164:167], v[84:87]\n\t"
  "buffer_load_dwordx4 v40, s[56:59], s29 offen offset:0 sc1 lds\n\t"
  "v_mfma_f32_16x16x32_bf16 v[88:91], a[236:239], v[160:163], v[88:91]\n\t"
  "ds_read_b128 a[224:227], v40 offset:40960\n\t"
  "v_cvt_pk_bf16_f32 v58, v60, v61\n\t"
  "v_mfma_f32_16x16x32_bf16 v[92:95], a[236:239], v[164:167], v[92:95]\n\t"
  "s_waitcnt lgkmcnt(3)\n\t"
  "v_cvt_pk_bf16_f32 v59, v62, v63\n\t"
  "v_accvgpr_write_b32 a48, v56\n\t"
  "v_mfma_f32_16x16x32_bf16 v[96:99], a[240:243], v[160:163], v[96:99]\n\t"
  "ds_read_b128 a[228:231], v40 offset:41984\n\t"
  "v_accvgpr_write_b32 a49, v57\n\t"
  "v_mfma_f32_16x16x32_bf16 v[100:103], a[240:243], v[164:167], v[100:103]\n\t"
  "buffer_load_dwordx4 v40, s[56:59], s29 offen offset:1024 sc1 lds\n\t"
  "v_mfma_f32_16x16x32_bf16 v[104:107], a[244:247], v[160:163], v[104:107]\n\t"
  "ds_read_b128 a[232:235], v40 offset:43008\n\t"
  "v_accvgpr_write_b32 a50, v58\n\t"
  "v_mfma_f32_16x16x32_bf16 v[108:111], a[244:247], v[164:167], v[108:111]\n\t"
  "s_waitcnt lgkmcnt(3)\n\t"
  "v_accvgpr_write_b32 a51, v59\n\t"
  "v_mfma_f32_16x16x32_bf16 v[112:115], a[248:251], v[160:163], v[112:115]\n\t"
  "ds_read_b128 a[236:239], v40 offset:44032\n\t"
  "v_mfma_f32_16x16x32_bf16 v[116:119], a[248:251], v[164:167], v[116:119]\n\t"
  "buffer_load_dwordx4 v40, s[56:59], s29 offen offset:2048 sc1 lds\n\t"
  "v_mfma_f32_16x16x32_bf16 v[64:67], a[252:255], v[168:171], v[64:67]\n\t"
  "ds_read_b128 a[240:243], v40 offset:45056\n\t"
  "v_mfma_f32_16x16x32_bf16 v[68:71], a[252:255], v[172:175], v[68:71]\n\t"
  "s_waitcnt lgkmcnt(3)\n\t"
  "v_mfma_f32_16x16x32_bf16 v[72:75], a[224:227], v[168:171], v[72:75]\n\t"
  "ds_read_b128 a[244:247], v40 offset:46080\n\t"
  "v_mfma_f32_16x16x32_bf16 v[76:79], a[224:227], v[172:175], v[76:79]\n\t"
  "v_mfma_f32_16x16x32_bf16 v[80:83], a[228:231], v[168:171], v[80:83]\n\t"
  "ds_read_b128 a[248:251], v40 offset:47104\n\t"
  "v_mfma_f32_16x16x32_bf16 v[84:87], a[228:231], v[172:175], v[84:87]\n\t"
  "s_waitcnt lgkmcnt(3)\n\t"
  "v_mfma_f32_16x16x32_bf16 v[88:91], a[232:235], v[168:171], v[88:91]\n\t"
  "ds_read_b128 a[252:255], v40 offset:48128\n\t"
  "v_mfma_f32_16x16x32_bf16 v[92:95], a[232:235], v[172:175], v[92:95]\n\t"
  "buffer_store_dwordx4 a[48:51], v40, s[56:59], s76 offen offset:2048 sc1\n\t"
  "v_mfma_f32_16x16x32_bf16 v[96:99], a[236:239], v[168:171], v[96:99]\n\t"
  "ds_read_b128 a[224:227], v40 offset:49152\n\t"
  "v_mfma_f32_16x16x32_bf16 v[100:103], a[236:239], v[172:175], v[100:103]\n\t"
  "s_waitcnt lgkmcnt(3)\n\t"
  "v_mfma_f32_16x16x32_bf16 v[104:107], a[240:243], v[168:171], v[104:107]\n\t"
  "ds_read_b128 a[228:231], v40 offset:50176\n\t"
  "v_mfma_f32_16x16x32_bf16 v[108:111], a[240:243], v[172:175], v[108:111]\n\t"
  "buffer_load_dwordx4 v40, s[56:59], s29 offen offset:3072 sc1 lds\n\t"
  "v_mfma_f32_16x16x32_bf16 v[112:115], a[244:247], v[168:171], v[112:115]\n\t"
  "s_add_u32 s29, s29, 0x1000\n\t"
  "ds_read_b128 a[232:235], v40 offset:51200\n\t"
  "v_mfma_f32_16x16x32_bf16 v[116:119], a[244:247], v[172:175], v[116:119]\n\t"
  "s_waitcnt lgkmcnt(3)\n\t"
  "v_mfma_f32_16x16x32_bf16 v[64:67], a[248:251], v[176:179], v[64:67]\n\t"
  "s_add_u32 m0, s87, 0x19000\n\t"
  "ds_read_b128 a[236:239], v40 offset:52224\n\t"
  "v_mfma_f32_16x16x32_bf16 v[68:71], a[248:251], v[180:183], v[68:71]\n\t"
  "buffer_load_dwordx4 v40, s[56:59], s29 offen offset:0 sc1 lds\n\t"
  "v_mfma_f32_16x16x32_bf16 v[72:75], a[252:255], v[176:179], v[72:75]\n\t"
  "ds_read_b128 a[240:243], v40 offset:53248\n\t"
  "v_mfma_f32_16x16x32_bf16 v[76:79], a[252:255], v[180:183], v[76:79]\n\t"
  "s_waitcnt lgkmcnt(3)\n\t"
  "v_mfma_f32_16x16x32_bf16 v[80:83], a[224:227], v[176:179], v[80:83]\n\t"
  "ds_read_b128 a[244:247], v40 offset:54272\n\t"
  "v_mfma_f32_16x16x32_bf16 v[84:87], a[224:227], v[180:183], v[84:87]\n\t"
  "v_mfma_f32_16x16x32_bf16 v[88:91], a[228:231], v[176:179], v[88:91]\n\t"
  "ds_read_b128 a[248:251], v40 offset:55296\n\t"
  "v_mfma_f32_16x16x32_bf16 v[92:95], a[228:231], v[180:183], v[92:95]\n\t"
  "s_waitcnt lgkmcnt(3)\n\t"
  "v_mfma_f32_16x16x32_bf16 v[96:99], a[232:235], v[176:179], v[96:99]\n\t"
  "ds_read_b128 a[252:255], v40 offset:56320\n\t"
  "v_mfma_f32_16x16x32_bf16 v[100:103], a[232:235], v[180:183], v[100:103]\n\t"
  "buffer_load_dwordx4 v40, s[56:59], s29 offen offset:1024 sc1 lds\n\t"
  "v_mfma_f32_16x16x32_bf16 v[104:107], a[236:239], v[176:179], v[104:107]\n\t"
  "ds_read_b128 a[224:227], v40 offset:57344\n\t"
  "v_mfma_f32_16x16x32_bf16 v[108:111], a[236:239], v[180:183], v[108:111]\n\t"
  "s_waitcnt lgkmcnt(3)\n\t"
  "v_mfma_f32_16x16x32_bf16 v[112:115], a[240:243], v[176:179], v[112:115]\n\t"
  "ds_read_b128 a[228:231], v40 offset:58368\n\t"
  "v_mfma_f32_16x16x32_bf16 v[116:119], a[240:243], v[180:183], v[116:119]\n\t"
  "buffer_load_dwordx4 v40, s[56:59], s29 offen offset:2048 sc1 lds\n\t"
  "v_mfma_f32_16x16x32_bf16 v[64:67], a[244:247], v[184:187], v[64:67]\n\t"
  "s_add_u32 s29, s29, 0x6000\n\t"
  "ds_read_b128 a[232:235], v40 offset:59392\n\t"
  "v_mfma_f32_16x16x32_bf16 v[68:71], a[244:247], v[188:191], v[68:71]\n\t"
  "s_waitcnt lgkmcnt(3)\n\t"
  "v_mfma_f32_16x16x32_bf16 v[72:75], a[248:251], v[184:187], v[72:75]\n\t"
  "s_waitcnt vmcnt(9)\n\t"
  "ds_read_b128 a[236:239], v40 offset:60416\n\t"
  "v_mfma_f32_16x16x32_bf16 v[76:79], a[248:251], v[188:191], v[76:79]\n\t"
  "s_barrier\n\t"
  "v_mfma_f32_16x16x32_bf16 v[80:83], a[252:255], v[184:187], v[80:83]\n\t"
  "ds_read_b128 a[240:243], v42 offset:0\n\t"
  "v_mfma_f32_16x16x32_bf16 v[84:87], a[252:255], v[188:191], v[84:87]\n\t"
  "s_waitcnt lgkmcnt(3)\n\t"
  "v_mfma_f32_16x16x32_bf16 v[88:91], a[224:227], v[184:187], v[88:91]\n\t"
  "ds_read_b128 a[244:247], v42 offset:1024\n\t"
  "v_mfma_f32_16x16x32_bf16 v[92:95], a[224:227], v[188:191], v[92:95]\n\t"
  "v_mfma_f32_16x16x32_bf16 v[96:99], a[228:231], v[184:187], v[96:99]\n\t"
  "ds_read_b128 a[248:251], v42 offset:2048\n\t"
  "v_mfma_f32_16x16x32_bf16 v[100:103], a[228:231], v[188:191], v[100:103]\n\t"
  "s_waitcnt lgkmcnt(3)\n\t"
  "v_mfma_f32_16x16x32_bf16 v[104:107], a[232:235], v[184:187], v[104:107]\n\t"
  "ds_read_b128 a[252:255], v42 offset:3072\n\t"
  "v_mfma_f32_16x16x32_bf16 v[108:111], a[232:235], v[188:191], v[108:111]\n\t"
  "v_mfma_f32_16x16x32_bf16 v[112:115], a[236:239], v[184:187], v[112:115]\n\t"
  "ds_read_b128 a[224:227], v42 offset:4096\n\t"
  "v_mfma_f32_16x16x32_bf16 v[116:119], a[236:239], v[188:191], v[116:119]\n\t"
  "s_waitcnt lgkmcnt(3)\n\t"
  "v_mfma_f32_16x16x32_bf16 v[64:67], a[240:243], v[192:195], v[64:67]\n\t"
  "ds_read_b128 a[228:231], v42 offset:5120\n\t"
  "v_mfma_f32_16x16x32_bf16 v[68:71], a[240:243], v[196:199], v[68:71]\n\t"
  "v_mfma_f32_16x16x32_bf16 v[72:75], a[244:247], v[192:195], v[72:75]\n\t"
  "s_add_u32 s29, s72, 2\n\t"
  "ds_read_b128 a[232:235], v42 offset:6144\n\t"
  "v_mfma_f32_16x16x32_bf16 v[76:79], a[244:247], v[196:199], v[76:79]\n\t"
  "s_waitcnt lgkmcnt(3)\n\t"
  "v_mfma_f32_16x16x32_bf16 v[80:83], a[248:251], v[192:195], v[80:83]\n\t"
  "s_and_b32 s29, s29, 3\n\t"
  "ds_read_b128 a[236:239], v42 offset:7168\n\t"
  "s_mul_i32 s29, s29, 0x1c000\n\t"
  "s_add_u32 s29, s29, s77\n\t"
  "s_add_u32 m0, s87, 0\n\t"
  "v_mfma_f32_16x16x32_bf16 v[84:87], a[248:251], v[196:199], v[84:87]\n\t"
  "buffer_load_dwordx4 v40, s[56:59], s29 offen offset:0 sc1 lds\n\t"
  "v_mfma_f32_16x16x32_bf16 v[88:91], a[252:255], v[192:195], v[88:91]\n\t"
  "ds_read_b128 a[240:243], v42 offset:8192\n\t"
  "v_mfma_f32_16x16x32_bf16 v[92:95], a[252:255], v[196:199], v[92:95]\n\t"
  "s_waitcnt lgkmcnt(3)\n\t"
  "v_mfma_f32_16x16x32_bf16 v[96:99], a[224:227], v[192:195], v[96:99]\n\t"
  "ds_read_b128 a[244:247], v42 offset:9216\n\t"
  "v_mfma_f32_16x16x32_bf16 v[100:103], a[224:227], v[196:199], v[100:103]\n\t"
  "buffer_load_dwordx4 v40, s[56:59], s29 offen offset:1024 sc1 lds\n\t"
  "v_mfma_f32_16x16x32_bf16 v[104:107], a[228:231], v[192:195], v[104:107]\n\t"
  "ds_read_b128 a[248:251], v42 offset:10240\n\t"
  "v_mfma_f32_16x16x32_bf16 v[108:111], a[228:231], v[196:199], v[108:111]\n\t"
  "s_waitcnt lgkmcnt(3)\n\t"
  "v_mfma_f32_16x16x32_bf16 v[112:115], a[232:235], v[192:195], v[112:115]\n\t"
  "ds_read_b128 a[252:255], v42 offset:11264\n\t"
  "v_mfma_f32_16x16x32_bf16 v[116:119], a[232:235], v[196:199], v[116:119]\n\t"
  "buffer_load_dwordx4 v40, s[56:59], s29 offen offset:2048 sc1 lds\n\t"
  "v_mfma_f32_16x16x32_bf16 v[64:67], a[236:239], v[200:203], v[64:67]\n\t"
  "ds_read_b128 a[224:227], v42 offset:12288\n\t"
  "v_mfma_f32_16x16x32_bf16 v[68:71], a[236:239], v[204:207], v[68:71]\n\t"
  "s_waitcnt lgkmcnt(3)\n\t"
  "v_mfma_f32_16x16x32_bf16 v[72:75], a[240:243], v[200:203], v[72:75]\n\t"
  "ds_read_b128 a[228:231], v42 offset:13312\n\t"
  "v_mfma_f32_16x16x32_bf16 v[76:79], a[240:243], v[204:207], v[76:79]\n\t"
  "v_mfma_f32_16x16x32_bf16 v[80:83], a[244:247], v[200:203], v[80:83]\n\t"
  "ds_read_b128 a[232:235], v42 offset:14336\n\t"
  "v_mfma_f32_16x16x32_bf16 v[84:87], a[244:247], v[204:207], v[84:87]\n\t"
  "s_waitcnt lgkmcnt(3)\n\t"
  "v_mfma_f32_16x16x32_bf16 v[88:91], a[248:251], v[200:203], v[88:91]\n\t"
  "ds_read_b128 a[236:239], v42 offset:15360\n\t"
  "v_mfma_f32_16x16x32_bf16 v[92:95], a[248:251], v[204:207], v[92:95]\n\t"
  "buffer_load_dwordx4 v40, s[56:59], s29 offen offset:3072 sc1 lds\n\t"
  "v_mfma_f32_16x16x32_bf16 v[96:99], a[252:255], v[200:203], v[96:99]\n\t"
  "s_add_u32 s29, s29, 0x1000\n\t"
  "ds_read_b128 a[240:243], v42 offset:16384\n\t"
  "v_mfma_f32_16x16x32_bf16 v[100:103], a[252:255], v[204:207], v[100:103]\n\t"
  "s_waitcnt lgkmcnt(3)\n\t"
  "v_mfma_f32_16x16x32_bf16 v[104:107], a[224:227], v[200:203], v[104:107]\n\t"
  "s_add_u32 m0, s87, 0x1000\n\t"
  "ds_read_b128 a[244:247], v42 offset:17408\n\t"
  "v_mfma_f32_16x16x32_bf16 v[108:111], a[224:227], v[204:207], v[108:111]\n\t"
  "buffer_load_dwordx4 v40, s[56:59], s29 offen offset:0 sc1 lds\n\t"
  "v_mfma_f32_16x16x32_bf16 v[112:115], a[228:231], v[200:203], v[112:115]\n\t"
  "ds_read_b128 a[248:251], v42 offset:18432\n\t"
  "v_mfma_f32_16x16x32_bf16 v[116:119], a[228:231], v[204:207], v[116:119]\n\t"
  "s_waitcnt lgkmcnt(3)\n\t"
  "v_mfma_f32_16x16x32_bf16 v[64:67], a[232:235], v[208:211], v[64:67]\n\t"
  "ds_read_b128 a[252:255], v42 offset:19456\n\t"
  "v_mfma_f32_16x16x32_bf16 v[68:71], a[232:235], v[212:215], v[68:71]\n\t"
  "v_mfma_f32_16x16x32_bf16 v[72:75], a[236:239], v[208:211], v[72:75]\n\t"
  "ds_read_b128 a[224:227], v42 offset:20480\n\t"
  "v_mfma_f32_16x16x32_bf16 v[76:79], a[236:239], v[212:215], v[76:79]\n\t"
  "s_waitcnt lgkmcnt(3)\n\t"
  "v_mfma_f32_16x16x32_bf16 v[80:83], a[240:243], v[208:211], v[80:83]\n\t"
  "ds_read_b128 a[228:231], v42 offset:21504\n\t"
  "v_mfma_f32_16x16x32_bf16 v[84:87], a[240:243], v[212:215], v[84:87]\n\t"
  "buffer_load_dwordx4 v40, s[56:59], s29 offen offset:1024 sc1 lds\n\t"
  "v_mfma_f32_16x16x32_bf16 v[88:91], a[244:247], v[208:211], v[88:91]\n\t"
  "ds_read_b128 a[232:235], v42 offset:22528\n\t"
  "v_mfma_f32_16x16x32_bf16 v[92:95], a[244:247], v[212:215], v[92:95]\n\t"
  "s_waitcnt lgkmcnt(3)\n\t"
  "v_mfma_f32_16x16x32_bf16 v[96:99], a[248:251], v[208:211], v[96:99]\n\t"
  "ds_read_b128 a[236:239], v42 offset:23552\n\t"
  "v_mfma_f32_16x16x32_bf16 v[100:103], a[248:251], v[212:215], v[100:103]\n\t"
  "buffer_load_dwordx4 v40, s[56:59], s29 offen offset:2048 sc1 lds\n\t"
  "v_mfma_f32_16x16x32_bf16 v[104:107], a[252:255], v[208:211], v[104:107]\n\t"
  "s_add_u32 s29, s29, 0x6000\n\t"
  "ds_read_b128 a[240:243], v42 offset:24576\n\t"
  "v_mfma_f32_16x16x32_bf16 v[108:111], a[252:255], v[212:215], v[108:111]\n\t"
  "s_waitcnt lgkmcnt(3)\n\t"
  "v_mfma_f32_16x16x32_bf16 v[112:115], a[224:227], v[208:211], v[112:115]\n\t"
  "s_add_u32 s27, s72, 0\n\t"
  "ds_read_b128 a[244:247], v42 offset:25600\n\t"
  "v_mfma_f32_16x16x32_bf16 v[116:119], a[224:227], v[212:215], v[116:119]\n\t"
  "s_and_b32 s27, s27, 3\n\t"
  "v_mfma_f32_16x16x32_bf16 v[64:67], a[228:231], v[216:219], v[64:67]\n\t"
  "s_lshl_b32 s27, s27, 7\n\t"
  "ds_read_b128 a[248:251], v42 offset:26624\n\t"
  "v_mfma_f32_16x16x32_bf16 v[68:71], a[228:231], v[220:223], v[68:71]\n\t"
  "s_waitcnt lgkmcnt(3)\n\t"
  "v_mfma_f32_16x16x32_bf16 v[72:75], a[232:235], v[216:219], v[72:75]\n\t"
  "s_waitcnt vmcnt(7)\n\t"
  "ds_read_b128 a[252:255], v42 offset:27648\n\t"
  "v_mfma_f32_16x16x32_bf16 v[76:79], a[232:235], v[220:223], v[76:79]\n\t"
  "s_add_u32 s27, s27, s13\n\t"
  "buffer_store_dwordx2 v[8:9], v12, s[56:59], s27 offen offset:0 sc1\n\t"
  "v_mfma_f32_16x16x32_bf16 v[80:83], a[236:239], v[216:219], v[80:83]\n\t"
  "s_barrier\n\t"
  "ds_read_b128 a[224:227], v42 offset:32768\n\t"
  "v_mfma_f32_16x16x32_bf16 v[84:87], a[236:239], v[220:223], v[84:87]\n\t"
  "s_waitcnt lgkmcnt(3)\n\t"
  "v_mfma_f32_16x16x32_bf16 v[88:91], a[240:243], v[216:219], v[88:91]\n\t"
  "ds_read_b128 a[228:231], v42 offset:39936\n\t"
  "v_mfma_f32_16x16x32_bf16 v[92:95], a[240:243], v[220:223], v[92:95]\n\t"
  "v_mfma_f32_16x16x32_bf16 v[96:99], a[244:247], v[216:219], v[96:99]\n\t"
  "ds_read_b128 a[232:235], v42 offset:47104\n\t"
  "v_mfma_f32_16x16x32_bf16 v[100:103], a[244:247], v[220:223], v[100:103]\n\t"
  "s_waitcnt lgkmcnt(3)\n\t"
  "v_mfma_f32_16x16x32_bf16 v[104:107], a[248:251], v[216:219], v[104:107]\n\t"
  "ds_read_b128 a[236:239], v42 offset:54272\n\t"
  "v_mfma_f32_16x16x32_bf16 v[108:111], a[248:251], v[220:223], v[108:111]\n\t"
  "v_mfma_f32_16x16x32_bf16 v[112:115], a[252:255], v[216:219], v[112:115]\n\t"
  "ds_read_b128 a[240:243], v42 offset:33792\n\t"
  "v_mfma_f32_16x16x32_bf16 v[116:119], a[252:255], v[220:223], v[116:119]\n\t"
  "s_waitcnt lgkmcnt(3)\n\t"
  "v_mfma_f32_16x16x32_bf16 v[64:67], a[224:227], v[224:227], v[64:67]\n\t"
  "s_add_u32 m0, s87, 0x8000\n\t"
  "ds_read_b128 a[244:247], v42 offset:40960\n\t"
  "v_mfma_f32_16x16x32_bf16 v[68:71], a[224:227], v[228:231], v[68:71]\n\t"
  "v_mfma_f32_16x16x32_bf16 v[64:67], a[228:231], v[232:235], v[64:67]\n\t"
  "ds_read_b128 a[248:251], v42 offset:48128\n\t"
  "v_mfma_f32_16x16x32_bf16 v[68:71], a[228:231], v[236:239], v[68:71]\n\t"
  "s_waitcnt lgkmcnt(3)\n\t"
  "buffer_load_dwordx4 v40, s[56:59], s29 offen offset:0 sc1 lds\n\t"
  "v_mfma_f32_16x16x32_bf16 v[64:67], a[232:235], v[240:243], v[64:67]\n\t"
  "ds_read_b128 a[252:255], v42 offset:55296\n\t"
  "v_mfma_f32_16x16x32_bf16 v[68:71], a[232:235], v[244:247], v[68:71]\n\t"
  "v_mfma_f32_16x16x32_bf16 v[64:67], a[236:239], v[248:251], v[64:67]\n\t"
  "ds_read_b128 a[224:227], v42 offset:34816\n\t"
  "v_mfma_f32_16x16x32_bf16 v[68:71], a[236:239], v[252:255], v[68:71]\n\t"
  "s_waitcnt lgkmcnt(3)\n\t"
  "buffer_load_dwordx4 v40, s[56:59], s29 offen offset:1024 sc1 lds\n\t"
  "v_mfma_f32_16x16x32_bf16 v[72:75], a[240:243], v[224:227], v[72:75]\n\t"
  "ds_read_b128 a[228:231], v42 offset:41984\n\t"
  "v_mfma_f32_16x16x32_bf16 v[76:79], a[240:243], v[228:231], v[76:79]\n\t"
  "v_mfma_f32_16x16x32_bf16 v[72:75], a[244:247], v[232:235], v[72:75]\n\t"
  "ds_read_b128 a[232:235], v42 offset:49152\n\t"
  "v_mul_f32 v64, v64, v50\n\t"
  "v_mfma_f32_16x16x32_bf16 v[76:79], a[244:247], v[236:239], v[76:79]\n\t"
  "s_waitcnt lgkmcnt(3)\n\t"
  "v_mul_f32 v65, v65, v50\n\t"
  "v_mul_f32 v66, v66, v50\n\t"
  "v_mfma_f32_16x16x32_bf16 v[72:75], a[248:251], v[240:243], v[72:75]\n\t"
  "ds_read_b128 a[236:239], v42 offset:56320\n\t"
  "v_mul_f32 v67, v67, v50\n\t"
  "v_mfma_f32_16x16x32_bf16 v[76:79], a[248:251], v[244:247], v[76:79]\n\t"
  "buffer_load_dwordx4 v40, s[56:59], s29 offen offset:2048 sc1 lds\n\t"
  "v_mfma_f32_16x16x32_bf16 v[72:75], a[252:255], v[248:251], v[72:75]\n\t"
  "ds_read_b128 a[240:243], v42 offset:35840\n\t"
  "v_mul_f32 v68, v68, v51\n\t"
  "v_mfma_f32_16x16x32_bf16 v[76:79], a[252:255], v[252:255], v[76:79]\n\t"
  "s_waitcnt lgkmcnt(3)\n\t"
  "v_mul_f32 v69, v69, v51\n\t"
  "v_mul_f32 v70, v70, v51\n\t"
  "v_mfma_f32_16x16x32_bf16 v[80:83], a[224:227], v[224:227], v[80:83]\n\t"
  "ds_read_b128 a[244:247], v42 offset:43008\n\t"
  "v_mul_f32 v71, v71, v51\n\t"
  "v_mfma_f32_16x16x32_bf16 v[84:87], a[224:227], v[228:231], v[84:87]\n\t"
  "v_cvt_pk_bf16_f32 v64, v64, v65\n\t"
  "v_cvt_pk_bf16_f32 v65, v66, v67\n\t"
  "v_mfma_f32_16x16x32_bf16 v[80:83], a[228:231], v[232:235], v[80:83]\n\t"
  "ds_read_b128 a[248:251], v42 offset:50176\n\t"
  "v_cvt_pk_bf16_f32 v66, v68, v69\n\t"
  "v_mfma_f32_16x16x32_bf16 v[84:87], a[228:231], v[236:239], v[84:87]\n\t"
  "s_waitcnt lgkmcnt(3)\n\t"
  "buffer_load_dwordx4 v40, s[56:59], s29 offen offset:3072 sc1 lds\n\t"
  "v_mfma_f32_16x16x32_bf16 v[80:83], a[232:235], v[240:243], v[80:83]\n\t"
  "s_add_u32 s29, s29, 0x1000\n\t"
  "ds_read_b128 a[252:255], v42 offset:57344\n\t"
  "s_add_u32 s75, s72, 1\n\t"
  "s_and_b32 s75, s75, 3\n\t"
  "s_mul_i32 s75, s75, 0x1c000\n\t"
  "s_add_u32 s75, s75, s86\n\t"
  "s_add_u32 s78, s75, 0x1000\n\t"
  "v_mfma_f32_16x16x32_bf16 v[84:87], a[232:235], v[244:247], v[84:87]\n\t"
  "v_cvt_pk_bf16_f32 v67, v70, v71\n\t"
  "v_mul_f32 v72, v72, v50\n\t"
  "v_mfma_f32_16x16x32_bf16 v[80:83], a[236:239], v[248:251], v[80:83]\n\t"
  "buffer_store_dwordx4 v[64:67], v40, s[56:59], s75 offen offset:0 sc1\n\t"
  "v_mfma_f32_16x16x32_bf16 v[84:87], a[236:239], v[252:255], v[84:87]\n\t"
  "s_waitcnt lgkmcnt(2)\n\t"
  "ds_read_b128 a[224:227], v42 offset:36864\n\t"
  "v_mul_f32 v73, v73, v50\n\t"
  "v_mfma_f32_16x16x32_bf16 v[88:91], a[240:243], v[224:227], v[88:91]\n\t"
  "s_add_u32 m0, s87, 0x9000\n\t"
  "ds_read_b128 a[228:231], v42 offset:44032\n\t"
  "v_mul_f32 v74, v74, v50\n\t"
  "v_mfma_f32_16x16x32_bf16 v[92:95], a[240:243], v[228:231], v[92:95]\n\t"
  "v_mul_f32 v75, v75, v50\n\t"
  "v_mul_f32 v76, v76, v51\n\t"
  "v_mfma_f32_16x16x32_bf16 v[88:91], a[244:247], v[232:235], v[88:91]\n\t"
  "ds_read_b128 a[232:235], v42 offset:51200\n\t"
  "v_mul_f32 v77, v77, v51\n\t"
  "v_mfma_f32_16x16x32_bf16 v[92:95], a[244:247], v[236:239], v[92:95]\n\t"
  "s_waitcnt lgkmcnt(3)\n\t"
  "buffer_load_dwordx4 v40, s[56:59], s29 offen offset:0 sc1 lds\n\t"
  "v_mfma_f32_16x16x32_bf16 v[88:91], a[248:251], v[240:243], v[88:91]\n\t"
  "ds_read_b128 a[236:239], v42 offset:58368\n\t"
  "v_mul_f32 v78, v78, v51\n\t"
  "v_mfma_f32_16x16x32_bf16 v[92:95], a[248:251], v[244:247], v[92:95]\n\t"
  "v_mul_f32 v79, v79, v51\n\t"
  "v_cvt_pk_bf16_f32 v72, v72, v73\n\t"
  "v_mfma_f32_16x16x32_bf16 v[88:91], a[252:255], v[248:251], v[88:91]\n\t"
  "ds_read_b128 a[240:243], v42 offset:37888\n\t"
  "v_cvt_pk_bf16_f32 v73, v74, v75\n\t"
  "v_mfma_f32_16x16x32_bf16 v[92:95], a[252:255], v[252:255], v[92:95]\n\t"
  "s_waitcnt lgkmcnt(3)\n\t"
  "v_cvt_pk_bf16_f32 v74, v76, v77\n\t"
  "v_cvt_pk_bf16_f32 v75, v78, v79\n\t"
  "v_mfma_f32_16x16x32_bf16 v[96:99], a[224:227], v[224:227], v[96:99]\n\t"
  "ds_read_b128 a[244:247], v42 offset:45056\n\t"
  "v_mul_f32 v80, v80, v50\n\t"
  "v_mfma_f32_16x16x32_bf16 v[100:103], a[224:227], v[228:231], v[100:103]\n\t"
  "buffer_load_dwordx4 v40, s[56:59], s29 offen offset:1024 sc1 lds\n\t"
  "v_mfma_f32_16x16x32_bf16 v[96:99], a[228:231], v[232:235], v[96:99]\n\t"
  "ds_read_b128 a[248:251], v42 offset:52224\n\t"
  "v_mul_f32 v81, v81, v50\n\t"
  "v_mfma_f32_16x16x32_bf16 v[100:103], a[228:231], v[236:239], v[100:103]\n\t"
  "s_waitcnt lgkmcnt(3)\n\t"
  "v_mul_f32 v82, v82, v50\n\t"
  "v_mul_f32 v83, v83, v50\n\t"
  "v_mfma_f32_16x16x32_bf16 v[96:99], a[232:235], v[240:243], v[96:99]\n\t"
  "ds_read_b128 a[252:255], v42 offset:59392\n\t"
  "v_mul_f32 v84, v84, v51\n\t"
  "v_mfma_f32_16x16x32_bf16 v[100:103], a[232:235], v[244:247], v[100:103]\n\t"
  "v_mul_f32 v85, v85, v51\n\t"
  "v_mul_f32 v86, v86, v51\n\t"
  "v_mfma_f32_16x16x32_bf16 v[96:99], a[236:239], v[248:251], v[96:99]\n\t"
  "ds_read_b128 a[224:227], v42 offset:38912\n\t"
  "v_mul_f32 v87, v87, v51\n\t"
  "v_mfma_f32_16x16x32_bf16 v[100:103], a[236:239], v[252:255], v[100:103]\n\t"
  "s_waitcnt lgkmcnt(3)\n\t"
  "buffer_store_dwordx4 v[72:75], v40, s[56:59], s75 offen offset:1024 sc1\n\t"
  "v_mfma_f32_16x16x32_bf16 v[104:107], a[240:243], v[224:227], v[104:107]\n\t"
  "ds_read_b128 a[228:231], v42 offset:46080\n\t"
  "v_cvt_pk_bf16_f32 v80, v80, v81\n\t"
  "v_mfma_f32_16x16x32_bf16 v[108:111], a[240:243], v[228:231], v[108:111]\n\t"
  "buffer_load_dwordx4 v40, s[56:59], s29 offen offset:2048 sc1 lds\n\t"
  "v_mfma_f32_16x16x32_bf16 v[104:107], a[244:247], v[232:235], v[104:107]\n\t"
  "s_add_u32 s29, s29, 0x6000\n\t"
  "ds_read_b128 a[232:235], v42 offset:53248\n\t"
  "v_cvt_pk_bf16_f32 v81, v82, v83\n\t"
  "v_mfma_f32_16x16x32_bf16 v[108:111], a[244:247], v[236:239], v[108:111]\n\t"
  "s_waitcnt lgkmcnt(3)\n\t"
  "v_cvt_pk_bf16_f32 v82, v84, v85\n\t"
  "v_cvt_pk_bf16_f32 v83, v86, v87\n\t"
  "v_mfma_f32_16x16x32_bf16 v[104:107], a[248:251], v[240:243], v[104:107]\n\t"
  "s_add_u32 s96, s96, 0x2000\n\t"
  "ds_read_b128 a[236:239], v42 offset:60416\n\t"
  "v_mul_f32 v88, v88, v50\n\t"
  "v_mfma_f32_16x16x32_bf16 v[108:111], a[248:251], v[244:247], v[108:111]\n\t"
  "s_addc_u32 s97, s97, 0\n\t"
  "v_mul_f32 v89, v89, v50\n\t"
  "v_mul_f32 v90, v90, v50\n\t"
  "v_mfma_f32_16x16x32_bf16 v[104:107], a[252:255], v[248:251], v[104:107]\n\t"
  "s_waitcnt vmcnt(10)\n\t"
  "v_mul_f32 v91, v91, v50\n\t"
  "v_mul_f32 v92, v92, v51\n\t"
  "v_mfma_f32_16x16x32_bf16 v[108:111], a[252:255], v[252:255], v[108:111]\n\t"
  "s_waitcnt lgkmcnt(2)\n\t"
  "global_load_dwordx4 v[32:35], v40, s[96:97] offset:-4096\n\t"
  "v_mfma_f32_16x16x32_bf16 v[112:115], a[224:227], v[224:227], v[112:115]\n\t"
  "s_barrier\n\t"
  "ds_read_b128 a[240:243], v40 offset:0\n\t"
  "ds_read_b128 a[244:247], v40 offset:1024\n\t"
  "v_mfma_f32_16x16x32_bf16 v[116:119], a[224:227], v[228:231], v[116:119]\n\t"
  "global_load_dwordx4 v[44:47], v40, s[96:97] offset:-3072\n\t"
  "v_mfma_f32_16x16x32_bf16 v[112:115], a[228:231], v[232:235], v[112:115]\n\t"
  "ds_read_b128 a[248:251], v40 offset:2048\n\t"
  "v_mul_f32 v93, v93, v51\n\t"
  "v_mfma_f32_16x16x32_bf16 v[116:119], a[228:231], v[236:239], v[116:119]\n\t"
  "s_waitcnt lgkmcnt(3)\n\t"
  "global_load_dwordx4 v[20:23], v40, s[96:97] offset:-2048\n\t"
  "v_mfma_f32_16x16x32_bf16 v[112:115], a[232:235], v[240:243], v[112:115]\n\t"
  "ds_read_b128 a[252:255], v40 offset:3072\n\t"
  "v_mul_f32 v94, v94, v51\n\t"
  "v_mfma_f32_16x16x32_bf16 v[116:119], a[232:235], v[244:247], v[116:119]\n\t"
  "global_load_dwordx4 v[24:27], v40, s[96:97] offset:-1024\n\t"
  "v_mfma_f32_16x16x32_bf16 v[112:115], a[236:239], v[248:251], v[112:115]\n\t"
  "ds_read_b128 a[224:227], v40 offset:4096\n\t"
  "v_mul_f32 v95, v95, v51\n\t"
  "v_mfma_f32_16x16x32_bf16 v[116:119], a[236:239], v[252:255], v[116:119]\n\t"
  "s_waitcnt lgkmcnt(3)\n\t"
  "v_cvt_pk_bf16_f32 v88, v88, v89\n\t"
  "v_cvt_pk_bf16_f32 v89, v90, v91\n\t"
  "v_mfma_f32_16x16x32_bf16 a[0:3], a[240:243], v[128:131], 0\n\t"
  "ds_read_b128 a[228:231], v40 offset:5120\n\t"
  "v_cvt_pk_bf16_f32 v90, v92, v93\n\t"
  "v_mfma_f32_16x16x32_bf16 a[4:7], a[240:243], v[132:135], 0\n\t"
  "v_cvt_pk_bf16_f32 v91, v94, v95\n\t"
  "v_mul_f32 v96, v96, v50\n\t"
  "v_mfma_f32_16x16x32_bf16 a[8:11], a[244:247], v[128:131], 0\n\t"
  "ds_read_b128 a[232:235], v40 offset:6144\n\t"
  "v_mul_f32 v97, v97, v50\n\t"
  "v_mfma_f32_16x16x32_bf16 a[12:15], a[244:247], v[132:135], 0\n\t"
  "s_waitcnt lgkmcnt(3)\n\t"
  "v_mul_f32 v98, v98, v50\n\t"
  "v_mul_f32 v99, v99, v50\n\t"
  "v_mfma_f32_16x16x32_bf16 a[16:19], a[248:251], v[128:131], 0\n\t"
  "s_sub_u32 s91, s19, 0\n\t"
  "ds_read_b128 a[236:239], v40 offset:7168\n\t"
  "v_mul_f32 v100, v100, v51\n\t"
  "v_mfma_f32_16x16x32_bf16 a[20:23], a[248:251], v[132:135], 0\n\t"
  "s_and_b32 s91, s91, 3\n\t"
  "buffer_store_dwordx4 v[80:83], v40, s[56:59], s75 offen offset:2048 sc1\n\t"
  "v_mfma_f32_16x16x32_bf16 a[24:27], a[252:255], v[128:131], 0\n\t"
  "s_lshl_b32 s91, s91, 3\n\t"
  "ds_read_b128 a[240:243], v40 offset:8192\n\t"
  "v_mul_f32 v101, v101, v51\n\t"
  "v_mfma_f32_16x16x32_bf16 a[28:31], a[252:255], v[132:135], 0\n\t"
  "s_waitcnt lgkmcnt(3)\n\t"
  "v_mul_f32 v102, v102, v51\n\t"
  "v_mul_f32 v103, v103, v51\n\t"
  "v_mfma_f32_16x16x32_bf16 a[32:35], a[224:227], v[128:131], 0\n\t"
  "s_add_u32 s91, s91, s15\n\t"
  "ds_read_b128 a[244:247], v40 offset:9216\n\t"
  "s_add_u32 m0, s87, 0x10000\n\t"
  "v_mfma_f32_16x16x32_bf16 a[36:39], a[224:227], v[132:135], 0\n\t"
  "buffer_load_dwordx4 v40, s[56:59], s29 offen offset:0 sc1 lds\n\t"
  "v_mfma_f32_16x16x32_bf16 a[40:43], a[228:231], v[128:131], 0\n\t"
  "ds_read_b128 a[248:251], v40 offset:10240\n\t"
  "v_cvt_pk_bf16_f32 v96, v96, v97\n\t"
  "v_mfma_f32_16x16x32_bf16 a[44:47], a[228:231], v[132:135], 0\n\t"
  "s_waitcnt lgkmcnt(3)\n\t"
  "buffer_load_dwordx2 v[48:49], v13, s[56:59], s91 offen offset:0 sc1\n\t"
  "v_mfma_f32_16x16x32_bf16 a[48:51], a[232:235], v[128:131], 0\n\t"
  "ds_read_b128 a[252:255], v40 offset:11264\n\t"
  "v_cvt_pk_bf16_f32 v97, v98, v99\n\t"
  "v_mfma_f32_16x16x32_bf16 a[52:55], a[232:235], v[132:135], 0\n\t"
  "buffer_load_dwordx4 v40, s[56:59], s29 offen offset:1024 sc1 lds\n\t"
  "v_mfma_f32_16x16x32_bf16 a[0:3], a[236:239], v[136:139], a[0:3]\n\t"
  "ds_read_b128 a[224:227], v40 offset:12288\n\t"
  "v_cvt_pk_bf16_f32 v98, v100, v101\n\t"
  "v_mfma_f32_16x16x32_bf16 a[4:7], a[236:239], v[140:143], a[4:7]\n\t"
  "s_waitcnt lgkmcnt(3)\n\t"
  "v_cvt_pk_bf16_f32 v99, v102, v103\n\t"
  "v_mul_f32 v104, v104, v50\n\t"
  "v_mfma_f32_16x16x32_bf16 a[8:11], a[240:243], v[136:139], a[8:11]\n\t"
  "ds_read_b128 a[228:231], v40 offset:13312\n\t"
  "v_mul_f32 v105, v105, v50\n\t"
  "v_mfma_f32_16x16x32_bf16 a[12:15], a[240:243], v[140:143], a[12:15]\n\t"
  "buffer_load_dwordx4 v40, s[56:59], s29 offen offset:2048 sc1 lds\n\t"
  "v_mfma_f32_16x16x32_bf16 a[16:19], a[244:247], v[136:139], a[16:19]\n\t"
  "ds_read_b128 a[232:235], v40 offset:14336\n\t"
  "v_mul_f32 v106, v106, v50\n\t"
  "v_mfma_f32_16x16x32_bf16 a[20:23], a[244:247], v[140:143], a[20:23]\n\t"
  "s_waitcnt lgkmcnt(3)\n\t"
  "v_mul_f32 v107, v107, v50\n\t"
  "v_mul_f32 v108, v108, v51\n\t"
  "v_mfma_f32_16x16x32_bf16 a[24:27], a[248:251], v[136:139], a[24:27]\n\t"
  "ds_read_b128 a[236:239], v40 offset:15360\n\t"
  "v_mul_f32 v109, v109, v51\n\t"
  "v_mfma_f32_16x16x32_bf16 a[28:31], a[248:251], v[140:143], a[28:31]\n\t"
  "buffer_store_dwordx4 v[88:91], v40, s[56:59], s75 offen offset:3072 sc1\n\t"
  "v_mfma_f32_16x16x32_bf16 a[32:35], a[252:255], v[136:139], a[32:35]\n\t"
  "ds_read_b128 a[240:243], v40 offset:16384\n\t"
  "v_mul_f32 v110, v110, v51\n\t"
  "v_mfma_f32_16x16x32_bf16 a[36:39], a[252:255], v[140:143], a[36:39]\n\t"
  "s_waitcnt lgkmcnt(3)\n\t"
  "v_mul_f32 v111, v111, v51\n\t"
  "v_cvt_pk_bf16_f32 v104, v104, v105\n\t"
  "v_mfma_f32_16x16x32_bf16 a[40:43], a[224:227], v[136:139], a[40:43]\n\t"
  "ds_read_b128 a[244:247], v40 offset:17408\n\t"
  "v_cvt_pk_bf16_f32 v105, v106, v107\n\t"
  "v_mfma_f32_16x16x32_bf16 a[44:47], a[224:227], v[140:143], a[44:47]\n\t"
  "buffer_load_dwordx4 v40, s[56:59], s29 offen offset:3072 sc1 lds\n\t"
  "v_mfma_f32_16x16x32_bf16 a[48:51], a[228:231], v[136:139], a[48:51]\n\t"
  "s_add_u32 s29, s29, 0x1000\n\t"
  "ds_read_b128 a[248:251], v40 offset:18432\n\t"
  "v_cvt_pk_bf16_f32 v106, v108, v109\n\t"
  "v_mfma_f32_16x16x32_bf16 a[52:55], a[228:231], v[140:143], a[52:55]\n\t"
  "s_waitcnt lgkmcnt(3)\n\t"
  "v_cvt_pk_bf16_f32 v107, v110, v111\n\t"
  "v_mul_f32 v112, v112, v50\n\t"
  "v_mfma_f32_16x16x32_bf16 a[0:3], a[232:235], v[144:147], a[0:3]\n\t"
  "s_add_u32 m0, s87, 0x11000\n\t"
  "ds_read_b128 a[252:255], v40 offset:19456\n\t"
  "v_mul_f32 v113, v113, v50\n\t"
  "v_mfma_f32_16x16x32_bf16 a[4:7], a[232:235], v[148:151], a[4:7]\n\t"
  "buffer_load_dwordx4 v40, s[56:59], s29 offen offset:0 sc1 lds\n\t"
  "v_mfma_f32_16x16x32_bf16 a[8:11], a[236:239], v[144:147], a[8:11]\n\t"
  "ds_read_b128 a[224:227], v40 offset:20480\n\t"
  "v_mul_f32 v114, v114, v50\n\t"
  "v_mfma_f32_16x16x32_bf16 a[12:15], a[236:239], v[148:151], a[12:15]\n\t"
  "s_waitcnt lgkmcnt(3)\n\t"
  "v_mul_f32 v115, v115, v50\n\t"
  "v_mul_f32 v116, v116, v51\n\t"
  "v_mfma_f32_16x16x32_bf16 a[16:19], a[240:243], v[144:147], a[16:19]\n\t"
  "ds_read_b128 a[228:231], v40 offset:21504\n\t"
  "v_mul_f32 v117, v117, v51\n\t"
  "v_mfma_f32_16x16x32_bf16 a[20:23], a[240:243], v[148:151], a[20:23]\n\t"
  "buffer_load_dwordx4 v40, s[56:59], s29 offen offset:1024 sc1 lds\n\t"
  "v_mfma_f32_16x16x32_bf16 a[24:27], a[244:247], v[144:147], a[24:27]\n\t"
  "ds_read_b128 a[232:235], v40 offset:22528\n\t"
  "v_mul_f32 v118, v118, v51\n\t"
  "v_mfma_f32_16x16x32_bf16 a[28:31], a[244:247], v[148:151], a[28:31]\n\t"
  "s_waitcnt lgkmcnt(3)\n\t"
  "v_mul_f32 v119, v119, v51\n\t"
  "v_cvt_pk_bf16_f32 v112, v112, v113\n\t"
  "v_mfma_f32_16x16x32_bf16 a[32:35], a[248:251], v[144:147], a[32:35]\n\t"
  "ds_read_b128 a[236:239], v40 offset:23552\n\t"
  "v_cvt_pk_bf16_f32 v113, v114, v115\n\t"
  "v_mfma_f32_16x16x32_bf16 a[36:39], a[248:251], v[148:151], a[36:39]\n\t"
  "buffer_store_dwordx4 v[96:99], v40, s[56:59], s78 offen offset:0 sc1\n\t"
  "v_mfma_f32_16x16x32_bf16 a[40:43], a[252:255], v[144:147], a[40:43]\n\t"
  "ds_read_b128 a[240:243], v40 offset:24576\n\t"
  "v_cvt_pk_bf16_f32 v114, v116, v117\n\t"
  "v_mfma_f32_16x16x32_bf16 a[44:47], a[252:255], v[148:151], a[44:47]\n\t"
  "s_waitcnt lgkmcnt(3)\n\t"
  "v_cvt_pk_bf16_f32 v115, v118, v119\n\t"
  "v_mfma_f32_16x16x32_bf16 a[48:51], a[224:227], v[144:147], a[48:51]\n\t"
  "ds_read_b128 a[244:247], v40 offset:25600\n\t"
  "v_mfma_f32_16x16x32_bf16 a[52:55], a[224:227], v[148:151], a[52:55]\n\t"
  "buffer_load_dwordx4 v40, s[56:59], s29 offen offset:2048 sc1 lds\n\t"
  "v_mfma_f32_16x16x32_bf16 a[0:3], a[228:231], v[152:155], a[0:3]\n\t"
  "s_add_u32 s29, s29, 0x6000\n\t"
  "ds_read_b128 a[248:251], v40 offset:26624\n\t"
  "v_mfma_f32_16x16x32_bf16 a[4:7], a[228:231], v[156:159], a[4:7]\n\t"
  "s_waitcnt lgkmcnt(3)\n\t"
  "v_mfma_f32_16x16x32_bf16 a[8:11], a[232:235], v[152:155], a[8:11]\n\t"
  "s_waitcnt vmcnt(15)\n\t"
  "ds_read_b128 a[252:255], v40 offset:27648\n\t"
  "v_mfma_f32_16x16x32_bf16 a[12:15], a[232:235], v[156:159], a[12:15]\n\t"
  "s_barrier\n\t"
  "v_mfma_f32_16x16x32_bf16 a[16:19], a[236:239], v[152:155], a[16:19]\n\t"
  "ds_read_b128 a[224:227], v40 offset:32768\n\t"
  "v_mfma_f32_16x16x32_bf16 a[20:23], a[236:239], v[156:159], a[20:23]\n\t"
  "s_waitcnt lgkmcnt(3)\n\t"
  "v_mfma_f32_16x16x32_bf16 a[24:27], a[240:243], v[152:155], a[24:27]\n\t"
  "ds_read_b128 a[228:231], v40 offset:33792\n\t"
  "v_mfma_f32_16x16x32_bf16 a[28:31], a[240:243], v[156:159], a[28:31]\n\t"
  "v_mfma_f32_16x16x32_bf16 a[32:35], a[244:247], v[152:155], a[32:35]\n\t"
  "ds_read_b128 a[232:235], v40 offset:34816\n\t"
  "v_mfma_f32_16x16x32_bf16 a[36:39], a[244:247], v[156:159], a[36:39]\n\t"
  "s_waitcnt lgkmcnt(3)\n\t"
  "v_mfma_f32_16x16x32_bf16 a[40:43], a[248:251], v[152:155], a[40:43]\n\t"
  "ds_read_b128 a[236:239], v40 offset:35840\n\t"
  "v_mfma_f32_16x16x32_bf16 a[44:47], a[248:251], v[156:159], a[44:47]\n\t"
  "buffer_store_dwordx4 v[104:107], v40, s[56:59], s78 offen offset:1024 sc1\n\t"
  "v_mfma_f32_16x16x32_bf16 a[48:51], a[252:255], v[152:155], a[48:51]\n\t"
  "ds_read_b128 a[240:243], v40 offset:36864\n\t"
  "v_mfma_f32_16x16x32_bf16 a[52:55], a[252:255], v[156:159], a[52:55]\n\t"
  "s_waitcnt lgkmcnt(3)\n\t"
  "v_mfma_f32_16x16x32_bf16 a[0:3], a[224:227], v[160:163], a[0:3]\n\t"
  "s_waitcnt vmcnt(9)\n\t"
  "ds_read_b128 a[244:247], v40 offset:37888\n\t"
  "v_cmp_ne_u64 vcc, v[48:49], v[8:9]\n\t"
  "v_mfma_f32_16x16x32_bf16 a[4:7], a[224:227], v[164:167], a[4:7]\n\t"
  "v_mfma_f32_16x16x32_bf16 a[8:11], a[228:231], v[160:163], a[8:11]\n\t"
  "s_cbranch_vccnz SPINS0_%=\n\t"
  "SRETS0_%=:\n\t"
  "v_mfma_f32_16x16x32_bf16 a[12:15], a[228:231], v[164:167], a[12:15]\n\t"
  "s_waitcnt lgkmcnt(2)\n\t"
  "ds_read_b128 a[248:251], v40 offset:38912\n\t"
  "v_mfma_f32_16x16x32_bf16 a[16:19], a[232:235], v[160:163], a[16:19]\n\t"
  "s_sub_u32 s22, s19, 0\n\t"
  "ds_read_b128 a[252:255], v40 offset:39936\n\t"
  "v_mfma_f32_16x16x32_bf16 a[20:23], a[232:235], v[164:167], a[20:23]\n\t"
  "s_and_b32 s22, s22, 3\n\t"
  "v_mfma_f32_16x16x32_bf16 a[24:27], a[236:239], v[160:163], a[24:27]\n\t"
  "s_mul_i32 s22, s22, 0x1c00\n\t"
  "s_nop 0\n\t"
  "s_cbranch_execz TRAMP_%=\n\t"
  "ds_read_b128 a[224:227], v40 offset:40960\n\t"
  "v_mfma_f32_16x16x32_bf16 a[28:31], a[236:239], v[164:167], a[28:31]\n\t"
  "s_add_u32 s22, s22, s16\n\t"
  "s_waitcnt lgkmcnt(3)\n\t"
  "buffer_load_dwordx4 a[56:59], v40, s[56:59], s22 offen offset:0 sc1\n\t"
  "v_mfma_f32_16x16x32_bf16 a[32:35], a[240:243], v[160:163], a[32:35]\n\t"
  "s_add_u32 s23, s22, 0x7000\n\t"
  "ds_read_b128 a[228:231], v40 offset:41984\n\t"
  "s_add_u32 s24, s23, 0x7000\n\t"
  "s_add_u32 s25, s24, 0x7000\n\t"
  "s_add_u32 m0, s87, 0x18000\n\t"
  "v_mfma_f32_16x16x32_bf16 a[36:39], a[240:243], v[164:167], a[36:39]\n\t"
  "buffer_load_dwordx4 v40, s[56:59], s29 offen offset:0 sc1 lds\n\t"
  "v_mfma_f32_16x16x32_bf16 a[40:43], a[244:247], v[160:163], a[40:43]\n\t"
  "ds_read_b128 a[232:235], v40 offset:43008\n\t"
  "v_mfma_f32_16x16x32_bf16 a[44:47], a[244:247], v[164:167], a[44:47]\n\t"
  "s_waitcnt lgkmcnt(3)\n\t"
  "v_mfma_f32_16x16x32_bf16 a[48:51], a[248:251], v[160:163], a[48:51]\n\t"
  "ds_read_b128 a[236:239], v40 offset:44032\n\t"
  "v_mfma_f32_16x16x32_bf16 a[52:55], a[248:251], v[164:167], a[52:55]\n\t"
  "buffer_load_dwordx4 v40, s[56:59], s29 offen offset:1024 sc1 lds\n\t"
  "v_mfma_f32_16x16x32_bf16 a[0:3], a[252:255], v[168:171], a[0:3]\n\t"
  "ds_read_b128 a[240:243], v40 offset:45056\n\t"
  "v_mfma_f32_16x16x32_bf16 a[4:7], a[252:255], v[172:175], a[4:7]\n\t"
  "s_waitcnt lgkmcnt(3)\n\t"
  "v_mfma_f32_16x16x32_bf16 a[8:11], a[224:227], v[168:171], a[8:11]\n\t"
  "ds_read_b128 a[244:247], v40 offset:46080\n\t"
  "v_mfma_f32_16x16x32_bf16 a[12:15], a[224:227], v[172:175], a[12:15]\n\t"
  "buffer_store_dwordx4 v[112:115], v40, s[56:59], s78 offen offset:2048 sc1\n\t"
  "v_mfma_f32_16x16x32_bf16 a[16:19], a[228:231], v[168:171], a[16:19]\n\t"
  "ds_read_b128 a[248:251], v40 offset:47104\n\t"
  "v_mfma_f32_16x16x32_bf16 a[20:23], a[228:231], v[172:175], a[20:23]\n\t"
  "s_waitcnt lgkmcnt(3)\n\t"
  "v_mfma_f32_16x16x32_bf16 a[24:27], a[232:235], v[168:171], a[24:27]\n\t"
  "ds_read_b128 a[252:255], v40 offset:48128\n\t"
  "v_mfma_f32_16x16x32_bf16 a[28:31], a[232:235], v[172:175], a[28:31]\n\t"
  "buffer_load_dwordx4 a[60:63], v10, s[56:59], s22 offen offset:1024 sc1\n\t"
  "v_mfma_f32_16x16x32_bf16 a[32:35], a[236:239], v[168:171], a[32:35]\n\t"
  "ds_read_b128 a[224:227], v40 offset:49152\n\t"
  "v_mfma_f32_16x16x32_bf16 a[36:39], a[236:239], v[172:175], a[36:39]\n\t"
  "s_waitcnt lgkmcnt(3)\n\t"
  "v_mfma_f32_16x16x32_bf16 a[40:43], a[240:243], v[168:171], a[40:43]\n\t"
  "ds_read_b128 a[228:231], v40 offset:50176\n\t"
  "v_mfma_f32_16x16x32_bf16 a[44:47], a[240:243], v[172:175], a[44:47]\n\t"
  "buffer_load_dwordx4 v40, s[56:59], s29 offen offset:2048 sc1 lds\n\t"
  "v_mfma_f32_16x16x32_bf16 a[48:51], a[244:247], v[168:171], a[48:51]\n\t"
  "ds_read_b128 a[232:235], v40 offset:51200\n\t"
  "v_mfma_f32_16x16x32_bf16 a[52:55], a[244:247], v[172:175], a[52:55]\n\t"
  "s_waitcnt lgkmcnt(3)\n\t"
  "v_mfma_f32_16x16x32_bf16 a[0:3], a[248:251], v[176:179], a[0:3]\n\t"
  "ds_read_b128 a[236:239], v40 offset:52224\n\t"
  "v_mfma_f32_16x16x32_bf16 a[4:7], a[248:251], v[180:183], a[4:7]\n\t"
  "buffer_load_dwordx4 v40, s[56:59], s29 offen offset:3072 sc1 lds\n\t"
  "v_mfma_f32_16x16x32_bf16 a[8:11], a[252:255], v[176:179], a[8:11]\n\t"
  "s_add_u32 s29, s29, 0x1000\n\t"
  "ds_read_b128 a[240:243], v40 offset:53248\n\t"
  "v_mfma_f32_16x16x32_bf16 a[12:15], a[252:255], v[180:183], a[12:15]\n\t"
  "s_waitcnt lgkmcnt(3)\n\t"
  "v_mfma_f32_16x16x32_bf16 a[16:19], a[224:227], v[176:179], a[16:19]\n\t"
  "ds_read_b128 a[244:247], v40 offset:54272\n\t"
  "v_mfma_f32_16x16x32_bf16 a[20:23], a[224:227], v[180:183], a[20:23]\n\t"
  "buffer_load_dwordx4 a[64:67], v40, s[56:59], s23 offen offset:0 sc1\n\t"
  "v_mfma_f32_16x16x32_bf16 a[24:27], a[228:231], v[176:179], a[24:27]\n\t"
  "s_add_u32 m0, s87, 0x19000\n\t"
  "ds_read_b128 a[248:251], v40 offset:55296\n\t"
  "v_mfma_f32_16x16x32_bf16 a[28:31], a[228:231], v[180:183], a[28:31]\n\t"
  "s_waitcnt lgkmcnt(3)\n\t"
  "v_mfma_f32_16x16x32_bf16 a[32:35], a[232:235], v[176:179], a[32:35]\n\t"
  "ds_read_b128 a[252:255], v40 offset:56320\n\t"
  "v_mfma_f32_16x16x32_bf16 a[36:39], a[232:235], v[180:183], a[36:39]\n\t"
  "buffer_load_dwordx4 v40, s[56:59], s29 offen offset:0 sc1 lds\n\t"
  "v_mfma_f32_16x16x32_bf16 a[40:43], a[236:239], v[176:179], a[40:43]\n\t"
  "ds_read_b128 a[224:227], v40 offset:57344\n\t"
  "v_mfma_f32_16x16x32_bf16 a[44:47], a[236:239], v[180:183], a[44:47]\n\t"
  "s_waitcnt lgkmcnt(3)\n\t"
  "v_mfma_f32_16x16x32_bf16 a[48:51], a[240:243], v[176:179], a[48:51]\n\t"
  "ds_read_b128 a[228:231], v40 offset:58368\n\t"
  "v_mfma_f32_16x16x32_bf16 a[52:55], a[240:243], v[180:183], a[52:55]\n\t"
  "buffer_load_dwordx4 v40, s[56:59], s29 offen offset:1024 sc1 lds\n\t"
  "v_mfma_f32_16x16x32_bf16 a[0:3], a[244:247], v[184:187], a[0:3]\n\t"
  "ds_read_b128 a[232:235], v40 offset:59392\n\t"
  "v_mfma_f32_16x16x32_bf16 a[4:7], a[244:247], v[188:191], a[4:7]\n\t"
  "s_waitcnt lgkmcnt(3)\n\t"
  "v_mfma_f32_16x16x32_bf16 a[8:11], a[248:251], v[184:187], a[8:11]\n\t"
  "s_waitcnt vmcnt(11)\n\t"
  "ds_read_b128 a[236:239], v40 offset:60416\n\t"
  "v_mfma_f32_16x16x32_bf16 a[12:15], a[248:251], v[188:191], a[12:15]\n\t"
  "s_barrier\n\t"
  "buffer_load_dwordx4 v40, s[56:59], s29 offen offset:2048 sc1 lds\n\t"
  "v_mfma_f32_16x16x32_bf16 a[16:19], a[252:255], v[184:187], a[16:19]\n\t"
  "s_add_u32 s29, s29, 0x6000\n\t"
  "ds_read_b128 a[240:243], v42 offset:0\n\t"
  "v_mfma_f32_16x16x32_bf16 a[20:23], a[252:255], v[188:191], a[20:23]\n\t"
  "s_waitcnt lgkmcnt(3)\n\t"
  "v_mfma_f32_16x16x32_bf16 a[24:27], a[224:227], v[184:187], a[24:27]\n\t"
  "ds_read_b128 a[244:247], v42 offset:1024\n\t"
  "v_mfma_f32_16x16x32_bf16 a[28:31], a[224:227], v[188:191], a[28:31]\n\t"
  "buffer_load_dwordx4 a[68:71], v10, s[56:59], s23 offen offset:1024 sc1\n\t"
  "v_mfma_f32_16x16x32_bf16 a[32:35], a[228:231], v[184:187], a[32:35]\n\t"
  "ds_read_b128 a[248:251], v42 offset:2048\n\t"
  "v_mfma_f32_16x16x32_bf16 a[36:39], a[228:231], v[188:191], a[36:39]\n\t"
  "s_waitcnt lgkmcnt(3)\n\t"
  "v_mfma_f32_16x16x32_bf16 a[40:43], a[232:235], v[184:187], a[40:43]\n\t"
  "ds_read_b128 a[252:255], v42 offset:3072\n\t"
  "v_mfma_f32_16x16x32_bf16 a[44:47], a[232:235], v[188:191], a[44:47]\n\t"
  "v_mfma_f32_16x16x32_bf16 a[48:51], a[236:239], v[184:187], a[48:51]\n\t"
  "ds_read_b128 a[224:227], v42 offset:4096\n\t"
  "v_mfma_f32_16x16x32_bf16 a[52:55], a[236:239], v[188:191], a[52:55]\n\t"
  "s_waitcnt lgkmcnt(3)\n\t"
  "v_mfma_f32_16x16x32_bf16 a[0:3], a[240:243], v[192:195], a[0:3]\n\t"
  "ds_read_b128 a[228:231], v42 offset:5120\n\t"
  "v_mfma_f32_16x16x32_bf16 a[4:7], a[240:243], v[196:199], a[4:7]\n\t"
  "v_mfma_f32_16x16x32_bf16 a[8:11], a[244:247], v[192:195], a[8:11]\n\t"
  "ds_read_b128 a[232:235], v42 offset:6144\n\t"
  "v_mfma_f32_16x16x32_bf16 a[12:15], a[244:247], v[196:199], a[12:15]\n\t"
  "s_waitcnt lgkmcnt(3)\n\t"
  "v_mfma_f32_16x16x32_bf16 a[16:19], a[248:251], v[192:195], a[16:19]\n\t"
  "ds_read_b128 a[236:239], v42 offset:7168\n\t"
  "v_mfma_f32_16x16x32_bf16 a[20:23], a[248:251], v[196:199], a[20:23]\n\t"
  "buffer_load_dwordx4 a[72:75], v40, s[56:59], s24 offen offset:0 sc1\n\t"
  "v_mfma_f32_16x16x32_bf16 a[24:27], a[252:255], v[192:195], a[24:27]\n\t"
  "s_add_u32 s29, s72, 3\n\t"
  "ds_read_b128 a[240:243], v42 offset:8192\n\t"
  "v_mfma_f32_16x16x32_bf16 a[28:31], a[252:255], v[196:199], a[28:31]\n\t"
  "s_waitcnt lgkmcnt(3)\n\t"
  "v_mfma_f32_16x16x32_bf16 a[32:35], a[224:227], v[192:195], a[32:35]\n\t"
  "s_and_b32 s29, s29, 3\n\t"
  "ds_read_b128 a[244:247], v42 offset:9216\n\t"
  "s_mul_i32 s29, s29, 0x1c000\n\t"
  "s_add_u32 s29, s29, s77\n\t"
  "s_add_u32 m0, s87, 0\n\t"
  "v_mfma_f32_16x16x32_bf16 a[36:39], a[224:227], v[196:199], a[36:39]\n\t"
  "buffer_load_dwordx4 v40, s[56:59], s29 offen offset:0 sc1 lds\n\t"
  "v_mfma_f32_16x16x32_bf16 a[40:43], a[228:231], v[192:195], a[40:43]\n\t"
  "ds_read_b128 a[248:251], v42 offset:10240\n\t"
  "v_mfma_f32_16x16x32_bf16 a[44:47], a[228:231], v[196:199], a[44:47]\n\t"
  "s_waitcnt lgkmcnt(3)\n\t"
  "v_mfma_f32_16x16x32_bf16 a[48:51], a[232:235], v[192:195], a[48:51]\n\t"
  "ds_read_b128 a[252:255], v42 offset:11264\n\t"
  "v_mfma_f32_16x16x32_bf16 a[52:55], a[232:235], v[196:199], a[52:55]\n\t"
  "buffer_load_dwordx4 v40, s[56:59], s29 offen offset:1024 sc1 lds\n\t"
  "v_mfma_f32_16x16x32_bf16 a[0:3], a[236:239], v[200:203], a[0:3]\n\t"
  "ds_read_b128 a[224:227], v42 offset:12288\n\t"
  "v_mfma_f32_16x16x32_bf16 a[4:7], a[236:239], v[204:207], a[4:7]\n\t"
  "s_waitcnt lgkmcnt(3)\n\t"
  "v_mfma_f32_16x16x32_bf16 a[8:11], a[240:243], v[200:203], a[8:11]\n\t"
  "ds_read_b128 a[228:231], v42 offset:13312\n\t"
  "v_mfma_f32_16x16x32_bf16 a[12:15], a[240:243], v[204:207], a[12:15]\n\t"
  "buffer_load_dwordx4 v40, s[56:59], s29 offen offset:2048 sc1 lds\n\t"
  "v_mfma_f32_16x16x32_bf16 a[16:19], a[244:247], v[200:203], a[16:19]\n\t"
  "ds_read_b128 a[232:235], v42 offset:14336\n\t"
  "v_mfma_f32_16x16x32_bf16 a[20:23], a[244:247], v[204:207], a[20:23]\n\t"
  "s_waitcnt lgkmcnt(3)\n\t"
  "v_mfma_f32_16x16x32_bf16 a[24:27], a[248:251], v[200:203], a[24:27]\n\t"
  "ds_read_b128 a[236:239], v42 offset:15360\n\t"
  "v_mfma_f32_16x16x32_bf16 a[28:31], a[248:251], v[204:207], a[28:31]\n\t"
  "buffer_load_dwordx4 a[76:79], v10, s[56:59], s24 offen offset:1024 sc1\n\t"
  "v_mfma_f32_16x16x32_bf16 a[32:35], a[252:255], v[200:203], a[32:35]\n\t"
  "ds_read_b128 a[240:243], v42 offset:16384\n\t"
  "v_mfma_f32_16x16x32_bf16 a[36:39], a[252:255], v[204:207], a[36:39]\n\t"
  "s_waitcnt lgkmcnt(3)\n\t"
  "v_mfma_f32_16x16x32_bf16 a[40:43], a[224:227], v[200:203], a[40:43]\n\t"
  "ds_read_b128 a[244:247], v42 offset:17408\n\t"
  "v_mfma_f32_16x16x32_bf16 a[44:47], a[224:227], v[204:207], a[44:47]\n\t"
  "buffer_load_dwordx4 v40, s[56:59], s29 offen offset:3072 sc1 lds\n\t"
  "v_mfma_f32_16x16x32_bf16 a[48:51], a[228:231], v[200:203], a[48:51]\n\t"
  "s_add_u32 s29, s29, 0x1000\n\t"
  "ds_read_b128 a[248:251], v42 offset:18432\n\t"
  "v_mfma_f32_16x16x32_bf16 a[52:55], a[228:231], v[204:207], a[52:55]\n\t"
  "s_waitcnt lgkmcnt(3)\n\t"
  "v_mfma_f32_16x16x32_bf16 a[0:3], a[232:235], v[208:211], a[0:3]\n\t"
  "s_add_u32 m0, s87, 0x1000\n\t"
  "ds_read_b128 a[252:255], v42 offset:19456\n\t"
  "v_mfma_f32_16x16x32_bf16 a[4:7], a[232:235], v[212:215], a[4:7]\n\t"
  "buffer_load_dwordx4 v40, s[56:59], s29 offen offset:0 sc1 lds\n\t"
  "v_mfma_f32_16x16x32_bf16 a[8:11], a[236:239], v[208:211], a[8:11]\n\t"
  "ds_read_b128 a[224:227], v42 offset:20480\n\t"
  "v_mfma_f32_16x16x32_bf16 a[12:15], a[236:239], v[212:215], a[12:15]\n\t"
  "s_waitcnt lgkmcnt(3)\n\t"
  "v_mfma_f32_16x16x32_bf16 a[16:19], a[240:243], v[208:211], a[16:19]\n\t"
  "ds_read_b128 a[228:231], v42 offset:21504\n\t"
  "v_mfma_f32_16x16x32_bf16 a[20:23], a[240:243], v[212:215], a[20:23]\n\t"
  "buffer_load_dwordx4 a[80:83], v40, s[56:59], s25 offen offset:0 sc1\n\t"
  "v_mfma_f32_16x16x32_bf16 a[24:27], a[244:247], v[208:211], a[24:27]\n\t"
  "ds_read_b128 a[232:235], v42 offset:22528\n\t"
  "v_mfma_f32_16x16x32_bf16 a[28:31], a[244:247], v[212:215], a[28:31]\n\t"
  "s_waitcnt lgkmcnt(3)\n\t"
  "v_mfma_f32_16x16x32_bf16 a[32:35], a[248:251], v[208:211], a[32:35]\n\t"
  "ds_read_b128 a[236:239], v42 offset:23552\n\t"
  "v_mfma_f32_16x16x32_bf16 a[36:39], a[248:251], v[212:215], a[36:39]\n\t"
  "buffer_load_dwordx4 v40, s[56:59], s29 offen offset:1024 sc1 lds\n\t"
  "v_mfma_f32_16x16x32_bf16 a[40:43], a[252:255], v[208:211], a[40:43]\n\t"
  "ds_read_b128 a[240:243], v42 offset:24576\n\t"
  "v_mfma_f32_16x16x32_bf16 a[44:47], a[252:255], v[212:215], a[44:47]\n\t"
  "s_waitcnt lgkmcnt(3)\n\t"
  "v_mfma_f32_16x16x32_bf16 a[48:51], a[224:227], v[208:211], a[48:51]\n\t"
  "ds_read_b128 a[244:247], v42 offset:25600\n\t"
  "v_mfma_f32_16x16x32_bf16 a[52:55], a[224:227], v[212:215], a[52:55]\n\t"
  "buffer_load_dwordx4 v40, s[56:59], s29 offen offset:2048 sc1 lds\n\t"
  "v_mfma_f32_16x16x32_bf16 a[0:3], a[228:231], v[216:219], a[0:3]\n\t"
  "s_add_u32 s29, s29, 0x6000\n\t"
  "ds_read_b128 a[248:251], v42 offset:26624\n\t"
  "v_mfma_f32_16x16x32_bf16 a[4:7], a[228:231], v[220:223], a[4:7]\n\t"
  "s_waitcnt lgkmcnt(3)\n\t"
  "v_mfma_f32_16x16x32_bf16 a[8:11], a[232:235], v[216:219], a[8:11]\n\t"
  "s_waitcnt vmcnt(11)\n\t"
  "ds_read_b128 a[252:255], v42 offset:27648\n\t"
  "v_mfma_f32_16x16x32_bf16 a[12:15], a[232:235], v[220:223], a[12:15]\n\t"
  "s_add_u32 s27, s72, 1\n\t"
  "v_mfma_f32_16x16x32_bf16 a[16:19], a[236:239], v[216:219], a[16:19]\n\t"
  "s_and_b32 s27, s27, 3\n\t"
  "v_mfma_f32_16x16x32_bf16 a[20:23], a[236:239], v[220:223], a[20:23]\n\t"
  "s_waitcnt lgkmcnt(2)\n\t"
  "v_mfma_f32_16x16x32_bf16 a[24:27], a[240:243], v[216:219], a[24:27]\n\t"
  "s_lshl_b32 s27, s27, 7\n\t"
  "v_mfma_f32_16x16x32_bf16 a[28:31], a[240:243], v[220:223], a[28:31]\n\t"
  "s_add_u32 s27, s27, s13\n\t"
  "buffer_load_dwordx4 a[84:87], v10, s[56:59], s25 offen offset:1024 sc1\n\t"
  "v_mfma_f32_16x16x32_bf16 a[32:35], a[244:247], v[216:219], a[32:35]\n\t"
  "s_barrier\n\t"
  "ds_read_b128 a[224:227], v42 offset:32768\n\t"
  "ds_read_b128 a[228:231], v42 offset:39936\n\t"
  "v_mfma_f32_16x16x32_bf16 a[36:39], a[244:247], v[220:223], a[36:39]\n\t"
  "s_waitcnt lgkmcnt(2)\n\t"
  "ds_read_b128 a[232:235], v42 offset:47104\n\t"
  "buffer_store_dwordx2 v[8:9], v12, s[56:59], s27 offen offset:0 sc1\n\t"
  "v_mfma_f32_16x16x32_bf16 a[40:43], a[248:251], v[216:219], a[40:43]\n\t"
  "ds_read_b128 a[236:239], v42 offset:54272\n\t"
  "v_mfma_f32_16x16x32_bf16 a[44:47], a[248:251], v[220:223], a[44:47]\n\t"
  "v_mfma_f32_16x16x32_bf16 a[48:51], a[252:255], v[216:219], a[48:51]\n\t"
  "ds_read_b128 a[240:243], v42 offset:33792\n\t"
  "v_mfma_f32_16x16x32_bf16 a[52:55], a[252:255], v[220:223], a[52:55]\n\t"
  "s_waitcnt lgkmcnt(3)\n\t"
  "v_mfma_f32_16x16x32_bf16 a[0:3], a[224:227], v[224:227], a[0:3]\n\t"
  "ds_read_b128 a[244:247], v42 offset:40960\n\t"
  "v_mfma_f32_16x16x32_bf16 a[4:7], a[224:227], v[228:231], a[4:7]\n\t"
  "v_mfma_f32_16x16x32_bf16 a[0:3], a[228:231], v[232:235], a[0:3]\n\t"
  "s_add_u32 m0, s87, 0x8000\n\t"
  "ds_read_b128 a[248:251], v42 offset:48128\n\t"
  "v_mfma_f32_16x16x32_bf16 a[4:7], a[228:231], v[236:239], a[4:7]\n\t"
  "s_waitcnt lgkmcnt(3)\n\t"
  "v_mfma_f32_16x16x32_bf16 a[0:3], a[232:235], v[240:243], a[0:3]\n\t"
  "ds_read_b128 a[252:255], v42 offset:55296\n\t"
  "v_mfma_f32_16x16x32_bf16 a[4:7], a[232:235], v[244:247], a[4:7]\n\t"
  "buffer_load_dwordx4 v40, s[56:59], s29 offen offset:0 sc1 lds\n\t"
  "v_mfma_f32_16x16x32_bf16 a[0:3], a[236:239], v[248:251], a[0:3]\n\t"
  "ds_read_b128 a[224:227], v42 offset:34816\n\t"
  "v_mfma_f32_16x16x32_bf16 a[4:7], a[236:239], v[252:255], a[4:7]\n\t"
  "s_waitcnt lgkmcnt(3)\n\t"
  "v_mfma_f32_16x16x32_bf16 a[8:11], a[240:243], v[224:227], a[8:11]\n\t"
  "ds_read_b128 a[228:231], v42 offset:41984\n\t"
  "v_mfma_f32_16x16x32_bf16 a[12:15], a[240:243], v[228:231], a[12:15]\n\t"
  "buffer_load_dwordx4 v40, s[56:59], s29 offen offset:1024 sc1 lds\n\t"
  "v_mfma_f32_16x16x32_bf16 a[8:11], a[244:247], v[232:235], a[8:11]\n\t"
  "ds_read_b128 a[232:235], v42 offset:49152\n\t"
  "v_accvgpr_read_b32 v56, a0\n\t"
  "v_mfma_f32_16x16x32_bf16 a[12:15], a[244:247], v[236:239], a[12:15]\n\t"
  "s_waitcnt lgkmcnt(3)\n\t"
  "v_accvgpr_read_b32 v57, a1\n\t"
  "v_accvgpr_read_b32 v58, a2\n\t"
  "v_mfma_f32_16x16x32_bf16 a[8:11], a[248:251], v[240:243], a[8:11]\n\t"
  "ds_read_b128 a[236:239], v42 offset:56320\n\t"
  "v_accvgpr_read_b32 v59, a3\n\t"
  "v_mfma_f32_16x16x32_bf16 a[12:15], a[248:251], v[244:247], a[12:15]\n\t"
  "buffer_load_dwordx4 v40, s[56:59], s29 offen offset:2048 sc1 lds\n\t"
  "v_mfma_f32_16x16x32_bf16 a[8:11], a[252:255], v[248:251], a[8:11]\n\t"
  "ds_read_b128 a[240:243], v42 offset:35840\n\t"
  "v_accvgpr_read_b32 v60, a4\n\t"
  "v_mfma_f32_16x16x32_bf16 a[12:15], a[252:255], v[252:255], a[12:15]\n\t"
  "s_waitcnt lgkmcnt(3)\n\t"
  "v_accvgpr_read_b32 v61, a5\n\t"
  "v_accvgpr_read_b32 v62, a6\n\t"
  "v_mfma_f32_16x16x32_bf16 a[16:19], a[224:227], v[224:227], a[16:19]\n\t"
  "ds_read_b128 a[244:247], v42 offset:43008\n\t"
  "v_accvgpr_read_b32 v63, a7\n\t"
  "v_mfma_f32_16x16x32_bf16 a[20:23], a[224:227], v[228:231], a[20:23]\n\t"
  "buffer_load_dwordx4 v40, s[56:59], s29 offen offset:3072 sc1 lds\n\t"
  "v_mfma_f32_16x16x32_bf16 a[16:19], a[228:231], v[232:235], a[16:19]\n\t"
  "s_add_u32 s29, s29, 0x1000\n\t"
  "ds_read_b128 a[248:251], v42 offset:50176\n\t"
  "v_mul_f32 v56, v56, v50\n\t"
  "v_mfma_f32_16x16x32_bf16 a[20:23], a[228:231], v[236:239], a[20:23]\n\t"
  "s_waitcnt lgkmcnt(3)\n\t"
  "v_mul_f32 v57, v57, v50\n\t"
  "v_mul_f32 v58, v58, v50\n\t"
  "v_mfma_f32_16x16x32_bf16 a[16:19], a[232:235], v[240:243], a[16:19]\n\t"
  "ds_read_b128 a[252:255], v42 offset:57344\n\t"
  "v_mul_f32 v59, v59, v50\n\t"
  "v_mfma_f32_16x16x32_bf16 a[20:23], a[232:235], v[244:247], a[20:23]\n\t"
  "v_mul_f32 v60, v60, v51\n\t"
  "v_mul_f32 v61, v61, v51\n\t"
  "v_mfma_f32_16x16x32_bf16 a[16:19], a[236:239], v[248:251], a[16:19]\n\t"
  "s_add_u32 s28, s72, 2\n\t"
  "ds_read_b128 a[224:227], v42 offset:36864\n\t"
  "v_mul_f32 v62, v62, v51\n\t"
  "v_mfma_f32_16x16x32_bf16 a[20:23], a[236:239], v[252:255], a[20:23]\n\t"
  "s_waitcnt lgkmcnt(3)\n\t"
  "v_mul_f32 v63, v63, v51\n\t"
  "v_cvt_pk_bf16_f32 v56, v56, v57\n\t"
  "v_mfma_f32_16x16x32_bf16 a[24:27], a[240:243], v[224:227], a[24:27]\n\t"
  "s_and_b32 s28, s28, 3\n\t"
  "ds_read_b128 a[228:231], v42 offset:44032\n\t"
  "s_mul_i32 s28, s28, 0x1c000\n\t"
  "s_add_u32 s28, s28, s86\n\t"
  "s_add_u32 s76, s28, 0x1000\n\t"
  "v_mfma_f32_16x16x32_bf16 a[28:31], a[240:243], v[228:231], a[28:31]\n\t"
  "v_cvt_pk_bf16_f32 v57, v58, v59\n\t"
  "v_cvt_pk_bf16_f32 v58, v60, v61\n\t"
  "v_mfma_f32_16x16x32_bf16 a[24:27], a[244:247], v[232:235], a[24:27]\n\t"
  "s_add_u32 m0, s87, 0x9000\n\t"
  "ds_read_b128 a[232:235], v42 offset:51200\n\t"
  "v_cvt_pk_bf16_f32 v59, v62, v63\n\t"
  "v_mfma_f32_16x16x32_bf16 a[28:31], a[244:247], v[236:239], a[28:31]\n\t"
  "s_waitcnt lgkmcnt(3)\n\t"
  "v_accvgpr_write_b32 a0, v56\n\t"
  "v_accvgpr_write_b32 a1, v57\n\t"
  "v_accvgpr_write_b32 a2, v58\n\t"
  "v_accvgpr_write_b32 a3, v59\n\t"
  "buffer_store_dwordx4 a[0:3], v40, s[56:59], s28 offen offset:0 sc1\n\t"
  "v_mfma_f32_16x16x32_bf16 a[24:27], a[248:251], v[240:243], a[24:27]\n\t"
  "ds_read_b128 a[236:239], v42 offset:58368\n\t"
  "v_accvgpr_read_b32 v120, a8\n\t"
  "v_mfma_f32_16x16x32_bf16 a[28:31], a[248:251], v[244:247], a[28:31]\n\t"
  "buffer_load_dwordx4 v40, s[56:59], s29 offen offset:0 sc1 lds\n\t"
  "v_mfma_f32_16x16x32_bf16 a[24:27], a[252:255], v[248:251], a[24:27]\n\t"
  "ds_read_b128 a[240:243], v42 offset:37888\n\t"
  "v_accvgpr_read_b32 v121, a9\n\t"
  "v_mfma_f32_16x16x32_bf16 a[28:31], a[252:255], v[252:255], a[28:31]\n\t"
  "s_waitcnt lgkmcnt(3)\n\t"
  "v_accvgpr_read_b32 v122, a10\n\t"
  "v_accvgpr_read_b32 v123, a11\n\t"
  "v_mfma_f32_16x16x32_bf16 a[32:35], a[224:227], v[224:227], a[32:35]\n\t"
  "ds_read_b128 a[244:247], v42 offset:45056\n\t"
  "v_accvgpr_read_b32 v124, a12\n\t"
  "v_mfma_f32_16x16x32_bf16 a[36:39], a[224:227], v[228:231], a[36:39]\n\t"
  "buffer_load_dwordx4 v40, s[56:59], s29 offen offset:1024 sc1 lds\n\t"
  "v_mfma_f32_16x16x32_bf16 a[32:35], a[228:231], v[232:235], a[32:35]\n\t"
  "ds_read_b128 a[248:251], v42 offset:52224\n\t"
  "v_accvgpr_read_b32 v125, a13\n\t"
  "v_mfma_f32_16x16x32_bf16 a[36:39], a[228:231], v[236:239], a[36:39]\n\t"
  "s_waitcnt lgkmcnt(3)\n\t"
  "v_accvgpr_read_b32 v126, a14\n\t"
  "v_accvgpr_read_b32 v127, a15\n\t"
  "v_mfma_f32_16x16x32_bf16 a[32:35], a[232:235], v[240:243], a[32:35]\n\t"
  "ds_read_b128 a[252:255], v42 offset:59392\n\t"
  "v_mul_f32 v120, v120, v50\n\t"
  "v_mfma_f32_16x16x32_bf16 a[36:39], a[232:235], v[244:247], a[36:39]\n\t"
  "buffer_load_dwordx4 v40, s[56:59], s29 offen offset:2048 sc1 lds\n\t"
  "v_mfma_f32_16x16x32_bf16 a[32:35], a[236:239], v[248:251], a[32:35]\n\t"
  "s_add_u32 s29, s29, 0x6000\n\t"
  "ds_read_b128 a[224:227], v42 offset:38912\n\t"
  "v_mul_f32 v121, v121, v50\n\t"
  "v_mfma_f32_16x16x32_bf16 a[36:39], a[236:239], v[252:255], a[36:39]\n\t"
  "s_waitcnt lgkmcnt(3)\n\t"
  "v_mul_f32 v122, v122, v50\n\t"
  "v_mul_f32 v123, v123, v50\n\t"
  "v_mfma_f32_16x16x32_bf16 a[40:43], a[240:243], v[224:227], a[40:43]\n\t"
  "ds_read_b128 a[228:231], v42 offset:46080\n\t"
  "v_mul_f32 v124, v124, v51\n\t"
  "v_mfma_f32_16x16x32_bf16 a[44:47], a[240:243], v[228:231], a[44:47]\n\t"
  "v_mul_f32 v125, v125, v51\n\t"
  "v_mul_f32 v126, v126, v51\n\t"
  "v_mfma_f32_16x16x32_bf16 a[40:43], a[244:247], v[232:235], a[40:43]\n\t"
  "ds_read_b128 a[232:235], v42 offset:53248\n\t"
  "v_mul_f32 v127, v127, v51\n\t"
  "v_mfma_f32_16x16x32_bf16 a[44:47], a[244:247], v[236:239], a[44:47]\n\t"
  "s_waitcnt lgkmcnt(3)\n\t"
  "v_cvt_pk_bf16_f32 v120, v120, v121\n\t"
  "v_cvt_pk_bf16_f32 v121, v122, v123\n\t"
  "v_mfma_f32_16x16x32_bf16 a[40:43], a[248:251], v[240:243], a[40:43]\n\t"
  "s_waitcnt vmcnt(10)\n\t"
  "ds_read_b128 a[236:239], v42 offset:60416\n\t"
  "v_cvt_pk_bf16_f32 v122, v124, v125\n\t"
  "v_mfma_f32_16x16x32_bf16 a[44:47], a[248:251], v[244:247], a[44:47]\n\t"
  "s_barrier\n\t"
  "v_cvt_pk_bf16_f32 v123, v126, v127\n\t"
  "v_accvgpr_write_b32 a8, v120\n\t"
  "v_mfma_f32_16x16x32_bf16 a[40:43], a[252:255], v[248:251], a[40:43]\n\t"
  "ds_read_b128 a[240:243], v40 offset:0\n\t"
  "v_accvgpr_write_b32 a9, v121\n\t"
  "v_mfma_f32_16x16x32_bf16 a[44:47], a[252:255], v[252:255], a[44:47]\n\t"
  "s_waitcnt lgkmcnt(3)\n\t"
  "v_accvgpr_write_b32 a10, v122\n\t"
  "v_accvgpr_write_b32 a11, v123\n\t"
  "buffer_store_dwordx4 a[8:11], v40, s[56:59], s28 offen offset:1024 sc1\n\t"
  "v_mfma_f32_16x16x32_bf16 a[48:51], a[224:227], v[224:227], a[48:51]\n\t"
  "ds_read_b128 a[244:247], v40 offset:1024\n\t"
  "v_accvgpr_read_b32 v56, a16\n\t"
  "v_mfma_f32_16x16x32_bf16 a[52:55], a[224:227], v[228:231], a[52:55]\n\t"
  "v_accvgpr_read_b32 v57, a17\n\t"
  "v_accvgpr_read_b32 v58, a18\n\t"
  "v_mfma_f32_16x16x32_bf16 a[48:51], a[228:231], v[232:235], a[48:51]\n\t"
  "ds_read_b128 a[248:251], v40 offset:2048\n\t"
  "v_accvgpr_read_b32 v59, a19\n\t"
  "v_mfma_f32_16x16x32_bf16 a[52:55], a[228:231], v[236:239], a[52:55]\n\t"
  "s_waitcnt lgkmcnt(3)\n\t"
  "v_accvgpr_read_b32 v60, a20\n\t"
  "v_accvgpr_read_b32 v61, a21\n\t"
  "v_mfma_f32_16x16x32_bf16 a[48:51], a[232:235], v[240:243], a[48:51]\n\t"
  "ds_read_b128 a[252:255], v40 offset:3072\n\t"
  "v_accvgpr_read_b32 v62, a22\n\t"
  "v_mfma_f32_16x16x32_bf16 a[52:55], a[232:235], v[244:247], a[52:55]\n\t"
  "v_accvgpr_read_b32 v63, a23\n\t"
  "v_mul_f32 v56, v56, v50\n\t"
  "v_mfma_f32_16x16x32_bf16 a[48:51], a[236:239], v[248:251], a[48:51]\n\t"
  "ds_read_b128 a[224:227], v40 offset:4096\n\t"
  "v_mul_f32 v57, v57, v50\n\t"
  "v_mfma_f32_16x16x32_bf16 a[52:55], a[236:239], v[252:255], a[52:55]\n\t"
  "s_waitcnt lgkmcnt(3)\n\t"
  "v_mul_f32 v58, v58, v50\n\t"
  "v_mul_f32 v59, v59, v50\n\t"
  "v_mfma_f32_16x16x32_bf16 v[64:67], a[240:243], v[128:131], 0\n\t"
  "ds_read_b128 a[228:231], v40 offset:5120\n\t"
  "v_mul_f32 v60, v60, v51\n\t"
  "v_mfma_f32_16x16x32_bf16 v[68:71], a[240:243], v[132:135], 0\n\t"
  "v_mul_f32 v61, v61, v51\n\t"
  "v_mul_f32 v62, v62, v51\n\t"
  "v_mfma_f32_16x16x32_bf16 v[72:75], a[244:247], v[128:131], 0\n\t"
  "ds_read_b128 a[232:235], v40 offset:6144\n\t"
  "v_mul_f32 v63, v63, v51\n\t"
  "v_mfma_f32_16x16x32_bf16 v[76:79], a[244:247], v[132:135], 0\n\t"
  "s_waitcnt lgkmcnt(3)\n\t"
  "v_cvt_pk_bf16_f32 v56, v56, v57\n\t"
  "v_cvt_pk_bf16_f32 v57, v58, v59\n\t"
  "v_mfma_f32_16x16x32_bf16 v[80:83], a[248:251], v[128:131], 0\n\t"
  "s_sub_u32 s91, s19, 1\n\t"
  "ds_read_b128 a[236:239], v40 offset:7168\n\t"
  "v_cvt_pk_bf16_f32 v58, v60, v61\n\t"
  "v_mfma_f32_16x16x32_bf16 v[84:87], a[248:251], v[132:135], 0\n\t"
  "s_and_b32 s91, s91, 3\n\t"
  "v_cvt_pk_bf16_f32 v59, v62, v63\n\t"
  "v_accvgpr_write_b32 a16, v56\n\t"
  "v_mfma_f32_16x16x32_bf16 v[88:91], a[252:255], v[128:131], 0\n\t"
  "s_lshl_b32 s91, s91, 3\n\t"
  "v_accvgpr_write_b32 a17, v57\n\t"
  "v_accvgpr_write_b32 a18, v58\n\t"
  "v_mfma_f32_16x16x32_bf16 v[92:95], a[252:255], v[132:135], 0\n\t"
  "s_waitcnt lgkmcnt(2)\n\t"
  "v_accvgpr_write_b32 a19, v59\n\t"
  "v_accvgpr_read_b32 v120, a24\n\t"
  "buffer_store_dwordx4 a[16:19], v40, s[56:59], s28 offen offset:2048 sc1\n\t"
  "v_mfma_f32_16x16x32_bf16 v[96:99], a[224:227], v[128:131], 0\n\t"
  "s_add_u32 s91, s91, s15\n\t"
  "ds_read_b128 a[240:243], v40 offset:8192\n\t"
  "s_add_u32 m0, s87, 0x10000\n\t"
  "v_mfma_f32_16x16x32_bf16 v[100:103], a[224:227], v[132:135], 0\n\t"
  "buffer_load_dwordx4 v40, s[56:59], s29 offen offset:0 sc1 lds\n\t"
  "v_mfma_f32_16x16x32_bf16 v[104:107], a[228:231], v[128:131], 0\n\t"
  "ds_read_b128 a[244:247], v40 offset:9216\n\t"
  "v_accvgpr_read_b32 v121, a25\n\t"
  "v_mfma_f32_16x16x32_bf16 v[108:111], a[228:231], v[132:135], 0\n\t"
  "s_waitcnt lgkmcnt(2)\n\t"
  "buffer_load_dwordx2 v[48:49], v13, s[56:59], s91 offen offset:0 sc1\n\t"
  "v_mfma_f32_16x16x32_bf16 v[112:115], a[232:235], v[128:131], 0\n\t"
  "v_accvgpr_read_b32 v122, a26\n\t"
  "v_accvgpr_read_b32 v123, a27\n\t"
  "v_mfma_f32_16x16x32_bf16 v[116:119], a[232:235], v[132:135], 0\n\t"
  "buffer_load_dwordx4 v40, s[56:59], s29 offen offset:1024 sc1 lds\n\t"
  "v_mfma_f32_16x16x32_bf16 v[64:67], a[236:239], v[136:139], v[64:67]\n\t"
  "ds_read_b128 a[248:251], v40 offset:10240\n\t"
  "v_accvgpr_read_b32 v124, a28\n\t"
  "v_mfma_f32_16x16x32_bf16 v[68:71], a[236:239], v[140:143], v[68:71]\n\t"
  "s_waitcnt vmcnt(14)\n\t"
  "v_accvgpr_read_b32 v125, a29\n\t"
  "v_accvgpr_read_b32 v126, a30\n\t"
  "v_mfma_f32_16x16x32_bf16 v[16:19], v[20:23], a[56:59], 0\n\t"
  "ds_read_b128 a[252:255], v40 offset:11264\n\t"
  "v_accvgpr_read_b32 v127, a31\n\t"
  "v_mfma_f32_16x16x32_bf16 v[36:39], v[24:27], a[56:59], 0\n\t"
  "s_waitcnt lgkmcnt(2)\n\t"
  "v_mul_f32 v120, v120, v50\n\t"
  "v_mul_f32 v121, v121, v50\n\t"
  "v_mfma_f32_16x16x32_bf16 v[72:75], a[240:243], v[136:139], v[72:75]\n\t"
  "v_mul_f32 v122, v122, v50\n\t"
  "v_mul_f32 v123, v123, v50\n\t"
  "v_mfma_f32_16x16x32_bf16 v[76:79], a[240:243], v[140:143], v[76:79]\n\t"
  "buffer_load_dwordx4 v40, s[56:59], s29 offen offset:2048 sc1 lds\n\t"
  "v_mfma_f32_16x16x32_bf16 v[80:83], a[244:247], v[136:139], v[80:83]\n\t"
  "ds_read_b128 a[224:227], v40 offset:12288\n\t"
  "v_mul_f32 v124, v124, v51\n\t"
  "v_mfma_f32_16x16x32_bf16 v[84:87], a[244:247], v[140:143], v[84:87]\n\t"
  "v_mul_f32 v125, v125, v51\n\t"
  "v_mul_f32 v126, v126, v51\n\t"
  "v_mfma_f32_16x16x32_bf16 v[28:31], v[20:23], a[60:63], 0\n\t"
  "ds_read_b128 a[228:231], v40 offset:13312\n\t"
  "v_mul_f32 v127, v127, v51\n\t"
  "v_mfma_f32_16x16x32_bf16 v[52:55], v[24:27], a[60:63], 0\n\t"
  "s_waitcnt lgkmcnt(2)\n\t"
  "v_cvt_pk_bf16_f32 v120, v120, v121\n\t"
  "v_cvt_pk_bf16_f32 v121, v122, v123\n\t"
  "v_mfma_f32_16x16x32_bf16 v[88:91], a[248:251], v[136:139], v[88:91]\n\t"
  "v_cvt_pk_bf16_f32 v122, v124, v125\n\t"
  "v_cvt_pk_bf16_f32 v123, v126, v127\n\t"
  "v_mfma_f32_16x16x32_bf16 v[92:95], a[248:251], v[140:143], v[92:95]\n\t"
  "v_accvgpr_write_b32 a24, v120\n\t"
  "v_accvgpr_write_b32 a25, v121\n\t"
  "v_mfma_f32_16x16x32_bf16 v[96:99], a[252:255], v[136:139], v[96:99]\n\t"
  "ds_read_b128 a[232:235], v40 offset:14336\n\t"
  "v_accvgpr_write_b32 a26, v122\n\t"
  "v_mfma_f32_16x16x32_bf16 v[100:103], a[252:255], v[140:143], v[100:103]\n\t"
  "v_accvgpr_write_b32 a27, v123\n\t"
  "v_accvgpr_read_b32 v56, a32\n\t"
  "buffer_store_dwordx4 a[24:27], v40, s[56:59], s28 offen offset:3072 sc1\n\t"
  "v_mfma_f32_16x16x32_bf16 v[16:19], v[20:23], a[64:67], v[16:19]\n\t"
  "ds_read_b128 a[236:239], v40 offset:15360\n\t"
  "v_accvgpr_read_b32 v57, a33\n\t"
  "v_mfma_f32_16x16x32_bf16 v[36:39], v[24:27], a[64:67], v[36:39]\n\t"
  "s_waitcnt lgkmcnt(2)\n\t"
  "v_accvgpr_read_b32 v58, a34\n\t"
  "v_accvgpr_read_b32 v59, a35\n\t"
  "v_mfma_f32_16x16x32_bf16 v[104:107], a[224:227], v[136:139], v[104:107]\n\t"
  "v_accvgpr_read_b32 v60, a36\n\t"
  "v_accvgpr_read_b32 v61, a37\n\t"
  "v_mfma_f32_16x16x32_bf16 v[108:111], a[224:227], v[140:143], v[108:111]\n\t"
  "buffer_load_dwordx4 v40, s[56:59], s29 offen offset:3072 sc1 lds\n\t"
  "v_mfma_f32_16x16x32_bf16 v[112:115], a[228:231], v[136:139], v[112:115]\n\t"
  "s_add_u32 s29, s29, 0x1000\n\t"
  "ds_read_b128 a[240:243], v40 offset:16384\n\t"
  "v_accvgpr_read_b32 v62, a38\n\t"
  "v_mfma_f32_16x16x32_bf16 v[116:119], a[228:231], v[140:143], v[116:119]\n\t"
  "v_accvgpr_read_b32 v63, a39\n\t"
  "v_mul_f32 v56, v56, v50\n\t"
  "v_mfma_f32_16x16x32_bf16 v[28:31], v[20:23], a[68:71], v[28:31]\n\t"
  "s_add_u32 m0, s87, 0x11000\n\t"
  "ds_read_b128 a[244:247], v40 offset:17408\n\t"
  "v_mul_f32 v57, v57, v50\n\t"
  "v_mfma_f32_16x16x32_bf16 v[52:55], v[24:27], a[68:71], v[52:55]\n\t"
  "s_waitcnt lgkmcnt(2)\n\t"
  "v_mul_f32 v58, v58, v50\n\t"
  "v_mul_f32 v59, v59, v50\n\t"
  "v_mfma_f32_16x16x32_bf16 v[64:67], a[232:235], v[144:147], v[64:67]\n\t"
  "v_mul_f32 v60, v60, v51\n\t"
  "v_mul_f32 v61, v61, v51\n\t"
  "v_mfma_f32_16x16x32_bf16 v[68:71], a[232:235], v[148:151], v[68:71]\n\t"
  "buffer_load_dwordx4 v40, s[56:59], s29 offen offset:0 sc1 lds\n\t"
  "v_mfma_f32_16x16x32_bf16 v[72:75], a[236:239], v[144:147], v[72:75]\n\t"
  "ds_read_b128 a[248:251], v40 offset:18432\n\t"
  "v_mul_f32 v62, v62, v51\n\t"
  "v_mfma_f32_16x16x32_bf16 v[76:79], a[236:239], v[148:151], v[76:79]\n\t"
  "v_mul_f32 v63, v63, v51\n\t"
  "v_cvt_pk_bf16_f32 v56, v56, v57\n\t"
  "v_mfma_f32_16x16x32_bf16 v[16:19], v[20:23], a[72:75], v[16:19]\n\t"
  "ds_read_b128 a[252:255], v40 offset:19456\n\t"
  "v_cvt_pk_bf16_f32 v57, v58, v59\n\t"
  "v_mfma_f32_16x16x32_bf16 v[36:39], v[24:27], a[72:75], v[36:39]\n\t"
  "s_waitcnt lgkmcnt(2)\n\t"
  "v_cvt_pk_bf16_f32 v58, v60, v61\n\t"
  "v_cvt_pk_bf16_f32 v59, v62, v63\n\t"
  "v_mfma_f32_16x16x32_bf16 v[80:83], a[240:243], v[144:147], v[80:83]\n\t"
  "v_accvgpr_write_b32 a32, v56\n\t"
  "v_accvgpr_write_b32 a33, v57\n\t"
  "v_mfma_f32_16x16x32_bf16 v[84:87], a[240:243], v[148:151], v[84:87]\n\t"
  "v_accvgpr_write_b32 a34, v58\n\t"
  "v_accvgpr_write_b32 a35, v59\n\t"
  "v_mfma_f32_16x16x32_bf16 v[88:91], a[244:247], v[144:147], v[88:91]\n\t"
  "buffer_store_dwordx4 a[32:35], v40, s[56:59], s76 offen offset:0 sc1\n\t"
  "v_mfma_f32_16x16x32_bf16 v[92:95], a[244:247], v[148:151], v[92:95]\n\t"
  "ds_read_b128 a[224:227], v40 offset:20480\n\t"
  "v_accvgpr_read_b32 v120, a40\n\t"
  "v_mfma_f32_16x16x32_bf16 v[28:31], v[20:23], a[76:79], v[28:31]\n\t"
  "ds_read_b128 a[228:231], v40 offset:21504\n\t"
  "v_accvgpr_read_b32 v121, a41\n\t"
  "v_mfma_f32_16x16x32_bf16 v[52:55], v[24:27], a[76:79], v[52:55]\n\t"
  "s_waitcnt lgkmcnt(2)\n\t"
  "v_accvgpr_read_b32 v122, a42\n\t"
  "v_accvgpr_read_b32 v123, a43\n\t"
  "v_mfma_f32_16x16x32_bf16 v[96:99], a[248:251], v[144:147], v[96:99]\n\t"
  "v_accvgpr_read_b32 v124, a44\n\t"
  "s_cbranch_execz TRAMP_%=\n\t"
  "v_accvgpr_read_b32 v125, a45\n\t"
  "v_mfma_f32_16x16x32_bf16 v[100:103], a[248:251], v[148:151], v[100:103]\n\t"
  "buffer_load_dwordx4 v40, s[56:59], s29 offen offset:1024 sc1 lds\n\t"
  "v_mfma_f32_16x16x32_bf16 v[104:107], a[252:255], v[144:147], v[104:107]\n\t"
  "ds_read_b128 a[232:235], v40 offset:22528\n\t"
  "v_accvgpr_read_b32 v126, a46\n\t"
  "v_mfma_f32_16x16x32_bf16 v[108:111], a[252:255], v[148:151], v[108:111]\n\t"
  "v_accvgpr_read_b32 v127, a47\n\t"
  "v_mul_f32 v120, v120, v50\n\t"
  "v_mfma_f32_16x16x32_bf16 v[16:19], v[20:23], a[80:83], v[16:19]\n\t"
  "ds_read_b128 a[236:239], v40 offset:23552\n\t"
  "v_mul_f32 v121, v121, v50\n\t"
  "v_mfma_f32_16x16x32_bf16 v[36:39], v[24:27], a[80:83], v[36:39]\n\t"
  "s_waitcnt lgkmcnt(2)\n\t"
  "v_mul_f32 v122, v122, v50\n\t"
  "v_mul_f32 v123, v123, v50\n\t"
  "v_mfma_f32_16x16x32_bf16 v[112:115], a[224:227], v[144:147], v[112:115]\n\t"
  "ds_read_b128 a[240:243], v40 offset:24576\n\t"
  "v_mul_f32 v124, v124, v51\n\t"
  "v_mfma_f32_16x16x32_bf16 v[116:119], a[224:227], v[148:151], v[116:119]\n\t"
  "buffer_load_dwordx4 v40, s[56:59], s29 offen offset:2048 sc1 lds\n\t"
  "v_mfma_f32_16x16x32_bf16 v[64:67], a[228:231], v[152:155], v[64:67]\n\t"
  "s_add_u32 s29, s29, 0x6000\n\t"
  "ds_read_b128 a[244:247], v40 offset:25600\n\t"
  "v_mul_f32 v125, v125, v51\n\t"
  "v_mfma_f32_16x16x32_bf16 v[68:71], a[228:231], v[156:159], v[68:71]\n\t"
  "v_mul_f32 v126, v126, v51\n\t"
  "v_mul_f32 v127, v127, v51\n\t"
  "v_mfma_f32_16x16x32_bf16 v[28:31], v[20:23], a[84:87], v[28:31]\n\t"
  "ds_read_b128 a[248:251], v40 offset:26624\n\t"
  "v_cvt_pk_bf16_f32 v120, v120, v121\n\t"
  "v_mfma_f32_16x16x32_bf16 v[52:55], v[24:27], a[84:87], v[52:55]\n\t"
  "s_waitcnt lgkmcnt(3)\n\t"
  "v_cvt_pk_bf16_f32 v121, v122, v123\n\t"
  "v_cvt_pk_bf16_f32 v122, v124, v125\n\t"
  "v_mfma_f32_16x16x32_bf16 v[72:75], a[232:235], v[152:155], v[72:75]\n\t"
  "s_waitcnt vmcnt(12)\n\t"
  "ds_read_b128 a[252:255], v40 offset:27648\n\t"
  "v_cvt_pk_bf16_f32 v123, v126, v127\n\t"
  "v_mfma_f32_16x16x32_bf16 v[76:79], a[232:235], v[156:159], v[76:79]\n\t"
  "s_barrier\n\t"
  "v_accvgpr_write_b32 a40, v120\n\t"
  "v_accvgpr_write_b32 a41, v121\n\t"
  "v_mfma_f32_16x16x32_bf16 v[80:83], a[236:239], v[152:155], v[80:83]\n\t"
  "ds_read_b128 a[224:227], v40 offset:32768\n\t"
  "v_accvgpr_write_b32 a42, v122\n\t"
  "v_mfma_f32_16x16x32_bf16 v[84:87], a[236:239], v[156:159], v[84:87]\n\t"
  "s_waitcnt lgkmcnt(3)\n\t"
  "v_accvgpr_write_b32 a43, v123\n\t"
  "v_accvgpr_read_b32 v56, a48\n\t"
  "buffer_store_dwordx4 a[40:43], v40, s[56:59], s76 offen offset:1024 sc1\n\t"
  "v_mfma_f32_16x16x32_bf16 v[88:91], a[240:243], v[152:155], v[88:91]\n\t"
  "ds_read_b128 a[228:231], v40 offset:33792\n\t"
  "v_accvgpr_read_b32 v57, a49\n\t"
  "v_mfma_f32_16x16x32_bf16 v[92:95], a[240:243], v[156:159], v[92:95]\n\t"
  "v_accvgpr_read_b32 v58, a50\n\t"
  "v_accvgpr_read_b32 v59, a51\n\t"
  "v_mfma_f32_16x16x32_bf16 v[96:99], a[244:247], v[152:155], v[96:99]\n\t"
  "ds_read_b128 a[232:235], v40 offset:34816\n\t"
  "v_accvgpr_read_b32 v60, a52\n\t"
  "v_mfma_f32_16x16x32_bf16 v[100:103], a[244:247], v[156:159], v[100:103]\n\t"
  "s_waitcnt lgkmcnt(3)\n\t"
  "v_accvgpr_read_b32 v61, a53\n\t"
  "v_accvgpr_read_b32 v62, a54\n\t"
  "v_mfma_f32_16x16x32_bf16 v[104:107], a[248:251], v[152:155], v[104:107]\n\t"
  "ds_read_b128 a[236:239], v40 offset:35840\n\t"
  "v_accvgpr_read_b32 v63, a55\n\t"
  "v_mfma_f32_16x16x32_bf16 v[108:111], a[248:251], v[156:159], v[108:111]\n\t"
  "v_mul_f32 v56, v56, v50\n\t"
  "v_mul_f32 v57, v57, v50\n\t"
  "v_mfma_f32_16x16x32_bf16 v[112:115], a[252:255], v[152:155], v[112:115]\n\t"
  "ds_read_b128 a[240:243], v40 offset:36864\n\t"
  "v_mul_f32 v58, v58, v50\n\t"
  "v_mfma_f32_16x16x32_bf16 v[116:119], a[252:255], v[156:159], v[116:119]\n\t"
  "s_waitcnt lgkmcnt(3)\n\t"
  "v_mul_f32 v59, v59, v50\n\t"
  "v_mul_f32 v60, v60, v51\n\t"
  "v_mfma_f32_16x16x32_bf16 v[64:67], a[224:227], v[160:163], v[64:67]\n\t"
  "s_waitcnt vmcnt(9)\n\t"
  "ds_read_b128 a[244:247], v40 offset:37888\n\t"
  "v_cmp_ne_u64 vcc, v[48:49], v[8:9]\n\t"
  "v_mfma_f32_16x16x32_bf16 v[68:71], a[224:227], v[164:167], v[68:71]\n\t"
  "v_mul_f32 v61, v61, v51\n\t"
  "v_mul_f32 v62, v62, v51\n\t"
  "v_mfma_f32_16x16x32_bf16 v[72:75], a[228:231], v[160:163], v[72:75]\n\t"
  "s_cbranch_vccnz SPINS1_%=\n\t"
  "SRETS1_%=:\n\t"
  "s_sub_u32 s40, s19, 1\n\t"
  "s_and_b32 s40, s40, 3\n\t"
  "s_mul_i32 s40, s40, 0x1c00\n\t"
  "s_add_u32 s40, s40, s16\n\t"
  "s_add_u32 s41, s40, 0x7000\n\t"
  "s_add_u32 s42, s41, 0x7000\n\t"
  "s_add_u32 s43, s42, 0x7000\n\t"
  "s_add_u32 m0, s87, 0x18000\n\t"
  "v_mfma_f32_16x16x32_bf16 v[76:79], a[228:231], v[164:167], v[76:79]\n\t"
  "s_waitcnt lgkmcnt(2)\n\t"
  "buffer_load_dwordx4 v40, s[56:59], s29 offen offset:0 sc1 lds\n\t"
  "v_mfma_f32_16x16x32_bf16 v[80:83], a[232:235], v[160:163], v[80:83]\n\t"
  "ds_read_b128 a[248:251], v40 offset:38912\n\t"
  "v_mul_f32 v63, v63, v51\n\t"
  "v_mfma_f32_16x16x32_bf16 v[84:87], a[232:235], v[164:167], v[84:87]\n\t"
  "ds_read_b128 a[252:255], v40 offset:39936\n\t"
  "v_cvt_pk_bf16_f32 v56, v56, v57\n\t"
  "v_mfma_f32_16x16x32_bf16 v[88:91], a[236:239], v[160:163], v[88:91]\n\t"
  "ds_read_b128 a[224:227], v40 offset:40960\n\t"
  "v_cvt_pk_bf16_f32 v57, v58, v59\n\t"
  "v_mfma_f32_16x16x32_bf16 v[92:95], a[236:239], v[164:167], v[92:95]\n\t"
  "s_waitcnt lgkmcnt(3)\n\t"
  "buffer_load_dwordx4 a[104:107], v40, s[56:59], s40 offen offset:0 sc1\n\t"
  "v_cvt_pk_bf16_f32 v58, v60, v61\n\t"
  "v_cvt_pk_bf16_f32 v59, v62, v63\n\t"
  "v_accvgpr_write_b32 a48, v56\n\t"
  "v_accvgpr_write_b32 a49, v57\n\t"
  "v_accvgpr_write_b32 a50, v58\n\t"
  "v_accvgpr_write_b32 a51, v59\n\t"
  "buffer_store_dwordx4 a[48:51], v40, s[56:59], s76 offen offset:2048 sc1\n\t"
  "v_mfma_f32_16x16x32_bf16 v[96:99], a[240:243], v[160:163], v[96:99]\n\t"
  "ds_read_b128 a[228:231], v40 offset:41984\n\t"
  "v_mfma_f32_16x16x32_bf16 v[100:103], a[240:243], v[164:167], v[100:103]\n\t"
  "v_mfma_f32_16x16x32_bf16 v[104:107], a[244:247], v[160:163], v[104:107]\n\t"
  "ds_read_b128 a[232:235], v40 offset:43008\n\t"
  "v_mfma_f32_16x16x32_bf16 v[108:111], a[244:247], v[164:167], v[108:111]\n\t"
  "s_waitcnt lgkmcnt(3)\n\t"
  "buffer_load_dwordx4 v40, s[56:59], s29 offen offset:1024 sc1 lds\n\t"
  "v_mfma_f32_16x16x32_bf16 v[112:115], a[248:251], v[160:163], v[112:115]\n\t"
  "ds_read_b128 a[236:239], v40 offset:44032\n\t"
  "v_mfma_f32_16x16x32_bf16 v[116:119], a[248:251], v[164:167], v[116:119]\n\t"
  "v_mfma_f32_16x16x32_bf16 v[64:67], a[252:255], v[168:171], v[64:67]\n\t"
  "ds_read_b128 a[240:243], v40 offset:45056\n\t"
  "v_mfma_f32_16x16x32_bf16 v[68:71], a[252:255], v[172:175], v[68:71]\n\t"
  "s_waitcnt lgkmcnt(3)\n\t"
  "buffer_load_dwordx4 v40, s[56:59], s29 offen offset:2048 sc1 lds\n\t"
  "v_mfma_f32_16x16x32_bf16 v[72:75], a[224:227], v[168:171], v[72:75]\n\t"
  "ds_read_b128 a[244:247], v40 offset:46080\n\t"
  "v_mfma_f32_16x16x32_bf16 v[76:79], a[224:227], v[172:175], v[76:79]\n\t"
  "buffer_load_dwordx4 a[108:111], v10, s[56:59], s40 offen offset:1024 sc1\n\t"
  "v_mfma_f32_16x16x32_bf16 v[80:83], a[228:231], v[168:171], v[80:83]\n\t"
  "ds_read_b128 a[248:251], v40 offset:47104\n\t"
  "v_mfma_f32_16x16x32_bf16 v[84:87], a[228:231], v[172:175], v[84:87]\n\t"
  "s_waitcnt lgkmcnt(3)\n\t"
  "v_mfma_f32_16x16x32_bf16 v[88:91], a[232:235], v[168:171], v[88:91]\n\t"
  "ds_read_b128 a[252:255], v40 offset:48128\n\t"
  "v_mfma_f32_16x16x32_bf16 v[92:95], a[232:235], v[172:175], v[92:95]\n\t"
  "v_mfma_f32_16x16x32_bf16 v[96:99], a[236:239], v[168:171], v[96:99]\n\t"
  "ds_read_b128 a[224:227], v40 offset:49152\n\t"
  "v_mfma_f32_16x16x32_bf16 v[100:103], a[236:239], v[172:175], v[100:103]\n\t"
  "s_waitcnt lgkmcnt(3)\n\t"
  "buffer_load_dwordx4 v40, s[56:59], s29 offen offset:3072 sc1 lds\n\t"
  "v_mfma_f32_16x16x32_bf16 v[104:107], a[240:243], v[168:171], v[104:107]\n\t"
  "s_add_u32 s29, s29, 0x1000\n\t"
  "ds_read_b128 a[228:231], v40 offset:50176\n\t"
  "v_mfma_f32_16x16x32_bf16 v[108:111], a[240:243], v[172:175], v[108:111]\n\t"
  "s_add_u32 m0, s87, 0x19000\n\t"
  "v_mfma_f32_16x16x32_bf16 v[112:115], a[244:247], v[168:171], v[112:115]\n\t"
  "ds_read_b128 a[232:235], v40 offset:51200\n\t"
  "v_mfma_f32_16x16x32_bf16 v[116:119], a[244:247], v[172:175], v[116:119]\n\t"
  "s_waitcnt lgkmcnt(3)\n\t"
  "buffer_load_dwordx4 v40, s[56:59], s29 offen offset:0 sc1 lds\n\t"
  "v_mfma_f32_16x16x32_bf16 v[64:67], a[248:251], v[176:179], v[64:67]\n\t"
  "ds_read_b128 a[236:239], v40 offset:52224\n\t"
  "v_mfma_f32_16x16x32_bf16 v[68:71], a[248:251], v[180:183], v[68:71]\n\t"
  "buffer_load_dwordx4 a[4:7], v40, s[56:59], s41 offen offset:0 sc1\n\t"
  "v_mfma_f32_16x16x32_bf16 v[72:75], a[252:255], v[176:179], v[72:75]\n\t"
  "ds_read_b128 a[240:243], v40 offset:53248\n\t"
  "v_mfma_f32_16x16x32_bf16 v[76:79], a[252:255], v[180:183], v[76:79]\n\t"
  "s_waitcnt lgkmcnt(3)\n\t"
  "v_mfma_f32_16x16x32_bf16 v[80:83], a[224:227], v[176:179], v[80:83]\n\t"
  "ds_read_b128 a[244:247], v40 offset:54272\n\t"
  "v_mfma_f32_16x16x32_bf16 v[84:87], a[224:227], v[180:183], v[84:87]\n\t"
  "v_mfma_f32_16x16x32_bf16 v[88:91], a[228:231], v[176:179], v[88:91]\n\t"
  "ds_read_b128 a[248:251], v40 offset:55296\n\t"
  "v_mfma_f32_16x16x32_bf16 v[92:95], a[228:231], v[180:183], v[92:95]\n\t"
  "s_waitcnt lgkmcnt(3)\n\t"
  "buffer_load_dwordx4 v40, s[56:59], s29 offen offset:1024 sc1 lds\n\t"
  "v_mfma_f32_16x16x32_bf16 v[96:99], a[232:235], v[176:179], v[96:99]\n\t"
  "ds_read_b128 a[252:255], v40 offset:56320\n\t"
  "v_mfma_f32_16x16x32_bf16 v[100:103], a[232:235], v[180:183], v[100:103]\n\t"
  "v_mfma_f32_16x16x32_bf16 v[104:107], a[236:239], v[176:179], v[104:107]\n\t"
  "ds_read_b128 a[224:227], v40 offset:57344\n\t"
  "v_mfma_f32_16x16x32_bf16 v[108:111], a[236:239], v[180:183], v[108:111]\n\t"
  "s_waitcnt lgkmcnt(3)\n\t"
  "buffer_load_dwordx4 v40, s[56:59], s29 offen offset:2048 sc1 lds\n\t"
  "v_mfma_f32_16x16x32_bf16 v[112:115], a[240:243], v[176:179], v[112:115]\n\t"
  "s_add_u32 s29, s29, 0x6000\n\t"
  "ds_read_b128 a[228:231], v40 offset:58368\n\t"
  "v_mfma_f32_16x16x32_bf16 v[116:119], a[240:243], v[180:183], v[116:119]\n\t"
  "s_add_u32 s27, s72, 2\n\t"
  "buffer_load_dwordx4 a[12:15], v10, s[56:59], s41 offen offset:1024 sc1\n\t"
  "v_mfma_f32_16x16x32_bf16 v[64:67], a[244:247], v[184:187], v[64:67]\n\t"
  "s_and_b32 s27, s27, 3\n\t"
  "ds_read_b128 a[232:235], v40 offset:59392\n\t"
  "v_mfma_f32_16x16x32_bf16 v[68:71], a[244:247], v[188:191], v[68:71]\n\t"
  "s_waitcnt lgkmcnt(3)\n\t"
  "v_mfma_f32_16x16x32_bf16 v[72:75], a[248:251], v[184:187], v[72:75]\n\t"
  "s_waitcnt vmcnt(9)\n\t"
  "ds_read_b128 a[236:239], v40 offset:60416\n\t"
  "v_mfma_f32_16x16x32_bf16 v[76:79], a[248:251], v[188:191], v[76:79]\n\t"
  "s_lshl_b32 s27, s27, 7\n\t"
  "v_mfma_f32_16x16x32_bf16 v[80:83], a[252:255], v[184:187], v[80:83]\n\t"
  "s_add_u32 s27, s27, s13\n\t"
  "buffer_store_dwordx2 v[8:9], v12, s[56:59], s27 offen offset:0 sc1\n\t"
  "v_mfma_f32_16x16x32_bf16 v[84:87], a[252:255], v[188:191], v[84:87]\n\t"
  "s_waitcnt lgkmcnt(2)\n\t"
  "v_mfma_f32_16x16x32_bf16 v[88:91], a[224:227], v[184:187], v[88:91]\n\t"
  "s_barrier\n\t"
  "ds_read_b128 a[240:243], v42 offset:0\n\t"
  "ds_read_b128 a[244:247], v42 offset:1024\n\t"
  "v_mfma_f32_16x16x32_bf16 v[92:95], a[224:227], v[188:191], v[92:95]\n\t"
  "v_mfma_f32_16x16x32_bf16 v[96:99], a[228:231], v[184:187], v[96:99]\n\t"
  "ds_read_b128 a[248:251], v42 offset:2048\n\t"
  "v_mfma_f32_16x16x32_bf16 v[100:103], a[228:231], v[188:191], v[100:103]\n\t"
  "s_waitcnt lgkmcnt(3)\n\t"
  "v_mfma_f32_16x16x32_bf16 v[104:107], a[232:235], v[184:187], v[104:107]\n\t"
  "ds_read_b128 a[252:255], v42 offset:3072\n\t"
  "v_mfma_f32_16x16x32_bf16 v[108:111], a[232:235], v[188:191], v[108:111]\n\t"
  "buffer_load_dwordx4 a[20:23], v40, s[56:59], s42 offen offset:0 sc1\n\t"
  "v_mfma_f32_16x16x32_bf16 v[112:115], a[236:239], v[184:187], v[112:115]\n\t"
  "ds_read_b128 a[224:227], v42 offset:4096\n\t"
  "v_mfma_f32_16x16x32_bf16 v[116:119], a[236:239], v[188:191], v[116:119]\n\t"
  "s_waitcnt lgkmcnt(3)\n\t"
  "v_mfma_f32_16x16x32_bf16 v[64:67], a[240:243], v[192:195], v[64:67]\n\t"
  "ds_read_b128 a[228:231], v42 offset:5120\n\t"
  "v_mfma_f32_16x16x32_bf16 v[68:71], a[240:243], v[196:199], v[68:71]\n\t"
  "v_mfma_f32_16x16x32_bf16 v[72:75], a[244:247], v[192:195], v[72:75]\n\t"
  "ds_read_b128 a[232:235], v42 offset:6144\n\t"
  "v_mfma_f32_16x16x32_bf16 v[76:79], a[244:247], v[196:199], v[76:79]\n\t"
  "s_waitcnt lgkmcnt(3)\n\t"
  "buffer_load_dwordx4 a[88:91], v40, s[56:59], s18 offen offset:0 sc1\n\t"
  "v_mfma_f32_16x16x32_bf16 v[80:83], a[248:251], v[192:195], v[80:83]\n\t"
  "ds_read_b128 a[236:239], v42 offset:7168\n\t"
  "v_mfma_f32_16x16x32_bf16 v[84:87], a[248:251], v[196:199], v[84:87]\n\t"
  "v_mfma_f32_16x16x32_bf16 v[88:91], a[252:255], v[192:195], v[88:91]\n\t"
  "ds_read_b128 a[240:243], v42 offset:8192\n\t"
  "v_mfma_f32_16x16x32_bf16 v[92:95], a[252:255], v[196:199], v[92:95]\n\t"
  "s_waitcnt lgkmcnt(3)\n\t"
  "v_mfma_f32_16x16x32_bf16 v[96:99], a[224:227], v[192:195], v[96:99]\n\t"
  "ds_read_b128 a[244:247], v42 offset:9216\n\t"
  "v_mfma_f32_16x16x32_bf16 v[100:103], a[224:227], v[196:199], v[100:103]\n\t"
  "buffer_load_dwordx4 a[92:95], v40, s[56:59], s18 offen offset:1024 sc1\n\t"
  "v_mfma_f32_16x16x32_bf16 v[104:107], a[228:231], v[192:195], v[104:107]\n\t"
  "s_add_u32 s18, s18, 0x800\n\t"
  "ds_read_b128 a[248:251], v42 offset:10240\n\t"
  "v_mfma_f32_16x16x32_bf16 v[108:111], a[228:231], v[196:199], v[108:111]\n\t"
  "s_waitcnt lgkmcnt(3)\n\t"
  "buffer_load_dwordx4 a[28:31], v10, s[56:59], s42 offen offset:1024 sc1\n\t"
  "v_mfma_f32_16x16x32_bf16 v[112:115], a[232:235], v[192:195], v[112:115]\n\t"
  "ds_read_b128 a[252:255], v42 offset:11264\n\t"
  "v_mfma_f32_16x16x32_bf16 v[116:119], a[232:235], v[196:199], v[116:119]\n\t"
  "v_mfma_f32_16x16x32_bf16 v[64:67], a[236:239], v[200:203], v[64:67]\n\t"
  "ds_read_b128 a[224:227], v42 offset:12288\n\t"
  "v_mfma_f32_16x16x32_bf16 v[68:71], a[236:239], v[204:207], v[68:71]\n\t"
  "s_waitcnt lgkmcnt(3)\n\t"
  "buffer_load_dwordx4 a[168:171], v40, s[56:59], s79 offen offset:0 sc1\n\t"
  "v_mfma_f32_16x16x32_bf16 v[72:75], a[240:243], v[200:203], v[72:75]\n\t"
  "ds_read_b128 a[228:231], v42 offset:13312\n\t"
  "v_mfma_f32_16x16x32_bf16 v[76:79], a[240:243], v[204:207], v[76:79]\n\t"
  "v_mfma_f32_16x16x32_bf16 v[80:83], a[244:247], v[200:203], v[80:83]\n\t"
  "ds_read_b128 a[232:235], v42 offset:14336\n\t"
  "v_mfma_f32_16x16x32_bf16 v[84:87], a[244:247], v[204:207], v[84:87]\n\t"
  "s_waitcnt lgkmcnt(3)\n\t"
  "v_mfma_f32_16x16x32_bf16 v[88:91], a[248:251], v[200:203], v[88:91]\n\t"
  "ds_read_b128 a[236:239], v42 offset:15360\n\t"
  "v_mfma_f32_16x16x32_bf16 v[92:95], a[248:251], v[204:207], v[92:95]\n\t"
  "buffer_load_dwordx4 a[172:175], v40, s[56:59], s79 offen offset:1024 sc1\n\t"
  "v_mfma_f32_16x16x32_bf16 v[96:99], a[252:255], v[200:203], v[96:99]\n\t"
  "ds_read_b128 a[240:243], v42 offset:16384\n\t"
  "v_mfma_f32_16x16x32_bf16 v[100:103], a[252:255], v[204:207], v[100:103]\n\t"
  "s_waitcnt lgkmcnt(3)\n\t"
  "buffer_load_dwordx4 a[36:39], v40, s[56:59], s43 offen offset:0 sc1\n\t"
  "v_mfma_f32_16x16x32_bf16 v[104:107], a[224:227], v[200:203], v[104:107]\n\t"
  "ds_read_b128 a[244:247], v42 offset:17408\n\t"
  "v_mfma_f32_16x16x32_bf16 v[108:111], a[224:227], v[204:207], v[108:111]\n\t"
  "v_mfma_f32_16x16x32_bf16 v[112:115], a[228:231], v[200:203], v[112:115]\n\t"
  "ds_read_b128 a[248:251], v42 offset:18432\n\t"
  "v_mfma_f32_16x16x32_bf16 v[116:119], a[228:231], v[204:207], v[116:119]\n\t"
  "s_waitcnt lgkmcnt(3)\n\t"
  "buffer_load_dwordx4 a[176:179], v40, s[56:59], s79 offen offset:2048 sc1\n\t"
  "v_mfma_f32_16x16x32_bf16 v[64:67], a[232:235], v[208:211], v[64:67]\n\t"
  "ds_read_b128 a[252:255], v42 offset:19456\n\t"
  "v_mfma_f32_16x16x32_bf16 v[68:71], a[232:235], v[212:215], v[68:71]\n\t"
  "v_mfma_f32_16x16x32_bf16 v[72:75], a[236:239], v[208:211], v[72:75]\n\t"
  "ds_read_b128 a[224:227], v42 offset:20480\n\t"
  "v_mfma_f32_16x16x32_bf16 v[76:79], a[236:239], v[212:215], v[76:79]\n\t"
  "s_waitcnt lgkmcnt(3)\n\t"
  "v_mfma_f32_16x16x32_bf16 v[80:83], a[240:243], v[208:211], v[80:83]\n\t"
  "ds_read_b128 a[228:231], v42 offset:21504\n\t"
  "v_mfma_f32_16x16x32_bf16 v[84:87], a[240:243], v[212:215], v[84:87]\n\t"
  "buffer_load_dwordx4 a[180:183], v40, s[56:59], s79 offen offset:3072 sc1\n\t"
  "v_mfma_f32_16x16x32_bf16 v[88:91], a[244:247], v[208:211], v[88:91]\n\t"
  "s_add_u32 s79, s79, 0x1000\n\t"
  "ds_read_b128 a[232:235], v42 offset:22528\n\t"
  "v_mfma_f32_16x16x32_bf16 v[92:95], a[244:247], v[212:215], v[92:95]\n\t"
  "s_waitcnt lgkmcnt(3)\n\t"
  "buffer_load_dwordx4 a[44:47], v10, s[56:59], s43 offen offset:1024 sc1\n\t"
  "v_mfma_f32_16x16x32_bf16 v[96:99], a[248:251], v[208:211], v[96:99]\n\t"
  "ds_read_b128 a[236:239], v42 offset:23552\n\t"
  "v_mfma_f32_16x16x32_bf16 v[100:103], a[248:251], v[212:215], v[100:103]\n\t"
  "v_mfma_f32_16x16x32_bf16 v[104:107], a[252:255], v[208:211], v[104:107]\n\t"
  "ds_read_b128 a[240:243], v42 offset:24576\n\t"
  "v_mfma_f32_16x16x32_bf16 v[108:111], a[252:255], v[212:215], v[108:111]\n\t"
  "s_waitcnt lgkmcnt(3)\n\t"
  "buffer_load_dwordx4 a[184:187], v40, s[56:59], s79 offen offset:0 sc1\n\t"
  "v_mfma_f32_16x16x32_bf16 v[112:115], a[224:227], v[208:211], v[112:115]\n\t"
  "ds_read_b128 a[244:247], v42 offset:25600\n\t"
  "v_mfma_f32_16x16x32_bf16 v[116:119], a[224:227], v[212:215], v[116:119]\n\t"
  "buffer_load_dwordx4 a[96:99], v40, s[56:59], s18 offen offset:0 sc1\n\t"
  "v_mfma_f32_16x16x32_bf16 v[64:67], a[228:231], v[216:219], v[64:67]\n\t"
  "ds_read_b128 a[248:251], v42 offset:26624\n\t"
  "v_mfma_f32_16x16x32_bf16 v[68:71], a[228:231], v[220:223], v[68:71]\n\t"
  "s_waitcnt lgkmcnt(3)\n\t"
  "v_mfma_f32_16x16x32_bf16 v[72:75], a[232:235], v[216:219], v[72:75]\n\t"
  "s_waitcnt vmcnt(14)\n\t"
  "ds_read_b128 a[252:255], v42 offset:27648\n\t"
  "v_mfma_f32_16x16x32_bf16 v[76:79], a[232:235], v[220:223], v[76:79]\n\t"
  "s_barrier\n\t"
  "buffer_load_dwordx4 a[188:191], v40, s[56:59], s79 offen offset:1024 sc1\n\t"
  "v_mfma_f32_16x16x32_bf16 v[80:83], a[236:239], v[216:219], v[80:83]\n\t"
  "ds_read_b128 a[224:227], v42 offset:32768\n\t"
  "v_mfma_f32_16x16x32_bf16 v[84:87], a[236:239], v[220:223], v[84:87]\n\t"
  "s_waitcnt lgkmcnt(3)\n\t"
  "buffer_load_dwordx4 a[100:103], v40, s[56:59], s18 offen offset:1024 sc1\n\t"
  "v_mfma_f32_16x16x32_bf16 v[88:91], a[240:243], v[216:219], v[88:91]\n\t"
  "s_add_u32 s18, s18, 0x800\n\t"
  "ds_read_b128 a[228:231], v42 offset:39936\n\t"
  "v_mfma_f32_16x16x32_bf16 v[92:95], a[240:243], v[220:223], v[92:95]\n\t"
  "v_mfma_f32_16x16x32_bf16 v[96:99], a[244:247], v[216:219], v[96:99]\n\t"
  "ds_read_b128 a[232:235], v42 offset:47104\n\t"
  "v_mfma_f32_16x16x32_bf16 v[100:103], a[244:247], v[220:223], v[100:103]\n\t"
  "s_waitcnt lgkmcnt(3)\n\t"
  "buffer_load_dwordx4 a[192:195], v40, s[56:59], s79 offen offset:2048 sc1\n\t"
  "v_mfma_f32_16x16x32_bf16 v[104:107], a[248:251], v[216:219], v[104:107]\n\t"
  "s_add_u32 s79, s79, 0xc00\n\t"
  "ds_read_b128 a[236:239], v42 offset:54272\n\t"
  "v_mfma_f32_16x16x32_bf16 v[108:111], a[248:251], v[220:223], v[108:111]\n\t"
  "buffer_load_dwordx4 a[196:199], v40, s[56:59], s79 offen offset:0 sc1\n\t"
  "v_mfma_f32_16x16x32_bf16 v[112:115], a[252:255], v[216:219], v[112:115]\n\t"
  "ds_read_b128 a[240:243], v42 offset:33792\n\t"
  "v_mfma_f32_16x16x32_bf16 v[116:119], a[252:255], v[220:223], v[116:119]\n\t"
  "s_waitcnt lgkmcnt(3)\n\t"
  "v_mfma_f32_16x16x32_bf16 v[64:67], a[224:227], v[224:227], v[64:67]\n\t"
  "ds_read_b128 a[244:247], v42 offset:40960\n\t"
  "v_mfma_f32_16x16x32_bf16 v[68:71], a[224:227], v[228:231], v[68:71]\n\t"
  "v_mfma_f32_16x16x32_bf16 v[64:67], a[228:231], v[232:235], v[64:67]\n\t"
  "ds_read_b128 a[248:251], v42 offset:48128\n\t"
  "v_mfma_f32_16x16x32_bf16 v[68:71], a[228:231], v[236:239], v[68:71]\n\t"
  "s_waitcnt lgkmcnt(3)\n\t"
  "v_mfma_f32_16x16x32_bf16 v[64:67], a[232:235], v[240:243], v[64:67]\n\t"
  "s_sub_u32 s91, s19, 2\n\t"
  "ds_read_b128 a[252:255], v42 offset:55296\n\t"
  "v_mfma_f32_16x16x32_bf16 v[68:71], a[232:235], v[244:247], v[68:71]\n\t"
  "s_and_b32 s91, s91, 3\n\t"
  "buffer_load_dwordx4 a[200:203], v40, s[56:59], s79 offen offset:1024 sc1\n\t"
  "v_mfma_f32_16x16x32_bf16 v[64:67], a[236:239], v[248:251], v[64:67]\n\t"
  "s_lshl_b32 s91, s91, 3\n\t"
  "ds_read_b128 a[224:227], v42 offset:34816\n\t"
  "v_mfma_f32_16x16x32_bf16 v[68:71], a[236:239], v[252:255], v[68:71]\n\t"
  "s_waitcnt lgkmcnt(3)\n\t"
  "v_mfma_f32_16x16x32_bf16 v[72:75], a[240:243], v[224:227], v[72:75]\n\t"
  "s_add_u32 s91, s91, s15\n\t"
  "ds_read_b128 a[228:231], v42 offset:41984\n\t"
  "v_mfma_f32_16x16x32_bf16 v[76:79], a[240:243], v[228:231], v[76:79]\n\t"
  "buffer_load_dwordx4 a[204:207], v40, s[56:59], s79 offen offset:2048 sc1\n\t"
  "v_mfma_f32_16x16x32_bf16 v[72:75], a[244:247], v[232:235], v[72:75]\n\t"
  "s_add_u32 s75, s72, 3\n\t"
  "ds_read_b128 a[232:235], v42 offset:49152\n\t"
  "v_mul_f32 v64, v64, v50\n\t"
  "v_mfma_f32_16x16x32_bf16 v[76:79], a[244:247], v[236:239], v[76:79]\n\t"
  "s_waitcnt lgkmcnt(3)\n\t"
  "buffer_load_dwordx2 v[48:49], v13, s[56:59], s91 offen offset:0 sc1\n\t"
  "v_mfma_f32_16x16x32_bf16 v[72:75], a[248:251], v[240:243], v[72:75]\n\t"
  "s_and_b32 s75, s75, 3\n\t"
  "ds_read_b128 a[236:239], v42 offset:56320\n\t"
  "s_mul_i32 s75, s75, 0x1c000\n\t"
  "s_add_u32 s75, s75, s86\n\t"
  "s_add_u32 s78, s75, 0x1000\n\t"
  "v_mfma_f32_16x16x32_bf16 v[76:79], a[248:251], v[244:247], v[76:79]\n\t"
  "v_mul_f32 v65, v65, v50\n\t"
  "v_mul_f32 v66, v66, v50\n\t"
  "v_mfma_f32_16x16x32_bf16 v[72:75], a[252:255], v[248:251], v[72:75]\n\t"
  "ds_read_b128 a[240:243], v42 offset:35840\n\t"
  "v_mul_f32 v67, v67, v50\n\t"
  "v_mfma_f32_16x16x32_bf16 v[76:79], a[252:255], v[252:255], v[76:79]\n\t"
  "s_waitcnt lgkmcnt(3)\n\t"
  "v_mul_f32 v68, v68, v51\n\t"
  "v_mul_f32 v69, v69, v51\n\t"
  "v_mul_f32 v70, v70, v51\n\t"
  "v_mul_f32 v71, v71, v51\n\t"
  "v_cvt_pk_bf16_f32 v64, v64, v65\n\t"
  "v_cvt_pk_bf16_f32 v65, v66, v67\n\t"
  "v_cvt_pk_bf16_f32 v66, v68, v69\n\t"
  "v_cvt_pk_bf16_f32 v67, v70, v71\n\t"
  "buffer_store_dwordx4 v[64:67], v40, s[56:59], s75 offen offset:0 sc1\n\t"
  "v_mfma_f32_16x16x32_bf16 v[80:83], a[224:227], v[224:227], v[80:83]\n\t"
  "ds_read_b128 a[244:247], v42 offset:43008\n\t"
  "v_mul_f32 v72, v72, v50\n\t"
  "v_mfma_f32_16x16x32_bf16 v[84:87], a[224:227], v[228:231], v[84:87]\n\t"
  "buffer_load_dwordx4 a[208:211], v40, s[56:59], s79 offen offset:3072 sc1\n\t"
  "v_mfma_f32_16x16x32_bf16 v[80:83], a[228:231], v[232:235], v[80:83]\n\t"
  "s_add_u32 s79, s79, 0x1000\n\t"
  "ds_read_b128 a[248:251], v42 offset:50176\n\t"
  "v_mul_f32 v73, v73, v50\n\t"
  "v_mfma_f32_16x16x32_bf16 v[84:87], a[228:231], v[236:239], v[84:87]\n\t"
  "s_waitcnt lgkmcnt(3)\n\t"
  "v_mul_f32 v74, v74, v50\n\t"
  "v_mul_f32 v75, v75, v50\n\t"
  "v_mfma_f32_16x16x32_bf16 v[80:83], a[232:235], v[240:243], v[80:83]\n\t"
  "ds_read_b128 a[252:255], v42 offset:57344\n\t"
  "v_mul_f32 v76, v76, v51\n\t"
  "v_mfma_f32_16x16x32_bf16 v[84:87], a[232:235], v[244:247], v[84:87]\n\t"
  "buffer_load_dwordx4 a[212:215], v40, s[56:59], s79 offen offset:0 sc1\n\t"
  "v_mfma_f32_16x16x32_bf16 v[80:83], a[236:239], v[248:251], v[80:83]\n\t"
  "ds_read_b128 a[224:227], v42 offset:36864\n\t"
  "v_mul_f32 v77, v77, v51\n\t"
  "v_mfma_f32_16x16x32_bf16 v[84:87], a[236:239], v[252:255], v[84:87]\n\t"
  "s_waitcnt lgkmcnt(3)\n\t"
  "v_mul_f32 v78, v78, v51\n\t"
  "v_mul_f32 v79, v79, v51\n\t"
  "v_mfma_f32_16x16x32_bf16 v[88:91], a[240:243], v[224:227], v[88:91]\n\t"
  "ds_read_b128 a[228:231], v42 offset:44032\n\t"
  "v_cvt_pk_bf16_f32 v72, v72, v73\n\t"
  "v_mfma_f32_16x16x32_bf16 v[92:95], a[240:243], v[228:231], v[92:95]\n\t"
  "v_cvt_pk_bf16_f32 v73, v74, v75\n\t"
  "v_cvt_pk_bf16_f32 v74, v76, v77\n\t"
  "v_mfma_f32_16x16x32_bf16 v[88:91], a[244:247], v[232:235], v[88:91]\n\t"
  "ds_read_b128 a[232:235], v42 offset:51200\n\t"
  "v_cvt_pk_bf16_f32 v75, v78, v79\n\t"
  "v_mfma_f32_16x16x32_bf16 v[92:95], a[244:247], v[236:239], v[92:95]\n\t"
  "s_waitcnt lgkmcnt(3)\n\t"
  "buffer_store_dwordx4 v[72:75], v40, s[56:59], s75 offen offset:1024 sc1\n\t"
  "v_mfma_f32_16x16x32_bf16 v[88:91], a[248:251], v[240:243], v[88:91]\n\t"
  "ds_read_b128 a[236:239], v42 offset:58368\n\t"
  "v_mul_f32 v80, v80, v50\n\t"
  "v_mfma_f32_16x16x32_bf16 v[92:95], a[248:251], v[244:247], v[92:95]\n\t"
  "buffer_load_dwordx4 a[216:219], v40, s[56:59], s79 offen offset:1024 sc1\n\t"
  "v_mfma_f32_16x16x32_bf16 v[88:91], a[252:255], v[248:251], v[88:91]\n\t"
  "ds_read_b128 a[240:243], v42 offset:37888\n\t"
  "v_mul_f32 v81, v81, v50\n\t"
  "v_mfma_f32_16x16x32_bf16 v[92:95], a[252:255], v[252:255], v[92:95]\n\t"
  "s_waitcnt lgkmcnt(3)\n\t"
  "v_mul_f32 v82, v82, v50\n\t"
  "v_mul_f32 v83, v83, v50\n\t"
  "v_mfma_f32_16x16x32_bf16 v[96:99], a[224:227], v[224:227], v[96:99]\n\t"
  "ds_read_b128 a[244:247], v42 offset:45056\n\t"
  "v_mul_f32 v84, v84, v51\n\t"
  "v_mfma_f32_16x16x32_bf16 v[100:103], a[224:227], v[228:231], v[100:103]\n\t"
  "v_mul_f32 v85, v85, v51\n\t"
  "v_mul_f32 v86, v86, v51\n\t"
  "v_mfma_f32_16x16x32_bf16 v[96:99], a[228:231], v[232:235], v[96:99]\n\t"
  "ds_read_b128 a[248:251], v42 offset:52224\n\t"
  "v_mul_f32 v87, v87, v51\n\t"
  "v_mfma_f32_16x16x32_bf16 v[100:103], a[228:231], v[236:239], v[100:103]\n\t"
  "s_waitcnt lgkmcnt(3)\n\t"
  "v_cvt_pk_bf16_f32 v80, v80, v81\n\t"
  "v_cvt_pk_bf16_f32 v81, v82, v83\n\t"
  "v_cvt_pk_bf16_f32 v82, v84, v85\n\t"
  "v_cvt_pk_bf16_f32 v83, v86, v87\n\t"
  "buffer_store_dwordx4 v[80:83], v40, s[56:59], s75 offen offset:2048 sc1\n\t"
  "v_mfma_f32_16x16x32_bf16 v[96:99], a[232:235], v[240:243], v[96:99]\n\t"
  "ds_read_b128 a[252:255], v42 offset:59392\n\t"
  "v_mul_f32 v88, v88, v50\n\t"
  "v_mfma_f32_16x16x32_bf16 v[100:103], a[232:235], v[244:247], v[100:103]\n\t"
  "buffer_load_dwordx4 a[220:223], v40, s[56:59], s79 offen offset:2048 sc1\n\t"
  "v_mfma_f32_16x16x32_bf16 v[96:99], a[236:239], v[248:251], v[96:99]\n\t"
  "s_add_u32 s79, s79, 0xc00\n\t"
  "ds_read_b128 a[224:227], v42 offset:38912\n\t"
  "v_mul_f32 v89, v89, v50\n\t"
  "v_mfma_f32_16x16x32_bf16 v[100:103], a[236:239], v[252:255], v[100:103]\n\t"
  "s_waitcnt lgkmcnt(3)\n\t"
  "v_mul_f32 v90, v90, v50\n\t"
  "v_mul_f32 v91, v91, v50\n\t"
  "v_mfma_f32_16x16x32_bf16 v[104:107], a[240:243], v[224:227], v[104:107]\n\t"
  "ds_read_b128 a[228:231], v42 offset:46080\n\t"
  "v_mul_f32 v92, v92, v51\n\t"
  "v_mfma_f32_16x16x32_bf16 v[108:111], a[240:243], v[228:231], v[108:111]\n\t"
  "v_mul_f32 v93, v93, v51\n\t"
  "v_mul_f32 v94, v94, v51\n\t"
  "v_mfma_f32_16x16x32_bf16 v[104:107], a[244:247], v[232:235], v[104:107]\n\t"
  "ds_read_b128 a[232:235], v42 offset:53248\n\t"
  "v_mul_f32 v95, v95, v51\n\t"
  "v_mfma_f32_16x16x32_bf16 v[108:111], a[244:247], v[236:239], v[108:111]\n\t"
  "s_waitcnt lgkmcnt(3)\n\t"
  "v_cvt_pk_bf16_f32 v88, v88, v89\n\t"
  "v_cvt_pk_bf16_f32 v89, v90, v91\n\t"
  "v_cvt_pk_bf16_f32 v90, v92, v93\n\t"
  "v_cvt_pk_bf16_f32 v91, v94, v95\n\t"
  "buffer_store_dwordx4 v[88:91], v40, s[56:59], s75 offen offset:3072 sc1\n\t"
  "v_mfma_f32_16x16x32_bf16 v[104:107], a[248:251], v[240:243], v[104:107]\n\t"
  "ds_read_b128 a[236:239], v42 offset:60416\n\t"
  "v_mul_f32 v96, v96, v50\n\t"
  "v_mfma_f32_16x16x32_bf16 v[108:111], a[248:251], v[244:247], v[108:111]\n\t"
  "v_mul_f32 v97, v97, v50\n\t"
  "v_mul_f32 v98, v98, v50\n\t"
  "v_mfma_f32_16x16x32_bf16 v[104:107], a[252:255], v[248:251], v[104:107]\n\t"
  "v_mul_f32 v99, v99, v50\n\t"
  "v_mul_f32 v100, v100, v51\n\t"
  "v_mfma_f32_16x16x32_bf16 v[108:111], a[252:255], v[252:255], v[108:111]\n\t"
  "s_waitcnt lgkmcnt(2)\n\t"
  "v_mul_f32 v101, v101, v51\n\t"
  "v_mul_f32 v102, v102, v51\n\t"
  "v_mul_f32 v103, v103, v51\n\t"
  "v_cvt_pk_bf16_f32 v96, v96, v97\n\t"
  "v_cvt_pk_bf16_f32 v97, v98, v99\n\t"
  "v_cvt_pk_bf16_f32 v98, v100, v101\n\t"
  "v_cvt_pk_bf16_f32 v99, v102, v103\n\t"
  "buffer_store_dwordx4 v[96:99], v40, s[56:59], s78 offen offset:0 sc1\n\t"
  "v_mfma_f32_16x16x32_bf16 v[112:115], a[224:227], v[224:227], v[112:115]\n\t"
  "v_mul_f32 v104, v104, v50\n\t"
  "v_mul_f32 v105, v105, v50\n\t"
  "v_mfma_f32_16x16x32_bf16 v[116:119], a[224:227], v[228:231], v[116:119]\n\t"
  "v_mul_f32 v106, v106, v50\n\t"
  "v_mul_f32 v107, v107, v50\n\t"
  "v_mfma_f32_16x16x32_bf16 v[112:115], a[228:231], v[232:235], v[112:115]\n\t"
  "v_mul_f32 v108, v108, v51\n\t"
  "v_mul_f32 v109, v109, v51\n\t"
  "v_mfma_f32_16x16x32_bf16 v[116:119], a[228:231], v[236:239], v[116:119]\n\t"
  "s_waitcnt lgkmcnt(0)\n\t"
  "v_mul_f32 v110, v110, v51\n\t"
  "v_mul_f32 v111, v111, v51\n\t"
  "v_mfma_f32_16x16x32_bf16 v[112:115], a[232:235], v[240:243], v[112:115]\n\t"
  "v_cvt_pk_bf16_f32 v104, v104, v105\n\t"
  "v_cvt_pk_bf16_f32 v105, v106, v107\n\t"
  "v_mfma_f32_16x16x32_bf16 v[116:119], a[232:235], v[244:247], v[116:119]\n\t"
  "v_cvt_pk_bf16_f32 v106, v108, v109\n\t"
  "v_cvt_pk_bf16_f32 v107, v110, v111\n\t"
  "v_mfma_f32_16x16x32_bf16 v[112:115], a[236:239], v[248:251], v[112:115]\n\t"
  "buffer_store_dwordx4 v[104:107], v40, s[56:59], s78 offen offset:1024 sc1\n\t"
  "v_mfma_f32_16x16x32_bf16 v[116:119], a[236:239], v[252:255], v[116:119]\n\t"
  "s_nop 5\n\t"
  "v_mul_f32 v112, v112, v50\n\t"
  "v_mul_f32 v113, v113, v50\n\t"
  "v_mul_f32 v114, v114, v50\n\t"
  "v_mul_f32 v115, v115, v50\n\t"
  "v_mul_f32 v116, v116, v51\n\t"
  "v_mul_f32 v117, v117, v51\n\t"
  "v_mul_f32 v118, v118, v51\n\t"
  "v_mul_f32 v119, v119, v51\n\t"
  "v_cvt_pk_bf16_f32 v112, v112, v113\n\t"
  "v_cvt_pk_bf16_f32 v113, v114, v115\n\t"
  "v_cvt_pk_bf16_f32 v114, v116, v117\n\t"
  "v_cvt_pk_bf16_f32 v115, v118, v119\n\t"
  "buffer_store_dwordx4 v[112:115], v40, s[56:59], s78 offen offset:2048 sc1\n\t"
  "s_barrier\n\t"
  "s_add_u32 m0, s84, 0x1c00\n\t"
  "s_nop 0\n\t"
  "buffer_load_dwordx4 v44, s[64:67], 0 offen lds\n\t"
  "s_add_u32 m0, s84, 0x9c00\n\t"
  "s_nop 0\n\t"
  "buffer_load_dwordx4 v44, s[64:67], s21 offen lds\n\t"
  "s_waitcnt vmcnt(17)\n\t"
  "v_mfma_f32_16x16x32_bf16 a[112:115], a[168:171], a[88:91], a[112:115]\n\t"
  "v_mfma_f32_16x16x32_bf16 a[116:119], a[168:171], a[92:95], a[116:119]\n\t"
  "v_mfma_f32_16x16x32_bf16 a[120:123], a[172:175], a[88:91], a[120:123]\n\t"
  "v_mfma_f32_16x16x32_bf16 a[124:127], a[172:175], a[92:95], a[124:127]\n\t"
  "v_mfma_f32_16x16x32_bf16 a[128:131], a[176:179], a[88:91], a[128:131]\n\t"
  "v_mfma_f32_16x16x32_bf16 a[132:135], a[176:179], a[92:95], a[132:135]\n\t"
  "v_mfma_f32_16x16x32_bf16 a[136:139], a[180:183], a[88:91], a[136:139]\n\t"
  "v_mfma_f32_16x16x32_bf16 a[140:143], a[180:183], a[92:95], a[140:143]\n\t"
  "v_mfma_f32_16x16x32_bf16 a[144:147], a[184:187], a[88:91], a[144:147]\n\t"
  "v_mfma_f32_16x16x32_bf16 a[148:151], a[184:187], a[92:95], a[148:151]\n\t"
  "v_mfma_f32_16x16x32_bf16 a[152:155], a[188:191], a[88:91], a[152:155]\n\t"
  "v_mfma_f32_16x16x32_bf16 a[156:159], a[188:191], a[92:95], a[156:159]\n\t"
  "v_mfma_f32_16x16x32_bf16 a[160:163], a[192:195], a[88:91], a[160:163]\n\t"
  "v_mfma_f32_16x16x32_bf16 a[164:167], a[192:195], a[92:95], a[164:167]\n\t"
  "s_waitcnt vmcnt(6)\n\t"
  "v_mfma_f32_16x16x32_bf16 a[112:115], a[196:199], a[96:99], a[112:115]\n\t"
  "v_mfma_f32_16x16x32_bf16 a[116:119], a[196:199], a[100:103], a[116:119]\n\t"
  "v_mfma_f32_16x16x32_bf16 a[120:123], a[200:203], a[96:99], a[120:123]\n\t"
  "v_mfma_f32_16x16x32_bf16 a[124:127], a[200:203], a[100:103], a[124:127]\n\t"
  "v_mfma_f32_16x16x32_bf16 a[128:131], a[204:207], a[96:99], a[128:131]\n\t"
  "v_mfma_f32_16x16x32_bf16 a[132:135], a[204:207], a[100:103], a[132:135]\n\t"
  "v_mfma_f32_16x16x32_bf16 a[136:139], a[208:211], a[96:99], a[136:139]\n\t"
  "v_mfma_f32_16x16x32_bf16 a[140:143], a[208:211], a[100:103], a[140:143]\n\t"
  "v_mfma_f32_16x16x32_bf16 a[144:147], a[212:215], a[96:99], a[144:147]\n\t"
  "v_mfma_f32_16x16x32_bf16 a[148:151], a[212:215], a[100:103], a[148:151]\n\t"
  "v_mfma_f32_16x16x32_bf16 a[152:155], a[216:219], a[96:99], a[152:155]\n\t"
  "v_mfma_f32_16x16x32_bf16 a[156:159], a[216:219], a[100:103], a[156:159]\n\t"
  "v_mfma_f32_16x16x32_bf16 a[160:163], a[220:223], a[96:99], a[160:163]\n\t"
  "v_mfma_f32_16x16x32_bf16 a[164:167], a[220:223], a[100:103], a[164:167]\n\t"
  "s_add_u32 s27, s72, 3\n\t"
  "s_and_b32 s27, s27, 3\n\t"
  "s_lshl_b32 s27, s27, 7\n\t"
  "s_add_u32 s27, s27, s13\n\t"
  "s_waitcnt vmcnt(2)\n\t"
  "buffer_store_dwordx2 v[8:9], v12, s[56:59], s27 offen offset:0 sc1\n\t"
  "s_waitcnt vmcnt(14)\n\t"
  "v_cmp_ne_u64 vcc, v[48:49], v[8:9]\n\t"
  "s_nop 1\n\t"
  "s_cbranch_vccnz SPINS2_%=\n\t"
  "SRETS2_%=:\n\t"
  "s_sub_u32 s44, s19, 2\n\t"
  "s_and_b32 s44, s44, 3\n\t"
  "s_nop 0\n\t"
  "s_cbranch_execz TRAMP_%=\n\t"
  "s_mul_i32 s44, s44, 0x1c00\n\t"
  "s_add_u32 s44, s44, s16\n\t"
  "s_add_u32 s45, s44, 0x7000\n\t"
  "s_add_u32 s46, s45, 0x7000\n\t"
  "s_add_u32 s47, s46, 0x7000\n\t"
  "buffer_load_dwordx4 v[176:179], v40, s[56:59], s44 offen offset:0 sc1\n\t"
  "s_waitcnt vmcnt(2)\n\t"
  "ds_read_b64 v[120:121], v45 offset:0\n\t"
  "buffer_load_dwordx4 v[180:183], v10, s[56:59], s44 offen offset:1024 sc1\n\t"
  "ds_read_b64 v[122:123], v45 offset:32768\n\t"
  "ds_read_b64 v[124:125], v46 offset:0\n\t"
  "buffer_load_dwordx4 v[184:187], v40, s[56:59], s45 offen offset:0 sc1\n\t"
  "ds_read_b64 v[126:127], v46 offset:32768\n\t"
  "ds_write_b128 v47, a[112:115] offset:0\n\t"
  "buffer_load_dwordx4 v[188:191], v10, s[56:59], s45 offen offset:1024 sc1\n\t"
  "ds_write_b128 v47, a[116:119] offset:1024\n\t"
  "ds_write_b128 v47, a[120:123] offset:2048\n\t"
  "ds_write_b128 v47, a[124:127] offset:3072\n\t"
  "s_sub_u32 s91, s19, 3\n\t"
  "s_and_b32 s91, s91, 3\n\t"
  "s_lshl_b32 s91, s91, 3\n\t"
  "s_add_u32 s91, s91, s15\n\t"
  "buffer_load_dwordx2 v[48:49], v13, s[56:59], s91 offen offset:0 sc1\n\t"
  "buffer_load_dwordx4 v[192:195], v40, s[56:59], s46 offen offset:0 sc1\n\t"
  "ds_write_b128 v47, a[128:131] offset:4096\n\t"
  "ds_write_b128 v47, a[132:135] offset:5120\n\t"
  "buffer_load_dwordx4 v[196:199], v10, s[56:59], s46 offen offset:1024 sc1\n\t"
  "ds_write_b128 v47, a[136:139] offset:6144\n\t"
  "ds_write_b128 v47, a[140:143] offset:7168\n\t"
  "ds_write_b128 v47, a[144:147] offset:32768\n\t"
  "buffer_load_dwordx4 v[200:203], v40, s[56:59], s47 offen offset:0 sc1\n\t"
  "ds_write_b128 v47, a[148:151] offset:33792\n\t"
  "ds_write_b128 v47, a[152:155] offset:34816\n\t"
  "buffer_load_dwordx4 v[204:207], v10, s[56:59], s47 offen offset:1024 sc1\n\t"
  "ds_write_b128 v47, a[156:159] offset:35840\n\t"
  "ds_write_b128 v47, a[160:163] offset:36864\n\t"
  "ds_write_b128 v47, a[164:167] offset:37888\n\t"
  "s_waitcnt vmcnt(53)\n\t"
  "v_mfma_f32_16x16x32_bf16 v[16:19], v[20:23], a[104:107], v[16:19]\n\t"
  "v_mfma_f32_16x16x32_bf16 v[36:39], v[24:27], a[104:107], v[36:39]\n\t"
  "s_waitcnt vmcnt(49)\n\t"
  "v_mfma_f32_16x16x32_bf16 v[28:31], v[20:23], a[108:111], v[28:31]\n\t"
  "v_mfma_f32_16x16x32_bf16 v[52:55], v[24:27], a[108:111], v[52:55]\n\t"
  "s_waitcnt vmcnt(46)\n\t"
  "v_mfma_f32_16x16x32_bf16 v[16:19], v[20:23], a[4:7], v[16:19]\n\t"
  "v_mfma_f32_16x16x32_bf16 v[36:39], v[24:27], a[4:7], v[36:39]\n\t"
  "s_waitcnt vmcnt(43)\n\t"
  "v_mfma_f32_16x16x32_bf16 v[28:31], v[20:23], a[12:15], v[28:31]\n\t"
  "v_mfma_f32_16x16x32_bf16 v[52:55], v[24:27], a[12:15], v[52:55]\n\t"
  "s_waitcnt vmcnt(41)\n\t"
  "v_mfma_f32_16x16x32_bf16 v[16:19], v[20:23], a[20:23], v[16:19]\n\t"
  "v_mfma_f32_16x16x32_bf16 v[36:39], v[24:27], a[20:23], v[36:39]\n\t"
  "s_waitcnt vmcnt(38)\n\t"
  "v_mfma_f32_16x16x32_bf16 v[28:31], v[20:23], a[28:31], v[28:31]\n\t"
  "v_mfma_f32_16x16x32_bf16 v[52:55], v[24:27], a[28:31], v[52:55]\n\t"
  "s_waitcnt vmcnt(35)\n\t"
  "v_mfma_f32_16x16x32_bf16 v[16:19], v[20:23], a[36:39], v[16:19]\n\t"
  "v_mfma_f32_16x16x32_bf16 v[36:39], v[24:27], a[36:39], v[36:39]\n\t"
  "s_waitcnt vmcnt(32)\n\t"
  "v_mfma_f32_16x16x32_bf16 v[28:31], v[20:23], a[44:47], v[28:31]\n\t"
  "v_mfma_f32_16x16x32_bf16 v[52:55], v[24:27], a[44:47], v[52:55]\n\t"
  "s_waitcnt lgkmcnt(14)\n\t"
  "v_lshlrev_b32 v128, 16, v120\n\t"
  "v_and_b32 v129, s83, v120\n\t"
  "v_lshlrev_b32 v130, 16, v121\n\t"
  "v_and_b32 v131, s83, v121\n\t"
  "v_lshlrev_b32 v132, 16, v122\n\t"
  "v_and_b32 v133, s83, v122\n\t"
  "v_lshlrev_b32 v134, 16, v123\n\t"
  "v_and_b32 v135, s83, v123\n\t"
  "v_lshlrev_b32 v136, 16, v124\n\t"
  "v_and_b32 v137, s83, v124\n\t"
  "v_lshlrev_b32 v138, 16, v125\n\t"
  "v_and_b32 v139, s83, v125\n\t"
  "v_lshlrev_b32 v140, 16, v126\n\t"
  "v_and_b32 v141, s83, v126\n\t"
  "v_lshlrev_b32 v142, 16, v127\n\t"
  "v_and_b32 v143, s83, v127\n\t"
  "s_waitcnt lgkmcnt(0)\n\t"
  "s_barrier\n\t"
  "ds_read_b128 v[64:67], v35 offset:0\n\t"
  "ds_read_b128 v[68:71], v35 offset:8192\n\t"
  "ds_read_b128 v[72:75], v35 offset:16384\n\t"
  "ds_read_b128 v[76:79], v35 offset:24576\n\t"
  "ds_read_b128 v[80:83], v35 offset:1024\n\t"
  "ds_read_b128 v[84:87], v35 offset:9216\n\t"
  "ds_read_b128 v[88:91], v35 offset:17408\n\t"
  "ds_read_b128 v[92:95], v35 offset:25600\n\t"
  "ds_read_b128 v[96:99], v35 offset:2048\n\t"
  "ds_read_b128 v[100:103], v35 offset:10240\n\t"
  "ds_read_b128 v[104:107], v35 offset:18432\n\t"
  "ds_read_b128 v[108:111], v35 offset:26624\n\t"
  "ds_read_b128 v[112:115], v35 offset:3072\n\t"
  "ds_read_b128 v[116:119], v35 offset:11264\n\t"
  "ds_read_b128 v[120:123], v35 offset:19456\n\t"
  "ds_read_b128 v[124:127], v35 offset:27648\n\t"
  "s_waitcnt vmcnt(4)\n\t"
  "v_cmp_ne_u64 vcc, v[48:49], v[8:9]\n\t"
  "s_nop 1\n\t"
  "s_cbranch_vccnz SPINS3_%=\n\t"
  "SRETS3_%=:\n\t"
  "s_sub_u32 s36, s19, 3\n\t"
  "s_and_b32 s36, s36, 3\n\t"
  "s_mul_i32 s36, s36, 0x1c00\n\t"
  "s_add_u32 s36, s36, s16\n\t"
  "s_add_u32 s37, s36, 0x7000\n\t"
  "s_add_u32 s38, s37, 0x7000\n\t"
  "s_add_u32 s39, s38, 0x7000\n\t"
  "buffer_load_dwordx4 v[144:147], v40, s[56:59], s36 offen offset:0 sc1\n\t"
  "buffer_load_dwordx4 v[148:151], v10, s[56:59], s36 offen offset:1024 sc1\n\t"
  "buffer_load_dwordx4 v[152:155], v40, s[56:59], s37 offen offset:0 sc1\n\t"
  "buffer_load_dwordx4 v[156:159], v10, s[56:59], s37 offen offset:1024 sc1\n\t"
  "buffer_load_dwordx4 v[160:163], v40, s[56:59], s38 offen offset:0 sc1\n\t"
  "buffer_load_dwordx4 v[164:167], v10, s[56:59], s38 offen offset:1024 sc1\n\t"
  "buffer_load_dwordx4 v[168:171], v40, s[56:59], s39 offen offset:0 sc1\n\t"
  "buffer_load_dwordx4 v[172:175], v10, s[56:59], s39 offen offset:1024 sc1\n\t"
  "s_waitcnt lgkmcnt(12)\n\t"
  "v_add_f32 v64, v64, v68\n\t"
  "v_add_f32 v65, v65, v69\n\t"
  "v_add_f32 v66, v66, v70\n\t"
  "v_add_f32 v67, v67, v71\n\t"
  "v_add_f32 v72, v72, v76\n\t"
  "v_add_f32 v73, v73, v77\n\t"
  "v_add_f32 v74, v74, v78\n\t"
  "v_add_f32 v75, v75, v79\n\t"
  "v_add_f32 v64, v64, v72\n\t"
  "v_add_f32 v65, v65, v73\n\t"
  "v_add_f32 v66, v66, v74\n\t"
  "v_add_f32 v67, v67, v75\n\t"
  "v_mul_f32 v68, 0xc3317218, v64\n\t"
  "v_mul_f32 v69, 0xc3317218, v65\n\t"
  "v_mul_f32 v70, 0xc3317218, v66\n\t"
  "v_mul_f32 v71, 0xc3317218, v67\n\t"
  "v_exp_f32 v68, v68\n\t"
  "v_exp_f32 v69, v69\n\t"
  "v_exp_f32 v70, v70\n\t"
  "v_exp_f32 v71, v71\n\t"
  "v_add_f32 v68, 1.0, v68\n\t"
  "v_add_f32 v69, 1.0, v69\n\t"
  "v_add_f32 v70, 1.0, v70\n\t"
  "v_add_f32 v71, 1.0, v71\n\t"
  "v_rcp_f32 v68, v68\n\t"
  "v_rcp_f32 v69, v69\n\t"
  "v_rcp_f32 v70, v70\n\t"
  "v_rcp_f32 v71, v71\n\t"
  "v_mul_f32 v64, v64, v68\n\t"
  "v_mul_f32 v65, v65, v69\n\t"
  "v_mul_f32 v66, v66, v70\n\t"
  "v_mul_f32 v67, v67, v71\n\t"
  "s_waitcnt lgkmcnt(8)\n\t"
  "v_add_f32 v80, v80, v84\n\t"
  "v_add_f32 v81, v81, v85\n\t"
  "v_add_f32 v82, v82, v86\n\t"
  "v_add_f32 v83, v83, v87\n\t"
  "v_add_f32 v88, v88, v92\n\t"
  "v_add_f32 v89, v89, v93\n\t"
  "v_add_f32 v90, v90, v94\n\t"
  "v_add_f32 v91, v91, v95\n\t"
  "v_add_f32 v80, v80, v88\n\t"
  "v_add_f32 v81, v81, v89\n\t"
  "v_add_f32 v82, v82, v90\n\t"
  "v_add_f32 v83, v83, v91\n\t"
  "v_mul_f32 v84, 0xc3317218, v80\n\t"
  "v_mul_f32 v85, 0xc3317218, v81\n\t"
  "v_mul_f32 v86, 0xc3317218, v82\n\t"
  "v_mul_f32 v87, 0xc3317218, v83\n\t"
  "v_exp_f32 v84, v84\n\t"
  "v_exp_f32 v85, v85\n\t"
  "v_exp_f32 v86, v86\n\t"
  "v_exp_f32 v87, v87\n\t"
  "v_add_f32 v84, 1.0, v84\n\t"
  "v_add_f32 v85, 1.0, v85\n\t"
  "v_add_f32 v86, 1.0, v86\n\t"
  "v_add_f32 v87, 1.0, v87\n\t"
  "v_rcp_f32 v84, v84\n\t"
  "v_rcp_f32 v85, v85\n\t"
  "v_rcp_f32 v86, v86\n\t"
  "v_rcp_f32 v87, v87\n\t"
  "v_mul_f32 v80, v80, v84\n\t"
  "v_mul_f32 v81, v81, v85\n\t"
  "v_mul_f32 v82, v82, v86\n\t"
  "v_mul_f32 v83, v83, v87\n\t"
  "s_waitcnt lgkmcnt(4)\n\t"
  "v_add_f32 v96, v96, v100\n\t"
  "v_add_f32 v97, v97, v101\n\t"
  "v_add_f32 v98, v98, v102\n\t"
  "v_add_f32 v99, v99, v103\n\t"
  "v_add_f32 v104, v104, v108\n\t"
  "v_add_f32 v105, v105, v109\n\t"
  "v_add_f32 v106, v106, v110\n\t"
  "v_add_f32 v107, v107, v111\n\t"
  "v_add_f32 v96, v96, v104\n\t"
  "v_add_f32 v97, v97, v105\n\t"
  "v_add_f32 v98, v98, v106\n\t"
  "v_add_f32 v99, v99, v107\n\t"
  "v_mul_f32 v100, 0xc3317218, v96\n\t"
  "v_mul_f32 v101, 0xc3317218, v97\n\t"
  "v_mul_f32 v102, 0xc3317218, v98\n\t"
  "v_mul_f32 v103, 0xc3317218, v99\n\t"
  "v_exp_f32 v100, v100\n\t"
  "v_exp_f32 v101, v101\n\t"
  "v_exp_f32 v102, v102\n\t"
  "v_exp_f32 v103, v103\n\t"
  "v_add_f32 v100, 1.0, v100\n\t"
  "v_add_f32 v101, 1.0, v101\n\t"
  "v_add_f32 v102, 1.0, v102\n\t"
  "v_add_f32 v103, 1.0, v103\n\t"
  "v_rcp_f32 v100, v100\n\t"
  "v_rcp_f32 v101, v101\n\t"
  "v_rcp_f32 v102, v102\n\t"
  "v_rcp_f32 v103, v103\n\t"
  "v_mul_f32 v96, v96, v100\n\t"
  "v_mul_f32 v97, v97, v101\n\t"
  "v_mul_f32 v98, v98, v102\n\t"
  "v_mul_f32 v99, v99, v103\n\t"
  "s_waitcnt lgkmcnt(0)\n\t"
  "v_add_f32 v112, v112, v116\n\t"
  "v_add_f32 v113, v113, v117\n\t"
  "v_add_f32 v114, v114, v118\n\t"
  "v_add_f32 v115, v115, v119\n\t"
  "v_add_f32 v120, v120, v124\n\t"
  "v_add_f32 v121, v121, v125\n\t"
  "v_add_f32 v122, v122, v126\n\t"
  "v_add_f32 v123, v123, v127\n\t"
  "v_add_f32 v112, v112, v120\n\t"
  "v_add_f32 v113, v113, v121\n\t"
  "v_add_f32 v114, v114, v122\n\t"
  "v_add_f32 v115, v115, v123\n\t"
  "v_mul_f32 v116, 0xc3317218, v112\n\t"
  "v_mul_f32 v117, 0xc3317218, v113\n\t"
  "v_mul_f32 v118, 0xc3317218, v114\n\t"
  "v_mul_f32 v119, 0xc3317218, v115\n\t"
  "v_exp_f32 v116, v116\n\t"
  "v_exp_f32 v117, v117\n\t"
  "v_exp_f32 v118, v118\n\t"
  "v_exp_f32 v119, v119\n\t"
  "v_add_f32 v116, 1.0, v116\n\t"
  "v_add_f32 v117, 1.0, v117\n\t"
  "v_add_f32 v118, 1.0, v118\n\t"
  "v_add_f32 v119, 1.0, v119\n\t"
  "v_rcp_f32 v116, v116\n\t"
  "v_rcp_f32 v117, v117\n\t"
  "v_rcp_f32 v118, v118\n\t"
  "v_rcp_f32 v119, v119\n\t"
  "v_mul_f32 v112, v112, v116\n\t"
  "v_mul_f32 v113, v113, v117\n\t"
  "v_mul_f32 v114, v114, v118\n\t"
  "v_mul_f32 v115, v115, v119\n\t"
  "s_waitcnt vmcnt(16)\n\t"
  "v_mfma_f32_16x16x32_bf16 v[16:19], v[20:23], v[176:179], v[16:19]\n\t"
  "v_mfma_f32_16x16x32_bf16 v[36:39], v[24:27], v[176:179], v[36:39]\n\t"
  "s_waitcnt vmcnt(15)\n\t"
  "v_mfma_f32_16x16x32_bf16 v[28:31], v[20:23], v[180:183], v[28:31]\n\t"
  "v_mfma_f32_16x16x32_bf16 v[52:55], v[24:27], v[180:183], v[52:55]\n\t"
  "s_waitcnt vmcnt(14)\n\t"
  "v_mfma_f32_16x16x32_bf16 v[16:19], v[20:23], v[184:187], v[16:19]\n\t"
  "v_mfma_f32_16x16x32_bf16 v[36:39], v[24:27], v[184:187], v[36:39]\n\t"
  "s_waitcnt vmcnt(13)\n\t"
  "v_mfma_f32_16x16x32_bf16 v[28:31], v[20:23], v[188:191], v[28:31]\n\t"
  "v_mfma_f32_16x16x32_bf16 v[52:55], v[24:27], v[188:191], v[52:55]\n\t"
  "s_waitcnt vmcnt(11)\n\t"
  "v_mfma_f32_16x16x32_bf16 v[16:19], v[20:23], v[192:195], v[16:19]\n\t"
  "v_mfma_f32_16x16x32_bf16 v[36:39], v[24:27], v[192:195], v[36:39]\n\t"
  "s_waitcnt vmcnt(10)\n\t"
  "v_mfma_f32_16x16x32_bf16 v[28:31], v[20:23], v[196:199], v[28:31]\n\t"
  "v_mfma_f32_16x16x32_bf16 v[52:55], v[24:27], v[196:199], v[52:55]\n\t"
  "s_waitcnt vmcnt(9)\n\t"
  "v_mfma_f32_16x16x32_bf16 v[16:19], v[20:23], v[200:203], v[16:19]\n\t"
  "v_mfma_f32_16x16x32_bf16 v[36:39], v[24:27], v[200:203], v[36:39]\n\t"
  "s_waitcnt vmcnt(8)\n\t"
  "v_mfma_f32_16x16x32_bf16 v[28:31], v[20:23], v[204:207], v[28:31]\n\t"
  "v_mfma_f32_16x16x32_bf16 v[52:55], v[24:27], v[204:207], v[52:55]\n\t"
  "s_waitcnt vmcnt(7)\n\t"
  "v_mfma_f32_16x16x32_bf16 v[16:19], v[20:23], v[144:147], v[16:19]\n\t"
  "v_mfma_f32_16x16x32_bf16 v[36:39], v[24:27], v[144:147], v[36:39]\n\t"
  "s_waitcnt vmcnt(6)\n\t"
  "v_mfma_f32_16x16x32_bf16 v[28:31], v[20:23], v[148:151], v[28:31]\n\t"
  "v_mfma_f32_16x16x32_bf16 v[52:55], v[24:27], v[148:151], v[52:55]\n\t"
  "s_waitcnt vmcnt(5)\n\t"
  "v_mfma_f32_16x16x32_bf16 v[16:19], v[20:23], v[152:155], v[16:19]\n\t"
  "v_mfma_f32_16x16x32_bf16 v[36:39], v[24:27], v[152:155], v[36:39]\n\t"
  "s_waitcnt vmcnt(4)\n\t"
  "v_mfma_f32_16x16x32_bf16 v[28:31], v[20:23], v[156:159], v[28:31]\n\t"
  "v_mfma_f32_16x16x32_bf16 v[52:55], v[24:27], v[156:159], v[52:55]\n\t"
  "s_waitcnt vmcnt(3)\n\t"
  "v_mfma_f32_16x16x32_bf16 v[16:19], v[20:23], v[160:163], v[16:19]\n\t"
  "v_mfma_f32_16x16x32_bf16 v[36:39], v[24:27], v[160:163], v[36:39]\n\t"
  "s_waitcnt vmcnt(2)\n\t"
  "v_mfma_f32_16x16x32_bf16 v[28:31], v[20:23], v[164:167], v[28:31]\n\t"
  "v_mfma_f32_16x16x32_bf16 v[52:55], v[24:27], v[164:167], v[52:55]\n\t"
  "s_waitcnt vmcnt(1)\n\t"
  "v_mfma_f32_16x16x32_bf16 v[16:19], v[20:23], v[168:171], v[16:19]\n\t"
  "v_mfma_f32_16x16x32_bf16 v[36:39], v[24:27], v[168:171], v[36:39]\n\t"
  "s_waitcnt vmcnt(0)\n\t"
  "v_mfma_f32_16x16x32_bf16 v[28:31], v[20:23], v[172:175], v[28:31]\n\t"
  "v_mfma_f32_16x16x32_bf16 v[52:55], v[24:27], v[172:175], v[52:55]\n\t"
  "s_nop 3\n\t"
  "v_fma_f32 v64, v64, v16, v128\n\t"
  "v_fma_f32 v65, v65, v17, v129\n\t"
  "v_fma_f32 v66, v66, v18, v130\n\t"
  "v_fma_f32 v67, v67, v19, v131\n\t"
  "v_cvt_pk_bf16_f32 v64, v64, v65\n\t"
  "v_cvt_pk_bf16_f32 v65, v66, v67\n\t"
  "ds_write_b64 v33, v[64:65] offset:0\n\t"
  "v_fma_f32 v80, v80, v36, v132\n\t"
  "v_fma_f32 v81, v81, v37, v133\n\t"
  "v_fma_f32 v82, v82, v38, v134\n\t"
  "v_fma_f32 v83, v83, v39, v135\n\t"
  "v_cvt_pk_bf16_f32 v80, v80, v81\n\t"
  "v_cvt_pk_bf16_f32 v81, v82, v83\n\t"
  "ds_write_b64 v33, v[80:81] offset:1280\n\t"
  "v_fma_f32 v96, v96, v28, v136\n\t"
  "v_fma_f32 v97, v97, v29, v137\n\t"
  "v_fma_f32 v98, v98, v30, v138\n\t"
  "v_fma_f32 v99, v99, v31, v139\n\t"
  "v_cvt_pk_bf16_f32 v96, v96, v97\n\t"
  "v_cvt_pk_bf16_f32 v97, v98, v99\n\t"
  "ds_write_b64 v33, v[96:97] offset:32\n\t"
  "v_fma_f32 v112, v112, v52, v140\n\t"
  "v_fma_f32 v113, v113, v53, v141\n\t"
  "v_fma_f32 v114, v114, v54, v142\n\t"
  "v_fma_f32 v115, v115, v55, v143\n\t"
  "v_cvt_pk_bf16_f32 v112, v112, v113\n\t"
  "v_cvt_pk_bf16_f32 v113, v114, v115\n\t"
  "ds_write_b64 v33, v[112:113] offset:1312\n\t"
  "ds_read_b128 v[160:163], v34 offset:0\n\t"
  "ds_read_b128 v[164:167], v34 offset:1280\n\t"
  "s_waitcnt lgkmcnt(1)\n\t"
  "buffer_store_dwordx4 v[160:163], v32, s[60:63], 0 offen offset:0\n\t"
  "s_waitcnt lgkmcnt(0)\n\t"
  "buffer_store_dwordx4 v[164:167], v32, s[60:63], s21 offen offset:0\n\t"
  "s_or_b32 s89, s10, s11\n\t"
  "s_cmp_lg_u32 s89, 0\n\t"
  "s_cbranch_scc1 EDONE_%=\n\t"
  "v_lshlrev_b32 v1, 5, v0\n\t"
  "s_mov_b32 s90, 12288\n\t"
  "s_mov_b32 s81, 0x40000\n\t"
  "ELOOP_%=:\n\t"
  "buffer_load_dwordx2 v[48:49], v1, s[56:59], s90 offen sc1\n\t"
  "s_waitcnt vmcnt(0)\n\t"
  "v_cmp_ne_u64 vcc, v[48:49], v[8:9]\n\t"
  "s_nop 4\n\t"
  "s_cbranch_vccz ENEXT_%=\n\t"
  "s_sub_u32 s81, s81, 1\n\t"
  "s_cbranch_scc0 ELOOP_%=\n\t"
  "ENEXT_%=:\n\t"
  "s_add_u32 s90, s90, 2048\n\t"
  "s_cmp_lt_u32 s90, 20480\n\t"
  "s_cbranch_scc1 ELOOP_%=\n\t"
  "buffer_store_dwordx2 v[8:9], v12, s[56:59], 0 offen sc1\n\t"
  "EDONE_%=:\n\t"
  "s_waitcnt vmcnt(0) lgkmcnt(0)\n\t"
  "s_endpgm\n\t"
  "SPINK_%=:\n\t"
  "s_mov_b32 s81, 0x40000\n\t"
  "SPLK_%=:\n\t"
  "s_sleep 2\n\t"
  "buffer_load_dwordx2 v[48:49], v11, s[56:59], s41 offen sc1\n\t"
  "s_waitcnt vmcnt(0)\n\t"
  "v_cmp_ne_u64 vcc, v[48:49], v[8:9]\n\t"
  "s_nop 4\n\t"
  "s_cbranch_vccz SRETK_%=\n\t"
  "s_sub_u32 s81, s81, 1\n\t"
  "s_cbranch_scc0 SPLK_%=\n\t"
  "s_branch SRETK_%=\n\t"
  "SPINN_%=:\n\t"
  "s_mov_b32 s81, 0x40000\n\t"
  "SPLN_%=:\n\t"
  "s_sleep 2\n\t"
  "buffer_load_dwordx2 v[48:49], v15, s[56:59], s55 offen sc1\n\t"
  "s_waitcnt vmcnt(0)\n\t"
  "v_cmp_ne_u64 vcc, v[48:49], v[8:9]\n\t"
  "s_nop 4\n\t"
  "s_cbranch_vccz SRETN_%=\n\t"
  "s_sub_u32 s81, s81, 1\n\t"
  "s_cbranch_scc0 SPLN_%=\n\t"
  "s_branch SRETN_%=\n\t"
  "SPINH_%=:\n\t"
  "s_mov_b32 s81, 0x40000\n\t"
  "SPLH_%=:\n\t"
  "s_sleep 2\n\t"
  "buffer_load_dwordx2 v[48:49], v14, s[56:59], s14 offen sc1\n\t"
  "s_waitcnt vmcnt(0)\n\t"
  "v_cmp_ne_u64 vcc, v[48:49], v[8:9]\n\t"
  "s_nop 4\n\t"
  "s_cbranch_vccz SRETH_%=\n\t"
  "s_sub_u32 s81, s81, 1\n\t"
  "s_cbranch_scc0 SPLH_%=\n\t"
  "s_branch SRETH_%=\n\t"
  "SPINW_%=:\n\t"
  "s_mov_b32 s81, 0x40000\n\t"
  "SPLW_%=:\n\t"
  "s_sleep 2\n\t"
  "buffer_load_dwordx2 v[48:49], v24, s[56:59], s54 offen sc1\n\t"
  "s_waitcnt vmcnt(0)\n\t"
  "v_cmp_ne_u64 vcc, v[48:49], v[8:9]\n\t"
  "s_nop 4\n\t"
  "s_cbranch_vccz SRETW_%=\n\t"
  "s_sub_u32 s81, s81, 1\n\t"
  "s_cbranch_scc0 SPLW_%=\n\t"
  "s_branch SRETW_%=\n\t"
  "SPINS0_%=:\n\t"
  "s_mov_b32 s81, 0x40000\n\t"
  "SPLS0_%=:\n\t"
  "s_sleep 2\n\t"
  "buffer_load_dwordx2 v[48:49], v13, s[56:59], s91 offen sc1\n\t"
  "s_waitcnt vmcnt(0)\n\t"
  "v_cmp_ne_u64 vcc, v[48:49], v[8:9]\n\t"
  "s_nop 4\n\t"
  "s_cbranch_vccz SRETS0_%=\n\t"
  "s_sub_u32 s81, s81, 1\n\t"
  "s_cbranch_scc0 SPLS0_%=\n\t"
  "s_branch SRETS0_%=\n\t"
  "SPINS1_%=:\n\t"
  "s_mov_b32 s81, 0x40000\n\t"
  "SPLS1_%=:\n\t"
  "s_sleep 2\n\t"
  "buffer_load_dwordx2 v[48:49], v13, s[56:59], s91 offen sc1\n\t"
  "s_waitcnt vmcnt(0)\n\t"
  "v_cmp_ne_u64 vcc, v[48:49], v[8:9]\n\t"
  "s_nop 4\n\t"
  "s_cbranch_vccz SRETS1_%=\n\t"
  "s_sub_u32 s81, s81, 1\n\t"
  "s_cbranch_scc0 SPLS1_%=\n\t"
  "s_branch SRETS1_%=\n\t"
  "SPINS2_%=:\n\t"
  "s_mov_b32 s81, 0x40000\n\t"
  "SPLS2_%=:\n\t"
  "s_sleep 2\n\t"
  "buffer_load_dwordx2 v[48:49], v13, s[56:59], s91 offen sc1\n\t"
  "s_waitcnt vmcnt(0)\n\t"
  "v_cmp_ne_u64 vcc, v[48:49], v[8:9]\n\t"
  "s_nop 4\n\t"
  "s_cbranch_vccz SRETS2_%=\n\t"
  "s_sub_u32 s81, s81, 1\n\t"
  "s_cbranch_scc0 SPLS2_%=\n\t"
  "s_branch SRETS2_%=\n\t"
  "SPINS3_%=:\n\t"
  "s_mov_b32 s81, 0x40000\n\t"
  "SPLS3_%=:\n\t"
  "s_sleep 2\n\t"
  "buffer_load_dwordx2 v[48:49], v13, s[56:59], s91 offen sc1\n\t"
  "s_waitcnt vmcnt(0)\n\t"
  "v_cmp_ne_u64 vcc, v[48:49], v[8:9]\n\t"
  "s_nop 4\n\t"
  "s_cbranch_vccz SRETS3_%=\n\t"
  "s_sub_u32 s81, s81, 1\n\t"
  "s_cbranch_scc0 SPLS3_%=\n\t"
  "s_branch SRETS3_%=\n\t"
  "STUBT1_%=:\n\t"
  "s_call_b64 s[98:99], RARE1_%=\n\t"
  "s_branch RETT1_%=\n\t"
  "STUBT2_%=:\n\t"
  "s_call_b64 s[98:99], RARE0_%=\n\t"
  "s_branch RETT2_%=\n\t"
  "STUBT3_%=:\n\t"
  "s_call_b64 s[98:99], RARE1_%=\n\t"
  "s_branch RETT3_%=\n\t"
  "STUBT4_%=:\n\t"
  "s_call_b64 s[98:99], RARE0_%=\n\t"
  "s_branch RETT4_%=\n\t"
  "STUBT5_%=:\n\t"
  "s_call_b64 s[98:99], RARE1_%=\n\t"
  "s_branch RETT5_%=\n\t"
  "STUBT6_%=:\n\t"
  "s_call_b64 s[98:99], RARE0_%=\n\t"
  "s_branch RETT6_%=\n\t"
  "STUBT7_%=:\n\t"
  "s_call_b64 s[98:99], RARE1_%=\n\t"
  "s_branch RETT7_%=\n\t"
  "STUBT8_%=:\n\t"
  "s_call_b64 s[98:99], RARE0_%=\n\t"
  "s_branch RETT8_%=\n\t"
  "STUBT9_%=:\n\t"
  "s_call_b64 s[98:99], RARE1_%=\n\t"
  "s_branch RETT9_%=\n\t"
  "STUBT10_%=:\n\t"
  "s_call_b64 s[98:99], RARE0_%=\n\t"
  "s_branch RETT10_%=\n\t"
  "STUBT11_%=:\n\t"
  "s_call_b64 s[98:99], RARE1_%=\n\t"
  "s_branch RETT11_%=\n\t"
  "STUBT12_%=:\n\t"
  "s_call_b64 s[98:99], RARE0_%=\n\t"
  "s_branch RETT12_%=\n\t"
  "STUBT13_%=:\n\t"
  "s_call_b64 s[98:99], RARE1_%=\n\t"
  "s_branch RETT13_%=\n\t"
  "STUBT14_%=:\n\t"
  "s_call_b64 s[98:99], RARE0_%=\n\t"
  "s_branch RETT14_%=\n\t"
  "STUBT15_%=:\n\t"
  "s_call_b64 s[98:99], RARE1_%=\n\t"
  "s_branch RETT15_%=\n\t"
  "RARE0_%=:\n\t"
  "s_nop 7\n\t"
  "s_nop 3\n\t"
  "v_accvgpr_write_b32 a64, v1\n\t"
  "v_accvgpr_write_b32 a65, v2\n\t"
  "v_accvgpr_write_b32 a66, v3\n\t"
  "v_max3_f32 v1, v80, v81, v82\n\t"
  "v_max3_f32 v1, v1, v83, v88\n\t"
  "v_max3_f32 v1, v1, v89, v90\n\t"
  "v_max_f32 v1, v1, v91\n\t"
  "v_mov_b32 v2, v1\n\t"
  "s_nop 1\n\t"
  "v_permlane32_swap_b32 v1, v2\n\t"
  "v_max_f32 v1, v1, v2\n\t"
  "v_mov_b32 v2, v1\n\t"
  "s_nop 1\n\t"
  "v_permlane16_swap_b32 v1, v2\n\t"
  "v_max_f32 v1, v1, v2\n\t"
  "v_max_f32 v1, 0, v1\n\t"
  "v_sub_f32 v112, v112, v1\n\t"
  "v_sub_f32 v113, v113, v1\n\t"
  "v_sub_f32 v114, v114, v1\n\t"
  "v_sub_f32 v115, v115, v1\n\t"
  "v_sub_f32 v80, v80, v1\n\t"
  "v_sub_f32 v81, v81, v1\n\t"
  "v_sub_f32 v82, v82, v1\n\t"
  "v_sub_f32 v83, v83, v1\n\t"
  "v_sub_f32 v88, v88, v1\n\t"
  "v_sub_f32 v89, v89, v1\n\t"
  "v_sub_f32 v90, v90, v1\n\t"
  "v_sub_f32 v91, v91, v1\n\t"
  "v_sub_f32 v96, v96, v1\n\t"
  "v_sub_f32 v97, v97, v1\n\t"
  "v_sub_f32 v98, v98, v1\n\t"
  "v_sub_f32 v99, v99, v1\n\t"
  "v_exp_f32 v3, -v1\n\t"
  "s_nop 0\n\t"
  "v_mul_f32 v56, v56, v3\n\t"
  "v_mul_f32 v57, v57, v3\n\t"
  "v_mul_f32 v58, v58, v3\n\t"
  "v_mul_f32 v59, v59, v3\n\t"
  "s_call_b64 s[68:69], RSC0_%=\n\t"
  "v_max3_f32 v1, v84, v85, v86\n\t"
  "v_max3_f32 v1, v1, v87, v92\n\t"
  "v_max3_f32 v1, v1, v93, v94\n\t"
  "v_max_f32 v1, v1, v95\n\t"
  "v_mov_b32 v2, v1\n\t"
  "s_nop 1\n\t"
  "v_permlane32_swap_b32 v1, v2\n\t"
  "v_max_f32 v1, v1, v2\n\t"
  "v_mov_b32 v2, v1\n\t"
  "s_nop 1\n\t"
  "v_permlane16_swap_b32 v1, v2\n\t"
  "v_max_f32 v1, v1, v2\n\t"
  "v_max_f32 v1, 0, v1\n\t"
  "v_sub_f32 v116, v116, v1\n\t"
  "v_sub_f32 v117, v117, v1\n\t"
  "v_sub_f32 v118, v118, v1\n\t"
  "v_sub_f32 v119, v119, v1\n\t"
  "v_sub_f32 v84, v84, v1\n\t"
  "v_sub_f32 v85, v85, v1\n\t"
  "v_sub_f32 v86, v86, v1\n\t"
  "v_sub_f32 v87, v87, v1\n\t"
  "v_sub_f32 v92, v92, v1\n\t"
  "v_sub_f32 v93, v93, v1\n\t"
  "v_sub_f32 v94, v94, v1\n\t"
  "v_sub_f32 v95, v95, v1\n\t"
  "v_sub_f32 v100, v100, v1\n\t"
  "v_sub_f32 v101, v101, v1\n\t"
  "v_sub_f32 v102, v102, v1\n\t"
  "v_sub_f32 v103, v103, v1\n\t"
  "v_exp_f32 v3, -v1\n\t"
  "s_nop 0\n\t"
  "v_mul_f32 v60, v60, v3\n\t"
  "v_mul_f32 v61, v61, v3\n\t"
  "v_mul_f32 v62, v62, v3\n\t"
  "v_mul_f32 v63, v63, v3\n\t"
  "s_call_b64 s[68:69], RSC1_%=\n\t"
  "v_accvgpr_read_b32 v1, a64\n\t"
  "v_accvgpr_read_b32 v2, a65\n\t"
  "v_accvgpr_read_b32 v3, a66\n\t"
  "s_nop 3\n\t"
  "s_setpc_b64 s[98:99]\n\t"
  "RARE1_%=:\n\t"
  "s_nop 7\n\t"
  "s_nop 3\n\t"
  "v_accvgpr_write_b32 a64, v1\n\t"
  "v_accvgpr_write_b32 a65, v2\n\t"
  "v_accvgpr_write_b32 a66, v3\n\t"
  "v_max3_f32 v1, v96, v97, v98\n\t"
  "v_max3_f32 v1, v1, v99, v104\n\t"
  "v_max3_f32 v1, v1, v105, v106\n\t"
  "v_max_f32 v1, v1, v107\n\t"
  "v_mov_b32 v2, v1\n\t"
  "s_nop 1\n\t"
  "v_permlane32_swap_b32 v1, v2\n\t"
  "v_max_f32 v1, v1, v2\n\t"
  "v_mov_b32 v2, v1\n\t"
  "s_nop 1\n\t"
  "v_permlane16_swap_b32 v1, v2\n\t"
  "v_max_f32 v1, v1, v2\n\t"
  "v_max_f32 v1, 0, v1\n\t"
  "v_sub_f32 v112, v112, v1\n\t"
  "v_sub_f32 v113, v113, v1\n\t"
  "v_sub_f32 v114, v114, v1\n\t"
  "v_sub_f32 v115, v115, v1\n\t"
  "v_sub_f32 v96, v96, v1\n\t"
  "v_sub_f32 v97, v97, v1\n\t"
  "v_sub_f32 v98, v98, v1\n\t"
  "v_sub_f32 v99, v99, v1\n\t"
  "v_sub_f32 v104, v104, v1\n\t"
  "v_sub_f32 v105, v105, v1\n\t"
  "v_sub_f32 v106, v106, v1\n\t"
  "v_sub_f32 v107, v107, v1\n\t"
  "v_sub_f32 v80, v80, v1\n\t"
  "v_sub_f32 v81, v81, v1\n\t"
  "v_sub_f32 v82, v82, v1\n\t"
  "v_sub_f32 v83, v83, v1\n\t"
  "v_exp_f32 v3, -v1\n\t"
  "s_nop 0\n\t"
  "v_mul_f32 v56, v56, v3\n\t"
  "v_mul_f32 v57, v57, v3\n\t"
  "v_mul_f32 v58, v58, v3\n\t"
  "v_mul_f32 v59, v59, v3\n\t"
  "s_call_b64 s[68:69], RSC0_%=\n\t"
  "v_max3_f32 v1, v100, v101, v102\n\t"
  "v_max3_f32 v1, v1, v103, v108\n\t"
  "v_max3_f32 v1, v1, v109, v110\n\t"
  "v_max_f32 v1, v1, v111\n\t"
  "v_mov_b32 v2, v1\n\t"
  "s_nop 1\n\t"
  "v_permlane32_swap_b32 v1, v2\n\t"
  "v_max_f32 v1, v1, v2\n\t"
  "v_mov_b32 v2, v1\n\t"
  "s_nop 1\n\t"
  "v_permlane16_swap_b32 v1, v2\n\t"
  "v_max_f32 v1, v1, v2\n\t"
  "v_max_f32 v1, 0, v1\n\t"
  "v_sub_f32 v116, v116, v1\n\t"
  "v_sub_f32 v117, v117, v1\n\t"
  "v_sub_f32 v118, v118, v1\n\t"
  "v_sub_f32 v119, v119, v1\n\t"
  "v_sub_f32 v100, v100, v1\n\t"
  "v_sub_f32 v101, v101, v1\n\t"
  "v_sub_f32 v102, v102, v1\n\t"
  "v_sub_f32 v103, v103, v1\n\t"
  "v_sub_f32 v108, v108, v1\n\t"
  "v_sub_f32 v109, v109, v1\n\t"
  "v_sub_f32 v110, v110, v1\n\t"
  "v_sub_f32 v111, v111, v1\n\t"
  "v_sub_f32 v84, v84, v1\n\t"
  "v_sub_f32 v85, v85, v1\n\t"
  "v_sub_f32 v86, v86, v1\n\t"
  "v_sub_f32 v87, v87, v1\n\t"
  "v_exp_f32 v3, -v1\n\t"
  "s_nop 0\n\t"
  "v_mul_f32 v60, v60, v3\n\t"
  "v_mul_f32 v61, v61, v3\n\t"
  "v_mul_f32 v62, v62, v3\n\t"
  "v_mul_f32 v63, v63, v3\n\t"
  "s_call_b64 s[68:69], RSC1_%=\n\t"
  "v_accvgpr_read_b32 v1, a64\n\t"
  "v_accvgpr_read_b32 v2, a65\n\t"
  "v_accvgpr_read_b32 v3, a66\n\t"
  "s_nop 3\n\t"
  "s_setpc_b64 s[98:99]\n\t"
  "RSC0_%=:\n\t"
  "v_lshlrev_b32 v1, 16, v128\n\t"
  "v_and_b32 v2, s83, v128\n\t"
  "v_mul_f32 v1, v1, v3\n\t"
  "v_mul_f32 v2, v2, v3\n\t"
  "v_cvt_pk_bf16_f32 v128, v1, v2\n\t"
  "v_lshlrev_b32 v1, 16, v129\n\t"
  "v_and_b32 v2, s83, v129\n\t"
  "v_mul_f32 v1, v1, v3\n\t"
  "v_mul_f32 v2, v2, v3\n\t"
  "v_cvt_pk_bf16_f32 v129, v1, v2\n\t"
  "v_lshlrev_b32 v1, 16, v130\n\t"
  "v_and_b32 v2, s83, v130\n\t"
  "v_mul_f32 v1, v1, v3\n\t"
  "v_mul_f32 v2, v2, v3\n\t"
  "v_cvt_pk_bf16_f32 v130, v1, v2\n\t"
  "v_lshlrev_b32 v1, 16, v131\n\t"
  "v_and_b32 v2, s83, v131\n\t"
  "v_mul_f32 v1, v1, v3\n\t"
  "v_mul_f32 v2, v2, v3\n\t"
  "v_cvt_pk_bf16_f32 v131, v1, v2\n\t"
  "v_lshlrev_b32 v1, 16, v136\n\t"
  "v_and_b32 v2, s83, v136\n\t"
  "v_mul_f32 v1, v1, v3\n\t"
  "v_mul_f32 v2, v2, v3\n\t"
  "v_cvt_pk_bf16_f32 v136, v1, v2\n\t"
  "v_lshlrev_b32 v1, 16, v137\n\t"
  "v_and_b32 v2, s83, v137\n\t"
  "v_mul_f32 v1, v1, v3\n\t"
  "v_mul_f32 v2, v2, v3\n\t"
  "v_cvt_pk_bf16_f32 v137, v1, v2\n\t"
  "v_lshlrev_b32 v1, 16, v138\n\t"
  "v_and_b32 v2, s83, v138\n\t"
  "v_mul_f32 v1, v1, v3\n\t"
  "v_mul_f32 v2, v2, v3\n\t"
  "v_cvt_pk_bf16_f32 v138, v1, v2\n\t"
  "v_lshlrev_b32 v1, 16, v139\n\t"
  "v_and_b32 v2, s83, v139\n\t"
  "v_mul_f32 v1, v1, v3\n\t"
  "v_mul_f32 v2, v2, v3\n\t"
  "v_cvt_pk_bf16_f32 v139, v1, v2\n\t"
  "v_lshlrev_b32 v1, 16, v144\n\t"
  "v_and_b32 v2, s83, v144\n\t"
  "v_mul_f32 v1, v1, v3\n\t"
  "v_mul_f32 v2, v2, v3\n\t"
  "v_cvt_pk_bf16_f32 v144, v1, v2\n\t"
  "v_lshlrev_b32 v1, 16, v145\n\t"
  "v_and_b32 v2, s83, v145\n\t"
  "v_mul_f32 v1, v1, v3\n\t"
  "v_mul_f32 v2, v2, v3\n\t"
  "v_cvt_pk_bf16_f32 v145, v1, v2\n\t"
  "v_lshlrev_b32 v1, 16, v146\n\t"
  "v_and_b32 v2, s83, v146\n\t"
  "v_mul_f32 v1, v1, v3\n\t"
  "v_mul_f32 v2, v2, v3\n\t"
  "v_cvt_pk_bf16_f32 v146, v1, v2\n\t"
  "v_lshlrev_b32 v1, 16, v147\n\t"
  "v_and_b32 v2, s83, v147\n\t"
  "v_mul_f32 v1, v1, v3\n\t"
  "v_mul_f32 v2, v2, v3\n\t"
  "v_cvt_pk_bf16_f32 v147, v1, v2\n\t"
  "v_lshlrev_b32 v1, 16, v152\n\t"
  "v_and_b32 v2, s83, v152\n\t"
  "v_mul_f32 v1, v1, v3\n\t"
  "v_mul_f32 v2, v2, v3\n\t"
  "v_cvt_pk_bf16_f32 v152, v1, v2\n\t"
  "v_lshlrev_b32 v1, 16, v153\n\t"
  "v_and_b32 v2, s83, v153\n\t"
  "v_mul_f32 v1, v1, v3\n\t"
  "v_mul_f32 v2, v2, v3\n\t"
  "v_cvt_pk_bf16_f32 v153, v1, v2\n\t"
  "v_lshlrev_b32 v1, 16, v154\n\t"
  "v_and_b32 v2, s83, v154\n\t"
  "v_mul_f32 v1, v1, v3\n\t"
  "v_mul_f32 v2, v2, v3\n\t"
  "v_cvt_pk_bf16_f32 v154, v1, v2\n\t"
  "v_lshlrev_b32 v1, 16, v155\n\t"
  "v_and_b32 v2, s83, v155\n\t"
  "v_mul_f32 v1, v1, v3\n\t"
  "v_mul_f32 v2, v2, v3\n\t"
  "v_cvt_pk_bf16_f32 v155, v1, v2\n\t"
  "v_lshlrev_b32 v1, 16, v160\n\t"
  "v_and_b32 v2, s83, v160\n\t"
  "v_mul_f32 v1, v1, v3\n\t"
  "v_mul_f32 v2, v2, v3\n\t"
  "v_cvt_pk_bf16_f32 v160, v1, v2\n\t"
  "v_lshlrev_b32 v1, 16, v161\n\t"
  "v_and_b32 v2, s83, v161\n\t"
  "v_mul_f32 v1, v1, v3\n\t"
  "v_mul_f32 v2, v2, v3\n\t"
  "v_cvt_pk_bf16_f32 v161, v1, v2\n\t"
  "v_lshlrev_b32 v1, 16, v162\n\t"
  "v_and_b32 v2, s83, v162\n\t"
  "v_mul_f32 v1, v1, v3\n\t"
  "v_mul_f32 v2, v2, v3\n\t"
  "v_cvt_pk_bf16_f32 v162, v1, v2\n\t"
  "v_lshlrev_b32 v1, 16, v163\n\t"
  "v_and_b32 v2, s83, v163\n\t"
  "v_mul_f32 v1, v1, v3\n\t"
  "v_mul_f32 v2, v2, v3\n\t"
  "v_cvt_pk_bf16_f32 v163, v1, v2\n\t"
  "v_lshlrev_b32 v1, 16, v168\n\t"
  "v_and_b32 v2, s83, v168\n\t"
  "v_mul_f32 v1, v1, v3\n\t"
  "v_mul_f32 v2, v2, v3\n\t"
  "v_cvt_pk_bf16_f32 v168, v1, v2\n\t"
  "v_lshlrev_b32 v1, 16, v169\n\t"
  "v_and_b32 v2, s83, v169\n\t"
  "v_mul_f32 v1, v1, v3\n\t"
  "v_mul_f32 v2, v2, v3\n\t"
  "v_cvt_pk_bf16_f32 v169, v1, v2\n\t"
  "v_lshlrev_b32 v1, 16, v170\n\t"
  "v_and_b32 v2, s83, v170\n\t"
  "v_mul_f32 v1, v1, v3\n\t"
  "v_mul_f32 v2, v2, v3\n\t"
  "v_cvt_pk_bf16_f32 v170, v1, v2\n\t"
  "v_lshlrev_b32 v1, 16, v171\n\t"
  "v_and_b32 v2, s83, v171\n\t"
  "v_mul_f32 v1, v1, v3\n\t"
  "v_mul_f32 v2, v2, v3\n\t"
  "v_cvt_pk_bf16_f32 v171, v1, v2\n\t"
  "v_lshlrev_b32 v1, 16, v176\n\t"
  "v_and_b32 v2, s83, v176\n\t"
  "v_mul_f32 v1, v1, v3\n\t"
  "v_mul_f32 v2, v2, v3\n\t"
  "v_cvt_pk_bf16_f32 v176, v1, v2\n\t"
  "v_lshlrev_b32 v1, 16, v177\n\t"
  "v_and_b32 v2, s83, v177\n\t"
  "v_mul_f32 v1, v1, v3\n\t"
  "v_mul_f32 v2, v2, v3\n\t"
  "v_cvt_pk_bf16_f32 v177, v1, v2\n\t"
  "v_lshlrev_b32 v1, 16, v178\n\t"
  "v_and_b32 v2, s83, v178\n\t"
  "v_mul_f32 v1, v1, v3\n\t"
  "v_mul_f32 v2, v2, v3\n\t"
  "v_cvt_pk_bf16_f32 v178, v1, v2\n\t"
  "v_lshlrev_b32 v1, 16, v179\n\t"
  "v_and_b32 v2, s83, v179\n\t"
  "v_mul_f32 v1, v1, v3\n\t"
  "v_mul_f32 v2, v2, v3\n\t"
  "v_cvt_pk_bf16_f32 v179, v1, v2\n\t"
  "v_lshlrev_b32 v1, 16, v184\n\t"
  "v_and_b32 v2, s83, v184\n\t"
  "v_mul_f32 v1, v1, v3\n\t"
  "v_mul_f32 v2, v2, v3\n\t"
  "v_cvt_pk_bf16_f32 v184, v1, v2\n\t"
  "v_lshlrev_b32 v1, 16, v185\n\t"
  "v_and_b32 v2, s83, v185\n\t"
  "v_mul_f32 v1, v1, v3\n\t"
  "v_mul_f32 v2, v2, v3\n\t"
  "v_cvt_pk_bf16_f32 v185, v1, v2\n\t"
  "v_lshlrev_b32 v1, 16, v186\n\t"
  "v_and_b32 v2, s83, v186\n\t"
  "v_mul_f32 v1, v1, v3\n\t"
  "v_mul_f32 v2, v2, v3\n\t"
  "v_cvt_pk_bf16_f32 v186, v1, v2\n\t"
  "v_lshlrev_b32 v1, 16, v187\n\t"
  "v_and_b32 v2, s83, v187\n\t"
  "v_mul_f32 v1, v1, v3\n\t"
  "v_mul_f32 v2, v2, v3\n\t"
  "v_cvt_pk_bf16_f32 v187, v1, v2\n\t"
  "v_lshlrev_b32 v1, 16, v192\n\t"
  "v_and_b32 v2, s83, v192\n\t"
  "v_mul_f32 v1, v1, v3\n\t"
  "s_cbranch_execz TRAMP_%=\n\t"
  "v_mul_f32 v2, v2, v3\n\t"
  "v_cvt_pk_bf16_f32 v192, v1, v2\n\t"
  "v_lshlrev_b32 v1, 16, v193\n\t"
  "v_and_b32 v2, s83, v193\n\t"
  "v_mul_f32 v1, v1, v3\n\t"
  "v_mul_f32 v2, v2, v3\n\t"
  "v_cvt_pk_bf16_f32 v193, v1, v2\n\t"
  "v_lshlrev_b32 v1, 16, v194\n\t"
  "v_and_b32 v2, s83, v194\n\t"
  "v_mul_f32 v1, v1, v3\n\t"
  "v_mul_f32 v2, v2, v3\n\t"
  "v_cvt_pk_bf16_f32 v194, v1, v2\n\t"
  "v_lshlrev_b32 v1, 16, v195\n\t"
  "v_and_b32 v2, s83, v195\n\t"
  "v_mul_f32 v1, v1, v3\n\t"
  "v_mul_f32 v2, v2, v3\n\t"
  "v_cvt_pk_bf16_f32 v195, v1, v2\n\t"
  "v_lshlrev_b32 v1, 16, v200\n\t"
  "v_and_b32 v2, s83, v200\n\t"
  "v_mul_f32 v1, v1, v3\n\t"
  "v_mul_f32 v2, v2, v3\n\t"
  "v_cvt_pk_bf16_f32 v200, v1, v2\n\t"
  "v_lshlrev_b32 v1, 16, v201\n\t"
  "v_and_b32 v2, s83, v201\n\t"
  "v_mul_f32 v1, v1, v3\n\t"
  "v_mul_f32 v2, v2, v3\n\t"
  "v_cvt_pk_bf16_f32 v201, v1, v2\n\t"
  "v_lshlrev_b32 v1, 16, v202\n\t"
  "v_and_b32 v2, s83, v202\n\t"
  "v_mul_f32 v1, v1, v3\n\t"
  "v_mul_f32 v2, v2, v3\n\t"
  "v_cvt_pk_bf16_f32 v202, v1, v2\n\t"
  "v_lshlrev_b32 v1, 16, v203\n\t"
  "v_and_b32 v2, s83, v203\n\t"
  "v_mul_f32 v1, v1, v3\n\t"
  "v_mul_f32 v2, v2, v3\n\t"
  "v_cvt_pk_bf16_f32 v203, v1, v2\n\t"
  "v_lshlrev_b32 v1, 16, v208\n\t"
  "v_and_b32 v2, s83, v208\n\t"
  "v_mul_f32 v1, v1, v3\n\t"
  "v_mul_f32 v2, v2, v3\n\t"
  "v_cvt_pk_bf16_f32 v208, v1, v2\n\t"
  "v_lshlrev_b32 v1, 16, v209\n\t"
  "v_and_b32 v2, s83, v209\n\t"
  "v_mul_f32 v1, v1, v3\n\t"
  "v_mul_f32 v2, v2, v3\n\t"
  "v_cvt_pk_bf16_f32 v209, v1, v2\n\t"
  "v_lshlrev_b32 v1, 16, v210\n\t"
  "v_and_b32 v2, s83, v210\n\t"
  "v_mul_f32 v1, v1, v3\n\t"
  "v_mul_f32 v2, v2, v3\n\t"
  "v_cvt_pk_bf16_f32 v210, v1, v2\n\t"
  "v_lshlrev_b32 v1, 16, v211\n\t"
  "v_and_b32 v2, s83, v211\n\t"
  "v_mul_f32 v1, v1, v3\n\t"
  "v_mul_f32 v2, v2, v3\n\t"
  "v_cvt_pk_bf16_f32 v211, v1, v2\n\t"
  "v_lshlrev_b32 v1, 16, v216\n\t"
  "v_and_b32 v2, s83, v216\n\t"
  "v_mul_f32 v1, v1, v3\n\t"
  "v_mul_f32 v2, v2, v3\n\t"
  "v_cvt_pk_bf16_f32 v216, v1, v2\n\t"
  "v_lshlrev_b32 v1, 16, v217\n\t"
  "v_and_b32 v2, s83, v217\n\t"
  "v_mul_f32 v1, v1, v3\n\t"
  "v_mul_f32 v2, v2, v3\n\t"
  "v_cvt_pk_bf16_f32 v217, v1, v2\n\t"
  "v_lshlrev_b32 v1, 16, v218\n\t"
  "v_and_b32 v2, s83, v218\n\t"
  "v_mul_f32 v1, v1, v3\n\t"
  "v_mul_f32 v2, v2, v3\n\t"
  "v_cvt_pk_bf16_f32 v218, v1, v2\n\t"
  "v_lshlrev_b32 v1, 16, v219\n\t"
  "v_and_b32 v2, s83, v219\n\t"
  "v_mul_f32 v1, v1, v3\n\t"
  "v_mul_f32 v2, v2, v3\n\t"
  "v_cvt_pk_bf16_f32 v219, v1, v2\n\t"
  "v_lshlrev_b32 v1, 16, v224\n\t"
  "v_and_b32 v2, s83, v224\n\t"
  "v_mul_f32 v1, v1, v3\n\t"
  "v_mul_f32 v2, v2, v3\n\t"
  "v_cvt_pk_bf16_f32 v224, v1, v2\n\t"
  "v_lshlrev_b32 v1, 16, v225\n\t"
  "v_and_b32 v2, s83, v225\n\t"
  "v_mul_f32 v1, v1, v3\n\t"
  "v_mul_f32 v2, v2, v3\n\t"
  "v_cvt_pk_bf16_f32 v225, v1, v2\n\t"
  "v_lshlrev_b32 v1, 16, v226\n\t"
  "v_and_b32 v2, s83, v226\n\t"
  "v_mul_f32 v1, v1, v3\n\t"
  "v_mul_f32 v2, v2, v3\n\t"
  "v_cvt_pk_bf16_f32 v226, v1, v2\n\t"
  "v_lshlrev_b32 v1, 16, v227\n\t"
  "v_and_b32 v2, s83, v227\n\t"
  "v_mul_f32 v1, v1, v3\n\t"
  "v_mul_f32 v2, v2, v3\n\t"
  "v_cvt_pk_bf16_f32 v227, v1, v2\n\t"
  "v_lshlrev_b32 v1, 16, v232\n\t"
  "v_and_b32 v2, s83, v232\n\t"
  "v_mul_f32 v1, v1, v3\n\t"
  "v_mul_f32 v2, v2, v3\n\t"
  "v_cvt_pk_bf16_f32 v232, v1, v2\n\t"
  "v_lshlrev_b32 v1, 16, v233\n\t"
  "v_and_b32 v2, s83, v233\n\t"
  "v_mul_f32 v1, v1, v3\n\t"
  "v_mul_f32 v2, v2, v3\n\t"
  "v_cvt_pk_bf16_f32 v233, v1, v2\n\t"
  "v_lshlrev_b32 v1, 16, v234\n\t"
  "v_and_b32 v2, s83, v234\n\t"
  "v_mul_f32 v1, v1, v3\n\t"
  "v_mul_f32 v2, v2, v3\n\t"
  "v_cvt_pk_bf16_f32 v234, v1, v2\n\t"
  "v_lshlrev_b32 v1, 16, v235\n\t"
  "v_and_b32 v2, s83, v235\n\t"
  "v_mul_f32 v1, v1, v3\n\t"
  "v_mul_f32 v2, v2, v3\n\t"
  "v_cvt_pk_bf16_f32 v235, v1, v2\n\t"
  "v_lshlrev_b32 v1, 16, v240\n\t"
  "v_and_b32 v2, s83, v240\n\t"
  "v_mul_f32 v1, v1, v3\n\t"
  "v_mul_f32 v2, v2, v3\n\t"
  "v_cvt_pk_bf16_f32 v240, v1, v2\n\t"
  "v_lshlrev_b32 v1, 16, v241\n\t"
  "v_and_b32 v2, s83, v241\n\t"
  "v_mul_f32 v1, v1, v3\n\t"
  "v_mul_f32 v2, v2, v3\n\t"
  "v_cvt_pk_bf16_f32 v241, v1, v2\n\t"
  "v_lshlrev_b32 v1, 16, v242\n\t"
  "v_and_b32 v2, s83, v242\n\t"
  "v_mul_f32 v1, v1, v3\n\t"
  "v_mul_f32 v2, v2, v3\n\t"
  "v_cvt_pk_bf16_f32 v242, v1, v2\n\t"
  "v_lshlrev_b32 v1, 16, v243\n\t"
  "v_and_b32 v2, s83, v243\n\t"
  "v_mul_f32 v1, v1, v3\n\t"
  "v_mul_f32 v2, v2, v3\n\t"
  "v_cvt_pk_bf16_f32 v243, v1, v2\n\t"
  "s_setpc_b64 s[68:69]\n\t"
  "RSC1_%=:\n\t"
  "v_lshlrev_b32 v1, 16, v132\n\t"
  "v_and_b32 v2, s83, v132\n\t"
  "v_mul_f32 v1, v1, v3\n\t"
  "v_mul_f32 v2, v2, v3\n\t"
  "v_cvt_pk_bf16_f32 v132, v1, v2\n\t"
  "v_lshlrev_b32 v1, 16, v133\n\t"
  "v_and_b32 v2, s83, v133\n\t"
  "v_mul_f32 v1, v1, v3\n\t"
  "v_mul_f32 v2, v2, v3\n\t"
  "v_cvt_pk_bf16_f32 v133, v1, v2\n\t"
  "v_lshlrev_b32 v1, 16, v134\n\t"
  "v_and_b32 v2, s83, v134\n\t"
  "v_mul_f32 v1, v1, v3\n\t"
  "v_mul_f32 v2, v2, v3\n\t"
  "v_cvt_pk_bf16_f32 v134, v1, v2\n\t"
  "v_lshlrev_b32 v1, 16, v135\n\t"
  "v_and_b32 v2, s83, v135\n\t"
  "v_mul_f32 v1, v1, v3\n\t"
  "v_mul_f32 v2, v2, v3\n\t"
  "v_cvt_pk_bf16_f32 v135, v1, v2\n\t"
  "v_lshlrev_b32 v1, 16, v140\n\t"
  "v_and_b32 v2, s83, v140\n\t"
  "v_mul_f32 v1, v1, v3\n\t"
  "v_mul_f32 v2, v2, v3\n\t"
  "v_cvt_pk_bf16_f32 v140, v1, v2\n\t"
  "v_lshlrev_b32 v1, 16, v141\n\t"
  "v_and_b32 v2, s83, v141\n\t"
  "v_mul_f32 v1, v1, v3\n\t"
  "v_mul_f32 v2, v2, v3\n\t"
  "v_cvt_pk_bf16_f32 v141, v1, v2\n\t"
  "v_lshlrev_b32 v1, 16, v142\n\t"
  "v_and_b32 v2, s83, v142\n\t"
  "v_mul_f32 v1, v1, v3\n\t"
  "v_mul_f32 v2, v2, v3\n\t"
  "v_cvt_pk_bf16_f32 v142, v1, v2\n\t"
  "v_lshlrev_b32 v1, 16, v143\n\t"
  "v_and_b32 v2, s83, v143\n\t"
  "v_mul_f32 v1, v1, v3\n\t"
  "v_mul_f32 v2, v2, v3\n\t"
  "v_cvt_pk_bf16_f32 v143, v1, v2\n\t"
  "v_lshlrev_b32 v1, 16, v148\n\t"
  "v_and_b32 v2, s83, v148\n\t"
  "v_mul_f32 v1, v1, v3\n\t"
  "v_mul_f32 v2, v2, v3\n\t"
  "v_cvt_pk_bf16_f32 v148, v1, v2\n\t"
  "v_lshlrev_b32 v1, 16, v149\n\t"
  "v_and_b32 v2, s83, v149\n\t"
  "v_mul_f32 v1, v1, v3\n\t"
  "v_mul_f32 v2, v2, v3\n\t"
  "v_cvt_pk_bf16_f32 v149, v1, v2\n\t"
  "v_lshlrev_b32 v1, 16, v150\n\t"
  "v_and_b32 v2, s83, v150\n\t"
  "v_mul_f32 v1, v1, v3\n\t"
  "v_mul_f32 v2, v2, v3\n\t"
  "v_cvt_pk_bf16_f32 v150, v1, v2\n\t"
  "v_lshlrev_b32 v1, 16, v151\n\t"
  "v_and_b32 v2, s83, v151\n\t"
  "v_mul_f32 v1, v1, v3\n\t"
  "v_mul_f32 v2, v2, v3\n\t"
  "v_cvt_pk_bf16_f32 v151, v1, v2\n\t"
  "v_lshlrev_b32 v1, 16, v156\n\t"
  "v_and_b32 v2, s83, v156\n\t"
  "v_mul_f32 v1, v1, v3\n\t"
  "v_mul_f32 v2, v2, v3\n\t"
  "v_cvt_pk_bf16_f32 v156, v1, v2\n\t"
  "v_lshlrev_b32 v1, 16, v157\n\t"
  "v_and_b32 v2, s83, v157\n\t"
  "v_mul_f32 v1, v1, v3\n\t"
  "v_mul_f32 v2, v2, v3\n\t"
  "v_cvt_pk_bf16_f32 v157, v1, v2\n\t"
  "v_lshlrev_b32 v1, 16, v158\n\t"
  "v_and_b32 v2, s83, v158\n\t"
  "v_mul_f32 v1, v1, v3\n\t"
  "v_mul_f32 v2, v2, v3\n\t"
  "v_cvt_pk_bf16_f32 v158, v1, v2\n\t"
  "v_lshlrev_b32 v1, 16, v159\n\t"
  "v_and_b32 v2, s83, v159\n\t"
  "v_mul_f32 v1, v1, v3\n\t"
  "v_mul_f32 v2, v2, v3\n\t"
  "v_cvt_pk_bf16_f32 v159, v1, v2\n\t"
  "v_lshlrev_b32 v1, 16, v164\n\t"
  "v_and_b32 v2, s83, v164\n\t"
  "v_mul_f32 v1, v1, v3\n\t"
  "v_mul_f32 v2, v2, v3\n\t"
  "v_cvt_pk_bf16_f32 v164, v1, v2\n\t"
  "v_lshlrev_b32 v1, 16, v165\n\t"
  "v_and_b32 v2, s83, v165\n\t"
  "v_mul_f32 v1, v1, v3\n\t"
  "v_mul_f32 v2, v2, v3\n\t"
  "v_cvt_pk_bf16_f32 v165, v1, v2\n\t"
  "v_lshlrev_b32 v1, 16, v166\n\t"
  "v_and_b32 v2, s83, v166\n\t"
  "v_mul_f32 v1, v1, v3\n\t"
  "v_mul_f32 v2, v2, v3\n\t"
  "v_cvt_pk_bf16_f32 v166, v1, v2\n\t"
  "v_lshlrev_b32 v1, 16, v167\n\t"
  "v_and_b32 v2, s83, v167\n\t"
  "v_mul_f32 v1, v1, v3\n\t"
  "v_mul_f32 v2, v2, v3\n\t"
  "v_cvt_pk_bf16_f32 v167, v1, v2\n\t"
  "v_lshlrev_b32 v1, 16, v172\n\t"
  "v_and_b32 v2, s83, v172\n\t"
  "v_mul_f32 v1, v1, v3\n\t"
  "v_mul_f32 v2, v2, v3\n\t"
  "v_cvt_pk_bf16_f32 v172, v1, v2\n\t"
  "v_lshlrev_b32 v1, 16, v173\n\t"
  "v_and_b32 v2, s83, v173\n\t"
  "v_mul_f32 v1, v1, v3\n\t"
  "v_mul_f32 v2, v2, v3\n\t"
  "v_cvt_pk_bf16_f32 v173, v1, v2\n\t"
  "v_lshlrev_b32 v1, 16, v174\n\t"
  "v_and_b32 v2, s83, v174\n\t"
  "v_mul_f32 v1, v1, v3\n\t"
  "v_mul_f32 v2, v2, v3\n\t"
  "v_cvt_pk_bf16_f32 v174, v1, v2\n\t"
  "v_lshlrev_b32 v1, 16, v175\n\t"
  "v_and_b32 v2, s83, v175\n\t"
  "v_mul_f32 v1, v1, v3\n\t"
  "v_mul_f32 v2, v2, v3\n\t"
  "v_cvt_pk_bf16_f32 v175, v1, v2\n\t"
  "v_lshlrev_b32 v1, 16, v180\n\t"
  "v_and_b32 v2, s83, v180\n\t"
  "v_mul_f32 v1, v1, v3\n\t"
  "v_mul_f32 v2, v2, v3\n\t"
  "v_cvt_pk_bf16_f32 v180, v1, v2\n\t"
  "v_lshlrev_b32 v1, 16, v181\n\t"
  "v_and_b32 v2, s83, v181\n\t"
  "v_mul_f32 v1, v1, v3\n\t"
  "v_mul_f32 v2, v2, v3\n\t"
  "v_cvt_pk_bf16_f32 v181, v1, v2\n\t"
  "v_lshlrev_b32 v1, 16, v182\n\t"
  "v_and_b32 v2, s83, v182\n\t"
  "v_mul_f32 v1, v1, v3\n\t"
  "v_mul_f32 v2, v2, v3\n\t"
  "v_cvt_pk_bf16_f32 v182, v1, v2\n\t"
  "v_lshlrev_b32 v1, 16, v183\n\t"
  "v_and_b32 v2, s83, v183\n\t"
  "v_mul_f32 v1, v1, v3\n\t"
  "v_mul_f32 v2, v2, v3\n\t"
  "v_cvt_pk_bf16_f32 v183, v1, v2\n\t"
  "v_lshlrev_b32 v1, 16, v188\n\t"
  "v_and_b32 v2, s83, v188\n\t"
  "v_mul_f32 v1, v1, v3\n\t"
  "v_mul_f32 v2, v2, v3\n\t"
  "v_cvt_pk_bf16_f32 v188, v1, v2\n\t"
  "v_lshlrev_b32 v1, 16, v189\n\t"
  "v_and_b32 v2, s83, v189\n\t"
  "v_mul_f32 v1, v1, v3\n\t"
  "v_mul_f32 v2, v2, v3\n\t"
  "v_cvt_pk_bf16_f32 v189, v1, v2\n\t"
  "v_lshlrev_b32 v1, 16, v190\n\t"
  "v_and_b32 v2, s83, v190\n\t"
  "v_mul_f32 v1, v1, v3\n\t"
  "v_mul_f32 v2, v2, v3\n\t"
  "v_cvt_pk_bf16_f32 v190, v1, v2\n\t"
  "v_lshlrev_b32 v1, 16, v191\n\t"
  "v_and_b32 v2, s83, v191\n\t"
  "v_mul_f32 v1, v1, v3\n\t"
  "v_mul_f32 v2, v2, v3\n\t"
  "v_cvt_pk_bf16_f32 v191, v1, v2\n\t"
  "v_lshlrev_b32 v1, 16, v196\n\t"
  "v_and_b32 v2, s83, v196\n\t"
  "v_mul_f32 v1, v1, v3\n\t"
  "v_mul_f32 v2, v2, v3\n\t"
  "v_cvt_pk_bf16_f32 v196, v1, v2\n\t"
  "v_lshlrev_b32 v1, 16, v197\n\t"
  "v_and_b32 v2, s83, v197\n\t"
  "v_mul_f32 v1, v1, v3\n\t"
  "v_mul_f32 v2, v2, v3\n\t"
  "v_cvt_pk_bf16_f32 v197, v1, v2\n\t"
  "v_lshlrev_b32 v1, 16, v198\n\t"
  "v_and_b32 v2, s83, v198\n\t"
  "v_mul_f32 v1, v1, v3\n\t"
  "v_mul_f32 v2, v2, v3\n\t"
  "v_cvt_pk_bf16_f32 v198, v1, v2\n\t"
  "v_lshlrev_b32 v1, 16, v199\n\t"
  "v_and_b32 v2, s83, v199\n\t"
  "v_mul_f32 v1, v1, v3\n\t"
  "v_mul_f32 v2, v2, v3\n\t"
  "v_cvt_pk_bf16_f32 v199, v1, v2\n\t"
  "v_lshlrev_b32 v1, 16, v204\n\t"
  "v_and_b32 v2, s83, v204\n\t"
  "v_mul_f32 v1, v1, v3\n\t"
  "v_mul_f32 v2, v2, v3\n\t"
  "v_cvt_pk_bf16_f32 v204, v1, v2\n\t"
  "v_lshlrev_b32 v1, 16, v205\n\t"
  "v_and_b32 v2, s83, v205\n\t"
  "v_mul_f32 v1, v1, v3\n\t"
  "v_mul_f32 v2, v2, v3\n\t"
  "v_cvt_pk_bf16_f32 v205, v1, v2\n\t"
  "v_lshlrev_b32 v1, 16, v206\n\t"
  "v_and_b32 v2, s83, v206\n\t"
  "v_mul_f32 v1, v1, v3\n\t"
  "v_mul_f32 v2, v2, v3\n\t"
  "v_cvt_pk_bf16_f32 v206, v1, v2\n\t"
  "v_lshlrev_b32 v1, 16, v207\n\t"
  "v_and_b32 v2, s83, v207\n\t"
  "v_mul_f32 v1, v1, v3\n\t"
  "v_mul_f32 v2, v2, v3\n\t"
  "v_cvt_pk_bf16_f32 v207, v1, v2\n\t"
  "v_lshlrev_b32 v1, 16, v212\n\t"
  "v_and_b32 v2, s83, v212\n\t"
  "v_mul_f32 v1, v1, v3\n\t"
  "v_mul_f32 v2, v2, v3\n\t"
  "v_cvt_pk_bf16_f32 v212, v1, v2\n\t"
  "v_lshlrev_b32 v1, 16, v213\n\t"
  "v_and_b32 v2, s83, v213\n\t"
  "v_mul_f32 v1, v1, v3\n\t"
  "v_mul_f32 v2, v2, v3\n\t"
  "v_cvt_pk_bf16_f32 v213, v1, v2\n\t"
  "v_lshlrev_b32 v1, 16, v214\n\t"
  "v_and_b32 v2, s83, v214\n\t"
  "v_mul_f32 v1, v1, v3\n\t"
  "v_mul_f32 v2, v2, v3\n\t"
  "v_cvt_pk_bf16_f32 v214, v1, v2\n\t"
  "v_lshlrev_b32 v1, 16, v215\n\t"
  "v_and_b32 v2, s83, v215\n\t"
  "v_mul_f32 v1, v1, v3\n\t"
  "v_mul_f32 v2, v2, v3\n\t"
  "v_cvt_pk_bf16_f32 v215, v1, v2\n\t"
  "v_lshlrev_b32 v1, 16, v220\n\t"
  "v_and_b32 v2, s83, v220\n\t"
  "v_mul_f32 v1, v1, v3\n\t"
  "v_mul_f32 v2, v2, v3\n\t"
  "v_cvt_pk_bf16_f32 v220, v1, v2\n\t"
  "v_lshlrev_b32 v1, 16, v221\n\t"
  "v_and_b32 v2, s83, v221\n\t"
  "v_mul_f32 v1, v1, v3\n\t"
  "v_mul_f32 v2, v2, v3\n\t"
  "v_cvt_pk_bf16_f32 v221, v1, v2\n\t"
  "v_lshlrev_b32 v1, 16, v222\n\t"
  "v_and_b32 v2, s83, v222\n\t"
  "v_mul_f32 v1, v1, v3\n\t"
  "v_mul_f32 v2, v2, v3\n\t"
  "v_cvt_pk_bf16_f32 v222, v1, v2\n\t"
  "v_lshlrev_b32 v1, 16, v223\n\t"
  "v_and_b32 v2, s83, v223\n\t"
  "v_mul_f32 v1, v1, v3\n\t"
  "v_mul_f32 v2, v2, v3\n\t"
  "v_cvt_pk_bf16_f32 v223, v1, v2\n\t"
  "v_lshlrev_b32 v1, 16, v228\n\t"
  "v_and_b32 v2, s83, v228\n\t"
  "v_mul_f32 v1, v1, v3\n\t"
  "v_mul_f32 v2, v2, v3\n\t"
  "v_cvt_pk_bf16_f32 v228, v1, v2\n\t"
  "v_lshlrev_b32 v1, 16, v229\n\t"
  "v_and_b32 v2, s83, v229\n\t"
  "v_mul_f32 v1, v1, v3\n\t"
  "v_mul_f32 v2, v2, v3\n\t"
  "v_cvt_pk_bf16_f32 v229, v1, v2\n\t"
  "v_lshlrev_b32 v1, 16, v230\n\t"
  "v_and_b32 v2, s83, v230\n\t"
  "v_mul_f32 v1, v1, v3\n\t"
  "v_mul_f32 v2, v2, v3\n\t"
  "v_cvt_pk_bf16_f32 v230, v1, v2\n\t"
  "v_lshlrev_b32 v1, 16, v231\n\t"
  "v_and_b32 v2, s83, v231\n\t"
  "v_mul_f32 v1, v1, v3\n\t"
  "v_mul_f32 v2, v2, v3\n\t"
  "v_cvt_pk_bf16_f32 v231, v1, v2\n\t"
  "v_lshlrev_b32 v1, 16, v236\n\t"
  "v_and_b32 v2, s83, v236\n\t"
  "v_mul_f32 v1, v1, v3\n\t"
  "v_mul_f32 v2, v2, v3\n\t"
  "v_cvt_pk_bf16_f32 v236, v1, v2\n\t"
  "v_lshlrev_b32 v1, 16, v237\n\t"
  "v_and_b32 v2, s83, v237\n\t"
  "v_mul_f32 v1, v1, v3\n\t"
  "v_mul_f32 v2, v2, v3\n\t"
  "v_cvt_pk_bf16_f32 v237, v1, v2\n\t"
  "v_lshlrev_b32 v1, 16, v238\n\t"
  "v_and_b32 v2, s83, v238\n\t"
  "v_mul_f32 v1, v1, v3\n\t"
  "v_mul_f32 v2, v2, v3\n\t"
  "v_cvt_pk_bf16_f32 v238, v1, v2\n\t"
  "v_lshlrev_b32 v1, 16, v239\n\t"
  "v_and_b32 v2, s83, v239\n\t"
  "v_mul_f32 v1, v1, v3\n\t"
  "v_mul_f32 v2, v2, v3\n\t"
  "v_cvt_pk_bf16_f32 v239, v1, v2\n\t"
  "v_lshlrev_b32 v1, 16, v244\n\t"
  "v_and_b32 v2, s83, v244\n\t"
  "v_mul_f32 v1, v1, v3\n\t"
  "v_mul_f32 v2, v2, v3\n\t"
  "v_cvt_pk_bf16_f32 v244, v1, v2\n\t"
  "v_lshlrev_b32 v1, 16, v245\n\t"
  "v_and_b32 v2, s83, v245\n\t"
  "v_mul_f32 v1, v1, v3\n\t"
  "v_mul_f32 v2, v2, v3\n\t"
  "v_cvt_pk_bf16_f32 v245, v1, v2\n\t"
  "v_lshlrev_b32 v1, 16, v246\n\t"
  "v_and_b32 v2, s83, v246\n\t"
  "v_mul_f32 v1, v1, v3\n\t"
  "v_mul_f32 v2, v2, v3\n\t"
  "v_cvt_pk_bf16_f32 v246, v1, v2\n\t"
  "v_lshlrev_b32 v1, 16, v247\n\t"
  "v_and_b32 v2, s83, v247\n\t"
  "v_mul_f32 v1, v1, v3\n\t"
  "v_mul_f32 v2, v2, v3\n\t"
  "v_cvt_pk_bf16_f32 v247, v1, v2\n\t"
  "s_setpc_b64 s[68:69]\n\t"
  :: "s"(kp), "s"(blockIdx.x), "s"(wid), "s"(lane_tab) : "memory","vcc","scc","v0","v1","v2","v3","v4","v5","v6","v7","v8","v9","v10","v11","v12","v13","v14","v15","v16","v17","v18","v19","v20","v21","v22","v23","v24","v25","v26","v27","v28","v29","v30","v31","v32","v33","v34","v35","v36","v37","v38","v39","v40","v41","v42","v43","v44","v45","v46","v47","v48","v49","v50","v51","v52","v53","v54","v55","v56","v57","v58","v59","v60","v61","v62","v63","v64","v65","v66","v67","v68","v69","v70","v71","v72","v73","v74","v75","v76","v77","v78","v79","v80","v81","v82","v83","v84","v85","v86","v87","v88","v89","v90","v91","v92","v93","v94","v95","v96","v97","v98","v99","v100","v101","v102","v103","v104","v105","v106","v107","v108","v109","v110","v111","v112","v113","v114","v115","v116","v117","v118","v119","v120","v121","v122","v123","v124","v125","v126","v127","v128","v129","v130","v131","v132","v133","v134","v135","v136","v137","v138","v139","v140","v141","v142","v143","v144","v145","v146","v147","v148","v149","v150","v151","v152","v153","v154","v155","v156","v157","v158","v159","v160","v161","v162","v163","v164","v165","v166","v167","v168","v169","v170","v171","v172","v173","v174","v175","v176","v177","v178","v179","v180","v181","v182","v183","v184","v185","v186","v187","v188","v189","v190","v191","v192","v193","v194","v195","v196","v197","v198","v199","v200","v201","v202","v203","v204","v205","v206","v207","v208","v209","v210","v211","v212","v213","v214","v215","v216","v217","v218","v219","v220","v221","v222","v223","v224","v225","v226","v227","v228","v229","v230","v231","v232","v233","v234","v235","v236","v237","v238","v239","v240","v241","v242","v243","v244","v245","v246","v247","v248","v249","v250","v251","v252","v253","v254","v255","a0","a1","a2","a3","a4","a5","a6","a7","a8","a9","a10","a11","a12","a13","a14","a15","a16","a17","a18","a19","a20","a21","a22","a23","a24","a25","a26","a27","a28","a29","a30","a31","a32","a33","a34","a35","a36","a37","a38","a39","a40","a41","a42","a43","a44","a45","a46","a47","a48","a49","a50","a51","a52","a53","a54","a55","a56","a57","a58","a59","a60","a61","a62","a63","a64","a65","a66","a67","a68","a69","a70","a71","a72","a73","a74","a75","a76","a77","a78","a79","a80","a81","a82","a83","a84","a85","a86","a87","a88","a89","a90","a91","a92","a93","a94","a95","a96","a97","a98","a99","a100","a101","a102","a103","a104","a105","a106","a107","a108","a109","a110","a111","a112","a113","a114","a115","a116","a117","a118","a119","a120","a121","a122","a123","a124","a125","a126","a127","a128","a129","a130","a131","a132","a133","a134","a135","a136","a137","a138","a139","a140","a141","a142","a143","a144","a145","a146","a147","a148","a149","a150","a151","a152","a153","a154","a155","a156","a157","a158","a159","a160","a161","a162","a163","a164","a165","a166","a167","a168","a169","a170","a171","a172","a173","a174","a175","a176","a177","a178","a179","a180","a181","a182","a183","a184","a185","a186","a187","a188","a189","a190","a191","a192","a193","a194","a195","a196","a197","a198","a199","a200","a201","a202","a203","a204","a205","a206","a207","a208","a209","a210","a211","a212","a213","a214","a215","a216","a217","a218","a219","a220","a221","a222","a223","a224","a225","a226","a227","a228","a229","a230","a231","a232","a233","a234","a235","a236","a237","a238","a239","a240","a241","a242","a243","a244","a245","a246","a247","a248","a249","a250","a251","a252","a253","a254","a255","s8","s9","s10","s11","s12","s13","s14","s15","s16","s17","s18","s19","s20","s21","s22","s23","s24","s25","s26","s27","s28","s29","s30","s31","s33","s34","s35","s36","s37","s38","s39","s40","s41","s42","s43","s44","s45","s46","s47","s48","s49","s50","s51","s52","s53","s54","s55","s56","s57","s58","s59","s60","s61","s62","s63","s64","s65","s66","s67","s68","s69","s70","s71","s72","s73","s74","s75","s76","s77","s78","s79","s80","s81","s82","s83","s84","s85","s86","s87","s88","s89","s90","s91","s92","s93","s94","s95","s96","s97","s98","s99");
}
extern "C" void kernel_launch(void* const* d_in, const int* in_sizes, int n_in, void* d_out, int out_size, void* d_ws, size_t ws_size, hipStream_t stream) {
  (void)hipFuncSetAttribute((const void*)fused_k, hipFuncAttributeMaxDynamicSharedMemorySize, 163840);
  hipLaunchKernelGGL(fused_k, dim3(256), dim3(256), 163840, stream, d_in[0], d_in[1], d_in[2], d_in[3], d_in[4], d_in[6], d_in[7], d_in[8], d_out, d_ws);
}
```
